```python
import torch

def _softmax(x, axis=-1):
    return torch.softmax(x, dim=axis)
def _gelu(x):
    return torch.nn.functional.gelu(x, approximate="tanh")
def _silu(x):
    return torch.nn.functional.silu(x)
def _sigmoid(x):
    return torch.sigmoid(x)
def _layernorm(x, axis=-1, eps=1e-5):
    m = x.mean(dim=axis, keepdim=True)
    v = x.var(dim=axis, keepdim=True, unbiased=False)
    return (x - m) / torch.sqrt(v + eps)
def _scatter_add(tgt, idx, src, axis=0):
    return tgt.clone().index_add_(axis, idx, src)
def _segment_sum(src, seg, n):
    out = torch.zeros((n,) + src.shape[1:], dtype=src.dtype)
    return out.index_add_(0, seg, src)
def _pad(x, pw):
    flat = tuple(v for pair in reversed(pw) for v in pair)
    return torch.nn.functional.pad(x, flat)

kernel_name = 'concat_heads__mha_block_cached__gqa_block_cached__near_ridge'

def setup_inputs():

    _w = (lambda a: (lambda t: t.to(torch.bfloat16) if t.is_floating_point() else t)(torch.as_tensor(a)))
    def _A_setup():
        B, Sq, D = 16, 96, 1024
        H, Sk, Dh = 32, 512, 128
        return {
            "x": _w((lambda *s: torch.randn(*s, dtype=torch.bfloat16) * 0.8)(B, Sq, D)),
            "k_cache": _w((lambda *s: torch.randn(*s, dtype=torch.bfloat16) * 0.8)(B, H, Sk, Dh)),
            "v_cache": _w((lambda *s: torch.randn(*s, dtype=torch.bfloat16) * 0.05)(B, H, Sk, Dh)),
            "wq": _w((lambda *s: torch.randn(*s, dtype=torch.bfloat16) * 0.8)(D, H * Dh) * (D ** -0.5)),
            "wo": _w((lambda *s: torch.randn(*s, dtype=torch.bfloat16) * 0.05)(H * Dh, D) * ((H * Dh) ** -0.5)),
        }
    _A_in = _A_setup()

    def _B_setup():
        B, Sq, D = 16, 96, 1024
        Hq, Hkv, Sk, Dh = 32, 32, 512, 128
        assert Hq % Hkv == 0, (Hq, Hkv)
        return {
            "x": _w((lambda *s: torch.randn(*s, dtype=torch.bfloat16) * 0.8)(B, Sq, D)),
            "k_cache": _w((lambda *s: torch.randn(*s, dtype=torch.bfloat16) * 0.8)(B, Hkv, Sk, Dh)),
            "v_cache": _w((lambda *s: torch.randn(*s, dtype=torch.bfloat16) * 0.05)(B, Hkv, Sk, Dh)),
            "wq": _w((lambda *s: torch.randn(*s, dtype=torch.bfloat16) * 0.8)(D, Hq * Dh) * (D ** -0.5)),
            "wo": _w((lambda *s: torch.randn(*s, dtype=torch.bfloat16) * 0.05)(Hq * Dh, D) * ((Hq * Dh) ** -0.5)),
        }
    _B_in = _B_setup()

    return {
        "A_x": _A_in["x"],
        "A_k_cache": _A_in["k_cache"],
        "A_v_cache": _A_in["v_cache"],
        "A_wq": _A_in["wq"],
        "A_wo": _A_in["wo"],
        "B_x": _B_in["x"],
        "B_k_cache": _B_in["k_cache"],
        "B_v_cache": _B_in["v_cache"],
        "B_wq": _B_in["wq"],
        "B_wo": _B_in["wo"],
    }

def reference(A_x, A_k_cache, A_v_cache, A_wq, A_wo, B_x, B_k_cache, B_v_cache, B_wq, B_wo):
    def _A_ref(x, k_cache, v_cache, wq, wo):
        B, Sq, D = x.shape
        H, Dh = 32, 128
        q = (lambda x, *d: x.permute(*d))((x @ wq).reshape(B, Sq, H, Dh), 0, 2, 1, 3)
        scores = torch.einsum("bhqd,bhkd->bhqk", q, k_cache) * (Dh ** -0.5)
        p = _softmax(scores, axis=-1)
        o = torch.einsum("bhqk,bhkd->bhqd", p, v_cache)
        return (lambda x, *d: x.permute(*d))(o, 0, 2, 1, 3).reshape(B, Sq, H * Dh) @ wo

    def _B_ref(x, k_cache, v_cache, wq, wo):
        B, Sq, D = x.shape
        Hq, Dh = 32, 128
        Hkv = k_cache.shape[1]
        rep = Hq // Hkv
        q = (lambda x, *d: x.permute(*d))((x @ wq).reshape(B, Sq, Hq, Dh), 0, 2, 1, 3)
        k = (lambda x, n, axis: torch.repeat_interleave(x, n, dim=axis))(k_cache, rep, axis=1)
        v = (lambda x, n, axis: torch.repeat_interleave(x, n, dim=axis))(v_cache, rep, axis=1)
        scores = torch.einsum("bhqd,bhkd->bhqk", q, k) * (Dh ** -0.5)
        p = _softmax(scores, axis=-1)
        o = torch.einsum("bhqk,bhkd->bhqd", p, v)
        return (lambda x, *d: x.permute(*d))(o, 0, 2, 1, 3).reshape(B, Sq, Hq * Dh) @ wo

    a = _A_ref(A_x, A_k_cache, A_v_cache, A_wq, A_wo)
    b = _B_ref(B_x, B_k_cache, B_v_cache, B_wq, B_wo)
    return torch.concatenate([a, b], axis=-1)
```

```cpp
#include <hip/hip_runtime.h>
#include <hip/hip_bf16.h>
#include <hip/hip_runtime.h>
#include <stdint.h>
typedef unsigned short u16;
__host__ __device__ inline float bf2f(u16 x){ union{uint32_t u; float f;} c; c.u = ((uint32_t)x)<<16; return c.f; }
__host__ __device__ inline u16 f2bf(float f){ union{uint32_t u; float f;} c; c.f=f; uint32_t u=c.u; return (u16)((u + 0x7FFF + ((u>>16)&1))>>16); }

__global__ void ref_gemm(const u16* A, const u16* B, u16* C, int M, int N, int K, int ldc){
  __shared__ float As[16][17], Bs[16][17];
  int tx=threadIdx.x, ty=threadIdx.y; int row=blockIdx.y*16+ty, col=blockIdx.x*16+tx;
  float acc=0;
  for(int k0=0;k0<K;k0+=16){
    As[ty][tx]=bf2f(A[(size_t)row*K+k0+tx]); Bs[ty][tx]=bf2f(B[(size_t)(k0+ty)*N+col]);
    __syncthreads();
    for(int k=0;k<16;k++) acc+=As[ty][k]*Bs[k][tx];
    __syncthreads();
  }
  C[(size_t)row*ldc+col]=f2bf(acc);
}
__global__ void ref_attn(const u16* Q, const u16* K, const u16* V, u16* O, int round_mode){
  int q=blockIdx.x, h=blockIdx.y, b=blockIdx.z; int t=threadIdx.x;
  __shared__ float qs[128], s[512], red[128];
  qs[t]=bf2f(Q[((size_t)(b*96+q))*4096+h*128+t]); __syncthreads();
  const u16* Kp=K+((size_t)(b*32+h))*512*128; const u16* Vp=V+((size_t)(b*32+h))*512*128;
  for(int j=t;j<512;j+=128){ float a=0; for(int d=0;d<128;d++) a+=qs[d]*bf2f(Kp[j*128+d]);
    if(round_mode){ a=bf2f(f2bf(a)); a=bf2f(f2bf(a*0.08838834764831845f)); } else a*=0.08838834764831845f; s[j]=a; }
  __syncthreads();
  float m=-1e30f; for(int j=0;j<512;j++) m=fmaxf(m,s[j]);
  float sum=0; for(int j=0;j<512;j++) sum+=expf(s[j]-m);
  __syncthreads();
  for(int j=t;j<512;j+=128){ float p=expf(s[j]-m)/sum; s[j]= round_mode? bf2f(f2bf(p)) : p; }
  __syncthreads();
  float a=0; for(int j=0;j<512;j++) a+=s[j]*bf2f(Vp[j*128+t]);
  O[((size_t)(b*96+q))*4096+h*128+t]=f2bf(a);
}
inline void ref_forward(void* const* d_in, void* d_out, void* ws, hipStream_t st, int round_mode){
  u16* Q=(u16*)ws; u16* O=Q+(size_t)1536*4096;
  for(int br=0;br<2;br++){
    const u16* x=(const u16*)d_in[br*5+0]; const u16* k=(const u16*)d_in[br*5+1]; const u16* v=(const u16*)d_in[br*5+2];
    const u16* wq=(const u16*)d_in[br*5+3]; const u16* wo=(const u16*)d_in[br*5+4];
    ref_gemm<<<dim3(4096/16,1536/16),dim3(16,16),0,st>>>(x,wq,Q,1536,4096,1024,4096);
    ref_attn<<<dim3(96,32,16),128,0,st>>>(Q,k,v,O,round_mode);
    ref_gemm<<<dim3(1024/16,1536/16),dim3(16,16),0,st>>>(O,wo,(u16*)d_out+br*1024,1536,1024,4096,2048);
  }
}
extern "C" void kernel_launch(void* const* d_in, const int* in_sizes, int n_in,
                              void* d_out, int out_size, void* d_ws, size_t ws_size,
                              hipStream_t stream) {
  ref_forward(d_in, d_out, d_ws, stream, 0);
}
```

```cpp
#include <hip/hip_runtime.h>
#include <hip/hip_bf16.h>
#include <hip/hip_runtime.h>
#include <stdint.h>
typedef unsigned short u16;
__host__ __device__ inline float bf2f(u16 x){ union{uint32_t u; float f;} c; c.u = ((uint32_t)x)<<16; return c.f; }
__host__ __device__ inline u16 f2bf(float f){ union{uint32_t u; float f;} c; c.f=f; uint32_t u=c.u; return (u16)((u + 0x7FFF + ((u>>16)&1))>>16); }

__global__ void ref_gemm(const u16* A, const u16* B, u16* C, int M, int N, int K, int ldc){
  __shared__ float As[16][17], Bs[16][17];
  int tx=threadIdx.x, ty=threadIdx.y; int row=blockIdx.y*16+ty, col=blockIdx.x*16+tx;
  float acc=0;
  for(int k0=0;k0<K;k0+=16){
    As[ty][tx]=bf2f(A[(size_t)row*K+k0+tx]); Bs[ty][tx]=bf2f(B[(size_t)(k0+ty)*N+col]);
    __syncthreads();
    for(int k=0;k<16;k++) acc+=As[ty][k]*Bs[k][tx];
    __syncthreads();
  }
  C[(size_t)row*ldc+col]=f2bf(acc);
}
__global__ void ref_attn(const u16* Q, const u16* K, const u16* V, u16* O, int round_mode){
  int q=blockIdx.x, h=blockIdx.y, b=blockIdx.z; int t=threadIdx.x;
  __shared__ float qs[128], s[512], red[128];
  qs[t]=bf2f(Q[((size_t)(b*96+q))*4096+h*128+t]); __syncthreads();
  const u16* Kp=K+((size_t)(b*32+h))*512*128; const u16* Vp=V+((size_t)(b*32+h))*512*128;
  for(int j=t;j<512;j+=128){ float a=0; for(int d=0;d<128;d++) a+=qs[d]*bf2f(Kp[j*128+d]);
    if(round_mode){ a=bf2f(f2bf(a)); a=bf2f(f2bf(a*0.08838834764831845f)); } else a*=0.08838834764831845f; s[j]=a; }
  __syncthreads();
  float m=-1e30f; for(int j=0;j<512;j++) m=fmaxf(m,s[j]);
  float sum=0; for(int j=0;j<512;j++) sum+=expf(s[j]-m);
  __syncthreads();
  for(int j=t;j<512;j+=128){ float p=expf(s[j]-m)/sum; s[j]= round_mode? bf2f(f2bf(p)) : p; }
  __syncthreads();
  float a=0; for(int j=0;j<512;j++) a+=s[j]*bf2f(Vp[j*128+t]);
  O[((size_t)(b*96+q))*4096+h*128+t]=f2bf(a);
}
inline void ref_forward(void* const* d_in, void* d_out, void* ws, hipStream_t st, int round_mode){
  u16* Q=(u16*)ws; u16* O=Q+(size_t)1536*4096;
  for(int br=0;br<2;br++){
    const u16* x=(const u16*)d_in[br*5+0]; const u16* k=(const u16*)d_in[br*5+1]; const u16* v=(const u16*)d_in[br*5+2];
    const u16* wq=(const u16*)d_in[br*5+3]; const u16* wo=(const u16*)d_in[br*5+4];
    ref_gemm<<<dim3(4096/16,1536/16),dim3(16,16),0,st>>>(x,wq,Q,1536,4096,1024,4096);
    ref_attn<<<dim3(96,32,16),128,0,st>>>(Q,k,v,O,round_mode);
    ref_gemm<<<dim3(1024/16,1536/16),dim3(16,16),0,st>>>(O,wo,(u16*)d_out+br*1024,1536,1024,4096,2048);
  }
}

__device__ __forceinline__ unsigned rfl(unsigned x){ return __builtin_amdgcn_readfirstlane(x); }
__global__ __launch_bounds__(256) void probe_k(const char* p0,const char* p1,const char* p2,const char* p3, unsigned per, unsigned wg_per_buf, unsigned long long* ts){
  __shared__ __attribute__((aligned(16))) char lds[98304];
  unsigned wid = rfl(threadIdx.x>>6), lane=threadIdx.x&63;
  unsigned long long t0 = __builtin_amdgcn_s_memrealtime();
  unsigned bi = blockIdx.x/wg_per_buf, r = blockIdx.x%wg_per_buf;
  const char* b = bi==0?p0:bi==1?p1:bi==2?p2:p3;
  __amdgpu_buffer_rsrc_t srd = __builtin_amdgcn_make_buffer_rsrc((void*)(b+(size_t)r*per),(short)0,(int)per,0x00020000);
  unsigned np = per/4096/4, ldsb = rfl((unsigned)(size_t)lds + wid*16*1024), slot=0, off=wid*4096, voff=lane*16;
  for(unsigned p=0;p<np;p++){
    asm volatile("s_mov_b32 m0, %0\n\ts_nop 0\n\tbuffer_load_dwordx4 %1, %2, %3 offen  lds\n\tbuffer_load_dwordx4 %1, %2, %3 offen offset:1024  lds\n\tbuffer_load_dwordx4 %1, %2, %3 offen offset:2048  lds\n\tbuffer_load_dwordx4 %1, %2, %3 offen offset:3072  lds" :: "s"(ldsb+slot*1024), "v"(voff), "s"(srd), "s"(off) : "memory");
    off+=4*4096; slot+=4; if(slot==16) slot=0;
    asm volatile("s_waitcnt vmcnt(12)":::"memory"); }
  asm volatile("s_waitcnt vmcnt(0)":::"memory");
  unsigned long long t1 = __builtin_amdgcn_s_memrealtime();
  if(lane==0){ ts[(blockIdx.x*4+wid)*2]=t0; ts[(blockIdx.x*4+wid)*2+1]=t1; }
}
__global__ void spin_k(const unsigned long long* ts, int n){
  unsigned long long mn=~0ull,mx=0; for(int i=0;i<n;i++){ mn=min(mn,ts[2*i]); mx=max(mx,ts[2*i+1]); }
  unsigned long long d=(mx-mn); if(d>20000) d=20000; d*=100;
  unsigned long long t0=__builtin_amdgcn_s_memrealtime();
  while(__builtin_amdgcn_s_memrealtime()-t0<d) __builtin_amdgcn_s_sleep(8);
}
extern "C" void kernel_launch(void* const* d_in, const int* in_sizes, int n_in,
                              void* d_out, int out_size, void* d_ws, size_t ws_size,
                              hipStream_t stream) {
  unsigned long long* ts=(unsigned long long*)((char*)d_ws+(64<<20));
  probe_k<<<64,256,0,stream>>>((const char*)d_in[3],(const char*)d_in[4],(const char*)d_in[8],(const char*)d_in[9],524288,16,ts);
  spin_k<<<1,1,0,stream>>>(ts,64*4);
  ref_forward(d_in, d_out, d_ws, stream, 0);
}
```

```cpp
#include <hip/hip_runtime.h>
#include <hip/hip_bf16.h>

typedef unsigned u32x4 __attribute__((ext_vector_type(4)));
typedef unsigned u32x8 __attribute__((ext_vector_type(8)));

static __device__ __forceinline__ unsigned rfl(unsigned x) { return __builtin_amdgcn_readfirstlane(x); }
static __device__ __forceinline__ u32x4 mk_srd(const void* p, unsigned bytes) {
  const unsigned long long a = (unsigned long long)p;
  u32x4 r = {rfl((unsigned)a), rfl((unsigned)(a >> 32) & 0xffffu), rfl(bytes), 0x00020000u};
  return r;
}

__global__ __launch_bounds__(256, 1) void fused_attn_kernel(const char* xA, const char* kA, const char* vA, const char* wqA, const char* woA,
                                                             const char* xB, const char* kB, const char* vB, const char* wqB, const char* woB,
                                                             char* out, char* ws) {
  __shared__ __attribute__((aligned(1024))) char lds[163840];
  const unsigned lb = (unsigned)(size_t)lds;
  const unsigned gid = blockIdx.x, xcd = gid & 7, c = gid >> 3;
  const unsigned br = xcd >> 2, bh = (xcd >> 1) & 1, hh = xcd & 1, bp = c >> 3, hp = c & 7;
  const unsigned b0 = 8 * bh + 2 * bp, h0 = 16 * hh + 2 * hp;
  const unsigned w = rfl(threadIdx.x >> 6), l = threadIdx.x & 63;
  const unsigned wb = w >> 1, wh = w & 1, b = b0 + wb, h = h0 + wh;
  const unsigned g4 = l >> 4, q4 = (l & 15) >> 2, p = l & 3, q = l & 15;
  unsigned sink;
  {
    asm volatile(
      "s_getpc_b64 s[84:85]\n\t"
      ".Lk_pc0:\n\t"
      "s_and_b32 s84, s84, 0xffffff80\n\t"
      "s_and_b32 s85, s85, 0xffff\n\t"
      "s_mov_b32 s86, .Lk_rare-.Lk_pc0\n\t"
      "s_mov_b32 s87, 0x20000\n\t"
      "buffer_load_dword %0, %1, s[84:87], 0 offen"
      : "={v239}"(sink) : "v"(l * 16384 + c * 512 + (threadIdx.x >> 6) * 128) : "s84", "s85", "s86", "s87", "scc", "memory");
    __builtin_amdgcn_sched_barrier(0);
  }
  u32x4 m0s = {rfl(lb + w * 4096), rfl(lb + 65536 + w * 6144), rfl(lb + 98304 + w * 8192), rfl(lb + w * 8192)};
  const unsigned rotq = (8 * (bh ^ hh) + 4 * br) ^ ((bp ^ hp) & 1);
  u32x4 ofs = {rfl(rotq * 128), rfl(rotq * 64 * 8192), rfl(((b + h) & 1) * 2048 + ((b + h) & 14) * 8192), rfl(hp)};
  const unsigned cw = q ^ ((g4 | (wh << 2)) << 1);
  u32x8 vx; u32x4 vw;
#pragma unroll
  for (unsigned i = 0; i < 6; ++i)
    vx[i] = (48 * w + 8 * i + (l >> 3)) * 2048 + 16 * ((l & 7) ^ ((4 * (i & 1) + (l >> 4)) & 7)) - 1024 * (i & 3);
#pragma unroll
  for (unsigned kk = 0; kk < 2; ++kk) vx[6 + kk] = lb + 65536 + 128 * (96 * wb + q) + 16 * ((4 * kk + g4) ^ ((q >> 1) & 7));
#pragma unroll
  for (unsigned i = 0; i < 4; ++i) vw[i] = (32 * (i >> 1) + 8 * w + 4 * (i & 1) + g4) * 8192 + (cw >> 3) * 256 + 16 * (cw & 7) - 1024 * i;
  asm volatile("" : "+{v[200:207]}"(vx), "+{v[208:211]}"(vw), "+{s[56:59]}"(m0s), "+{s[60:63]}"(ofs));
  __builtin_amdgcn_sched_barrier(0);
  u32x4 srdX = mk_srd((br ? xB : xA) + (size_t)b0 * 96 * 2048, 192 * 2048);
  u32x4 srdW = mk_srd((br ? wqB : wqA) + h0 * 256, 1024 * 8192 - h0 * 256);
  asm volatile(
    "s_nop 4\n\t"
    "s_add_u32 m0, s57, 0\n\t"
    "s_nop 0\n\t"
    "s_nop 0\n\t"
    "buffer_load_dwordx4 v200, s[36:39], s60 offen lds\n\t"
    "buffer_load_dwordx4 v201, s[36:39], s60 offen offset:1024 lds\n\t"
    "buffer_load_dwordx4 v202, s[36:39], s60 offen offset:2048 lds\n\t"
    "buffer_load_dwordx4 v203, s[36:39], s60 offen offset:3072 lds\n\t"
    "s_add_u32 m0, s57, 4096\n\t"
    "s_nop 0\n\t"
    "s_nop 0\n\t"
    "buffer_load_dwordx4 v204, s[36:39], s60 offen lds\n\t"
    "buffer_load_dwordx4 v205, s[36:39], s60 offen offset:1024 lds\n\t"
    "s_xor_b32 s60, s60, 128\n\t"
    "s_add_u32 m0, s56, 0\n\t"
    "s_nop 0\n\t"
    "buffer_load_dwordx4 v208, s[40:43], s61 offen lds\n\t"
    "buffer_load_dwordx4 v209, s[40:43], s61 offen offset:1024 lds\n\t"
    "buffer_load_dwordx4 v210, s[40:43], s61 offen offset:2048 lds\n\t"
    "buffer_load_dwordx4 v211, s[40:43], s61 offen offset:3072 lds\n\t"
    "s_xor_b32 s61, s61, 128\n\t"
    : "+{s[60:63]}"(ofs)
    : "{s[36:39]}"(srdX), "{s[40:43]}"(srdW), "{s[56:59]}"(m0s), "{v[200:207]}"(vx), "{v[208:211]}"(vw)
    : "scc", "memory");
  __builtin_amdgcn_sched_barrier(0);
  u32x4 srdK = mk_srd((br ? kB : kA) + (size_t)(b * 32 + h) * 131072, 131072);
  u32x4 srdV = mk_srd((br ? vB : vA) + (size_t)(b * 32 + h) * 131072, 131072);
  u32x4 srdOW = mk_srd(ws + (size_t)((br * 16 + b) * 2 + hh) * 397312 + wh * 6144, 393216 - wh * 6144);
  u32x4 srdWO = mk_srd((br ? woB : woA) + (size_t)hh * 2048 * 2048 + hp * 256, 2048 * 2048 - hp * 256);
  u32x4 srdF = mk_srd(ws + (48u << 20), 16u << 20);
  u32x4 srdOut = mk_srd(out + (size_t)b * 96 * 4096 + br * 2048 + hp * 256 + (4 * wh + 2 * hh) * 32, 95 * 4096 + 64);
  u32x4 of2 = {0u, rfl(hp * 12288), rfl((gid * 4 + w) * 64), rfl((8u << 20) + (hh << 22) + ((gid >> 1) * 4 + w) * 6144)};
  u32x8 va, vk, vv, vo; u32x4 vkd;
  const unsigned ftr = (q4 | ((g4 & 1) << 2)) << 1;
#pragma unroll
  for (unsigned u = 0; u < 4; ++u) va[u] = lb + 4096 * g4 + 256 * q4 + 16 * ((8 * wh + 2 * u + (p >> 1)) ^ ftr) + 8 * (p & 1);
#pragma unroll
  for (unsigned i = 0; i < 4; ++i)
    va[4 + i] = (4 * w + 16 * (i & 1) + g4 + 128 * (cw >> 3)) * 2048 + ((i >> 1) ^ (cw >> 3)) * 128 + 16 * (cw & 7) - 1024 * (i & 1);
#pragma unroll
  for (unsigned i = 0; i < 4; ++i) vkd[i] = g4 * 4096 + 16 * (q ^ (4 * i + g4));
#pragma unroll
  for (unsigned u = 0; u < 8; ++u) vk[u] = lb + w * 8192 + 256 * (4 * g4 + q4) + 16 * ((2 * u + (p >> 1)) ^ ftr) + 8 * (p & 1);
#pragma unroll
  for (unsigned s = 0; s < 4; ++s) vv[s] = lb + 98304 + w * 8192 + 256 * q + 16 * ((4 * s + 2 * (g4 & 1) + (g4 >> 1)) ^ q);
  vv[4] = g4 * 4096 + 16 * (q ^ (g4 << 1));
  vv[5] = l * 16;
  vv[6] = (q == 8 ? ((gid ^ 1) * 4 + w) * 64 : ((xcd + 8 * (bp * 8 + ((hp + (q < 8 ? q : 0)) & 7))) * 4 + w) * 64) +
          (g4 == 1 ? 131072 : q == 8 ? 65536 : 0);
  vv[7] = 0;
  vo[0] = lb + 8192 * g4 + 256 * q4 + 16 * ((8 * wh + 4 * hh + (p >> 1)) ^ ftr) + 8 * (p & 1);
  vo[1] = l * 16 + 4096;
  vo[2] = l * 16384;
  vo[3] = lb + w * 24576 + l * 16;
  vo[4] = lb + (w ^ 1) * 24576 + l * 16;
  vo[5] = q * 4096 + (g4 & 1) * 32 + (g4 >> 1) * 16;
  vo[6] = l < 16 ? (((bh * 4 + bp) * 4 + w) * 8 + (q >> 1)) * 2048 + (l & 1) * 128 : 0x40000000u;
  vo[7] = 1;
  asm volatile(
    ".p2align 3\n\t"
    "s_nop 4\n\t"
    "s_mov_b32 s20, 0x3e0293ee\n\t"
    "s_mov_b32 s21, 0x41000000\n\t"
    "s_mov_b32 s18, s62\n\t"
    "s_mov_b32 s27, 1048576\n\t"
    "v_xor_b32 v238, 128, v228\n\t"
    "s_getpc_b64 s[12:13]\n\t"
    ".Lk_pc:\n\t"
    "s_and_b32 s12, s12, 0xffffff80\n\t"
    "s_and_b32 s13, s13, 0xffff\n\t"
    "s_mov_b32 s14, .Lk_rare-.Lk_pc\n\t"
    "s_mov_b32 s15, 0x20000\n\t"
    "s_lshr_b32 s11, s78, 11\n\t"
    "s_lshl_b32 s11, s11, 9\n\t"
    "s_bfe_u32 s24, s78, 0x20006\n\t"
    "s_lshl_b32 s24, s24, 7\n\t"
    "s_add_u32 s11, s11, s24\n\t"
    "s_add_u32 m0, s56, 16384\n\t"
    "s_nop 0\n\t"
    "buffer_load_dwordx4 v208, s[40:43], s61 offen lds\n\t"
    "buffer_load_dwordx4 v209, s[40:43], s61 offen offset:1024 lds\n\t"
    "buffer_load_dwordx4 v210, s[40:43], s61 offen offset:2048 lds\n\t"
    "buffer_load_dwordx4 v211, s[40:43], s61 offen offset:3072 lds\n\t"
    "s_xor_b32 s61, s61, 524416\n\t"
    "s_waitcnt vmcnt(4)\n\t"
    "s_barrier\n\t"
    "ds_read_b128 a[64:67], v206\n\t"
    "s_add_u32 m0, s57, 24576\n\t"
    "s_nop 0\n\t"
    "s_nop 0\n\t"
    "buffer_load_dwordx4 v200, s[36:39], s60 offen lds\n\t"
    "ds_read_b128 a[88:91], v207\n\t"
    "ds_read_b128 a[68:71], v206 offset:2048\n\t"
    "buffer_load_dwordx4 v201, s[36:39], s60 offen offset:1024 lds\n\t"
    "ds_read_b128 a[92:95], v207 offset:2048\n\t"
    "ds_read_b64_tr_b16 a[0:1], v192\n\t"
    "ds_read_b64_tr_b16 a[2:3], v192 offset:1024\n\t"
    "buffer_load_dwordx4 v202, s[36:39], s60 offen offset:2048 lds\n\t"
    "ds_read_b64_tr_b16 a[4:5], v192 offset:2048\n\t"
    "ds_read_b64_tr_b16 a[6:7], v192 offset:3072\n\t"
    "ds_read_b64_tr_b16 a[8:9], v193\n\t"
    "buffer_load_dwordx4 v203, s[36:39], s60 offen offset:3072 lds\n\t"
    "ds_read_b64_tr_b16 a[10:11], v193 offset:1024\n\t"
    "ds_read_b64_tr_b16 a[12:13], v193 offset:2048\n\t"
    "ds_read_b64_tr_b16 a[14:15], v193 offset:3072\n\t"
    "s_add_u32 m0, s57, 28672\n\t"
    "s_nop 0\n\t"
    "s_nop 0\n\t"
    "buffer_load_dwordx4 v204, s[36:39], s60 offen lds\n\t"
    "ds_read_b64_tr_b16 a[16:17], v194\n\t"
    "ds_read_b64_tr_b16 a[18:19], v194 offset:1024\n\t"
    "ds_read_b64_tr_b16 a[20:21], v194 offset:2048\n\t"
    "buffer_load_dwordx4 v205, s[36:39], s60 offen offset:1024 lds\n\t"
    "s_xor_b32 s60, s60, 384\n\t"
    "ds_read_b64_tr_b16 a[22:23], v194 offset:3072\n\t"
    "ds_read_b64_tr_b16 a[24:25], v195\n\t"
    "s_add_u32 m0, s56, 32768\n\t"
    "s_nop 0\n\t"
    "s_nop 0\n\t"
    "buffer_load_dwordx4 v208, s[40:43], s61 offen lds\n\t"
    "ds_read_b64_tr_b16 a[26:27], v195 offset:1024\n\t"
    "ds_read_b64_tr_b16 a[28:29], v195 offset:2048\n\t"
    "ds_read_b64_tr_b16 a[30:31], v195 offset:3072\n\t"
    "buffer_load_dwordx4 v209, s[40:43], s61 offen offset:1024 lds\n\t"
    "ds_read_b128 a[72:75], v206 offset:4096\n\t"
    "ds_read_b128 a[96:99], v207 offset:4096\n\t"
    "ds_read_b128 a[76:79], v206 offset:6144\n\t"
    "buffer_load_dwordx4 v210, s[40:43], s61 offen offset:2048 lds\n\t"
    "ds_read_b128 a[100:103], v207 offset:6144\n\t"
    "ds_read_b128 a[80:83], v206 offset:8192\n\t"
    "ds_read_b128 a[104:107], v207 offset:8192\n\t"
    "buffer_load_dwordx4 v211, s[40:43], s61 offen offset:3072 lds\n\t"
    "s_xor_b32 s61, s61, 128\n\t"
    "ds_read_b128 a[84:87], v206 offset:10240\n\t"
    "ds_read_b128 a[108:111], v207 offset:10240\n\t"
    "s_add_u32 m0, s56, 49152\n\t"
    "s_nop 0\n\t"
    "s_nop 0\n\t"
    "buffer_load_dwordx4 v208, s[40:43], s61 offen lds\n\t"
    "buffer_load_dwordx4 v209, s[40:43], s61 offen offset:1024 lds\n\t"
    "buffer_load_dwordx4 v210, s[40:43], s61 offen offset:2048 lds\n\t"
    "buffer_load_dwordx4 v211, s[40:43], s61 offen offset:3072 lds\n\t"
    "s_xor_b32 s61, s61, 1572992\n\t"
    "buffer_load_dword v231, v230, s[68:71], 0 offen sc1\n\t"
    "s_waitcnt vmcnt(15) lgkmcnt(0)\n\t"
    "s_barrier\n\t"
    "v_mfma_f32_16x16x32_bf16 v[0:3], a[0:3], a[64:67], 0\n\t"
    "ds_read_b64_tr_b16 a[32:33], v192 offset:16384\n\t"
    "v_mfma_f32_16x16x32_bf16 v[0:3], a[4:7], a[88:91], v[0:3]\n\t"
    "buffer_load_dword v239, v246, s[64:67], s76 offen\n\t"
    "s_add_u32 s76, s76, 524288\n\t"
    "v_mfma_f32_16x16x32_bf16 v[8:11], a[4:7], a[92:95], 0\n\t"
    "ds_read_b64_tr_b16 a[34:35], v192 offset:17408\n\t"
    "v_mfma_f32_16x16x32_bf16 v[8:11], a[0:3], a[68:71], v[8:11]\n\t"
    "v_mfma_f32_16x16x32_bf16 v[16:19], a[0:3], a[72:75], 0\n\t"
    "ds_read_b64_tr_b16 a[36:37], v192 offset:18432\n\t"
    "s_add_u32 m0, s57, 0\n\t"
    "s_nop 0\n\t"
    "v_mfma_f32_16x16x32_bf16 v[16:19], a[4:7], a[96:99], v[16:19]\n\t"
    "buffer_load_dwordx4 v200, s[36:39], s60 offen lds\n\t"
    "v_mfma_f32_16x16x32_bf16 v[24:27], a[4:7], a[100:103], 0\n\t"
    "ds_read_b64_tr_b16 a[38:39], v192 offset:19456\n\t"
    "v_mfma_f32_16x16x32_bf16 v[24:27], a[0:3], a[76:79], v[24:27]\n\t"
    "v_mfma_f32_16x16x32_bf16 v[32:35], a[0:3], a[80:83], 0\n\t"
    "ds_read_b64_tr_b16 a[40:41], v193 offset:16384\n\t"
    "v_mfma_f32_16x16x32_bf16 v[32:35], a[4:7], a[104:107], v[32:35]\n\t"
    "buffer_load_dwordx4 v201, s[36:39], s60 offen offset:1024 lds\n\t"
    "v_mfma_f32_16x16x32_bf16 v[40:43], a[4:7], a[108:111], 0\n\t"
    "ds_read_b64_tr_b16 a[42:43], v193 offset:17408\n\t"
    "v_mfma_f32_16x16x32_bf16 v[40:43], a[0:3], a[84:87], v[40:43]\n\t"
    "v_mfma_f32_16x16x32_bf16 v[4:7], a[8:11], a[64:67], 0\n\t"
    "ds_read_b64_tr_b16 a[44:45], v193 offset:18432\n\t"
    "v_mfma_f32_16x16x32_bf16 v[4:7], a[12:15], a[88:91], v[4:7]\n\t"
    "buffer_load_dwordx4 v202, s[36:39], s60 offen offset:2048 lds\n\t"
    "v_mfma_f32_16x16x32_bf16 v[12:15], a[12:15], a[92:95], 0\n\t"
    "ds_read_b64_tr_b16 a[46:47], v193 offset:19456\n\t"
    "v_mfma_f32_16x16x32_bf16 v[12:15], a[8:11], a[68:71], v[12:15]\n\t"
    "v_mfma_f32_16x16x32_bf16 v[20:23], a[8:11], a[72:75], 0\n\t"
    "ds_read_b64_tr_b16 a[48:49], v194 offset:16384\n\t"
    "v_mfma_f32_16x16x32_bf16 v[20:23], a[12:15], a[96:99], v[20:23]\n\t"
    "buffer_load_dwordx4 v203, s[36:39], s60 offen offset:3072 lds\n\t"
    "v_mfma_f32_16x16x32_bf16 v[28:31], a[12:15], a[100:103], 0\n\t"
    "ds_read_b64_tr_b16 a[50:51], v194 offset:17408\n\t"
    "v_mfma_f32_16x16x32_bf16 v[28:31], a[8:11], a[76:79], v[28:31]\n\t"
    "v_mfma_f32_16x16x32_bf16 v[36:39], a[8:11], a[80:83], 0\n\t"
    "ds_read_b64_tr_b16 a[52:53], v194 offset:18432\n\t"
    "s_add_u32 m0, s57, 4096\n\t"
    "v_mfma_f32_16x16x32_bf16 v[36:39], a[12:15], a[104:107], v[36:39]\n\t"
    "buffer_load_dwordx4 v204, s[36:39], s60 offen lds\n\t"
    "v_mfma_f32_16x16x32_bf16 v[44:47], a[12:15], a[108:111], 0\n\t"
    "ds_read_b64_tr_b16 a[54:55], v194 offset:19456\n\t"
    "v_mfma_f32_16x16x32_bf16 v[44:47], a[8:11], a[84:87], v[44:47]\n\t"
    "v_mfma_f32_16x16x32_bf16 v[48:51], a[16:19], a[64:67], 0\n\t"
    "ds_read_b64_tr_b16 a[56:57], v195 offset:16384\n\t"
    "v_mfma_f32_16x16x32_bf16 v[48:51], a[20:23], a[88:91], v[48:51]\n\t"
    "buffer_load_dwordx4 v205, s[36:39], s60 offen offset:1024 lds\n\t"
    "s_xor_b32 s60, s60, 128\n\t"
    "v_mfma_f32_16x16x32_bf16 v[56:59], a[20:23], a[92:95], 0\n\t"
    "ds_read_b64_tr_b16 a[58:59], v195 offset:17408\n\t"
    "v_mfma_f32_16x16x32_bf16 v[56:59], a[16:19], a[68:71], v[56:59]\n\t"
    "v_mfma_f32_16x16x32_bf16 v[64:67], a[16:19], a[72:75], 0\n\t"
    "ds_read_b64_tr_b16 a[60:61], v195 offset:18432\n\t"
    "s_add_u32 m0, s56, 0\n\t"
    "s_nop 0\n\t"
    "v_mfma_f32_16x16x32_bf16 v[64:67], a[20:23], a[96:99], v[64:67]\n\t"
    "buffer_load_dwordx4 v208, s[40:43], s61 offen lds\n\t"
    "v_mfma_f32_16x16x32_bf16 v[72:75], a[20:23], a[100:103], 0\n\t"
    "ds_read_b64_tr_b16 a[62:63], v195 offset:19456\n\t"
    "v_mfma_f32_16x16x32_bf16 v[72:75], a[16:19], a[76:79], v[72:75]\n\t"
    "v_mfma_f32_16x16x32_bf16 v[80:83], a[16:19], a[80:83], 0\n\t"
    "v_mfma_f32_16x16x32_bf16 v[80:83], a[20:23], a[104:107], v[80:83]\n\t"
    "buffer_load_dwordx4 v209, s[40:43], s61 offen offset:1024 lds\n\t"
    "v_mfma_f32_16x16x32_bf16 v[88:91], a[20:23], a[108:111], 0\n\t"
    "v_mfma_f32_16x16x32_bf16 v[88:91], a[16:19], a[84:87], v[88:91]\n\t"
    "v_mfma_f32_16x16x32_bf16 v[52:55], a[24:27], a[64:67], 0\n\t"
    "v_mfma_f32_16x16x32_bf16 v[52:55], a[28:31], a[88:91], v[52:55]\n\t"
    "buffer_load_dwordx4 v210, s[40:43], s61 offen offset:2048 lds\n\t"
    "v_mfma_f32_16x16x32_bf16 v[60:63], a[28:31], a[92:95], 0\n\t"
    "v_mfma_f32_16x16x32_bf16 v[60:63], a[24:27], a[68:71], v[60:63]\n\t"
    "v_mfma_f32_16x16x32_bf16 v[68:71], a[24:27], a[72:75], 0\n\t"
    "v_mfma_f32_16x16x32_bf16 v[68:71], a[28:31], a[96:99], v[68:71]\n\t"
    "buffer_load_dwordx4 v211, s[40:43], s61 offen offset:3072 lds\n\t"
    "s_xor_b32 s61, s61, 128\n\t"
    "v_mfma_f32_16x16x32_bf16 v[76:79], a[28:31], a[100:103], 0\n\t"
    "v_mfma_f32_16x16x32_bf16 v[76:79], a[24:27], a[76:79], v[76:79]\n\t"
    "v_mfma_f32_16x16x32_bf16 v[84:87], a[24:27], a[80:83], 0\n\t"
    "v_mfma_f32_16x16x32_bf16 v[84:87], a[28:31], a[104:107], v[84:87]\n\t"
    "v_mfma_f32_16x16x32_bf16 v[92:95], a[28:31], a[108:111], 0\n\t"
    "v_mfma_f32_16x16x32_bf16 v[92:95], a[24:27], a[84:87], v[92:95]\n\t"
    "s_waitcnt vmcnt(16) lgkmcnt(0)\n\t"
    "s_barrier\n\t"
    "v_mfma_f32_16x16x32_bf16 v[96:99], a[32:35], a[64:67], 0\n\t"
    "ds_read_b64_tr_b16 a[0:1], v192 offset:32768\n\t"
    "v_mfma_f32_16x16x32_bf16 v[96:99], a[36:39], a[88:91], v[96:99]\n\t"
    "buffer_load_dword v239, v246, s[64:67], s76 offen\n\t"
    "s_add_u32 s76, s76, 524288\n\t"
    "v_mfma_f32_16x16x32_bf16 v[104:107], a[36:39], a[92:95], 0\n\t"
    "ds_read_b64_tr_b16 a[2:3], v192 offset:33792\n\t"
    "v_mfma_f32_16x16x32_bf16 v[104:107], a[32:35], a[68:71], v[104:107]\n\t"
    "ds_read_b128 a[112:115], v206 offset:24576\n\t"
    "v_mfma_f32_16x16x32_bf16 v[112:115], a[32:35], a[72:75], 0\n\t"
    "ds_read_b64_tr_b16 a[4:5], v192 offset:34816\n\t"
    "v_mfma_f32_16x16x32_bf16 v[112:115], a[36:39], a[96:99], v[112:115]\n\t"
    "ds_read_b128 a[136:139], v207 offset:24576\n\t"
    "v_mfma_f32_16x16x32_bf16 v[120:123], a[36:39], a[100:103], 0\n\t"
    "ds_read_b64_tr_b16 a[6:7], v192 offset:35840\n\t"
    "v_mfma_f32_16x16x32_bf16 v[120:123], a[32:35], a[76:79], v[120:123]\n\t"
    "ds_read_b128 a[116:119], v206 offset:26624\n\t"
    "v_mfma_f32_16x16x32_bf16 v[128:131], a[32:35], a[80:83], 0\n\t"
    "ds_read_b64_tr_b16 a[8:9], v193 offset:32768\n\t"
    "v_mfma_f32_16x16x32_bf16 v[128:131], a[36:39], a[104:107], v[128:131]\n\t"
    "ds_read_b128 a[140:143], v207 offset:26624\n\t"
    "v_mfma_f32_16x16x32_bf16 v[136:139], a[36:39], a[108:111], 0\n\t"
    "ds_read_b64_tr_b16 a[10:11], v193 offset:33792\n\t"
    "v_mfma_f32_16x16x32_bf16 v[136:139], a[32:35], a[84:87], v[136:139]\n\t"
    "ds_read_b128 a[120:123], v206 offset:28672\n\t"
    "v_mfma_f32_16x16x32_bf16 v[100:103], a[40:43], a[64:67], 0\n\t"
    "ds_read_b64_tr_b16 a[12:13], v193 offset:34816\n\t"
    "v_mfma_f32_16x16x32_bf16 v[100:103], a[44:47], a[88:91], v[100:103]\n\t"
    "ds_read_b128 a[144:147], v207 offset:28672\n\t"
    "v_mfma_f32_16x16x32_bf16 v[108:111], a[44:47], a[92:95], 0\n\t"
    "ds_read_b64_tr_b16 a[14:15], v193 offset:35840\n\t"
    "v_mfma_f32_16x16x32_bf16 v[108:111], a[40:43], a[68:71], v[108:111]\n\t"
    "ds_read_b128 a[124:127], v206 offset:30720\n\t"
    "v_mfma_f32_16x16x32_bf16 v[116:119], a[40:43], a[72:75], 0\n\t"
    "ds_read_b64_tr_b16 a[16:17], v194 offset:32768\n\t"
    "v_mfma_f32_16x16x32_bf16 v[116:119], a[44:47], a[96:99], v[116:119]\n\t"
    "ds_read_b128 a[148:151], v207 offset:30720\n\t"
    "v_mfma_f32_16x16x32_bf16 v[124:127], a[44:47], a[100:103], 0\n\t"
    "ds_read_b64_tr_b16 a[18:19], v194 offset:33792\n\t"
    "v_mfma_f32_16x16x32_bf16 v[124:127], a[40:43], a[76:79], v[124:127]\n\t"
    "ds_read_b128 a[128:131], v206 offset:32768\n\t"
    "v_mfma_f32_16x16x32_bf16 v[132:135], a[40:43], a[80:83], 0\n\t"
    "ds_read_b64_tr_b16 a[20:21], v194 offset:34816\n\t"
    "v_mfma_f32_16x16x32_bf16 v[132:135], a[44:47], a[104:107], v[132:135]\n\t"
    "ds_read_b128 a[152:155], v207 offset:32768\n\t"
    "v_mfma_f32_16x16x32_bf16 v[140:143], a[44:47], a[108:111], 0\n\t"
    "ds_read_b64_tr_b16 a[22:23], v194 offset:35840\n\t"
    "v_mfma_f32_16x16x32_bf16 v[140:143], a[40:43], a[84:87], v[140:143]\n\t"
    "ds_read_b128 a[132:135], v206 offset:34816\n\t"
    "v_mfma_f32_16x16x32_bf16 v[144:147], a[48:51], a[64:67], 0\n\t"
    "ds_read_b64_tr_b16 a[24:25], v195 offset:32768\n\t"
    "v_mfma_f32_16x16x32_bf16 v[144:147], a[52:55], a[88:91], v[144:147]\n\t"
    "ds_read_b128 a[156:159], v207 offset:34816\n\t"
    "v_mfma_f32_16x16x32_bf16 v[152:155], a[52:55], a[92:95], 0\n\t"
    "ds_read_b64_tr_b16 a[26:27], v195 offset:33792\n\t"
    "v_mfma_f32_16x16x32_bf16 v[152:155], a[48:51], a[68:71], v[152:155]\n\t"
    "v_mfma_f32_16x16x32_bf16 v[160:163], a[48:51], a[72:75], 0\n\t"
    "ds_read_b64_tr_b16 a[28:29], v195 offset:34816\n\t"
    "s_add_u32 m0, s56, 16384\n\t"
    "v_mfma_f32_16x16x32_bf16 v[160:163], a[52:55], a[96:99], v[160:163]\n\t"
    "buffer_load_dwordx4 v208, s[40:43], s61 offen lds\n\t"
    "v_mfma_f32_16x16x32_bf16 v[168:171], a[52:55], a[100:103], 0\n\t"
    "ds_read_b64_tr_b16 a[30:31], v195 offset:35840\n\t"
    "v_mfma_f32_16x16x32_bf16 v[168:171], a[48:51], a[76:79], v[168:171]\n\t"
    "v_mfma_f32_16x16x32_bf16 v[176:179], a[48:51], a[80:83], 0\n\t"
    "v_mfma_f32_16x16x32_bf16 v[176:179], a[52:55], a[104:107], v[176:179]\n\t"
    "buffer_load_dwordx4 v209, s[40:43], s61 offen offset:1024 lds\n\t"
    "v_mfma_f32_16x16x32_bf16 v[184:187], a[52:55], a[108:111], 0\n\t"
    "v_mfma_f32_16x16x32_bf16 v[184:187], a[48:51], a[84:87], v[184:187]\n\t"
    "v_mfma_f32_16x16x32_bf16 v[148:151], a[56:59], a[64:67], 0\n\t"
    "v_mfma_f32_16x16x32_bf16 v[148:151], a[60:63], a[88:91], v[148:151]\n\t"
    "buffer_load_dwordx4 v210, s[40:43], s61 offen offset:2048 lds\n\t"
    "v_mfma_f32_16x16x32_bf16 v[156:159], a[60:63], a[92:95], 0\n\t"
    "v_mfma_f32_16x16x32_bf16 v[156:159], a[56:59], a[68:71], v[156:159]\n\t"
    "v_mfma_f32_16x16x32_bf16 v[164:167], a[56:59], a[72:75], 0\n\t"
    "v_mfma_f32_16x16x32_bf16 v[164:167], a[60:63], a[96:99], v[164:167]\n\t"
    "buffer_load_dwordx4 v211, s[40:43], s61 offen offset:3072 lds\n\t"
    "s_xor_b32 s61, s61, 524416\n\t"
    "v_mfma_f32_16x16x32_bf16 v[172:175], a[60:63], a[100:103], 0\n\t"
    "v_mfma_f32_16x16x32_bf16 v[172:175], a[56:59], a[76:79], v[172:175]\n\t"
    "v_mfma_f32_16x16x32_bf16 v[180:183], a[56:59], a[80:83], 0\n\t"
    "v_mfma_f32_16x16x32_bf16 v[180:183], a[60:63], a[104:107], v[180:183]\n\t"
    "v_mfma_f32_16x16x32_bf16 v[188:191], a[60:63], a[108:111], 0\n\t"
    "v_mfma_f32_16x16x32_bf16 v[188:191], a[56:59], a[84:87], v[188:191]\n\t"
    "s_waitcnt vmcnt(17) lgkmcnt(0)\n\t"
    "s_barrier\n\t"
    "v_mfma_f32_16x16x32_bf16 v[0:3], a[0:3], a[112:115], v[0:3]\n\t"
    "ds_read_b64_tr_b16 a[32:33], v192 offset:49152\n\t"
    "v_mfma_f32_16x16x32_bf16 v[0:3], a[4:7], a[136:139], v[0:3]\n\t"
    "buffer_load_dword v239, v246, s[64:67], s76 offen\n\t"
    "s_add_u32 s76, s76, 524288\n\t"
    "v_mfma_f32_16x16x32_bf16 v[8:11], a[4:7], a[140:143], v[8:11]\n\t"
    "ds_read_b64_tr_b16 a[34:35], v192 offset:50176\n\t"
    "v_mfma_f32_16x16x32_bf16 v[8:11], a[0:3], a[116:119], v[8:11]\n\t"
    "v_mfma_f32_16x16x32_bf16 v[16:19], a[0:3], a[120:123], v[16:19]\n\t"
    "ds_read_b64_tr_b16 a[36:37], v192 offset:51200\n\t"
    "s_add_u32 m0, s57, 24576\n\t"
    "v_mfma_f32_16x16x32_bf16 v[16:19], a[4:7], a[144:147], v[16:19]\n\t"
    "buffer_load_dwordx4 v200, s[36:39], s60 offen lds\n\t"
    "v_mfma_f32_16x16x32_bf16 v[24:27], a[4:7], a[148:151], v[24:27]\n\t"
    "ds_read_b64_tr_b16 a[38:39], v192 offset:52224\n\t"
    "v_mfma_f32_16x16x32_bf16 v[24:27], a[0:3], a[124:127], v[24:27]\n\t"
    "v_mfma_f32_16x16x32_bf16 v[32:35], a[0:3], a[128:131], v[32:35]\n\t"
    "ds_read_b64_tr_b16 a[40:41], v193 offset:49152\n\t"
    "v_mfma_f32_16x16x32_bf16 v[32:35], a[4:7], a[152:155], v[32:35]\n\t"
    "buffer_load_dwordx4 v201, s[36:39], s60 offen offset:1024 lds\n\t"
    "v_mfma_f32_16x16x32_bf16 v[40:43], a[4:7], a[156:159], v[40:43]\n\t"
    "ds_read_b64_tr_b16 a[42:43], v193 offset:50176\n\t"
    "v_mfma_f32_16x16x32_bf16 v[40:43], a[0:3], a[132:135], v[40:43]\n\t"
    "v_mfma_f32_16x16x32_bf16 v[4:7], a[8:11], a[112:115], v[4:7]\n\t"
    "ds_read_b64_tr_b16 a[44:45], v193 offset:51200\n\t"
    "v_mfma_f32_16x16x32_bf16 v[4:7], a[12:15], a[136:139], v[4:7]\n\t"
    "buffer_load_dwordx4 v202, s[36:39], s60 offen offset:2048 lds\n\t"
    "v_mfma_f32_16x16x32_bf16 v[12:15], a[12:15], a[140:143], v[12:15]\n\t"
    "ds_read_b64_tr_b16 a[46:47], v193 offset:52224\n\t"
    "v_mfma_f32_16x16x32_bf16 v[12:15], a[8:11], a[116:119], v[12:15]\n\t"
    "v_mfma_f32_16x16x32_bf16 v[20:23], a[8:11], a[120:123], v[20:23]\n\t"
    "ds_read_b64_tr_b16 a[48:49], v194 offset:49152\n\t"
    "v_mfma_f32_16x16x32_bf16 v[20:23], a[12:15], a[144:147], v[20:23]\n\t"
    "buffer_load_dwordx4 v203, s[36:39], s60 offen offset:3072 lds\n\t"
    "v_mfma_f32_16x16x32_bf16 v[28:31], a[12:15], a[148:151], v[28:31]\n\t"
    "ds_read_b64_tr_b16 a[50:51], v194 offset:50176\n\t"
    "v_mfma_f32_16x16x32_bf16 v[28:31], a[8:11], a[124:127], v[28:31]\n\t"
    "v_mfma_f32_16x16x32_bf16 v[36:39], a[8:11], a[128:131], v[36:39]\n\t"
    "ds_read_b64_tr_b16 a[52:53], v194 offset:51200\n\t"
    "s_add_u32 m0, s57, 28672\n\t"
    "v_mfma_f32_16x16x32_bf16 v[36:39], a[12:15], a[152:155], v[36:39]\n\t"
    "buffer_load_dwordx4 v204, s[36:39], s60 offen lds\n\t"
    "v_mfma_f32_16x16x32_bf16 v[44:47], a[12:15], a[156:159], v[44:47]\n\t"
    "ds_read_b64_tr_b16 a[54:55], v194 offset:52224\n\t"
    "v_mfma_f32_16x16x32_bf16 v[44:47], a[8:11], a[132:135], v[44:47]\n\t"
    "v_mfma_f32_16x16x32_bf16 v[48:51], a[16:19], a[112:115], v[48:51]\n\t"
    "ds_read_b64_tr_b16 a[56:57], v195 offset:49152\n\t"
    "v_mfma_f32_16x16x32_bf16 v[48:51], a[20:23], a[136:139], v[48:51]\n\t"
    "buffer_load_dwordx4 v205, s[36:39], s60 offen offset:1024 lds\n\t"
    "s_xor_b32 s60, s60, 896\n\t"
    "v_mfma_f32_16x16x32_bf16 v[56:59], a[20:23], a[140:143], v[56:59]\n\t"
    "ds_read_b64_tr_b16 a[58:59], v195 offset:50176\n\t"
    "v_mfma_f32_16x16x32_bf16 v[56:59], a[16:19], a[116:119], v[56:59]\n\t"
    "v_mfma_f32_16x16x32_bf16 v[64:67], a[16:19], a[120:123], v[64:67]\n\t"
    "ds_read_b64_tr_b16 a[60:61], v195 offset:51200\n\t"
    "s_add_u32 m0, s56, 32768\n\t"
    "v_mfma_f32_16x16x32_bf16 v[64:67], a[20:23], a[144:147], v[64:67]\n\t"
    "buffer_load_dwordx4 v208, s[40:43], s61 offen lds\n\t"
    "v_mfma_f32_16x16x32_bf16 v[72:75], a[20:23], a[148:151], v[72:75]\n\t"
    "ds_read_b64_tr_b16 a[62:63], v195 offset:52224\n\t"
    "v_mfma_f32_16x16x32_bf16 v[72:75], a[16:19], a[124:127], v[72:75]\n\t"
    "v_mfma_f32_16x16x32_bf16 v[80:83], a[16:19], a[128:131], v[80:83]\n\t"
    "v_mfma_f32_16x16x32_bf16 v[80:83], a[20:23], a[152:155], v[80:83]\n\t"
    "buffer_load_dwordx4 v209, s[40:43], s61 offen offset:1024 lds\n\t"
    "v_mfma_f32_16x16x32_bf16 v[88:91], a[20:23], a[156:159], v[88:91]\n\t"
    "v_mfma_f32_16x16x32_bf16 v[88:91], a[16:19], a[132:135], v[88:91]\n\t"
    "v_mfma_f32_16x16x32_bf16 v[52:55], a[24:27], a[112:115], v[52:55]\n\t"
    "v_mfma_f32_16x16x32_bf16 v[52:55], a[28:31], a[136:139], v[52:55]\n\t"
    "buffer_load_dwordx4 v210, s[40:43], s61 offen offset:2048 lds\n\t"
    "v_mfma_f32_16x16x32_bf16 v[60:63], a[28:31], a[140:143], v[60:63]\n\t"
    "v_mfma_f32_16x16x32_bf16 v[60:63], a[24:27], a[116:119], v[60:63]\n\t"
    "v_mfma_f32_16x16x32_bf16 v[68:71], a[24:27], a[120:123], v[68:71]\n\t"
    "v_mfma_f32_16x16x32_bf16 v[68:71], a[28:31], a[144:147], v[68:71]\n\t"
    "buffer_load_dwordx4 v211, s[40:43], s61 offen offset:3072 lds\n\t"
    "s_xor_b32 s61, s61, 128\n\t"
    "v_mfma_f32_16x16x32_bf16 v[76:79], a[28:31], a[148:151], v[76:79]\n\t"
    "v_mfma_f32_16x16x32_bf16 v[76:79], a[24:27], a[124:127], v[76:79]\n\t"
    "v_mfma_f32_16x16x32_bf16 v[84:87], a[24:27], a[128:131], v[84:87]\n\t"
    "v_mfma_f32_16x16x32_bf16 v[84:87], a[28:31], a[152:155], v[84:87]\n\t"
    "v_mfma_f32_16x16x32_bf16 v[92:95], a[28:31], a[156:159], v[92:95]\n\t"
    "s_waitcnt vmcnt(27)\n\t"
    "s_add_u32 s24, s78, 131072\n\t"
    "s_mov_b64 exec, 1\n\t"
    "buffer_atomic_add v247, off, s[68:71], s24 sc1\n\t"
    "s_mov_b64 exec, -1\n\t"
    "s_nop 0\n\t"
    "v_mfma_f32_16x16x32_bf16 v[92:95], a[24:27], a[132:135], v[92:95]\n\t"
    "s_waitcnt vmcnt(17) lgkmcnt(0)\n\t"
    "s_barrier\n\t"
    "v_mfma_f32_16x16x32_bf16 v[96:99], a[32:35], a[112:115], v[96:99]\n\t"
    "ds_read_b64_tr_b16 a[0:1], v192\n\t"
    "s_add_u32 m0, s58, 32768\n\t"
    "v_mfma_f32_16x16x32_bf16 v[96:99], a[36:39], a[136:139], v[96:99]\n\t"
    "buffer_load_dwordx4 v212, s[44:47], s62 offen nt lds\n\t"
    "v_mfma_f32_16x16x32_bf16 v[104:107], a[36:39], a[140:143], v[104:107]\n\t"
    "ds_read_b64_tr_b16 a[2:3], v192 offset:1024\n\t"
    "s_add_u32 m0, s58, 33536\n\t"
    "v_mfma_f32_16x16x32_bf16 v[104:107], a[32:35], a[116:119], v[104:107]\n\t"
    "buffer_load_dwordx4 v213, s[44:47], s62 offen offset:256 nt lds\n\t"
    "v_mfma_f32_16x16x32_bf16 v[112:115], a[32:35], a[120:123], v[112:115]\n\t"
    "ds_read_b64_tr_b16 a[4:5], v192 offset:2048\n\t"
    "s_add_u32 m0, s58, 34304\n\t"
    "v_mfma_f32_16x16x32_bf16 v[112:115], a[36:39], a[144:147], v[112:115]\n\t"
    "buffer_load_dwordx4 v214, s[44:47], s62 offen offset:512 nt lds\n\t"
    "v_mfma_f32_16x16x32_bf16 v[120:123], a[36:39], a[148:151], v[120:123]\n\t"
    "ds_read_b64_tr_b16 a[6:7], v192 offset:3072\n\t"
    "s_add_u32 m0, s58, 35072\n\t"
    "v_mfma_f32_16x16x32_bf16 v[120:123], a[32:35], a[124:127], v[120:123]\n\t"
    "buffer_load_dwordx4 v215, s[44:47], s62 offen offset:768 nt lds\n\t"
    "v_mfma_f32_16x16x32_bf16 v[128:131], a[32:35], a[128:131], v[128:131]\n\t"
    "ds_read_b64_tr_b16 a[8:9], v193\n\t"
    "v_mfma_f32_16x16x32_bf16 v[128:131], a[36:39], a[152:155], v[128:131]\n\t"
    "ds_read_b128 a[64:67], v206\n\t"
    "v_mfma_f32_16x16x32_bf16 v[136:139], a[36:39], a[156:159], v[136:139]\n\t"
    "ds_read_b64_tr_b16 a[10:11], v193 offset:1024\n\t"
    "v_mfma_f32_16x16x32_bf16 v[136:139], a[32:35], a[132:135], v[136:139]\n\t"
    "ds_read_b128 a[88:91], v207\n\t"
    "v_mfma_f32_16x16x32_bf16 v[100:103], a[40:43], a[112:115], v[100:103]\n\t"
    "ds_read_b64_tr_b16 a[12:13], v193 offset:2048\n\t"
    "v_mfma_f32_16x16x32_bf16 v[100:103], a[44:47], a[136:139], v[100:103]\n\t"
    "ds_read_b128 a[68:71], v206 offset:2048\n\t"
    "v_mfma_f32_16x16x32_bf16 v[108:111], a[44:47], a[140:143], v[108:111]\n\t"
    "ds_read_b64_tr_b16 a[14:15], v193 offset:3072\n\t"
    "v_mfma_f32_16x16x32_bf16 v[108:111], a[40:43], a[116:119], v[108:111]\n\t"
    "ds_read_b128 a[92:95], v207 offset:2048\n\t"
    "v_mfma_f32_16x16x32_bf16 v[116:119], a[40:43], a[120:123], v[116:119]\n\t"
    "ds_read_b64_tr_b16 a[16:17], v194\n\t"
    "v_mfma_f32_16x16x32_bf16 v[116:119], a[44:47], a[144:147], v[116:119]\n\t"
    "ds_read_b128 a[72:75], v206 offset:4096\n\t"
    "v_mfma_f32_16x16x32_bf16 v[124:127], a[44:47], a[148:151], v[124:127]\n\t"
    "ds_read_b64_tr_b16 a[18:19], v194 offset:1024\n\t"
    "v_mfma_f32_16x16x32_bf16 v[124:127], a[40:43], a[124:127], v[124:127]\n\t"
    "ds_read_b128 a[96:99], v207 offset:4096\n\t"
    "v_mfma_f32_16x16x32_bf16 v[132:135], a[40:43], a[128:131], v[132:135]\n\t"
    "ds_read_b64_tr_b16 a[20:21], v194 offset:2048\n\t"
    "v_mfma_f32_16x16x32_bf16 v[132:135], a[44:47], a[152:155], v[132:135]\n\t"
    "ds_read_b128 a[76:79], v206 offset:6144\n\t"
    "v_mfma_f32_16x16x32_bf16 v[140:143], a[44:47], a[156:159], v[140:143]\n\t"
    "ds_read_b64_tr_b16 a[22:23], v194 offset:3072\n\t"
    "v_mfma_f32_16x16x32_bf16 v[140:143], a[40:43], a[132:135], v[140:143]\n\t"
    "ds_read_b128 a[100:103], v207 offset:6144\n\t"
    "v_mfma_f32_16x16x32_bf16 v[144:147], a[48:51], a[112:115], v[144:147]\n\t"
    "ds_read_b64_tr_b16 a[24:25], v195\n\t"
    "v_mfma_f32_16x16x32_bf16 v[144:147], a[52:55], a[136:139], v[144:147]\n\t"
    "ds_read_b128 a[80:83], v206 offset:8192\n\t"
    "v_mfma_f32_16x16x32_bf16 v[152:155], a[52:55], a[140:143], v[152:155]\n\t"
    "ds_read_b64_tr_b16 a[26:27], v195 offset:1024\n\t"
    "v_mfma_f32_16x16x32_bf16 v[152:155], a[48:51], a[116:119], v[152:155]\n\t"
    "ds_read_b128 a[104:107], v207 offset:8192\n\t"
    "v_mfma_f32_16x16x32_bf16 v[160:163], a[48:51], a[120:123], v[160:163]\n\t"
    "ds_read_b64_tr_b16 a[28:29], v195 offset:2048\n\t"
    "v_mfma_f32_16x16x32_bf16 v[160:163], a[52:55], a[144:147], v[160:163]\n\t"
    "ds_read_b128 a[84:87], v206 offset:10240\n\t"
    "v_mfma_f32_16x16x32_bf16 v[168:171], a[52:55], a[148:151], v[168:171]\n\t"
    "ds_read_b64_tr_b16 a[30:31], v195 offset:3072\n\t"
    "v_mfma_f32_16x16x32_bf16 v[168:171], a[48:51], a[124:127], v[168:171]\n\t"
    "ds_read_b128 a[108:111], v207 offset:10240\n\t"
    "v_mfma_f32_16x16x32_bf16 v[176:179], a[48:51], a[128:131], v[176:179]\n\t"
    "s_add_u32 m0, s56, 49152\n\t"
    "v_mfma_f32_16x16x32_bf16 v[176:179], a[52:55], a[152:155], v[176:179]\n\t"
    "buffer_load_dwordx4 v208, s[40:43], s61 offen lds\n\t"
    "v_mfma_f32_16x16x32_bf16 v[184:187], a[52:55], a[156:159], v[184:187]\n\t"
    "v_mfma_f32_16x16x32_bf16 v[184:187], a[48:51], a[132:135], v[184:187]\n\t"
    "buffer_load_dwordx4 v209, s[40:43], s61 offen offset:1024 lds\n\t"
    "v_mfma_f32_16x16x32_bf16 v[148:151], a[56:59], a[112:115], v[148:151]\n\t"
    "v_mfma_f32_16x16x32_bf16 v[148:151], a[60:63], a[136:139], v[148:151]\n\t"
    "buffer_load_dwordx4 v210, s[40:43], s61 offen offset:2048 lds\n\t"
    "v_mfma_f32_16x16x32_bf16 v[156:159], a[60:63], a[140:143], v[156:159]\n\t"
    "v_mfma_f32_16x16x32_bf16 v[156:159], a[56:59], a[116:119], v[156:159]\n\t"
    "buffer_load_dwordx4 v211, s[40:43], s61 offen offset:3072 lds\n\t"
    "s_xor_b32 s61, s61, 3670144\n\t"
    "v_mfma_f32_16x16x32_bf16 v[164:167], a[56:59], a[120:123], v[164:167]\n\t"
    "v_mfma_f32_16x16x32_bf16 v[164:167], a[60:63], a[144:147], v[164:167]\n\t"
    "v_mfma_f32_16x16x32_bf16 v[172:175], a[60:63], a[148:151], v[172:175]\n\t"
    "v_mfma_f32_16x16x32_bf16 v[172:175], a[56:59], a[124:127], v[172:175]\n\t"
    "v_mfma_f32_16x16x32_bf16 v[180:183], a[56:59], a[128:131], v[180:183]\n\t"
    "v_mfma_f32_16x16x32_bf16 v[180:183], a[60:63], a[152:155], v[180:183]\n\t"
    "v_mfma_f32_16x16x32_bf16 v[188:191], a[60:63], a[156:159], v[188:191]\n\t"
    "v_mfma_f32_16x16x32_bf16 v[188:191], a[56:59], a[132:135], v[188:191]\n\t"
    "s_waitcnt vmcnt(20) lgkmcnt(0)\n\t"
    "s_barrier\n\t"
    "v_mfma_f32_16x16x32_bf16 v[0:3], a[0:3], a[64:67], v[0:3]\n\t"
    "ds_read_b64_tr_b16 a[32:33], v192 offset:16384\n\t"
    "v_mfma_f32_16x16x32_bf16 v[0:3], a[4:7], a[88:91], v[0:3]\n\t"
    "v_mfma_f32_16x16x32_bf16 v[8:11], a[4:7], a[92:95], v[8:11]\n\t"
    "ds_read_b64_tr_b16 a[34:35], v192 offset:17408\n\t"
    "s_add_u32 m0, s57, 0\n\t"
    "s_nop 0\n\t"
    "v_mfma_f32_16x16x32_bf16 v[8:11], a[0:3], a[68:71], v[8:11]\n\t"
    "buffer_load_dwordx4 v200, s[36:39], s60 offen lds\n\t"
    "v_mfma_f32_16x16x32_bf16 v[16:19], a[0:3], a[72:75], v[16:19]\n\t"
    "ds_read_b64_tr_b16 a[36:37], v192 offset:18432\n\t"
    "v_mfma_f32_16x16x32_bf16 v[16:19], a[4:7], a[96:99], v[16:19]\n\t"
    "v_mfma_f32_16x16x32_bf16 v[24:27], a[4:7], a[100:103], v[24:27]\n\t"
    "ds_read_b64_tr_b16 a[38:39], v192 offset:19456\n\t"
    "v_mfma_f32_16x16x32_bf16 v[24:27], a[0:3], a[76:79], v[24:27]\n\t"
    "buffer_load_dwordx4 v201, s[36:39], s60 offen offset:1024 lds\n\t"
    "v_mfma_f32_16x16x32_bf16 v[32:35], a[0:3], a[80:83], v[32:35]\n\t"
    "ds_read_b64_tr_b16 a[40:41], v193 offset:16384\n\t"
    "v_mfma_f32_16x16x32_bf16 v[32:35], a[4:7], a[104:107], v[32:35]\n\t"
    "v_mfma_f32_16x16x32_bf16 v[40:43], a[4:7], a[108:111], v[40:43]\n\t"
    "ds_read_b64_tr_b16 a[42:43], v193 offset:17408\n\t"
    "v_mfma_f32_16x16x32_bf16 v[40:43], a[0:3], a[84:87], v[40:43]\n\t"
    "buffer_load_dwordx4 v202, s[36:39], s60 offen offset:2048 lds\n\t"
    "v_mfma_f32_16x16x32_bf16 v[4:7], a[8:11], a[64:67], v[4:7]\n\t"
    "ds_read_b64_tr_b16 a[44:45], v193 offset:18432\n\t"
    "v_mfma_f32_16x16x32_bf16 v[4:7], a[12:15], a[88:91], v[4:7]\n\t"
    "v_mfma_f32_16x16x32_bf16 v[12:15], a[12:15], a[92:95], v[12:15]\n\t"
    "ds_read_b64_tr_b16 a[46:47], v193 offset:19456\n\t"
    "v_mfma_f32_16x16x32_bf16 v[12:15], a[8:11], a[68:71], v[12:15]\n\t"
    "buffer_load_dwordx4 v203, s[36:39], s60 offen offset:3072 lds\n\t"
    "v_mfma_f32_16x16x32_bf16 v[20:23], a[8:11], a[72:75], v[20:23]\n\t"
    "ds_read_b64_tr_b16 a[48:49], v194 offset:16384\n\t"
    "v_mfma_f32_16x16x32_bf16 v[20:23], a[12:15], a[96:99], v[20:23]\n\t"
    "v_mfma_f32_16x16x32_bf16 v[28:31], a[12:15], a[100:103], v[28:31]\n\t"
    "ds_read_b64_tr_b16 a[50:51], v194 offset:17408\n\t"
    "s_add_u32 m0, s57, 4096\n\t"
    "v_mfma_f32_16x16x32_bf16 v[28:31], a[8:11], a[76:79], v[28:31]\n\t"
    "buffer_load_dwordx4 v204, s[36:39], s60 offen lds\n\t"
    "v_mfma_f32_16x16x32_bf16 v[36:39], a[8:11], a[80:83], v[36:39]\n\t"
    "ds_read_b64_tr_b16 a[52:53], v194 offset:18432\n\t"
    "v_mfma_f32_16x16x32_bf16 v[36:39], a[12:15], a[104:107], v[36:39]\n\t"
    "v_mfma_f32_16x16x32_bf16 v[44:47], a[12:15], a[108:111], v[44:47]\n\t"
    "ds_read_b64_tr_b16 a[54:55], v194 offset:19456\n\t"
    "v_mfma_f32_16x16x32_bf16 v[44:47], a[8:11], a[84:87], v[44:47]\n\t"
    "buffer_load_dwordx4 v205, s[36:39], s60 offen offset:1024 lds\n\t"
    "s_xor_b32 s60, s60, 128\n\t"
    "v_mfma_f32_16x16x32_bf16 v[48:51], a[16:19], a[64:67], v[48:51]\n\t"
    "ds_read_b64_tr_b16 a[56:57], v195 offset:16384\n\t"
    "v_mfma_f32_16x16x32_bf16 v[48:51], a[20:23], a[88:91], v[48:51]\n\t"
    "v_mfma_f32_16x16x32_bf16 v[56:59], a[20:23], a[92:95], v[56:59]\n\t"
    "ds_read_b64_tr_b16 a[58:59], v195 offset:17408\n\t"
    "s_add_u32 m0, s56, 0\n\t"
    "s_nop 0\n\t"
    "v_mfma_f32_16x16x32_bf16 v[56:59], a[16:19], a[68:71], v[56:59]\n\t"
    "buffer_load_dwordx4 v208, s[40:43], s61 offen lds\n\t"
    "v_mfma_f32_16x16x32_bf16 v[64:67], a[16:19], a[72:75], v[64:67]\n\t"
    "ds_read_b64_tr_b16 a[60:61], v195 offset:18432\n\t"
    "v_mfma_f32_16x16x32_bf16 v[64:67], a[20:23], a[96:99], v[64:67]\n\t"
    "v_mfma_f32_16x16x32_bf16 v[72:75], a[20:23], a[100:103], v[72:75]\n\t"
    "ds_read_b64_tr_b16 a[62:63], v195 offset:19456\n\t"
    "v_mfma_f32_16x16x32_bf16 v[72:75], a[16:19], a[76:79], v[72:75]\n\t"
    "buffer_load_dwordx4 v209, s[40:43], s61 offen offset:1024 lds\n\t"
    "v_mfma_f32_16x16x32_bf16 v[80:83], a[16:19], a[80:83], v[80:83]\n\t"
    "v_mfma_f32_16x16x32_bf16 v[80:83], a[20:23], a[104:107], v[80:83]\n\t"
    "v_mfma_f32_16x16x32_bf16 v[88:91], a[20:23], a[108:111], v[88:91]\n\t"
    "v_mfma_f32_16x16x32_bf16 v[88:91], a[16:19], a[84:87], v[88:91]\n\t"
    "buffer_load_dwordx4 v210, s[40:43], s61 offen offset:2048 lds\n\t"
    "v_mfma_f32_16x16x32_bf16 v[52:55], a[24:27], a[64:67], v[52:55]\n\t"
    "v_mfma_f32_16x16x32_bf16 v[52:55], a[28:31], a[88:91], v[52:55]\n\t"
    "v_mfma_f32_16x16x32_bf16 v[60:63], a[28:31], a[92:95], v[60:63]\n\t"
    "v_mfma_f32_16x16x32_bf16 v[60:63], a[24:27], a[68:71], v[60:63]\n\t"
    "buffer_load_dwordx4 v211, s[40:43], s61 offen offset:3072 lds\n\t"
    "s_xor_b32 s61, s61, 128\n\t"
    "v_mfma_f32_16x16x32_bf16 v[68:71], a[24:27], a[72:75], v[68:71]\n\t"
    "v_mfma_f32_16x16x32_bf16 v[68:71], a[28:31], a[96:99], v[68:71]\n\t"
    "v_mfma_f32_16x16x32_bf16 v[76:79], a[28:31], a[100:103], v[76:79]\n\t"
    "v_mfma_f32_16x16x32_bf16 v[76:79], a[24:27], a[76:79], v[76:79]\n\t"
    "v_mfma_f32_16x16x32_bf16 v[84:87], a[24:27], a[80:83], v[84:87]\n\t"
    "v_mfma_f32_16x16x32_bf16 v[84:87], a[28:31], a[104:107], v[84:87]\n\t"
    "v_mfma_f32_16x16x32_bf16 v[92:95], a[28:31], a[108:111], v[92:95]\n\t"
    "v_mfma_f32_16x16x32_bf16 v[92:95], a[24:27], a[84:87], v[92:95]\n\t"
    "s_waitcnt vmcnt(19) lgkmcnt(0)\n\t"
    "s_barrier\n\t"
    "v_mfma_f32_16x16x32_bf16 v[96:99], a[32:35], a[64:67], v[96:99]\n\t"
    "ds_read_b64_tr_b16 a[0:1], v192 offset:32768\n\t"
    "v_mfma_f32_16x16x32_bf16 v[96:99], a[36:39], a[88:91], v[96:99]\n\t"
    "buffer_load_dword v239, v246, s[64:67], s76 offen\n\t"
    "s_add_u32 s76, s76, 524288\n\t"
    "v_mfma_f32_16x16x32_bf16 v[104:107], a[36:39], a[92:95], v[104:107]\n\t"
    "ds_read_b64_tr_b16 a[2:3], v192 offset:33792\n\t"
    "v_mfma_f32_16x16x32_bf16 v[104:107], a[32:35], a[68:71], v[104:107]\n\t"
    "ds_read_b128 a[112:115], v206 offset:24576\n\t"
    "v_mfma_f32_16x16x32_bf16 v[112:115], a[32:35], a[72:75], v[112:115]\n\t"
    "ds_read_b64_tr_b16 a[4:5], v192 offset:34816\n\t"
    "v_mfma_f32_16x16x32_bf16 v[112:115], a[36:39], a[96:99], v[112:115]\n\t"
    "ds_read_b128 a[136:139], v207 offset:24576\n\t"
    "v_mfma_f32_16x16x32_bf16 v[120:123], a[36:39], a[100:103], v[120:123]\n\t"
    "ds_read_b64_tr_b16 a[6:7], v192 offset:35840\n\t"
    "v_mfma_f32_16x16x32_bf16 v[120:123], a[32:35], a[76:79], v[120:123]\n\t"
    "ds_read_b128 a[116:119], v206 offset:26624\n\t"
    "v_mfma_f32_16x16x32_bf16 v[128:131], a[32:35], a[80:83], v[128:131]\n\t"
    "ds_read_b64_tr_b16 a[8:9], v193 offset:32768\n\t"
    "v_mfma_f32_16x16x32_bf16 v[128:131], a[36:39], a[104:107], v[128:131]\n\t"
    "ds_read_b128 a[140:143], v207 offset:26624\n\t"
    "v_mfma_f32_16x16x32_bf16 v[136:139], a[36:39], a[108:111], v[136:139]\n\t"
    "ds_read_b64_tr_b16 a[10:11], v193 offset:33792\n\t"
    "v_mfma_f32_16x16x32_bf16 v[136:139], a[32:35], a[84:87], v[136:139]\n\t"
    "ds_read_b128 a[120:123], v206 offset:28672\n\t"
    "v_mfma_f32_16x16x32_bf16 v[100:103], a[40:43], a[64:67], v[100:103]\n\t"
    "ds_read_b64_tr_b16 a[12:13], v193 offset:34816\n\t"
    "v_mfma_f32_16x16x32_bf16 v[100:103], a[44:47], a[88:91], v[100:103]\n\t"
    "ds_read_b128 a[144:147], v207 offset:28672\n\t"
    "v_mfma_f32_16x16x32_bf16 v[108:111], a[44:47], a[92:95], v[108:111]\n\t"
    "ds_read_b64_tr_b16 a[14:15], v193 offset:35840\n\t"
    "v_mfma_f32_16x16x32_bf16 v[108:111], a[40:43], a[68:71], v[108:111]\n\t"
    "ds_read_b128 a[124:127], v206 offset:30720\n\t"
    "v_mfma_f32_16x16x32_bf16 v[116:119], a[40:43], a[72:75], v[116:119]\n\t"
    "ds_read_b64_tr_b16 a[16:17], v194 offset:32768\n\t"
    "v_mfma_f32_16x16x32_bf16 v[116:119], a[44:47], a[96:99], v[116:119]\n\t"
    "ds_read_b128 a[148:151], v207 offset:30720\n\t"
    "v_mfma_f32_16x16x32_bf16 v[124:127], a[44:47], a[100:103], v[124:127]\n\t"
    "ds_read_b64_tr_b16 a[18:19], v194 offset:33792\n\t"
    "v_mfma_f32_16x16x32_bf16 v[124:127], a[40:43], a[76:79], v[124:127]\n\t"
    "ds_read_b128 a[128:131], v206 offset:32768\n\t"
    "v_mfma_f32_16x16x32_bf16 v[132:135], a[40:43], a[80:83], v[132:135]\n\t"
    "ds_read_b64_tr_b16 a[20:21], v194 offset:34816\n\t"
    "v_mfma_f32_16x16x32_bf16 v[132:135], a[44:47], a[104:107], v[132:135]\n\t"
    "ds_read_b128 a[152:155], v207 offset:32768\n\t"
    "v_mfma_f32_16x16x32_bf16 v[140:143], a[44:47], a[108:111], v[140:143]\n\t"
    "ds_read_b64_tr_b16 a[22:23], v194 offset:35840\n\t"
    "v_mfma_f32_16x16x32_bf16 v[140:143], a[40:43], a[84:87], v[140:143]\n\t"
    "ds_read_b128 a[132:135], v206 offset:34816\n\t"
    "v_mfma_f32_16x16x32_bf16 v[144:147], a[48:51], a[64:67], v[144:147]\n\t"
    "ds_read_b64_tr_b16 a[24:25], v195 offset:32768\n\t"
    "v_mfma_f32_16x16x32_bf16 v[144:147], a[52:55], a[88:91], v[144:147]\n\t"
    "ds_read_b128 a[156:159], v207 offset:34816\n\t"
    "v_mfma_f32_16x16x32_bf16 v[152:155], a[52:55], a[92:95], v[152:155]\n\t"
    "ds_read_b64_tr_b16 a[26:27], v195 offset:33792\n\t"
    "v_mfma_f32_16x16x32_bf16 v[152:155], a[48:51], a[68:71], v[152:155]\n\t"
    "v_mfma_f32_16x16x32_bf16 v[160:163], a[48:51], a[72:75], v[160:163]\n\t"
    "ds_read_b64_tr_b16 a[28:29], v195 offset:34816\n\t"
    "s_add_u32 m0, s56, 16384\n\t"
    "v_mfma_f32_16x16x32_bf16 v[160:163], a[52:55], a[96:99], v[160:163]\n\t"
    "buffer_load_dwordx4 v208, s[40:43], s61 offen lds\n\t"
    "v_mfma_f32_16x16x32_bf16 v[168:171], a[52:55], a[100:103], v[168:171]\n\t"
    "ds_read_b64_tr_b16 a[30:31], v195 offset:35840\n\t"
    "v_mfma_f32_16x16x32_bf16 v[168:171], a[48:51], a[76:79], v[168:171]\n\t"
    "v_mfma_f32_16x16x32_bf16 v[176:179], a[48:51], a[80:83], v[176:179]\n\t"
    "v_mfma_f32_16x16x32_bf16 v[176:179], a[52:55], a[104:107], v[176:179]\n\t"
    "buffer_load_dwordx4 v209, s[40:43], s61 offen offset:1024 lds\n\t"
    "v_mfma_f32_16x16x32_bf16 v[184:187], a[52:55], a[108:111], v[184:187]\n\t"
    "v_mfma_f32_16x16x32_bf16 v[184:187], a[48:51], a[84:87], v[184:187]\n\t"
    "v_mfma_f32_16x16x32_bf16 v[148:151], a[56:59], a[64:67], v[148:151]\n\t"
    "v_mfma_f32_16x16x32_bf16 v[148:151], a[60:63], a[88:91], v[148:151]\n\t"
    "buffer_load_dwordx4 v210, s[40:43], s61 offen offset:2048 lds\n\t"
    "v_mfma_f32_16x16x32_bf16 v[156:159], a[60:63], a[92:95], v[156:159]\n\t"
    "v_mfma_f32_16x16x32_bf16 v[156:159], a[56:59], a[68:71], v[156:159]\n\t"
    "v_mfma_f32_16x16x32_bf16 v[164:167], a[56:59], a[72:75], v[164:167]\n\t"
    "v_mfma_f32_16x16x32_bf16 v[164:167], a[60:63], a[96:99], v[164:167]\n\t"
    "buffer_load_dwordx4 v211, s[40:43], s61 offen offset:3072 lds\n\t"
    "s_xor_b32 s61, s61, 524416\n\t"
    "v_mfma_f32_16x16x32_bf16 v[172:175], a[60:63], a[100:103], v[172:175]\n\t"
    "v_mfma_f32_16x16x32_bf16 v[172:175], a[56:59], a[76:79], v[172:175]\n\t"
    "v_mfma_f32_16x16x32_bf16 v[180:183], a[56:59], a[80:83], v[180:183]\n\t"
    "v_mfma_f32_16x16x32_bf16 v[180:183], a[60:63], a[104:107], v[180:183]\n\t"
    "v_mfma_f32_16x16x32_bf16 v[188:191], a[60:63], a[108:111], v[188:191]\n\t"
    "v_mfma_f32_16x16x32_bf16 v[188:191], a[56:59], a[84:87], v[188:191]\n\t"
    "s_waitcnt vmcnt(15) lgkmcnt(0)\n\t"
    "s_barrier\n\t"
    "v_mfma_f32_16x16x32_bf16 v[0:3], a[0:3], a[112:115], v[0:3]\n\t"
    "ds_read_b64_tr_b16 a[32:33], v192 offset:49152\n\t"
    "v_mfma_f32_16x16x32_bf16 v[0:3], a[4:7], a[136:139], v[0:3]\n\t"
    "v_mfma_f32_16x16x32_bf16 v[8:11], a[4:7], a[140:143], v[8:11]\n\t"
    "ds_read_b64_tr_b16 a[34:35], v192 offset:50176\n\t"
    "s_add_u32 m0, s57, 24576\n\t"
    "v_mfma_f32_16x16x32_bf16 v[8:11], a[0:3], a[116:119], v[8:11]\n\t"
    "buffer_load_dwordx4 v200, s[36:39], s60 offen lds\n\t"
    "v_mfma_f32_16x16x32_bf16 v[16:19], a[0:3], a[120:123], v[16:19]\n\t"
    "ds_read_b64_tr_b16 a[36:37], v192 offset:51200\n\t"
    "v_mfma_f32_16x16x32_bf16 v[16:19], a[4:7], a[144:147], v[16:19]\n\t"
    "v_mfma_f32_16x16x32_bf16 v[24:27], a[4:7], a[148:151], v[24:27]\n\t"
    "ds_read_b64_tr_b16 a[38:39], v192 offset:52224\n\t"
    "v_mfma_f32_16x16x32_bf16 v[24:27], a[0:3], a[124:127], v[24:27]\n\t"
    "buffer_load_dwordx4 v201, s[36:39], s60 offen offset:1024 lds\n\t"
    "v_mfma_f32_16x16x32_bf16 v[32:35], a[0:3], a[128:131], v[32:35]\n\t"
    "ds_read_b64_tr_b16 a[40:41], v193 offset:49152\n\t"
    "v_mfma_f32_16x16x32_bf16 v[32:35], a[4:7], a[152:155], v[32:35]\n\t"
    "v_mfma_f32_16x16x32_bf16 v[40:43], a[4:7], a[156:159], v[40:43]\n\t"
    "ds_read_b64_tr_b16 a[42:43], v193 offset:50176\n\t"
    "v_mfma_f32_16x16x32_bf16 v[40:43], a[0:3], a[132:135], v[40:43]\n\t"
    "buffer_load_dwordx4 v202, s[36:39], s60 offen offset:2048 lds\n\t"
    "v_mfma_f32_16x16x32_bf16 v[4:7], a[8:11], a[112:115], v[4:7]\n\t"
    "ds_read_b64_tr_b16 a[44:45], v193 offset:51200\n\t"
    "v_mfma_f32_16x16x32_bf16 v[4:7], a[12:15], a[136:139], v[4:7]\n\t"
    "v_mfma_f32_16x16x32_bf16 v[12:15], a[12:15], a[140:143], v[12:15]\n\t"
    "ds_read_b64_tr_b16 a[46:47], v193 offset:52224\n\t"
    "v_mfma_f32_16x16x32_bf16 v[12:15], a[8:11], a[116:119], v[12:15]\n\t"
    "buffer_load_dwordx4 v203, s[36:39], s60 offen offset:3072 lds\n\t"
    "v_mfma_f32_16x16x32_bf16 v[20:23], a[8:11], a[120:123], v[20:23]\n\t"
    "ds_read_b64_tr_b16 a[48:49], v194 offset:49152\n\t"
    "v_mfma_f32_16x16x32_bf16 v[20:23], a[12:15], a[144:147], v[20:23]\n\t"
    "v_mfma_f32_16x16x32_bf16 v[28:31], a[12:15], a[148:151], v[28:31]\n\t"
    "ds_read_b64_tr_b16 a[50:51], v194 offset:50176\n\t"
    "s_add_u32 m0, s57, 28672\n\t"
    "v_mfma_f32_16x16x32_bf16 v[28:31], a[8:11], a[124:127], v[28:31]\n\t"
    "buffer_load_dwordx4 v204, s[36:39], s60 offen lds\n\t"
    "v_mfma_f32_16x16x32_bf16 v[36:39], a[8:11], a[128:131], v[36:39]\n\t"
    "ds_read_b64_tr_b16 a[52:53], v194 offset:51200\n\t"
    "v_mfma_f32_16x16x32_bf16 v[36:39], a[12:15], a[152:155], v[36:39]\n\t"
    "v_mfma_f32_16x16x32_bf16 v[44:47], a[12:15], a[156:159], v[44:47]\n\t"
    "ds_read_b64_tr_b16 a[54:55], v194 offset:52224\n\t"
    "v_mfma_f32_16x16x32_bf16 v[44:47], a[8:11], a[132:135], v[44:47]\n\t"
    "buffer_load_dwordx4 v205, s[36:39], s60 offen offset:1024 lds\n\t"
    "s_xor_b32 s60, s60, 384\n\t"
    "v_mfma_f32_16x16x32_bf16 v[48:51], a[16:19], a[112:115], v[48:51]\n\t"
    "ds_read_b64_tr_b16 a[56:57], v195 offset:49152\n\t"
    "v_mfma_f32_16x16x32_bf16 v[48:51], a[20:23], a[136:139], v[48:51]\n\t"
    "v_mfma_f32_16x16x32_bf16 v[56:59], a[20:23], a[140:143], v[56:59]\n\t"
    "ds_read_b64_tr_b16 a[58:59], v195 offset:50176\n\t"
    "s_add_u32 m0, s56, 32768\n\t"
    "v_mfma_f32_16x16x32_bf16 v[56:59], a[16:19], a[116:119], v[56:59]\n\t"
    "buffer_load_dwordx4 v208, s[40:43], s61 offen lds\n\t"
    "v_mfma_f32_16x16x32_bf16 v[64:67], a[16:19], a[120:123], v[64:67]\n\t"
    "ds_read_b64_tr_b16 a[60:61], v195 offset:51200\n\t"
    "v_mfma_f32_16x16x32_bf16 v[64:67], a[20:23], a[144:147], v[64:67]\n\t"
    "v_mfma_f32_16x16x32_bf16 v[72:75], a[20:23], a[148:151], v[72:75]\n\t"
    "ds_read_b64_tr_b16 a[62:63], v195 offset:52224\n\t"
    "v_mfma_f32_16x16x32_bf16 v[72:75], a[16:19], a[124:127], v[72:75]\n\t"
    "buffer_load_dwordx4 v209, s[40:43], s61 offen offset:1024 lds\n\t"
    "v_mfma_f32_16x16x32_bf16 v[80:83], a[16:19], a[128:131], v[80:83]\n\t"
    "v_mfma_f32_16x16x32_bf16 v[80:83], a[20:23], a[152:155], v[80:83]\n\t"
    "v_mfma_f32_16x16x32_bf16 v[88:91], a[20:23], a[156:159], v[88:91]\n\t"
    "v_mfma_f32_16x16x32_bf16 v[88:91], a[16:19], a[132:135], v[88:91]\n\t"
    "buffer_load_dwordx4 v210, s[40:43], s61 offen offset:2048 lds\n\t"
    "v_mfma_f32_16x16x32_bf16 v[52:55], a[24:27], a[112:115], v[52:55]\n\t"
    "v_mfma_f32_16x16x32_bf16 v[52:55], a[28:31], a[136:139], v[52:55]\n\t"
    "v_mfma_f32_16x16x32_bf16 v[60:63], a[28:31], a[140:143], v[60:63]\n\t"
    "v_mfma_f32_16x16x32_bf16 v[60:63], a[24:27], a[116:119], v[60:63]\n\t"
    "buffer_load_dwordx4 v211, s[40:43], s61 offen offset:3072 lds\n\t"
    "s_xor_b32 s61, s61, 128\n\t"
    "v_mfma_f32_16x16x32_bf16 v[68:71], a[24:27], a[120:123], v[68:71]\n\t"
    "v_mfma_f32_16x16x32_bf16 v[68:71], a[28:31], a[144:147], v[68:71]\n\t"
    "v_mfma_f32_16x16x32_bf16 v[76:79], a[28:31], a[148:151], v[76:79]\n\t"
    "v_mfma_f32_16x16x32_bf16 v[76:79], a[24:27], a[124:127], v[76:79]\n\t"
    "v_mfma_f32_16x16x32_bf16 v[84:87], a[24:27], a[128:131], v[84:87]\n\t"
    "v_mfma_f32_16x16x32_bf16 v[84:87], a[28:31], a[152:155], v[84:87]\n\t"
    "v_mfma_f32_16x16x32_bf16 v[92:95], a[28:31], a[156:159], v[92:95]\n\t"
    "v_mfma_f32_16x16x32_bf16 v[92:95], a[24:27], a[132:135], v[92:95]\n\t"
    "s_waitcnt vmcnt(15) lgkmcnt(0)\n\t"
    "s_barrier\n\t"
    "v_mfma_f32_16x16x32_bf16 v[96:99], a[32:35], a[112:115], v[96:99]\n\t"
    "ds_read_b64_tr_b16 a[0:1], v192\n\t"
    "s_add_u32 m0, s58, 35840\n\t"
    "v_mfma_f32_16x16x32_bf16 v[96:99], a[36:39], a[136:139], v[96:99]\n\t"
    "buffer_load_dwordx4 v212, s[44:47], s62 offen offset:1024 nt lds\n\t"
    "v_mfma_f32_16x16x32_bf16 v[104:107], a[36:39], a[140:143], v[104:107]\n\t"
    "ds_read_b64_tr_b16 a[2:3], v192 offset:1024\n\t"
    "s_add_u32 m0, s58, 36608\n\t"
    "v_mfma_f32_16x16x32_bf16 v[104:107], a[32:35], a[116:119], v[104:107]\n\t"
    "buffer_load_dwordx4 v213, s[44:47], s62 offen offset:1280 nt lds\n\t"
    "v_mfma_f32_16x16x32_bf16 v[112:115], a[32:35], a[120:123], v[112:115]\n\t"
    "ds_read_b64_tr_b16 a[4:5], v192 offset:2048\n\t"
    "s_add_u32 m0, s58, 37376\n\t"
    "v_mfma_f32_16x16x32_bf16 v[112:115], a[36:39], a[144:147], v[112:115]\n\t"
    "buffer_load_dwordx4 v214, s[44:47], s62 offen offset:1536 nt lds\n\t"
    "v_mfma_f32_16x16x32_bf16 v[120:123], a[36:39], a[148:151], v[120:123]\n\t"
    "ds_read_b64_tr_b16 a[6:7], v192 offset:3072\n\t"
    "s_add_u32 m0, s58, 38144\n\t"
    "v_mfma_f32_16x16x32_bf16 v[120:123], a[32:35], a[124:127], v[120:123]\n\t"
    "buffer_load_dwordx4 v215, s[44:47], s62 offen offset:1792 nt lds\n\t"
    "s_or_b32 s62, s62, 12288\n\t"
    "s_add_u32 s62, s62, 2048\n\t"
    "s_and_b32 s62, s62, 116736\n\t"
    "v_mfma_f32_16x16x32_bf16 v[128:131], a[32:35], a[128:131], v[128:131]\n\t"
    "ds_read_b64_tr_b16 a[8:9], v193\n\t"
    "v_mfma_f32_16x16x32_bf16 v[128:131], a[36:39], a[152:155], v[128:131]\n\t"
    "ds_read_b128 a[64:67], v206\n\t"
    "v_mfma_f32_16x16x32_bf16 v[136:139], a[36:39], a[156:159], v[136:139]\n\t"
    "ds_read_b64_tr_b16 a[10:11], v193 offset:1024\n\t"
    "v_mfma_f32_16x16x32_bf16 v[136:139], a[32:35], a[132:135], v[136:139]\n\t"
    "ds_read_b128 a[88:91], v207\n\t"
    "v_mfma_f32_16x16x32_bf16 v[100:103], a[40:43], a[112:115], v[100:103]\n\t"
    "ds_read_b64_tr_b16 a[12:13], v193 offset:2048\n\t"
    "v_mfma_f32_16x16x32_bf16 v[100:103], a[44:47], a[136:139], v[100:103]\n\t"
    "ds_read_b128 a[68:71], v206 offset:2048\n\t"
    "v_mfma_f32_16x16x32_bf16 v[108:111], a[44:47], a[140:143], v[108:111]\n\t"
    "ds_read_b64_tr_b16 a[14:15], v193 offset:3072\n\t"
    "v_mfma_f32_16x16x32_bf16 v[108:111], a[40:43], a[116:119], v[108:111]\n\t"
    "ds_read_b128 a[92:95], v207 offset:2048\n\t"
    "v_mfma_f32_16x16x32_bf16 v[116:119], a[40:43], a[120:123], v[116:119]\n\t"
    "ds_read_b64_tr_b16 a[16:17], v194\n\t"
    "v_mfma_f32_16x16x32_bf16 v[116:119], a[44:47], a[144:147], v[116:119]\n\t"
    "ds_read_b128 a[72:75], v206 offset:4096\n\t"
    "v_mfma_f32_16x16x32_bf16 v[124:127], a[44:47], a[148:151], v[124:127]\n\t"
    "ds_read_b64_tr_b16 a[18:19], v194 offset:1024\n\t"
    "v_mfma_f32_16x16x32_bf16 v[124:127], a[40:43], a[124:127], v[124:127]\n\t"
    "ds_read_b128 a[96:99], v207 offset:4096\n\t"
    "v_mfma_f32_16x16x32_bf16 v[132:135], a[40:43], a[128:131], v[132:135]\n\t"
    "ds_read_b64_tr_b16 a[20:21], v194 offset:2048\n\t"
    "v_mfma_f32_16x16x32_bf16 v[132:135], a[44:47], a[152:155], v[132:135]\n\t"
    "ds_read_b128 a[76:79], v206 offset:6144\n\t"
    "v_mfma_f32_16x16x32_bf16 v[140:143], a[44:47], a[156:159], v[140:143]\n\t"
    "ds_read_b64_tr_b16 a[22:23], v194 offset:3072\n\t"
    "v_mfma_f32_16x16x32_bf16 v[140:143], a[40:43], a[132:135], v[140:143]\n\t"
    "ds_read_b128 a[100:103], v207 offset:6144\n\t"
    "v_mfma_f32_16x16x32_bf16 v[144:147], a[48:51], a[112:115], v[144:147]\n\t"
    "ds_read_b64_tr_b16 a[24:25], v195\n\t"
    "v_mfma_f32_16x16x32_bf16 v[144:147], a[52:55], a[136:139], v[144:147]\n\t"
    "ds_read_b128 a[80:83], v206 offset:8192\n\t"
    "v_mfma_f32_16x16x32_bf16 v[152:155], a[52:55], a[140:143], v[152:155]\n\t"
    "ds_read_b64_tr_b16 a[26:27], v195 offset:1024\n\t"
    "v_mfma_f32_16x16x32_bf16 v[152:155], a[48:51], a[116:119], v[152:155]\n\t"
    "ds_read_b128 a[104:107], v207 offset:8192\n\t"
    "v_mfma_f32_16x16x32_bf16 v[160:163], a[48:51], a[120:123], v[160:163]\n\t"
    "ds_read_b64_tr_b16 a[28:29], v195 offset:2048\n\t"
    "v_mfma_f32_16x16x32_bf16 v[160:163], a[52:55], a[144:147], v[160:163]\n\t"
    "ds_read_b128 a[84:87], v206 offset:10240\n\t"
    "v_mfma_f32_16x16x32_bf16 v[168:171], a[52:55], a[148:151], v[168:171]\n\t"
    "ds_read_b64_tr_b16 a[30:31], v195 offset:3072\n\t"
    "v_mfma_f32_16x16x32_bf16 v[168:171], a[48:51], a[124:127], v[168:171]\n\t"
    "ds_read_b128 a[108:111], v207 offset:10240\n\t"
    "v_mfma_f32_16x16x32_bf16 v[176:179], a[48:51], a[128:131], v[176:179]\n\t"
    "s_add_u32 m0, s56, 49152\n\t"
    "v_mfma_f32_16x16x32_bf16 v[176:179], a[52:55], a[152:155], v[176:179]\n\t"
    "buffer_load_dwordx4 v208, s[40:43], s61 offen lds\n\t"
    "v_mfma_f32_16x16x32_bf16 v[184:187], a[52:55], a[156:159], v[184:187]\n\t"
    "v_mfma_f32_16x16x32_bf16 v[184:187], a[48:51], a[132:135], v[184:187]\n\t"
    "buffer_load_dwordx4 v209, s[40:43], s61 offen offset:1024 lds\n\t"
    "v_mfma_f32_16x16x32_bf16 v[148:151], a[56:59], a[112:115], v[148:151]\n\t"
    "v_mfma_f32_16x16x32_bf16 v[148:151], a[60:63], a[136:139], v[148:151]\n\t"
    "buffer_load_dwordx4 v210, s[40:43], s61 offen offset:2048 lds\n\t"
    "v_mfma_f32_16x16x32_bf16 v[156:159], a[60:63], a[140:143], v[156:159]\n\t"
    "v_mfma_f32_16x16x32_bf16 v[156:159], a[56:59], a[116:119], v[156:159]\n\t"
    "buffer_load_dwordx4 v211, s[40:43], s61 offen offset:3072 lds\n\t"
    "s_xor_b32 s61, s61, 1572992\n\t"
    "v_mfma_f32_16x16x32_bf16 v[164:167], a[56:59], a[120:123], v[164:167]\n\t"
    "v_mfma_f32_16x16x32_bf16 v[164:167], a[60:63], a[144:147], v[164:167]\n\t"
    "v_mfma_f32_16x16x32_bf16 v[172:175], a[60:63], a[148:151], v[172:175]\n\t"
    "v_mfma_f32_16x16x32_bf16 v[172:175], a[56:59], a[124:127], v[172:175]\n\t"
    "v_mfma_f32_16x16x32_bf16 v[180:183], a[56:59], a[128:131], v[180:183]\n\t"
    "v_mfma_f32_16x16x32_bf16 v[180:183], a[60:63], a[152:155], v[180:183]\n\t"
    "v_mfma_f32_16x16x32_bf16 v[188:191], a[60:63], a[156:159], v[188:191]\n\t"
    "v_mfma_f32_16x16x32_bf16 v[188:191], a[56:59], a[132:135], v[188:191]\n\t"
    "s_waitcnt vmcnt(18) lgkmcnt(0)\n\t"
    "s_barrier\n\t"
    "v_mfma_f32_16x16x32_bf16 v[0:3], a[0:3], a[64:67], v[0:3]\n\t"
    "ds_read_b64_tr_b16 a[32:33], v192 offset:16384\n\t"
    "v_mfma_f32_16x16x32_bf16 v[0:3], a[4:7], a[88:91], v[0:3]\n\t"
    "v_mfma_f32_16x16x32_bf16 v[8:11], a[4:7], a[92:95], v[8:11]\n\t"
    "ds_read_b64_tr_b16 a[34:35], v192 offset:17408\n\t"
    "s_add_u32 m0, s57, 0\n\t"
    "s_nop 0\n\t"
    "v_mfma_f32_16x16x32_bf16 v[8:11], a[0:3], a[68:71], v[8:11]\n\t"
    "buffer_load_dwordx4 v200, s[36:39], s60 offen lds\n\t"
    "v_mfma_f32_16x16x32_bf16 v[16:19], a[0:3], a[72:75], v[16:19]\n\t"
    "ds_read_b64_tr_b16 a[36:37], v192 offset:18432\n\t"
    "v_mfma_f32_16x16x32_bf16 v[16:19], a[4:7], a[96:99], v[16:19]\n\t"
    "v_mfma_f32_16x16x32_bf16 v[24:27], a[4:7], a[100:103], v[24:27]\n\t"
    "ds_read_b64_tr_b16 a[38:39], v192 offset:19456\n\t"
    "v_mfma_f32_16x16x32_bf16 v[24:27], a[0:3], a[76:79], v[24:27]\n\t"
    "buffer_load_dwordx4 v201, s[36:39], s60 offen offset:1024 lds\n\t"
    "v_mfma_f32_16x16x32_bf16 v[32:35], a[0:3], a[80:83], v[32:35]\n\t"
    "ds_read_b64_tr_b16 a[40:41], v193 offset:16384\n\t"
    "v_mfma_f32_16x16x32_bf16 v[32:35], a[4:7], a[104:107], v[32:35]\n\t"
    "v_mfma_f32_16x16x32_bf16 v[40:43], a[4:7], a[108:111], v[40:43]\n\t"
    "ds_read_b64_tr_b16 a[42:43], v193 offset:17408\n\t"
    "v_mfma_f32_16x16x32_bf16 v[40:43], a[0:3], a[84:87], v[40:43]\n\t"
    "buffer_load_dwordx4 v202, s[36:39], s60 offen offset:2048 lds\n\t"
    "v_mfma_f32_16x16x32_bf16 v[4:7], a[8:11], a[64:67], v[4:7]\n\t"
    "ds_read_b64_tr_b16 a[44:45], v193 offset:18432\n\t"
    "v_mfma_f32_16x16x32_bf16 v[4:7], a[12:15], a[88:91], v[4:7]\n\t"
    "v_mfma_f32_16x16x32_bf16 v[12:15], a[12:15], a[92:95], v[12:15]\n\t"
    "ds_read_b64_tr_b16 a[46:47], v193 offset:19456\n\t"
    "v_mfma_f32_16x16x32_bf16 v[12:15], a[8:11], a[68:71], v[12:15]\n\t"
    "buffer_load_dwordx4 v203, s[36:39], s60 offen offset:3072 lds\n\t"
    "v_mfma_f32_16x16x32_bf16 v[20:23], a[8:11], a[72:75], v[20:23]\n\t"
    "ds_read_b64_tr_b16 a[48:49], v194 offset:16384\n\t"
    "v_mfma_f32_16x16x32_bf16 v[20:23], a[12:15], a[96:99], v[20:23]\n\t"
    "v_mfma_f32_16x16x32_bf16 v[28:31], a[12:15], a[100:103], v[28:31]\n\t"
    "ds_read_b64_tr_b16 a[50:51], v194 offset:17408\n\t"
    "s_add_u32 m0, s57, 4096\n\t"
    "v_mfma_f32_16x16x32_bf16 v[28:31], a[8:11], a[76:79], v[28:31]\n\t"
    "buffer_load_dwordx4 v204, s[36:39], s60 offen lds\n\t"
    "v_mfma_f32_16x16x32_bf16 v[36:39], a[8:11], a[80:83], v[36:39]\n\t"
    "ds_read_b64_tr_b16 a[52:53], v194 offset:18432\n\t"
    "v_mfma_f32_16x16x32_bf16 v[36:39], a[12:15], a[104:107], v[36:39]\n\t"
    "v_mfma_f32_16x16x32_bf16 v[44:47], a[12:15], a[108:111], v[44:47]\n\t"
    "ds_read_b64_tr_b16 a[54:55], v194 offset:19456\n\t"
    "v_mfma_f32_16x16x32_bf16 v[44:47], a[8:11], a[84:87], v[44:47]\n\t"
    "buffer_load_dwordx4 v205, s[36:39], s60 offen offset:1024 lds\n\t"
    "s_xor_b32 s60, s60, 128\n\t"
    "v_mfma_f32_16x16x32_bf16 v[48:51], a[16:19], a[64:67], v[48:51]\n\t"
    "ds_read_b64_tr_b16 a[56:57], v195 offset:16384\n\t"
    "v_mfma_f32_16x16x32_bf16 v[48:51], a[20:23], a[88:91], v[48:51]\n\t"
    "v_mfma_f32_16x16x32_bf16 v[56:59], a[20:23], a[92:95], v[56:59]\n\t"
    "ds_read_b64_tr_b16 a[58:59], v195 offset:17408\n\t"
    "s_add_u32 m0, s56, 0\n\t"
    "s_nop 0\n\t"
    "v_mfma_f32_16x16x32_bf16 v[56:59], a[16:19], a[68:71], v[56:59]\n\t"
    "buffer_load_dwordx4 v208, s[40:43], s61 offen lds\n\t"
    "v_mfma_f32_16x16x32_bf16 v[64:67], a[16:19], a[72:75], v[64:67]\n\t"
    "ds_read_b64_tr_b16 a[60:61], v195 offset:18432\n\t"
    "v_mfma_f32_16x16x32_bf16 v[64:67], a[20:23], a[96:99], v[64:67]\n\t"
    "v_mfma_f32_16x16x32_bf16 v[72:75], a[20:23], a[100:103], v[72:75]\n\t"
    "ds_read_b64_tr_b16 a[62:63], v195 offset:19456\n\t"
    "v_mfma_f32_16x16x32_bf16 v[72:75], a[16:19], a[76:79], v[72:75]\n\t"
    "buffer_load_dwordx4 v209, s[40:43], s61 offen offset:1024 lds\n\t"
    "v_mfma_f32_16x16x32_bf16 v[80:83], a[16:19], a[80:83], v[80:83]\n\t"
    "v_mfma_f32_16x16x32_bf16 v[80:83], a[20:23], a[104:107], v[80:83]\n\t"
    "v_mfma_f32_16x16x32_bf16 v[88:91], a[20:23], a[108:111], v[88:91]\n\t"
    "v_mfma_f32_16x16x32_bf16 v[88:91], a[16:19], a[84:87], v[88:91]\n\t"
    "buffer_load_dwordx4 v210, s[40:43], s61 offen offset:2048 lds\n\t"
    "v_mfma_f32_16x16x32_bf16 v[52:55], a[24:27], a[64:67], v[52:55]\n\t"
    "v_mfma_f32_16x16x32_bf16 v[52:55], a[28:31], a[88:91], v[52:55]\n\t"
    "v_mfma_f32_16x16x32_bf16 v[60:63], a[28:31], a[92:95], v[60:63]\n\t"
    "v_mfma_f32_16x16x32_bf16 v[60:63], a[24:27], a[68:71], v[60:63]\n\t"
    "buffer_load_dwordx4 v211, s[40:43], s61 offen offset:3072 lds\n\t"
    "s_xor_b32 s61, s61, 128\n\t"
    "v_mfma_f32_16x16x32_bf16 v[68:71], a[24:27], a[72:75], v[68:71]\n\t"
    "v_mfma_f32_16x16x32_bf16 v[68:71], a[28:31], a[96:99], v[68:71]\n\t"
    "v_mfma_f32_16x16x32_bf16 v[76:79], a[28:31], a[100:103], v[76:79]\n\t"
    "v_mfma_f32_16x16x32_bf16 v[76:79], a[24:27], a[76:79], v[76:79]\n\t"
    "v_mfma_f32_16x16x32_bf16 v[84:87], a[24:27], a[80:83], v[84:87]\n\t"
    "v_mfma_f32_16x16x32_bf16 v[84:87], a[28:31], a[104:107], v[84:87]\n\t"
    "v_mfma_f32_16x16x32_bf16 v[92:95], a[28:31], a[108:111], v[92:95]\n\t"
    "v_mfma_f32_16x16x32_bf16 v[92:95], a[24:27], a[84:87], v[92:95]\n\t"
    "s_waitcnt vmcnt(18) lgkmcnt(0)\n\t"
    "s_barrier\n\t"
    "v_mfma_f32_16x16x32_bf16 v[96:99], a[32:35], a[64:67], v[96:99]\n\t"
    "ds_read_b64_tr_b16 a[0:1], v192 offset:32768\n\t"
    "v_mfma_f32_16x16x32_bf16 v[96:99], a[36:39], a[88:91], v[96:99]\n\t"
    "buffer_load_dword v239, v246, s[64:67], s76 offen\n\t"
    "s_add_u32 s76, s76, 524288\n\t"
    "v_mfma_f32_16x16x32_bf16 v[104:107], a[36:39], a[92:95], v[104:107]\n\t"
    "ds_read_b64_tr_b16 a[2:3], v192 offset:33792\n\t"
    "v_mfma_f32_16x16x32_bf16 v[104:107], a[32:35], a[68:71], v[104:107]\n\t"
    "ds_read_b128 a[112:115], v206 offset:24576\n\t"
    "v_mfma_f32_16x16x32_bf16 v[112:115], a[32:35], a[72:75], v[112:115]\n\t"
    "ds_read_b64_tr_b16 a[4:5], v192 offset:34816\n\t"
    "v_mfma_f32_16x16x32_bf16 v[112:115], a[36:39], a[96:99], v[112:115]\n\t"
    "ds_read_b128 a[136:139], v207 offset:24576\n\t"
    "v_mfma_f32_16x16x32_bf16 v[120:123], a[36:39], a[100:103], v[120:123]\n\t"
    "ds_read_b64_tr_b16 a[6:7], v192 offset:35840\n\t"
    "v_mfma_f32_16x16x32_bf16 v[120:123], a[32:35], a[76:79], v[120:123]\n\t"
    "ds_read_b128 a[116:119], v206 offset:26624\n\t"
    "v_mfma_f32_16x16x32_bf16 v[128:131], a[32:35], a[80:83], v[128:131]\n\t"
    "ds_read_b64_tr_b16 a[8:9], v193 offset:32768\n\t"
    "v_mfma_f32_16x16x32_bf16 v[128:131], a[36:39], a[104:107], v[128:131]\n\t"
    "ds_read_b128 a[140:143], v207 offset:26624\n\t"
    "v_mfma_f32_16x16x32_bf16 v[136:139], a[36:39], a[108:111], v[136:139]\n\t"
    "ds_read_b64_tr_b16 a[10:11], v193 offset:33792\n\t"
    "v_mfma_f32_16x16x32_bf16 v[136:139], a[32:35], a[84:87], v[136:139]\n\t"
    "ds_read_b128 a[120:123], v206 offset:28672\n\t"
    "v_mfma_f32_16x16x32_bf16 v[100:103], a[40:43], a[64:67], v[100:103]\n\t"
    "ds_read_b64_tr_b16 a[12:13], v193 offset:34816\n\t"
    "v_mfma_f32_16x16x32_bf16 v[100:103], a[44:47], a[88:91], v[100:103]\n\t"
    "ds_read_b128 a[144:147], v207 offset:28672\n\t"
    "v_mfma_f32_16x16x32_bf16 v[108:111], a[44:47], a[92:95], v[108:111]\n\t"
    "ds_read_b64_tr_b16 a[14:15], v193 offset:35840\n\t"
    "v_mfma_f32_16x16x32_bf16 v[108:111], a[40:43], a[68:71], v[108:111]\n\t"
    "ds_read_b128 a[124:127], v206 offset:30720\n\t"
    "v_mfma_f32_16x16x32_bf16 v[116:119], a[40:43], a[72:75], v[116:119]\n\t"
    "ds_read_b64_tr_b16 a[16:17], v194 offset:32768\n\t"
    "v_mfma_f32_16x16x32_bf16 v[116:119], a[44:47], a[96:99], v[116:119]\n\t"
    "ds_read_b128 a[148:151], v207 offset:30720\n\t"
    "v_mfma_f32_16x16x32_bf16 v[124:127], a[44:47], a[100:103], v[124:127]\n\t"
    "ds_read_b64_tr_b16 a[18:19], v194 offset:33792\n\t"
    "v_mfma_f32_16x16x32_bf16 v[124:127], a[40:43], a[76:79], v[124:127]\n\t"
    "ds_read_b128 a[128:131], v206 offset:32768\n\t"
    "v_mfma_f32_16x16x32_bf16 v[132:135], a[40:43], a[80:83], v[132:135]\n\t"
    "ds_read_b64_tr_b16 a[20:21], v194 offset:34816\n\t"
    "v_mfma_f32_16x16x32_bf16 v[132:135], a[44:47], a[104:107], v[132:135]\n\t"
    "ds_read_b128 a[152:155], v207 offset:32768\n\t"
    "v_mfma_f32_16x16x32_bf16 v[140:143], a[44:47], a[108:111], v[140:143]\n\t"
    "ds_read_b64_tr_b16 a[22:23], v194 offset:35840\n\t"
    "v_mfma_f32_16x16x32_bf16 v[140:143], a[40:43], a[84:87], v[140:143]\n\t"
    "ds_read_b128 a[132:135], v206 offset:34816\n\t"
    "v_mfma_f32_16x16x32_bf16 v[144:147], a[48:51], a[64:67], v[144:147]\n\t"
    "ds_read_b64_tr_b16 a[24:25], v195 offset:32768\n\t"
    "v_mfma_f32_16x16x32_bf16 v[144:147], a[52:55], a[88:91], v[144:147]\n\t"
    "ds_read_b128 a[156:159], v207 offset:34816\n\t"
    "v_mfma_f32_16x16x32_bf16 v[152:155], a[52:55], a[92:95], v[152:155]\n\t"
    "ds_read_b64_tr_b16 a[26:27], v195 offset:33792\n\t"
    "v_mfma_f32_16x16x32_bf16 v[152:155], a[48:51], a[68:71], v[152:155]\n\t"
    "v_mfma_f32_16x16x32_bf16 v[160:163], a[48:51], a[72:75], v[160:163]\n\t"
    "ds_read_b64_tr_b16 a[28:29], v195 offset:34816\n\t"
    "s_add_u32 m0, s56, 16384\n\t"
    "v_mfma_f32_16x16x32_bf16 v[160:163], a[52:55], a[96:99], v[160:163]\n\t"
    "buffer_load_dwordx4 v208, s[40:43], s61 offen lds\n\t"
    "v_mfma_f32_16x16x32_bf16 v[168:171], a[52:55], a[100:103], v[168:171]\n\t"
    "ds_read_b64_tr_b16 a[30:31], v195 offset:35840\n\t"
    "v_mfma_f32_16x16x32_bf16 v[168:171], a[48:51], a[76:79], v[168:171]\n\t"
    "v_mfma_f32_16x16x32_bf16 v[176:179], a[48:51], a[80:83], v[176:179]\n\t"
    "v_mfma_f32_16x16x32_bf16 v[176:179], a[52:55], a[104:107], v[176:179]\n\t"
    "buffer_load_dwordx4 v209, s[40:43], s61 offen offset:1024 lds\n\t"
    "v_mfma_f32_16x16x32_bf16 v[184:187], a[52:55], a[108:111], v[184:187]\n\t"
    "v_mfma_f32_16x16x32_bf16 v[184:187], a[48:51], a[84:87], v[184:187]\n\t"
    "v_mfma_f32_16x16x32_bf16 v[148:151], a[56:59], a[64:67], v[148:151]\n\t"
    "v_mfma_f32_16x16x32_bf16 v[148:151], a[60:63], a[88:91], v[148:151]\n\t"
    "buffer_load_dwordx4 v210, s[40:43], s61 offen offset:2048 lds\n\t"
    "v_mfma_f32_16x16x32_bf16 v[156:159], a[60:63], a[92:95], v[156:159]\n\t"
    "v_mfma_f32_16x16x32_bf16 v[156:159], a[56:59], a[68:71], v[156:159]\n\t"
    "v_mfma_f32_16x16x32_bf16 v[164:167], a[56:59], a[72:75], v[164:167]\n\t"
    "v_mfma_f32_16x16x32_bf16 v[164:167], a[60:63], a[96:99], v[164:167]\n\t"
    "buffer_load_dwordx4 v211, s[40:43], s61 offen offset:3072 lds\n\t"
    "s_xor_b32 s61, s61, 524416\n\t"
    "v_mfma_f32_16x16x32_bf16 v[172:175], a[60:63], a[100:103], v[172:175]\n\t"
    "v_mfma_f32_16x16x32_bf16 v[172:175], a[56:59], a[76:79], v[172:175]\n\t"
    "v_mfma_f32_16x16x32_bf16 v[180:183], a[56:59], a[80:83], v[180:183]\n\t"
    "v_mfma_f32_16x16x32_bf16 v[180:183], a[60:63], a[104:107], v[180:183]\n\t"
    "v_mfma_f32_16x16x32_bf16 v[188:191], a[60:63], a[108:111], v[188:191]\n\t"
    "v_mfma_f32_16x16x32_bf16 v[188:191], a[56:59], a[84:87], v[188:191]\n\t"
    "s_waitcnt vmcnt(15) lgkmcnt(0)\n\t"
    "s_barrier\n\t"
    "v_mfma_f32_16x16x32_bf16 v[0:3], a[0:3], a[112:115], v[0:3]\n\t"
    "ds_read_b64_tr_b16 a[32:33], v192 offset:49152\n\t"
    "v_mfma_f32_16x16x32_bf16 v[0:3], a[4:7], a[136:139], v[0:3]\n\t"
    "ds_read_b128 a[192:195], v224 offset:32768\n\t"
    "v_mfma_f32_16x16x32_bf16 v[8:11], a[4:7], a[140:143], v[8:11]\n\t"
    "ds_read_b64_tr_b16 a[34:35], v192 offset:50176\n\t"
    "s_add_u32 m0, s57, 24576\n\t"
    "v_mfma_f32_16x16x32_bf16 v[8:11], a[0:3], a[116:119], v[8:11]\n\t"
    "buffer_load_dwordx4 v200, s[36:39], s60 offen lds\n\t"
    "v_mfma_f32_16x16x32_bf16 v[16:19], a[0:3], a[120:123], v[16:19]\n\t"
    "ds_read_b64_tr_b16 a[36:37], v192 offset:51200\n\t"
    "v_mfma_f32_16x16x32_bf16 v[16:19], a[4:7], a[144:147], v[16:19]\n\t"
    "ds_read_b128 a[196:199], v225 offset:32768\n\t"
    "v_mfma_f32_16x16x32_bf16 v[24:27], a[4:7], a[148:151], v[24:27]\n\t"
    "ds_read_b64_tr_b16 a[38:39], v192 offset:52224\n\t"
    "v_mfma_f32_16x16x32_bf16 v[24:27], a[0:3], a[124:127], v[24:27]\n\t"
    "buffer_load_dwordx4 v201, s[36:39], s60 offen offset:1024 lds\n\t"
    "v_mfma_f32_16x16x32_bf16 v[32:35], a[0:3], a[128:131], v[32:35]\n\t"
    "ds_read_b64_tr_b16 a[40:41], v193 offset:49152\n\t"
    "v_mfma_f32_16x16x32_bf16 v[32:35], a[4:7], a[152:155], v[32:35]\n\t"
    "ds_read_b128 a[200:203], v226 offset:32768\n\t"
    "v_mfma_f32_16x16x32_bf16 v[40:43], a[4:7], a[156:159], v[40:43]\n\t"
    "ds_read_b64_tr_b16 a[42:43], v193 offset:50176\n\t"
    "v_mfma_f32_16x16x32_bf16 v[40:43], a[0:3], a[132:135], v[40:43]\n\t"
    "buffer_load_dwordx4 v202, s[36:39], s60 offen offset:2048 lds\n\t"
    "v_mfma_f32_16x16x32_bf16 v[4:7], a[8:11], a[112:115], v[4:7]\n\t"
    "ds_read_b64_tr_b16 a[44:45], v193 offset:51200\n\t"
    "v_mfma_f32_16x16x32_bf16 v[4:7], a[12:15], a[136:139], v[4:7]\n\t"
    "ds_read_b128 a[204:207], v227 offset:32768\n\t"
    "v_mfma_f32_16x16x32_bf16 v[12:15], a[12:15], a[140:143], v[12:15]\n\t"
    "ds_read_b64_tr_b16 a[46:47], v193 offset:52224\n\t"
    "v_mfma_f32_16x16x32_bf16 v[12:15], a[8:11], a[116:119], v[12:15]\n\t"
    "buffer_load_dwordx4 v203, s[36:39], s60 offen offset:3072 lds\n\t"
    "v_mfma_f32_16x16x32_bf16 v[20:23], a[8:11], a[120:123], v[20:23]\n\t"
    "ds_read_b64_tr_b16 a[48:49], v194 offset:49152\n\t"
    "v_mfma_f32_16x16x32_bf16 v[20:23], a[12:15], a[144:147], v[20:23]\n\t"
    "ds_read_b128 a[208:211], v224 offset:36864\n\t"
    "v_mfma_f32_16x16x32_bf16 v[28:31], a[12:15], a[148:151], v[28:31]\n\t"
    "ds_read_b64_tr_b16 a[50:51], v194 offset:50176\n\t"
    "s_add_u32 m0, s57, 28672\n\t"
    "v_mfma_f32_16x16x32_bf16 v[28:31], a[8:11], a[124:127], v[28:31]\n\t"
    "buffer_load_dwordx4 v204, s[36:39], s60 offen lds\n\t"
    "v_mfma_f32_16x16x32_bf16 v[36:39], a[8:11], a[128:131], v[36:39]\n\t"
    "ds_read_b64_tr_b16 a[52:53], v194 offset:51200\n\t"
    "v_mfma_f32_16x16x32_bf16 v[36:39], a[12:15], a[152:155], v[36:39]\n\t"
    "ds_read_b128 a[212:215], v225 offset:36864\n\t"
    "v_mfma_f32_16x16x32_bf16 v[44:47], a[12:15], a[156:159], v[44:47]\n\t"
    "ds_read_b64_tr_b16 a[54:55], v194 offset:52224\n\t"
    "v_mfma_f32_16x16x32_bf16 v[44:47], a[8:11], a[132:135], v[44:47]\n\t"
    "buffer_load_dwordx4 v205, s[36:39], s60 offen offset:1024 lds\n\t"
    "s_xor_b32 s60, s60, 1920\n\t"
    "v_mfma_f32_16x16x32_bf16 v[48:51], a[16:19], a[112:115], v[48:51]\n\t"
    "ds_read_b64_tr_b16 a[56:57], v195 offset:49152\n\t"
    "v_mfma_f32_16x16x32_bf16 v[48:51], a[20:23], a[136:139], v[48:51]\n\t"
    "ds_read_b128 a[216:219], v226 offset:36864\n\t"
    "v_mfma_f32_16x16x32_bf16 v[56:59], a[20:23], a[140:143], v[56:59]\n\t"
    "ds_read_b64_tr_b16 a[58:59], v195 offset:50176\n\t"
    "s_add_u32 m0, s56, 32768\n\t"
    "v_mfma_f32_16x16x32_bf16 v[56:59], a[16:19], a[116:119], v[56:59]\n\t"
    "buffer_load_dwordx4 v208, s[40:43], s61 offen lds\n\t"
    "v_mfma_f32_16x16x32_bf16 v[64:67], a[16:19], a[120:123], v[64:67]\n\t"
    "ds_read_b64_tr_b16 a[60:61], v195 offset:51200\n\t"
    "v_mfma_f32_16x16x32_bf16 v[64:67], a[20:23], a[144:147], v[64:67]\n\t"
    "ds_read_b128 a[220:223], v227 offset:36864\n\t"
    "v_mfma_f32_16x16x32_bf16 v[72:75], a[20:23], a[148:151], v[72:75]\n\t"
    "ds_read_b64_tr_b16 a[62:63], v195 offset:52224\n\t"
    "v_mfma_f32_16x16x32_bf16 v[72:75], a[16:19], a[124:127], v[72:75]\n\t"
    "buffer_load_dwordx4 v209, s[40:43], s61 offen offset:1024 lds\n\t"
    "v_mfma_f32_16x16x32_bf16 v[80:83], a[16:19], a[128:131], v[80:83]\n\t"
    "v_mfma_f32_16x16x32_bf16 v[80:83], a[20:23], a[152:155], v[80:83]\n\t"
    "v_mfma_f32_16x16x32_bf16 v[88:91], a[20:23], a[156:159], v[88:91]\n\t"
    "v_mfma_f32_16x16x32_bf16 v[88:91], a[16:19], a[132:135], v[88:91]\n\t"
    "buffer_load_dwordx4 v210, s[40:43], s61 offen offset:2048 lds\n\t"
    "v_mfma_f32_16x16x32_bf16 v[52:55], a[24:27], a[112:115], v[52:55]\n\t"
    "v_mfma_f32_16x16x32_bf16 v[52:55], a[28:31], a[136:139], v[52:55]\n\t"
    "v_mfma_f32_16x16x32_bf16 v[60:63], a[28:31], a[140:143], v[60:63]\n\t"
    "v_mfma_f32_16x16x32_bf16 v[60:63], a[24:27], a[116:119], v[60:63]\n\t"
    "buffer_load_dwordx4 v211, s[40:43], s61 offen offset:3072 lds\n\t"
    "s_xor_b32 s61, s61, 128\n\t"
    "v_mfma_f32_16x16x32_bf16 v[68:71], a[24:27], a[120:123], v[68:71]\n\t"
    "v_mfma_f32_16x16x32_bf16 v[68:71], a[28:31], a[144:147], v[68:71]\n\t"
    "v_mfma_f32_16x16x32_bf16 v[76:79], a[28:31], a[148:151], v[76:79]\n\t"
    "v_mfma_f32_16x16x32_bf16 v[76:79], a[24:27], a[124:127], v[76:79]\n\t"
    "v_mfma_f32_16x16x32_bf16 v[84:87], a[24:27], a[128:131], v[84:87]\n\t"
    "v_mfma_f32_16x16x32_bf16 v[84:87], a[28:31], a[152:155], v[84:87]\n\t"
    "v_mfma_f32_16x16x32_bf16 v[92:95], a[28:31], a[156:159], v[92:95]\n\t"
    "v_mfma_f32_16x16x32_bf16 v[92:95], a[24:27], a[132:135], v[92:95]\n\t"
    "s_waitcnt vmcnt(15) lgkmcnt(0)\n\t"
    "s_barrier\n\t"
    "v_mfma_f32_16x16x32_bf16 v[96:99], a[32:35], a[112:115], v[96:99]\n\t"
    "ds_read_b64_tr_b16 a[0:1], v192\n\t"
    "s_add_u32 m0, s58, 32768\n\t"
    "v_mfma_f32_16x16x32_bf16 v[96:99], a[36:39], a[136:139], v[96:99]\n\t"
    "buffer_load_dwordx4 v212, s[44:47], s62 offen nt lds\n\t"
    "v_mfma_f32_16x16x32_bf16 v[104:107], a[36:39], a[140:143], v[104:107]\n\t"
    "ds_read_b64_tr_b16 a[2:3], v192 offset:1024\n\t"
    "s_add_u32 m0, s58, 33536\n\t"
    "v_mfma_f32_16x16x32_bf16 v[104:107], a[32:35], a[116:119], v[104:107]\n\t"
    "buffer_load_dwordx4 v213, s[44:47], s62 offen offset:256 nt lds\n\t"
    "v_mfma_f32_16x16x32_bf16 v[112:115], a[32:35], a[120:123], v[112:115]\n\t"
    "ds_read_b64_tr_b16 a[4:5], v192 offset:2048\n\t"
    "s_add_u32 m0, s58, 34304\n\t"
    "v_mfma_f32_16x16x32_bf16 v[112:115], a[36:39], a[144:147], v[112:115]\n\t"
    "buffer_load_dwordx4 v214, s[44:47], s62 offen offset:512 nt lds\n\t"
    "v_mfma_f32_16x16x32_bf16 v[120:123], a[36:39], a[148:151], v[120:123]\n\t"
    "ds_read_b64_tr_b16 a[6:7], v192 offset:3072\n\t"
    "s_add_u32 m0, s58, 35072\n\t"
    "v_mfma_f32_16x16x32_bf16 v[120:123], a[32:35], a[124:127], v[120:123]\n\t"
    "buffer_load_dwordx4 v215, s[44:47], s62 offen offset:768 nt lds\n\t"
    "v_mfma_f32_16x16x32_bf16 v[128:131], a[32:35], a[128:131], v[128:131]\n\t"
    "ds_read_b64_tr_b16 a[8:9], v193\n\t"
    "v_mfma_f32_16x16x32_bf16 v[128:131], a[36:39], a[152:155], v[128:131]\n\t"
    "ds_read_b128 a[64:67], v206\n\t"
    "v_mfma_f32_16x16x32_bf16 v[136:139], a[36:39], a[156:159], v[136:139]\n\t"
    "ds_read_b64_tr_b16 a[10:11], v193 offset:1024\n\t"
    "v_mfma_f32_16x16x32_bf16 v[136:139], a[32:35], a[132:135], v[136:139]\n\t"
    "ds_read_b128 a[88:91], v207\n\t"
    "v_mfma_f32_16x16x32_bf16 v[100:103], a[40:43], a[112:115], v[100:103]\n\t"
    "ds_read_b64_tr_b16 a[12:13], v193 offset:2048\n\t"
    "v_mfma_f32_16x16x32_bf16 v[100:103], a[44:47], a[136:139], v[100:103]\n\t"
    "ds_read_b128 a[68:71], v206 offset:2048\n\t"
    "v_mfma_f32_16x16x32_bf16 v[108:111], a[44:47], a[140:143], v[108:111]\n\t"
    "ds_read_b64_tr_b16 a[14:15], v193 offset:3072\n\t"
    "v_mfma_f32_16x16x32_bf16 v[108:111], a[40:43], a[116:119], v[108:111]\n\t"
    "ds_read_b128 a[92:95], v207 offset:2048\n\t"
    "v_mfma_f32_16x16x32_bf16 v[116:119], a[40:43], a[120:123], v[116:119]\n\t"
    "ds_read_b64_tr_b16 a[16:17], v194\n\t"
    "v_mfma_f32_16x16x32_bf16 v[116:119], a[44:47], a[144:147], v[116:119]\n\t"
    "ds_read_b128 a[72:75], v206 offset:4096\n\t"
    "v_mfma_f32_16x16x32_bf16 v[124:127], a[44:47], a[148:151], v[124:127]\n\t"
    "ds_read_b64_tr_b16 a[18:19], v194 offset:1024\n\t"
    "v_mfma_f32_16x16x32_bf16 v[124:127], a[40:43], a[124:127], v[124:127]\n\t"
    "ds_read_b128 a[96:99], v207 offset:4096\n\t"
    "v_mfma_f32_16x16x32_bf16 v[132:135], a[40:43], a[128:131], v[132:135]\n\t"
    "ds_read_b64_tr_b16 a[20:21], v194 offset:2048\n\t"
    "v_mfma_f32_16x16x32_bf16 v[132:135], a[44:47], a[152:155], v[132:135]\n\t"
    "ds_read_b128 a[76:79], v206 offset:6144\n\t"
    "v_mfma_f32_16x16x32_bf16 v[140:143], a[44:47], a[156:159], v[140:143]\n\t"
    "ds_read_b64_tr_b16 a[22:23], v194 offset:3072\n\t"
    "v_mfma_f32_16x16x32_bf16 v[140:143], a[40:43], a[132:135], v[140:143]\n\t"
    "ds_read_b128 a[100:103], v207 offset:6144\n\t"
    "v_mfma_f32_16x16x32_bf16 v[144:147], a[48:51], a[112:115], v[144:147]\n\t"
    "ds_read_b64_tr_b16 a[24:25], v195\n\t"
    "v_mfma_f32_16x16x32_bf16 v[144:147], a[52:55], a[136:139], v[144:147]\n\t"
    "ds_read_b128 a[80:83], v206 offset:8192\n\t"
    "v_mfma_f32_16x16x32_bf16 v[152:155], a[52:55], a[140:143], v[152:155]\n\t"
    "ds_read_b64_tr_b16 a[26:27], v195 offset:1024\n\t"
    "v_mfma_f32_16x16x32_bf16 v[152:155], a[48:51], a[116:119], v[152:155]\n\t"
    "ds_read_b128 a[104:107], v207 offset:8192\n\t"
    "v_mfma_f32_16x16x32_bf16 v[160:163], a[48:51], a[120:123], v[160:163]\n\t"
    "ds_read_b64_tr_b16 a[28:29], v195 offset:2048\n\t"
    "v_mfma_f32_16x16x32_bf16 v[160:163], a[52:55], a[144:147], v[160:163]\n\t"
    "ds_read_b128 a[84:87], v206 offset:10240\n\t"
    "v_mfma_f32_16x16x32_bf16 v[168:171], a[52:55], a[148:151], v[168:171]\n\t"
    "ds_read_b64_tr_b16 a[30:31], v195 offset:3072\n\t"
    "v_mfma_f32_16x16x32_bf16 v[168:171], a[48:51], a[124:127], v[168:171]\n\t"
    "ds_read_b128 a[108:111], v207 offset:10240\n\t"
    "v_mfma_f32_16x16x32_bf16 v[176:179], a[48:51], a[128:131], v[176:179]\n\t"
    "s_add_u32 m0, s56, 49152\n\t"
    "v_mfma_f32_16x16x32_bf16 v[176:179], a[52:55], a[152:155], v[176:179]\n\t"
    "buffer_load_dwordx4 v208, s[40:43], s61 offen lds\n\t"
    "v_mfma_f32_16x16x32_bf16 v[184:187], a[52:55], a[156:159], v[184:187]\n\t"
    "v_mfma_f32_16x16x32_bf16 v[184:187], a[48:51], a[132:135], v[184:187]\n\t"
    "buffer_load_dwordx4 v209, s[40:43], s61 offen offset:1024 lds\n\t"
    "v_mfma_f32_16x16x32_bf16 v[148:151], a[56:59], a[112:115], v[148:151]\n\t"
    "v_mfma_f32_16x16x32_bf16 v[148:151], a[60:63], a[136:139], v[148:151]\n\t"
    "buffer_load_dwordx4 v210, s[40:43], s61 offen offset:2048 lds\n\t"
    "v_mfma_f32_16x16x32_bf16 v[156:159], a[60:63], a[140:143], v[156:159]\n\t"
    "v_mfma_f32_16x16x32_bf16 v[156:159], a[56:59], a[116:119], v[156:159]\n\t"
    "buffer_load_dwordx4 v211, s[40:43], s61 offen offset:3072 lds\n\t"
    "s_xor_b32 s61, s61, 7864448\n\t"
    "v_mfma_f32_16x16x32_bf16 v[164:167], a[56:59], a[120:123], v[164:167]\n\t"
    "v_mfma_f32_16x16x32_bf16 v[164:167], a[60:63], a[144:147], v[164:167]\n\t"
    "v_mfma_f32_16x16x32_bf16 v[172:175], a[60:63], a[148:151], v[172:175]\n\t"
    "v_mfma_f32_16x16x32_bf16 v[172:175], a[56:59], a[124:127], v[172:175]\n\t"
    "v_mfma_f32_16x16x32_bf16 v[180:183], a[56:59], a[128:131], v[180:183]\n\t"
    "v_mfma_f32_16x16x32_bf16 v[180:183], a[60:63], a[152:155], v[180:183]\n\t"
    "v_mfma_f32_16x16x32_bf16 v[188:191], a[60:63], a[156:159], v[188:191]\n\t"
    "v_mfma_f32_16x16x32_bf16 v[188:191], a[56:59], a[132:135], v[188:191]\n\t"
    "s_waitcnt vmcnt(18) lgkmcnt(0)\n\t"
    "s_barrier\n\t"
    "v_mfma_f32_16x16x32_bf16 v[0:3], a[0:3], a[64:67], v[0:3]\n\t"
    "ds_read_b64_tr_b16 a[32:33], v192 offset:16384\n\t"
    "v_mfma_f32_16x16x32_bf16 v[0:3], a[4:7], a[88:91], v[0:3]\n\t"
    "v_mfma_f32_16x16x32_bf16 v[8:11], a[4:7], a[92:95], v[8:11]\n\t"
    "ds_read_b64_tr_b16 a[34:35], v192 offset:17408\n\t"
    "s_add_u32 m0, s57, 0\n\t"
    "s_nop 0\n\t"
    "v_mfma_f32_16x16x32_bf16 v[8:11], a[0:3], a[68:71], v[8:11]\n\t"
    "buffer_load_dwordx4 v200, s[36:39], s60 offen lds\n\t"
    "v_mfma_f32_16x16x32_bf16 v[16:19], a[0:3], a[72:75], v[16:19]\n\t"
    "ds_read_b64_tr_b16 a[36:37], v192 offset:18432\n\t"
    "v_mfma_f32_16x16x32_bf16 v[16:19], a[4:7], a[96:99], v[16:19]\n\t"
    "v_mfma_f32_16x16x32_bf16 v[24:27], a[4:7], a[100:103], v[24:27]\n\t"
    "ds_read_b64_tr_b16 a[38:39], v192 offset:19456\n\t"
    "v_mfma_f32_16x16x32_bf16 v[24:27], a[0:3], a[76:79], v[24:27]\n\t"
    "buffer_load_dwordx4 v201, s[36:39], s60 offen offset:1024 lds\n\t"
    "v_mfma_f32_16x16x32_bf16 v[32:35], a[0:3], a[80:83], v[32:35]\n\t"
    "ds_read_b64_tr_b16 a[40:41], v193 offset:16384\n\t"
    "v_mfma_f32_16x16x32_bf16 v[32:35], a[4:7], a[104:107], v[32:35]\n\t"
    "v_mfma_f32_16x16x32_bf16 v[40:43], a[4:7], a[108:111], v[40:43]\n\t"
    "ds_read_b64_tr_b16 a[42:43], v193 offset:17408\n\t"
    "v_mfma_f32_16x16x32_bf16 v[40:43], a[0:3], a[84:87], v[40:43]\n\t"
    "buffer_load_dwordx4 v202, s[36:39], s60 offen offset:2048 lds\n\t"
    "v_mfma_f32_16x16x32_bf16 v[4:7], a[8:11], a[64:67], v[4:7]\n\t"
    "ds_read_b64_tr_b16 a[44:45], v193 offset:18432\n\t"
    "v_mfma_f32_16x16x32_bf16 v[4:7], a[12:15], a[88:91], v[4:7]\n\t"
    "v_mfma_f32_16x16x32_bf16 v[12:15], a[12:15], a[92:95], v[12:15]\n\t"
    "ds_read_b64_tr_b16 a[46:47], v193 offset:19456\n\t"
    "v_mfma_f32_16x16x32_bf16 v[12:15], a[8:11], a[68:71], v[12:15]\n\t"
    "buffer_load_dwordx4 v203, s[36:39], s60 offen offset:3072 lds\n\t"
    "v_mfma_f32_16x16x32_bf16 v[20:23], a[8:11], a[72:75], v[20:23]\n\t"
    "ds_read_b64_tr_b16 a[48:49], v194 offset:16384\n\t"
    "v_mfma_f32_16x16x32_bf16 v[20:23], a[12:15], a[96:99], v[20:23]\n\t"
    "v_mfma_f32_16x16x32_bf16 v[28:31], a[12:15], a[100:103], v[28:31]\n\t"
    "ds_read_b64_tr_b16 a[50:51], v194 offset:17408\n\t"
    "s_add_u32 m0, s57, 4096\n\t"
    "v_mfma_f32_16x16x32_bf16 v[28:31], a[8:11], a[76:79], v[28:31]\n\t"
    "buffer_load_dwordx4 v204, s[36:39], s60 offen lds\n\t"
    "v_mfma_f32_16x16x32_bf16 v[36:39], a[8:11], a[80:83], v[36:39]\n\t"
    "ds_read_b64_tr_b16 a[52:53], v194 offset:18432\n\t"
    "v_mfma_f32_16x16x32_bf16 v[36:39], a[12:15], a[104:107], v[36:39]\n\t"
    "v_mfma_f32_16x16x32_bf16 v[44:47], a[12:15], a[108:111], v[44:47]\n\t"
    "ds_read_b64_tr_b16 a[54:55], v194 offset:19456\n\t"
    "v_mfma_f32_16x16x32_bf16 v[44:47], a[8:11], a[84:87], v[44:47]\n\t"
    "buffer_load_dwordx4 v205, s[36:39], s60 offen offset:1024 lds\n\t"
    "s_xor_b32 s60, s60, 128\n\t"
    "v_mfma_f32_16x16x32_bf16 v[48:51], a[16:19], a[64:67], v[48:51]\n\t"
    "ds_read_b64_tr_b16 a[56:57], v195 offset:16384\n\t"
    "v_mfma_f32_16x16x32_bf16 v[48:51], a[20:23], a[88:91], v[48:51]\n\t"
    "v_mfma_f32_16x16x32_bf16 v[56:59], a[20:23], a[92:95], v[56:59]\n\t"
    "ds_read_b64_tr_b16 a[58:59], v195 offset:17408\n\t"
    "s_add_u32 m0, s56, 0\n\t"
    "s_nop 0\n\t"
    "v_mfma_f32_16x16x32_bf16 v[56:59], a[16:19], a[68:71], v[56:59]\n\t"
    "buffer_load_dwordx4 v208, s[40:43], s61 offen lds\n\t"
    "v_mfma_f32_16x16x32_bf16 v[64:67], a[16:19], a[72:75], v[64:67]\n\t"
    "ds_read_b64_tr_b16 a[60:61], v195 offset:18432\n\t"
    "v_mfma_f32_16x16x32_bf16 v[64:67], a[20:23], a[96:99], v[64:67]\n\t"
    "v_mfma_f32_16x16x32_bf16 v[72:75], a[20:23], a[100:103], v[72:75]\n\t"
    "ds_read_b64_tr_b16 a[62:63], v195 offset:19456\n\t"
    "v_mfma_f32_16x16x32_bf16 v[72:75], a[16:19], a[76:79], v[72:75]\n\t"
    "buffer_load_dwordx4 v209, s[40:43], s61 offen offset:1024 lds\n\t"
    "v_mfma_f32_16x16x32_bf16 v[80:83], a[16:19], a[80:83], v[80:83]\n\t"
    "v_mfma_f32_16x16x32_bf16 v[80:83], a[20:23], a[104:107], v[80:83]\n\t"
    "v_mfma_f32_16x16x32_bf16 v[88:91], a[20:23], a[108:111], v[88:91]\n\t"
    "v_mfma_f32_16x16x32_bf16 v[88:91], a[16:19], a[84:87], v[88:91]\n\t"
    "buffer_load_dwordx4 v210, s[40:43], s61 offen offset:2048 lds\n\t"
    "v_mfma_f32_16x16x32_bf16 v[52:55], a[24:27], a[64:67], v[52:55]\n\t"
    "v_mfma_f32_16x16x32_bf16 v[52:55], a[28:31], a[88:91], v[52:55]\n\t"
    "v_mfma_f32_16x16x32_bf16 v[60:63], a[28:31], a[92:95], v[60:63]\n\t"
    "v_mfma_f32_16x16x32_bf16 v[60:63], a[24:27], a[68:71], v[60:63]\n\t"
    "buffer_load_dwordx4 v211, s[40:43], s61 offen offset:3072 lds\n\t"
    "s_xor_b32 s61, s61, 128\n\t"
    "v_mfma_f32_16x16x32_bf16 v[68:71], a[24:27], a[72:75], v[68:71]\n\t"
    "v_mfma_f32_16x16x32_bf16 v[68:71], a[28:31], a[96:99], v[68:71]\n\t"
    "v_mfma_f32_16x16x32_bf16 v[76:79], a[28:31], a[100:103], v[76:79]\n\t"
    "v_mfma_f32_16x16x32_bf16 v[76:79], a[24:27], a[76:79], v[76:79]\n\t"
    "v_mfma_f32_16x16x32_bf16 v[84:87], a[24:27], a[80:83], v[84:87]\n\t"
    "v_mfma_f32_16x16x32_bf16 v[84:87], a[28:31], a[104:107], v[84:87]\n\t"
    "v_mfma_f32_16x16x32_bf16 v[92:95], a[28:31], a[108:111], v[92:95]\n\t"
    "v_mfma_f32_16x16x32_bf16 v[92:95], a[24:27], a[84:87], v[92:95]\n\t"
    "s_waitcnt vmcnt(18) lgkmcnt(0)\n\t"
    "s_barrier\n\t"
    "v_mfma_f32_16x16x32_bf16 v[96:99], a[32:35], a[64:67], v[96:99]\n\t"
    "ds_read_b64_tr_b16 a[0:1], v192 offset:32768\n\t"
    "v_mfma_f32_16x16x32_bf16 v[96:99], a[36:39], a[88:91], v[96:99]\n\t"
    "buffer_load_dword v239, v246, s[64:67], s76 offen\n\t"
    "s_add_u32 s76, s76, 524288\n\t"
    "v_mfma_f32_16x16x32_bf16 v[104:107], a[36:39], a[92:95], v[104:107]\n\t"
    "ds_read_b64_tr_b16 a[2:3], v192 offset:33792\n\t"
    "v_mfma_f32_16x16x32_bf16 v[104:107], a[32:35], a[68:71], v[104:107]\n\t"
    "ds_read_b128 a[112:115], v206 offset:24576\n\t"
    "v_mfma_f32_16x16x32_bf16 v[112:115], a[32:35], a[72:75], v[112:115]\n\t"
    "ds_read_b64_tr_b16 a[4:5], v192 offset:34816\n\t"
    "v_mfma_f32_16x16x32_bf16 v[112:115], a[36:39], a[96:99], v[112:115]\n\t"
    "ds_read_b128 a[136:139], v207 offset:24576\n\t"
    "v_mfma_f32_16x16x32_bf16 v[120:123], a[36:39], a[100:103], v[120:123]\n\t"
    "ds_read_b64_tr_b16 a[6:7], v192 offset:35840\n\t"
    "v_mfma_f32_16x16x32_bf16 v[120:123], a[32:35], a[76:79], v[120:123]\n\t"
    "ds_read_b128 a[116:119], v206 offset:26624\n\t"
    "v_mfma_f32_16x16x32_bf16 v[128:131], a[32:35], a[80:83], v[128:131]\n\t"
    "ds_read_b64_tr_b16 a[8:9], v193 offset:32768\n\t"
    "v_mfma_f32_16x16x32_bf16 v[128:131], a[36:39], a[104:107], v[128:131]\n\t"
    "ds_read_b128 a[140:143], v207 offset:26624\n\t"
    "v_mfma_f32_16x16x32_bf16 v[136:139], a[36:39], a[108:111], v[136:139]\n\t"
    "ds_read_b64_tr_b16 a[10:11], v193 offset:33792\n\t"
    "v_mfma_f32_16x16x32_bf16 v[136:139], a[32:35], a[84:87], v[136:139]\n\t"
    "ds_read_b128 a[120:123], v206 offset:28672\n\t"
    "v_mfma_f32_16x16x32_bf16 v[100:103], a[40:43], a[64:67], v[100:103]\n\t"
    "ds_read_b64_tr_b16 a[12:13], v193 offset:34816\n\t"
    "v_mfma_f32_16x16x32_bf16 v[100:103], a[44:47], a[88:91], v[100:103]\n\t"
    "ds_read_b128 a[144:147], v207 offset:28672\n\t"
    "v_mfma_f32_16x16x32_bf16 v[108:111], a[44:47], a[92:95], v[108:111]\n\t"
    "ds_read_b64_tr_b16 a[14:15], v193 offset:35840\n\t"
    "v_mfma_f32_16x16x32_bf16 v[108:111], a[40:43], a[68:71], v[108:111]\n\t"
    "ds_read_b128 a[124:127], v206 offset:30720\n\t"
    "v_mfma_f32_16x16x32_bf16 v[116:119], a[40:43], a[72:75], v[116:119]\n\t"
    "ds_read_b64_tr_b16 a[16:17], v194 offset:32768\n\t"
    "v_mfma_f32_16x16x32_bf16 v[116:119], a[44:47], a[96:99], v[116:119]\n\t"
    "ds_read_b128 a[148:151], v207 offset:30720\n\t"
    "v_mfma_f32_16x16x32_bf16 v[124:127], a[44:47], a[100:103], v[124:127]\n\t"
    "ds_read_b64_tr_b16 a[18:19], v194 offset:33792\n\t"
    "v_mfma_f32_16x16x32_bf16 v[124:127], a[40:43], a[76:79], v[124:127]\n\t"
    "ds_read_b128 a[128:131], v206 offset:32768\n\t"
    "v_mfma_f32_16x16x32_bf16 v[132:135], a[40:43], a[80:83], v[132:135]\n\t"
    "ds_read_b64_tr_b16 a[20:21], v194 offset:34816\n\t"
    "v_mfma_f32_16x16x32_bf16 v[132:135], a[44:47], a[104:107], v[132:135]\n\t"
    "ds_read_b128 a[152:155], v207 offset:32768\n\t"
    "v_mfma_f32_16x16x32_bf16 v[140:143], a[44:47], a[108:111], v[140:143]\n\t"
    "ds_read_b64_tr_b16 a[22:23], v194 offset:35840\n\t"
    "v_mfma_f32_16x16x32_bf16 v[140:143], a[40:43], a[84:87], v[140:143]\n\t"
    "ds_read_b128 a[132:135], v206 offset:34816\n\t"
    "v_mfma_f32_16x16x32_bf16 v[144:147], a[48:51], a[64:67], v[144:147]\n\t"
    "ds_read_b64_tr_b16 a[24:25], v195 offset:32768\n\t"
    "v_mfma_f32_16x16x32_bf16 v[144:147], a[52:55], a[88:91], v[144:147]\n\t"
    "ds_read_b128 a[156:159], v207 offset:34816\n\t"
    "v_mfma_f32_16x16x32_bf16 v[152:155], a[52:55], a[92:95], v[152:155]\n\t"
    "ds_read_b64_tr_b16 a[26:27], v195 offset:33792\n\t"
    "v_mfma_f32_16x16x32_bf16 v[152:155], a[48:51], a[68:71], v[152:155]\n\t"
    "v_mfma_f32_16x16x32_bf16 v[160:163], a[48:51], a[72:75], v[160:163]\n\t"
    "ds_read_b64_tr_b16 a[28:29], v195 offset:34816\n\t"
    "s_add_u32 m0, s56, 16384\n\t"
    "v_mfma_f32_16x16x32_bf16 v[160:163], a[52:55], a[96:99], v[160:163]\n\t"
    "buffer_load_dwordx4 v208, s[40:43], s61 offen lds\n\t"
    "v_mfma_f32_16x16x32_bf16 v[168:171], a[52:55], a[100:103], v[168:171]\n\t"
    "ds_read_b64_tr_b16 a[30:31], v195 offset:35840\n\t"
    "v_mfma_f32_16x16x32_bf16 v[168:171], a[48:51], a[76:79], v[168:171]\n\t"
    "v_mfma_f32_16x16x32_bf16 v[176:179], a[48:51], a[80:83], v[176:179]\n\t"
    "v_mfma_f32_16x16x32_bf16 v[176:179], a[52:55], a[104:107], v[176:179]\n\t"
    "buffer_load_dwordx4 v209, s[40:43], s61 offen offset:1024 lds\n\t"
    "v_mfma_f32_16x16x32_bf16 v[184:187], a[52:55], a[108:111], v[184:187]\n\t"
    "v_mfma_f32_16x16x32_bf16 v[184:187], a[48:51], a[84:87], v[184:187]\n\t"
    "v_mfma_f32_16x16x32_bf16 v[148:151], a[56:59], a[64:67], v[148:151]\n\t"
    "v_mfma_f32_16x16x32_bf16 v[148:151], a[60:63], a[88:91], v[148:151]\n\t"
    "buffer_load_dwordx4 v210, s[40:43], s61 offen offset:2048 lds\n\t"
    "v_mfma_f32_16x16x32_bf16 v[156:159], a[60:63], a[92:95], v[156:159]\n\t"
    "v_mfma_f32_16x16x32_bf16 v[156:159], a[56:59], a[68:71], v[156:159]\n\t"
    "v_mfma_f32_16x16x32_bf16 v[164:167], a[56:59], a[72:75], v[164:167]\n\t"
    "v_mfma_f32_16x16x32_bf16 v[164:167], a[60:63], a[96:99], v[164:167]\n\t"
    "buffer_load_dwordx4 v211, s[40:43], s61 offen offset:3072 lds\n\t"
    "s_xor_b32 s61, s61, 524416\n\t"
    "v_mfma_f32_16x16x32_bf16 v[172:175], a[60:63], a[100:103], v[172:175]\n\t"
    "v_mfma_f32_16x16x32_bf16 v[172:175], a[56:59], a[76:79], v[172:175]\n\t"
    "v_mfma_f32_16x16x32_bf16 v[180:183], a[56:59], a[80:83], v[180:183]\n\t"
    "v_mfma_f32_16x16x32_bf16 v[180:183], a[60:63], a[104:107], v[180:183]\n\t"
    "v_mfma_f32_16x16x32_bf16 v[188:191], a[60:63], a[108:111], v[188:191]\n\t"
    "v_mfma_f32_16x16x32_bf16 v[188:191], a[56:59], a[84:87], v[188:191]\n\t"
    "s_waitcnt vmcnt(15) lgkmcnt(0)\n\t"
    "s_barrier\n\t"
    "v_mfma_f32_16x16x32_bf16 v[0:3], a[0:3], a[112:115], v[0:3]\n\t"
    "ds_read_b64_tr_b16 a[32:33], v192 offset:49152\n\t"
    "v_mfma_f32_16x16x32_bf16 v[0:3], a[4:7], a[136:139], v[0:3]\n\t"
    "v_mfma_f32_16x16x32_bf16 v[8:11], a[4:7], a[140:143], v[8:11]\n\t"
    "ds_read_b64_tr_b16 a[34:35], v192 offset:50176\n\t"
    "s_add_u32 m0, s57, 24576\n\t"
    "v_mfma_f32_16x16x32_bf16 v[8:11], a[0:3], a[116:119], v[8:11]\n\t"
    "buffer_load_dwordx4 v200, s[36:39], s60 offen lds\n\t"
    "v_mfma_f32_16x16x32_bf16 v[16:19], a[0:3], a[120:123], v[16:19]\n\t"
    "ds_read_b64_tr_b16 a[36:37], v192 offset:51200\n\t"
    "v_mfma_f32_16x16x32_bf16 v[16:19], a[4:7], a[144:147], v[16:19]\n\t"
    "v_mfma_f32_16x16x32_bf16 v[24:27], a[4:7], a[148:151], v[24:27]\n\t"
    "ds_read_b64_tr_b16 a[38:39], v192 offset:52224\n\t"
    "v_mfma_f32_16x16x32_bf16 v[24:27], a[0:3], a[124:127], v[24:27]\n\t"
    "buffer_load_dwordx4 v201, s[36:39], s60 offen offset:1024 lds\n\t"
    "v_mfma_f32_16x16x32_bf16 v[32:35], a[0:3], a[128:131], v[32:35]\n\t"
    "ds_read_b64_tr_b16 a[40:41], v193 offset:49152\n\t"
    "v_mfma_f32_16x16x32_bf16 v[32:35], a[4:7], a[152:155], v[32:35]\n\t"
    "v_mfma_f32_16x16x32_bf16 v[40:43], a[4:7], a[156:159], v[40:43]\n\t"
    "ds_read_b64_tr_b16 a[42:43], v193 offset:50176\n\t"
    "v_mfma_f32_16x16x32_bf16 v[40:43], a[0:3], a[132:135], v[40:43]\n\t"
    "buffer_load_dwordx4 v202, s[36:39], s60 offen offset:2048 lds\n\t"
    "v_mfma_f32_16x16x32_bf16 v[4:7], a[8:11], a[112:115], v[4:7]\n\t"
    "ds_read_b64_tr_b16 a[44:45], v193 offset:51200\n\t"
    "v_mfma_f32_16x16x32_bf16 v[4:7], a[12:15], a[136:139], v[4:7]\n\t"
    "v_mfma_f32_16x16x32_bf16 v[12:15], a[12:15], a[140:143], v[12:15]\n\t"
    "ds_read_b64_tr_b16 a[46:47], v193 offset:52224\n\t"
    "v_mfma_f32_16x16x32_bf16 v[12:15], a[8:11], a[116:119], v[12:15]\n\t"
    "buffer_load_dwordx4 v203, s[36:39], s60 offen offset:3072 lds\n\t"
    "v_mfma_f32_16x16x32_bf16 v[20:23], a[8:11], a[120:123], v[20:23]\n\t"
    "ds_read_b64_tr_b16 a[48:49], v194 offset:49152\n\t"
    "v_mfma_f32_16x16x32_bf16 v[20:23], a[12:15], a[144:147], v[20:23]\n\t"
    "v_mfma_f32_16x16x32_bf16 v[28:31], a[12:15], a[148:151], v[28:31]\n\t"
    "ds_read_b64_tr_b16 a[50:51], v194 offset:50176\n\t"
    "s_add_u32 m0, s57, 28672\n\t"
    "v_mfma_f32_16x16x32_bf16 v[28:31], a[8:11], a[124:127], v[28:31]\n\t"
    "buffer_load_dwordx4 v204, s[36:39], s60 offen lds\n\t"
    "v_mfma_f32_16x16x32_bf16 v[36:39], a[8:11], a[128:131], v[36:39]\n\t"
    "ds_read_b64_tr_b16 a[52:53], v194 offset:51200\n\t"
    "v_mfma_f32_16x16x32_bf16 v[36:39], a[12:15], a[152:155], v[36:39]\n\t"
    "v_mfma_f32_16x16x32_bf16 v[44:47], a[12:15], a[156:159], v[44:47]\n\t"
    "ds_read_b64_tr_b16 a[54:55], v194 offset:52224\n\t"
    "v_mfma_f32_16x16x32_bf16 v[44:47], a[8:11], a[132:135], v[44:47]\n\t"
    "buffer_load_dwordx4 v205, s[36:39], s60 offen offset:1024 lds\n\t"
    "s_xor_b32 s60, s60, 384\n\t"
    "v_mfma_f32_16x16x32_bf16 v[48:51], a[16:19], a[112:115], v[48:51]\n\t"
    "ds_read_b64_tr_b16 a[56:57], v195 offset:49152\n\t"
    "v_mfma_f32_16x16x32_bf16 v[48:51], a[20:23], a[136:139], v[48:51]\n\t"
    "v_mfma_f32_16x16x32_bf16 v[56:59], a[20:23], a[140:143], v[56:59]\n\t"
    "ds_read_b64_tr_b16 a[58:59], v195 offset:50176\n\t"
    "s_add_u32 m0, s56, 32768\n\t"
    "v_mfma_f32_16x16x32_bf16 v[56:59], a[16:19], a[116:119], v[56:59]\n\t"
    "buffer_load_dwordx4 v208, s[40:43], s61 offen lds\n\t"
    "v_mfma_f32_16x16x32_bf16 v[64:67], a[16:19], a[120:123], v[64:67]\n\t"
    "ds_read_b64_tr_b16 a[60:61], v195 offset:51200\n\t"
    "v_mfma_f32_16x16x32_bf16 v[64:67], a[20:23], a[144:147], v[64:67]\n\t"
    "v_mfma_f32_16x16x32_bf16 v[72:75], a[20:23], a[148:151], v[72:75]\n\t"
    "ds_read_b64_tr_b16 a[62:63], v195 offset:52224\n\t"
    "v_mfma_f32_16x16x32_bf16 v[72:75], a[16:19], a[124:127], v[72:75]\n\t"
    "buffer_load_dwordx4 v209, s[40:43], s61 offen offset:1024 lds\n\t"
    "v_mfma_f32_16x16x32_bf16 v[80:83], a[16:19], a[128:131], v[80:83]\n\t"
    "v_mfma_f32_16x16x32_bf16 v[80:83], a[20:23], a[152:155], v[80:83]\n\t"
    "v_mfma_f32_16x16x32_bf16 v[88:91], a[20:23], a[156:159], v[88:91]\n\t"
    "v_mfma_f32_16x16x32_bf16 v[88:91], a[16:19], a[132:135], v[88:91]\n\t"
    "buffer_load_dwordx4 v210, s[40:43], s61 offen offset:2048 lds\n\t"
    "v_mfma_f32_16x16x32_bf16 v[52:55], a[24:27], a[112:115], v[52:55]\n\t"
    "v_mfma_f32_16x16x32_bf16 v[52:55], a[28:31], a[136:139], v[52:55]\n\t"
    "v_mfma_f32_16x16x32_bf16 v[60:63], a[28:31], a[140:143], v[60:63]\n\t"
    "v_mfma_f32_16x16x32_bf16 v[60:63], a[24:27], a[116:119], v[60:63]\n\t"
    "buffer_load_dwordx4 v211, s[40:43], s61 offen offset:3072 lds\n\t"
    "s_xor_b32 s61, s61, 128\n\t"
    "v_mfma_f32_16x16x32_bf16 v[68:71], a[24:27], a[120:123], v[68:71]\n\t"
    "v_mfma_f32_16x16x32_bf16 v[68:71], a[28:31], a[144:147], v[68:71]\n\t"
    "v_mfma_f32_16x16x32_bf16 v[76:79], a[28:31], a[148:151], v[76:79]\n\t"
    "v_mfma_f32_16x16x32_bf16 v[76:79], a[24:27], a[124:127], v[76:79]\n\t"
    "v_mfma_f32_16x16x32_bf16 v[84:87], a[24:27], a[128:131], v[84:87]\n\t"
    "v_mfma_f32_16x16x32_bf16 v[84:87], a[28:31], a[152:155], v[84:87]\n\t"
    "v_mfma_f32_16x16x32_bf16 v[92:95], a[28:31], a[156:159], v[92:95]\n\t"
    "v_mfma_f32_16x16x32_bf16 v[92:95], a[24:27], a[132:135], v[92:95]\n\t"
    "s_waitcnt vmcnt(15) lgkmcnt(0)\n\t"
    "s_barrier\n\t"
    "v_mfma_f32_16x16x32_bf16 v[96:99], a[32:35], a[112:115], v[96:99]\n\t"
    "ds_read_b64_tr_b16 a[0:1], v192\n\t"
    "s_add_u32 m0, s58, 35840\n\t"
    "v_mfma_f32_16x16x32_bf16 v[96:99], a[36:39], a[136:139], v[96:99]\n\t"
    "buffer_load_dwordx4 v212, s[44:47], s62 offen offset:1024 nt lds\n\t"
    "v_mfma_f32_16x16x32_bf16 v[104:107], a[36:39], a[140:143], v[104:107]\n\t"
    "ds_read_b64_tr_b16 a[2:3], v192 offset:1024\n\t"
    "s_add_u32 m0, s58, 36608\n\t"
    "v_mfma_f32_16x16x32_bf16 v[104:107], a[32:35], a[116:119], v[104:107]\n\t"
    "buffer_load_dwordx4 v213, s[44:47], s62 offen offset:1280 nt lds\n\t"
    "v_mfma_f32_16x16x32_bf16 v[112:115], a[32:35], a[120:123], v[112:115]\n\t"
    "ds_read_b64_tr_b16 a[4:5], v192 offset:2048\n\t"
    "s_add_u32 m0, s58, 37376\n\t"
    "v_mfma_f32_16x16x32_bf16 v[112:115], a[36:39], a[144:147], v[112:115]\n\t"
    "buffer_load_dwordx4 v214, s[44:47], s62 offen offset:1536 nt lds\n\t"
    "v_mfma_f32_16x16x32_bf16 v[120:123], a[36:39], a[148:151], v[120:123]\n\t"
    "ds_read_b64_tr_b16 a[6:7], v192 offset:3072\n\t"
    "s_add_u32 m0, s58, 38144\n\t"
    "v_mfma_f32_16x16x32_bf16 v[120:123], a[32:35], a[124:127], v[120:123]\n\t"
    "buffer_load_dwordx4 v215, s[44:47], s62 offen offset:1792 nt lds\n\t"
    "s_or_b32 s62, s62, 12288\n\t"
    "s_add_u32 s62, s62, 2048\n\t"
    "s_and_b32 s62, s62, 116736\n\t"
    "v_mfma_f32_16x16x32_bf16 v[128:131], a[32:35], a[128:131], v[128:131]\n\t"
    "ds_read_b64_tr_b16 a[8:9], v193\n\t"
    "v_mfma_f32_16x16x32_bf16 v[128:131], a[36:39], a[152:155], v[128:131]\n\t"
    "ds_read_b128 a[64:67], v206\n\t"
    "v_mfma_f32_16x16x32_bf16 v[136:139], a[36:39], a[156:159], v[136:139]\n\t"
    "ds_read_b64_tr_b16 a[10:11], v193 offset:1024\n\t"
    "v_mfma_f32_16x16x32_bf16 v[136:139], a[32:35], a[132:135], v[136:139]\n\t"
    "ds_read_b128 a[88:91], v207\n\t"
    "v_mfma_f32_16x16x32_bf16 v[100:103], a[40:43], a[112:115], v[100:103]\n\t"
    "ds_read_b64_tr_b16 a[12:13], v193 offset:2048\n\t"
    "v_mfma_f32_16x16x32_bf16 v[100:103], a[44:47], a[136:139], v[100:103]\n\t"
    "ds_read_b128 a[68:71], v206 offset:2048\n\t"
    "v_mfma_f32_16x16x32_bf16 v[108:111], a[44:47], a[140:143], v[108:111]\n\t"
    "ds_read_b64_tr_b16 a[14:15], v193 offset:3072\n\t"
    "v_mfma_f32_16x16x32_bf16 v[108:111], a[40:43], a[116:119], v[108:111]\n\t"
    "ds_read_b128 a[92:95], v207 offset:2048\n\t"
    "v_mfma_f32_16x16x32_bf16 v[116:119], a[40:43], a[120:123], v[116:119]\n\t"
    "ds_read_b64_tr_b16 a[16:17], v194\n\t"
    "v_mfma_f32_16x16x32_bf16 v[116:119], a[44:47], a[144:147], v[116:119]\n\t"
    "ds_read_b128 a[72:75], v206 offset:4096\n\t"
    "v_mfma_f32_16x16x32_bf16 v[124:127], a[44:47], a[148:151], v[124:127]\n\t"
    "ds_read_b64_tr_b16 a[18:19], v194 offset:1024\n\t"
    "v_mfma_f32_16x16x32_bf16 v[124:127], a[40:43], a[124:127], v[124:127]\n\t"
    "ds_read_b128 a[96:99], v207 offset:4096\n\t"
    "v_mfma_f32_16x16x32_bf16 v[132:135], a[40:43], a[128:131], v[132:135]\n\t"
    "ds_read_b64_tr_b16 a[20:21], v194 offset:2048\n\t"
    "v_mfma_f32_16x16x32_bf16 v[132:135], a[44:47], a[152:155], v[132:135]\n\t"
    "ds_read_b128 a[76:79], v206 offset:6144\n\t"
    "v_mfma_f32_16x16x32_bf16 v[140:143], a[44:47], a[156:159], v[140:143]\n\t"
    "ds_read_b64_tr_b16 a[22:23], v194 offset:3072\n\t"
    "v_mfma_f32_16x16x32_bf16 v[140:143], a[40:43], a[132:135], v[140:143]\n\t"
    "ds_read_b128 a[100:103], v207 offset:6144\n\t"
    "v_mfma_f32_16x16x32_bf16 v[144:147], a[48:51], a[112:115], v[144:147]\n\t"
    "ds_read_b64_tr_b16 a[24:25], v195\n\t"
    "v_mfma_f32_16x16x32_bf16 v[144:147], a[52:55], a[136:139], v[144:147]\n\t"
    "ds_read_b128 a[80:83], v206 offset:8192\n\t"
    "v_mfma_f32_16x16x32_bf16 v[152:155], a[52:55], a[140:143], v[152:155]\n\t"
    "ds_read_b64_tr_b16 a[26:27], v195 offset:1024\n\t"
    "v_mfma_f32_16x16x32_bf16 v[152:155], a[48:51], a[116:119], v[152:155]\n\t"
    "ds_read_b128 a[104:107], v207 offset:8192\n\t"
    "v_mfma_f32_16x16x32_bf16 v[160:163], a[48:51], a[120:123], v[160:163]\n\t"
    "ds_read_b64_tr_b16 a[28:29], v195 offset:2048\n\t"
    "v_mfma_f32_16x16x32_bf16 v[160:163], a[52:55], a[144:147], v[160:163]\n\t"
    "ds_read_b128 a[84:87], v206 offset:10240\n\t"
    "v_mfma_f32_16x16x32_bf16 v[168:171], a[52:55], a[148:151], v[168:171]\n\t"
    "ds_read_b64_tr_b16 a[30:31], v195 offset:3072\n\t"
    "v_mfma_f32_16x16x32_bf16 v[168:171], a[48:51], a[124:127], v[168:171]\n\t"
    "ds_read_b128 a[108:111], v207 offset:10240\n\t"
    "v_mfma_f32_16x16x32_bf16 v[176:179], a[48:51], a[128:131], v[176:179]\n\t"
    "s_add_u32 m0, s56, 49152\n\t"
    "v_mfma_f32_16x16x32_bf16 v[176:179], a[52:55], a[152:155], v[176:179]\n\t"
    "buffer_load_dwordx4 v208, s[40:43], s61 offen lds\n\t"
    "v_mfma_f32_16x16x32_bf16 v[184:187], a[52:55], a[156:159], v[184:187]\n\t"
    "v_mfma_f32_16x16x32_bf16 v[184:187], a[48:51], a[132:135], v[184:187]\n\t"
    "buffer_load_dwordx4 v209, s[40:43], s61 offen offset:1024 lds\n\t"
    "v_mfma_f32_16x16x32_bf16 v[148:151], a[56:59], a[112:115], v[148:151]\n\t"
    "v_mfma_f32_16x16x32_bf16 v[148:151], a[60:63], a[136:139], v[148:151]\n\t"
    "buffer_load_dwordx4 v210, s[40:43], s61 offen offset:2048 lds\n\t"
    "v_mfma_f32_16x16x32_bf16 v[156:159], a[60:63], a[140:143], v[156:159]\n\t"
    "v_mfma_f32_16x16x32_bf16 v[156:159], a[56:59], a[116:119], v[156:159]\n\t"
    "buffer_load_dwordx4 v211, s[40:43], s61 offen offset:3072 lds\n\t"
    "s_xor_b32 s61, s61, 1572992\n\t"
    "v_mfma_f32_16x16x32_bf16 v[164:167], a[56:59], a[120:123], v[164:167]\n\t"
    "v_mfma_f32_16x16x32_bf16 v[164:167], a[60:63], a[144:147], v[164:167]\n\t"
    "v_mfma_f32_16x16x32_bf16 v[172:175], a[60:63], a[148:151], v[172:175]\n\t"
    "v_mfma_f32_16x16x32_bf16 v[172:175], a[56:59], a[124:127], v[172:175]\n\t"
    "v_mfma_f32_16x16x32_bf16 v[180:183], a[56:59], a[128:131], v[180:183]\n\t"
    "v_mfma_f32_16x16x32_bf16 v[180:183], a[60:63], a[152:155], v[180:183]\n\t"
    "v_mfma_f32_16x16x32_bf16 v[188:191], a[60:63], a[156:159], v[188:191]\n\t"
    "v_mfma_f32_16x16x32_bf16 v[188:191], a[56:59], a[132:135], v[188:191]\n\t"
    "s_waitcnt vmcnt(18) lgkmcnt(0)\n\t"
    "s_barrier\n\t"
    "v_mfma_f32_16x16x32_bf16 v[0:3], a[0:3], a[64:67], v[0:3]\n\t"
    "ds_read_b64_tr_b16 a[32:33], v192 offset:16384\n\t"
    "v_mfma_f32_16x16x32_bf16 v[0:3], a[4:7], a[88:91], v[0:3]\n\t"
    "v_mfma_f32_16x16x32_bf16 v[8:11], a[4:7], a[92:95], v[8:11]\n\t"
    "ds_read_b64_tr_b16 a[34:35], v192 offset:17408\n\t"
    "s_add_u32 m0, s57, 0\n\t"
    "s_nop 0\n\t"
    "v_mfma_f32_16x16x32_bf16 v[8:11], a[0:3], a[68:71], v[8:11]\n\t"
    "buffer_load_dwordx4 v200, s[36:39], s60 offen lds\n\t"
    "v_mfma_f32_16x16x32_bf16 v[16:19], a[0:3], a[72:75], v[16:19]\n\t"
    "ds_read_b64_tr_b16 a[36:37], v192 offset:18432\n\t"
    "v_mfma_f32_16x16x32_bf16 v[16:19], a[4:7], a[96:99], v[16:19]\n\t"
    "v_mfma_f32_16x16x32_bf16 v[24:27], a[4:7], a[100:103], v[24:27]\n\t"
    "ds_read_b64_tr_b16 a[38:39], v192 offset:19456\n\t"
    "v_mfma_f32_16x16x32_bf16 v[24:27], a[0:3], a[76:79], v[24:27]\n\t"
    "buffer_load_dwordx4 v201, s[36:39], s60 offen offset:1024 lds\n\t"
    "v_mfma_f32_16x16x32_bf16 v[32:35], a[0:3], a[80:83], v[32:35]\n\t"
    "ds_read_b64_tr_b16 a[40:41], v193 offset:16384\n\t"
    "v_mfma_f32_16x16x32_bf16 v[32:35], a[4:7], a[104:107], v[32:35]\n\t"
    "v_mfma_f32_16x16x32_bf16 v[40:43], a[4:7], a[108:111], v[40:43]\n\t"
    "ds_read_b64_tr_b16 a[42:43], v193 offset:17408\n\t"
    "v_mfma_f32_16x16x32_bf16 v[40:43], a[0:3], a[84:87], v[40:43]\n\t"
    "buffer_load_dwordx4 v202, s[36:39], s60 offen offset:2048 lds\n\t"
    "v_mfma_f32_16x16x32_bf16 v[4:7], a[8:11], a[64:67], v[4:7]\n\t"
    "ds_read_b64_tr_b16 a[44:45], v193 offset:18432\n\t"
    "v_mfma_f32_16x16x32_bf16 v[4:7], a[12:15], a[88:91], v[4:7]\n\t"
    "v_mfma_f32_16x16x32_bf16 v[12:15], a[12:15], a[92:95], v[12:15]\n\t"
    "ds_read_b64_tr_b16 a[46:47], v193 offset:19456\n\t"
    "v_mfma_f32_16x16x32_bf16 v[12:15], a[8:11], a[68:71], v[12:15]\n\t"
    "buffer_load_dwordx4 v203, s[36:39], s60 offen offset:3072 lds\n\t"
    "v_mfma_f32_16x16x32_bf16 v[20:23], a[8:11], a[72:75], v[20:23]\n\t"
    "ds_read_b64_tr_b16 a[48:49], v194 offset:16384\n\t"
    "v_mfma_f32_16x16x32_bf16 v[20:23], a[12:15], a[96:99], v[20:23]\n\t"
    "v_mfma_f32_16x16x32_bf16 v[28:31], a[12:15], a[100:103], v[28:31]\n\t"
    "ds_read_b64_tr_b16 a[50:51], v194 offset:17408\n\t"
    "s_add_u32 m0, s57, 4096\n\t"
    "v_mfma_f32_16x16x32_bf16 v[28:31], a[8:11], a[76:79], v[28:31]\n\t"
    "buffer_load_dwordx4 v204, s[36:39], s60 offen lds\n\t"
    "v_mfma_f32_16x16x32_bf16 v[36:39], a[8:11], a[80:83], v[36:39]\n\t"
    "ds_read_b64_tr_b16 a[52:53], v194 offset:18432\n\t"
    "v_mfma_f32_16x16x32_bf16 v[36:39], a[12:15], a[104:107], v[36:39]\n\t"
    "v_mfma_f32_16x16x32_bf16 v[44:47], a[12:15], a[108:111], v[44:47]\n\t"
    "ds_read_b64_tr_b16 a[54:55], v194 offset:19456\n\t"
    "v_mfma_f32_16x16x32_bf16 v[44:47], a[8:11], a[84:87], v[44:47]\n\t"
    "buffer_load_dwordx4 v205, s[36:39], s60 offen offset:1024 lds\n\t"
    "s_xor_b32 s60, s60, 128\n\t"
    "v_mfma_f32_16x16x32_bf16 v[48:51], a[16:19], a[64:67], v[48:51]\n\t"
    "ds_read_b64_tr_b16 a[56:57], v195 offset:16384\n\t"
    "v_mfma_f32_16x16x32_bf16 v[48:51], a[20:23], a[88:91], v[48:51]\n\t"
    "v_mfma_f32_16x16x32_bf16 v[56:59], a[20:23], a[92:95], v[56:59]\n\t"
    "ds_read_b64_tr_b16 a[58:59], v195 offset:17408\n\t"
    "s_add_u32 m0, s56, 0\n\t"
    "s_nop 0\n\t"
    "v_mfma_f32_16x16x32_bf16 v[56:59], a[16:19], a[68:71], v[56:59]\n\t"
    "buffer_load_dwordx4 v208, s[40:43], s61 offen lds\n\t"
    "v_mfma_f32_16x16x32_bf16 v[64:67], a[16:19], a[72:75], v[64:67]\n\t"
    "ds_read_b64_tr_b16 a[60:61], v195 offset:18432\n\t"
    "v_mfma_f32_16x16x32_bf16 v[64:67], a[20:23], a[96:99], v[64:67]\n\t"
    "v_mfma_f32_16x16x32_bf16 v[72:75], a[20:23], a[100:103], v[72:75]\n\t"
    "ds_read_b64_tr_b16 a[62:63], v195 offset:19456\n\t"
    "v_mfma_f32_16x16x32_bf16 v[72:75], a[16:19], a[76:79], v[72:75]\n\t"
    "buffer_load_dwordx4 v209, s[40:43], s61 offen offset:1024 lds\n\t"
    "v_mfma_f32_16x16x32_bf16 v[80:83], a[16:19], a[80:83], v[80:83]\n\t"
    "v_mfma_f32_16x16x32_bf16 v[80:83], a[20:23], a[104:107], v[80:83]\n\t"
    "v_mfma_f32_16x16x32_bf16 v[88:91], a[20:23], a[108:111], v[88:91]\n\t"
    "v_mfma_f32_16x16x32_bf16 v[88:91], a[16:19], a[84:87], v[88:91]\n\t"
    "buffer_load_dwordx4 v210, s[40:43], s61 offen offset:2048 lds\n\t"
    "v_mfma_f32_16x16x32_bf16 v[52:55], a[24:27], a[64:67], v[52:55]\n\t"
    "v_mfma_f32_16x16x32_bf16 v[52:55], a[28:31], a[88:91], v[52:55]\n\t"
    "v_mfma_f32_16x16x32_bf16 v[60:63], a[28:31], a[92:95], v[60:63]\n\t"
    "v_mfma_f32_16x16x32_bf16 v[60:63], a[24:27], a[68:71], v[60:63]\n\t"
    "buffer_load_dwordx4 v211, s[40:43], s61 offen offset:3072 lds\n\t"
    "s_xor_b32 s61, s61, 128\n\t"
    "v_mfma_f32_16x16x32_bf16 v[68:71], a[24:27], a[72:75], v[68:71]\n\t"
    "v_mfma_f32_16x16x32_bf16 v[68:71], a[28:31], a[96:99], v[68:71]\n\t"
    "v_mfma_f32_16x16x32_bf16 v[76:79], a[28:31], a[100:103], v[76:79]\n\t"
    "v_mfma_f32_16x16x32_bf16 v[76:79], a[24:27], a[76:79], v[76:79]\n\t"
    "v_mfma_f32_16x16x32_bf16 v[84:87], a[24:27], a[80:83], v[84:87]\n\t"
    "v_mfma_f32_16x16x32_bf16 v[84:87], a[28:31], a[104:107], v[84:87]\n\t"
    "v_mfma_f32_16x16x32_bf16 v[92:95], a[28:31], a[108:111], v[92:95]\n\t"
    "v_mfma_f32_16x16x32_bf16 v[92:95], a[24:27], a[84:87], v[92:95]\n\t"
    "s_waitcnt vmcnt(18) lgkmcnt(0)\n\t"
    "s_barrier\n\t"
    "v_mfma_f32_16x16x32_bf16 v[96:99], a[32:35], a[64:67], v[96:99]\n\t"
    "ds_read_b64_tr_b16 a[0:1], v192 offset:32768\n\t"
    "v_mfma_f32_16x16x32_bf16 v[96:99], a[36:39], a[88:91], v[96:99]\n\t"
    "buffer_load_dword v239, v246, s[64:67], s76 offen\n\t"
    "s_add_u32 s76, s76, 524288\n\t"
    "v_mfma_f32_16x16x32_bf16 v[104:107], a[36:39], a[92:95], v[104:107]\n\t"
    "ds_read_b64_tr_b16 a[2:3], v192 offset:33792\n\t"
    "v_mfma_f32_16x16x32_bf16 v[104:107], a[32:35], a[68:71], v[104:107]\n\t"
    "ds_read_b128 a[112:115], v206 offset:24576\n\t"
    "v_mfma_f32_16x16x32_bf16 v[112:115], a[32:35], a[72:75], v[112:115]\n\t"
    "ds_read_b64_tr_b16 a[4:5], v192 offset:34816\n\t"
    "v_mfma_f32_16x16x32_bf16 v[112:115], a[36:39], a[96:99], v[112:115]\n\t"
    "ds_read_b128 a[136:139], v207 offset:24576\n\t"
    "v_mfma_f32_16x16x32_bf16 v[120:123], a[36:39], a[100:103], v[120:123]\n\t"
    "ds_read_b64_tr_b16 a[6:7], v192 offset:35840\n\t"
    "v_mfma_f32_16x16x32_bf16 v[120:123], a[32:35], a[76:79], v[120:123]\n\t"
    "ds_read_b128 a[116:119], v206 offset:26624\n\t"
    "v_mfma_f32_16x16x32_bf16 v[128:131], a[32:35], a[80:83], v[128:131]\n\t"
    "ds_read_b64_tr_b16 a[8:9], v193 offset:32768\n\t"
    "v_mfma_f32_16x16x32_bf16 v[128:131], a[36:39], a[104:107], v[128:131]\n\t"
    "ds_read_b128 a[140:143], v207 offset:26624\n\t"
    "v_mfma_f32_16x16x32_bf16 v[136:139], a[36:39], a[108:111], v[136:139]\n\t"
    "ds_read_b64_tr_b16 a[10:11], v193 offset:33792\n\t"
    "v_mfma_f32_16x16x32_bf16 v[136:139], a[32:35], a[84:87], v[136:139]\n\t"
    "ds_read_b128 a[120:123], v206 offset:28672\n\t"
    "v_mfma_f32_16x16x32_bf16 v[100:103], a[40:43], a[64:67], v[100:103]\n\t"
    "ds_read_b64_tr_b16 a[12:13], v193 offset:34816\n\t"
    "v_mfma_f32_16x16x32_bf16 v[100:103], a[44:47], a[88:91], v[100:103]\n\t"
    "ds_read_b128 a[144:147], v207 offset:28672\n\t"
    "v_mfma_f32_16x16x32_bf16 v[108:111], a[44:47], a[92:95], v[108:111]\n\t"
    "ds_read_b64_tr_b16 a[14:15], v193 offset:35840\n\t"
    "v_mfma_f32_16x16x32_bf16 v[108:111], a[40:43], a[68:71], v[108:111]\n\t"
    "ds_read_b128 a[124:127], v206 offset:30720\n\t"
    "v_mfma_f32_16x16x32_bf16 v[116:119], a[40:43], a[72:75], v[116:119]\n\t"
    "ds_read_b64_tr_b16 a[16:17], v194 offset:32768\n\t"
    "v_mfma_f32_16x16x32_bf16 v[116:119], a[44:47], a[96:99], v[116:119]\n\t"
    "ds_read_b128 a[148:151], v207 offset:30720\n\t"
    "v_mfma_f32_16x16x32_bf16 v[124:127], a[44:47], a[100:103], v[124:127]\n\t"
    "ds_read_b64_tr_b16 a[18:19], v194 offset:33792\n\t"
    "v_mfma_f32_16x16x32_bf16 v[124:127], a[40:43], a[76:79], v[124:127]\n\t"
    "ds_read_b128 a[128:131], v206 offset:32768\n\t"
    "v_mfma_f32_16x16x32_bf16 v[132:135], a[40:43], a[80:83], v[132:135]\n\t"
    "ds_read_b64_tr_b16 a[20:21], v194 offset:34816\n\t"
    "v_mfma_f32_16x16x32_bf16 v[132:135], a[44:47], a[104:107], v[132:135]\n\t"
    "ds_read_b128 a[152:155], v207 offset:32768\n\t"
    "v_mfma_f32_16x16x32_bf16 v[140:143], a[44:47], a[108:111], v[140:143]\n\t"
    "ds_read_b64_tr_b16 a[22:23], v194 offset:35840\n\t"
    "v_mfma_f32_16x16x32_bf16 v[140:143], a[40:43], a[84:87], v[140:143]\n\t"
    "ds_read_b128 a[132:135], v206 offset:34816\n\t"
    "v_mfma_f32_16x16x32_bf16 v[144:147], a[48:51], a[64:67], v[144:147]\n\t"
    "ds_read_b64_tr_b16 a[24:25], v195 offset:32768\n\t"
    "v_mfma_f32_16x16x32_bf16 v[144:147], a[52:55], a[88:91], v[144:147]\n\t"
    "ds_read_b128 a[156:159], v207 offset:34816\n\t"
    "v_mfma_f32_16x16x32_bf16 v[152:155], a[52:55], a[92:95], v[152:155]\n\t"
    "ds_read_b64_tr_b16 a[26:27], v195 offset:33792\n\t"
    "v_mfma_f32_16x16x32_bf16 v[152:155], a[48:51], a[68:71], v[152:155]\n\t"
    "v_mfma_f32_16x16x32_bf16 v[160:163], a[48:51], a[72:75], v[160:163]\n\t"
    "ds_read_b64_tr_b16 a[28:29], v195 offset:34816\n\t"
    "s_add_u32 m0, s56, 16384\n\t"
    "v_mfma_f32_16x16x32_bf16 v[160:163], a[52:55], a[96:99], v[160:163]\n\t"
    "buffer_load_dwordx4 v208, s[40:43], s61 offen lds\n\t"
    "v_mfma_f32_16x16x32_bf16 v[168:171], a[52:55], a[100:103], v[168:171]\n\t"
    "ds_read_b64_tr_b16 a[30:31], v195 offset:35840\n\t"
    "v_mfma_f32_16x16x32_bf16 v[168:171], a[48:51], a[76:79], v[168:171]\n\t"
    "v_mfma_f32_16x16x32_bf16 v[176:179], a[48:51], a[80:83], v[176:179]\n\t"
    "v_mfma_f32_16x16x32_bf16 v[176:179], a[52:55], a[104:107], v[176:179]\n\t"
    "buffer_load_dwordx4 v209, s[40:43], s61 offen offset:1024 lds\n\t"
    "v_mfma_f32_16x16x32_bf16 v[184:187], a[52:55], a[108:111], v[184:187]\n\t"
    "v_mfma_f32_16x16x32_bf16 v[184:187], a[48:51], a[84:87], v[184:187]\n\t"
    "v_mfma_f32_16x16x32_bf16 v[148:151], a[56:59], a[64:67], v[148:151]\n\t"
    "v_mfma_f32_16x16x32_bf16 v[148:151], a[60:63], a[88:91], v[148:151]\n\t"
    "buffer_load_dwordx4 v210, s[40:43], s61 offen offset:2048 lds\n\t"
    "v_mfma_f32_16x16x32_bf16 v[156:159], a[60:63], a[92:95], v[156:159]\n\t"
    "v_mfma_f32_16x16x32_bf16 v[156:159], a[56:59], a[68:71], v[156:159]\n\t"
    "v_mfma_f32_16x16x32_bf16 v[164:167], a[56:59], a[72:75], v[164:167]\n\t"
    "v_mfma_f32_16x16x32_bf16 v[164:167], a[60:63], a[96:99], v[164:167]\n\t"
    "buffer_load_dwordx4 v211, s[40:43], s61 offen offset:3072 lds\n\t"
    "s_xor_b32 s61, s61, 524416\n\t"
    "v_mfma_f32_16x16x32_bf16 v[172:175], a[60:63], a[100:103], v[172:175]\n\t"
    "v_mfma_f32_16x16x32_bf16 v[172:175], a[56:59], a[76:79], v[172:175]\n\t"
    "v_mfma_f32_16x16x32_bf16 v[180:183], a[56:59], a[80:83], v[180:183]\n\t"
    "v_mfma_f32_16x16x32_bf16 v[180:183], a[60:63], a[104:107], v[180:183]\n\t"
    "v_mfma_f32_16x16x32_bf16 v[188:191], a[60:63], a[108:111], v[188:191]\n\t"
    "v_mfma_f32_16x16x32_bf16 v[188:191], a[56:59], a[84:87], v[188:191]\n\t"
    "s_waitcnt vmcnt(15) lgkmcnt(0)\n\t"
    "s_barrier\n\t"
    "v_mfma_f32_16x16x32_bf16 v[0:3], a[0:3], a[112:115], v[0:3]\n\t"
    "ds_read_b64_tr_b16 a[32:33], v192 offset:49152\n\t"
    "v_mfma_f32_16x16x32_bf16 v[0:3], a[4:7], a[136:139], v[0:3]\n\t"
    "ds_read_b128 a[160:163], v224 offset:32768\n\t"
    "v_mfma_f32_16x16x32_bf16 v[8:11], a[4:7], a[140:143], v[8:11]\n\t"
    "ds_read_b64_tr_b16 a[34:35], v192 offset:50176\n\t"
    "s_add_u32 m0, s57, 24576\n\t"
    "v_mfma_f32_16x16x32_bf16 v[8:11], a[0:3], a[116:119], v[8:11]\n\t"
    "buffer_load_dwordx4 v200, s[36:39], s60 offen lds\n\t"
    "v_mfma_f32_16x16x32_bf16 v[16:19], a[0:3], a[120:123], v[16:19]\n\t"
    "ds_read_b64_tr_b16 a[36:37], v192 offset:51200\n\t"
    "v_mfma_f32_16x16x32_bf16 v[16:19], a[4:7], a[144:147], v[16:19]\n\t"
    "ds_read_b128 a[164:167], v225 offset:32768\n\t"
    "v_mfma_f32_16x16x32_bf16 v[24:27], a[4:7], a[148:151], v[24:27]\n\t"
    "ds_read_b64_tr_b16 a[38:39], v192 offset:52224\n\t"
    "v_mfma_f32_16x16x32_bf16 v[24:27], a[0:3], a[124:127], v[24:27]\n\t"
    "buffer_load_dwordx4 v201, s[36:39], s60 offen offset:1024 lds\n\t"
    "v_mfma_f32_16x16x32_bf16 v[32:35], a[0:3], a[128:131], v[32:35]\n\t"
    "ds_read_b64_tr_b16 a[40:41], v193 offset:49152\n\t"
    "v_mfma_f32_16x16x32_bf16 v[32:35], a[4:7], a[152:155], v[32:35]\n\t"
    "ds_read_b128 a[168:171], v226 offset:32768\n\t"
    "v_mfma_f32_16x16x32_bf16 v[40:43], a[4:7], a[156:159], v[40:43]\n\t"
    "ds_read_b64_tr_b16 a[42:43], v193 offset:50176\n\t"
    "v_mfma_f32_16x16x32_bf16 v[40:43], a[0:3], a[132:135], v[40:43]\n\t"
    "buffer_load_dwordx4 v202, s[36:39], s60 offen offset:2048 lds\n\t"
    "v_mfma_f32_16x16x32_bf16 v[4:7], a[8:11], a[112:115], v[4:7]\n\t"
    "ds_read_b64_tr_b16 a[44:45], v193 offset:51200\n\t"
    "v_mfma_f32_16x16x32_bf16 v[4:7], a[12:15], a[136:139], v[4:7]\n\t"
    "ds_read_b128 a[172:175], v227 offset:32768\n\t"
    "v_mfma_f32_16x16x32_bf16 v[12:15], a[12:15], a[140:143], v[12:15]\n\t"
    "ds_read_b64_tr_b16 a[46:47], v193 offset:52224\n\t"
    "v_mfma_f32_16x16x32_bf16 v[12:15], a[8:11], a[116:119], v[12:15]\n\t"
    "buffer_load_dwordx4 v203, s[36:39], s60 offen offset:3072 lds\n\t"
    "v_mfma_f32_16x16x32_bf16 v[20:23], a[8:11], a[120:123], v[20:23]\n\t"
    "ds_read_b64_tr_b16 a[48:49], v194 offset:49152\n\t"
    "v_mfma_f32_16x16x32_bf16 v[20:23], a[12:15], a[144:147], v[20:23]\n\t"
    "ds_read_b128 a[176:179], v224 offset:36864\n\t"
    "v_mfma_f32_16x16x32_bf16 v[28:31], a[12:15], a[148:151], v[28:31]\n\t"
    "ds_read_b64_tr_b16 a[50:51], v194 offset:50176\n\t"
    "s_add_u32 m0, s57, 28672\n\t"
    "v_mfma_f32_16x16x32_bf16 v[28:31], a[8:11], a[124:127], v[28:31]\n\t"
    "buffer_load_dwordx4 v204, s[36:39], s60 offen lds\n\t"
    "v_mfma_f32_16x16x32_bf16 v[36:39], a[8:11], a[128:131], v[36:39]\n\t"
    "ds_read_b64_tr_b16 a[52:53], v194 offset:51200\n\t"
    "v_mfma_f32_16x16x32_bf16 v[36:39], a[12:15], a[152:155], v[36:39]\n\t"
    "ds_read_b128 a[180:183], v225 offset:36864\n\t"
    "v_mfma_f32_16x16x32_bf16 v[44:47], a[12:15], a[156:159], v[44:47]\n\t"
    "ds_read_b64_tr_b16 a[54:55], v194 offset:52224\n\t"
    "v_mfma_f32_16x16x32_bf16 v[44:47], a[8:11], a[132:135], v[44:47]\n\t"
    "buffer_load_dwordx4 v205, s[36:39], s60 offen offset:1024 lds\n\t"
    "s_xor_b32 s60, s60, 896\n\t"
    "v_mfma_f32_16x16x32_bf16 v[48:51], a[16:19], a[112:115], v[48:51]\n\t"
    "ds_read_b64_tr_b16 a[56:57], v195 offset:49152\n\t"
    "v_mfma_f32_16x16x32_bf16 v[48:51], a[20:23], a[136:139], v[48:51]\n\t"
    "ds_read_b128 a[184:187], v226 offset:36864\n\t"
    "v_mfma_f32_16x16x32_bf16 v[56:59], a[20:23], a[140:143], v[56:59]\n\t"
    "ds_read_b64_tr_b16 a[58:59], v195 offset:50176\n\t"
    "s_add_u32 m0, s56, 32768\n\t"
    "v_mfma_f32_16x16x32_bf16 v[56:59], a[16:19], a[116:119], v[56:59]\n\t"
    "buffer_load_dwordx4 v208, s[40:43], s61 offen lds\n\t"
    "v_mfma_f32_16x16x32_bf16 v[64:67], a[16:19], a[120:123], v[64:67]\n\t"
    "ds_read_b64_tr_b16 a[60:61], v195 offset:51200\n\t"
    "v_mfma_f32_16x16x32_bf16 v[64:67], a[20:23], a[144:147], v[64:67]\n\t"
    "ds_read_b128 a[188:191], v227 offset:36864\n\t"
    "v_mfma_f32_16x16x32_bf16 v[72:75], a[20:23], a[148:151], v[72:75]\n\t"
    "ds_read_b64_tr_b16 a[62:63], v195 offset:52224\n\t"
    "v_mfma_f32_16x16x32_bf16 v[72:75], a[16:19], a[124:127], v[72:75]\n\t"
    "buffer_load_dwordx4 v209, s[40:43], s61 offen offset:1024 lds\n\t"
    "v_mfma_f32_16x16x32_bf16 v[80:83], a[16:19], a[128:131], v[80:83]\n\t"
    "v_mfma_f32_16x16x32_bf16 v[80:83], a[20:23], a[152:155], v[80:83]\n\t"
    "v_mfma_f32_16x16x32_bf16 v[88:91], a[20:23], a[156:159], v[88:91]\n\t"
    "v_mfma_f32_16x16x32_bf16 v[88:91], a[16:19], a[132:135], v[88:91]\n\t"
    "buffer_load_dwordx4 v210, s[40:43], s61 offen offset:2048 lds\n\t"
    "v_mfma_f32_16x16x32_bf16 v[52:55], a[24:27], a[112:115], v[52:55]\n\t"
    "v_mfma_f32_16x16x32_bf16 v[52:55], a[28:31], a[136:139], v[52:55]\n\t"
    "v_mfma_f32_16x16x32_bf16 v[60:63], a[28:31], a[140:143], v[60:63]\n\t"
    "v_mfma_f32_16x16x32_bf16 v[60:63], a[24:27], a[116:119], v[60:63]\n\t"
    "buffer_load_dwordx4 v211, s[40:43], s61 offen offset:3072 lds\n\t"
    "s_xor_b32 s61, s61, 128\n\t"
    "v_mfma_f32_16x16x32_bf16 v[68:71], a[24:27], a[120:123], v[68:71]\n\t"
    "v_mfma_f32_16x16x32_bf16 v[68:71], a[28:31], a[144:147], v[68:71]\n\t"
    "v_mfma_f32_16x16x32_bf16 v[76:79], a[28:31], a[148:151], v[76:79]\n\t"
    "v_mfma_f32_16x16x32_bf16 v[76:79], a[24:27], a[124:127], v[76:79]\n\t"
    "v_mfma_f32_16x16x32_bf16 v[84:87], a[24:27], a[128:131], v[84:87]\n\t"
    "v_mfma_f32_16x16x32_bf16 v[84:87], a[28:31], a[152:155], v[84:87]\n\t"
    "v_mfma_f32_16x16x32_bf16 v[92:95], a[28:31], a[156:159], v[92:95]\n\t"
    "v_mfma_f32_16x16x32_bf16 v[92:95], a[24:27], a[132:135], v[92:95]\n\t"
    "s_waitcnt vmcnt(15) lgkmcnt(0)\n\t"
    "s_barrier\n\t"
    "v_mfma_f32_16x16x32_bf16 v[96:99], a[32:35], a[112:115], v[96:99]\n\t"
    "ds_read_b64_tr_b16 a[0:1], v192\n\t"
    "s_add_u32 m0, s58, 32768\n\t"
    "v_mfma_f32_16x16x32_bf16 v[96:99], a[36:39], a[136:139], v[96:99]\n\t"
    "buffer_load_dwordx4 v228, s[48:51], s18 offen nt lds\n\t"
    "v_mfma_f32_16x16x32_bf16 v[104:107], a[36:39], a[140:143], v[104:107]\n\t"
    "ds_read_b64_tr_b16 a[2:3], v192 offset:1024\n\t"
    "s_add_u32 m0, s58, 33536\n\t"
    "v_mfma_f32_16x16x32_bf16 v[104:107], a[32:35], a[116:119], v[104:107]\n\t"
    "buffer_load_dwordx4 v238, s[48:51], s18 offen offset:256 nt lds\n\t"
    "v_mfma_f32_16x16x32_bf16 v[112:115], a[32:35], a[120:123], v[112:115]\n\t"
    "ds_read_b64_tr_b16 a[4:5], v192 offset:2048\n\t"
    "s_add_u32 m0, s58, 34304\n\t"
    "v_mfma_f32_16x16x32_bf16 v[112:115], a[36:39], a[144:147], v[112:115]\n\t"
    "buffer_load_dwordx4 v228, s[48:51], s18 offen offset:512 nt lds\n\t"
    "v_mfma_f32_16x16x32_bf16 v[120:123], a[36:39], a[148:151], v[120:123]\n\t"
    "ds_read_b64_tr_b16 a[6:7], v192 offset:3072\n\t"
    "s_add_u32 m0, s58, 35072\n\t"
    "v_mfma_f32_16x16x32_bf16 v[120:123], a[32:35], a[124:127], v[120:123]\n\t"
    "buffer_load_dwordx4 v238, s[48:51], s18 offen offset:768 nt lds\n\t"
    "v_mfma_f32_16x16x32_bf16 v[128:131], a[32:35], a[128:131], v[128:131]\n\t"
    "ds_read_b64_tr_b16 a[8:9], v193\n\t"
    "v_mfma_f32_16x16x32_bf16 v[128:131], a[36:39], a[152:155], v[128:131]\n\t"
    "ds_read_b128 a[64:67], v206\n\t"
    "v_mfma_f32_16x16x32_bf16 v[136:139], a[36:39], a[156:159], v[136:139]\n\t"
    "ds_read_b64_tr_b16 a[10:11], v193 offset:1024\n\t"
    "v_mfma_f32_16x16x32_bf16 v[136:139], a[32:35], a[132:135], v[136:139]\n\t"
    "ds_read_b128 a[88:91], v207\n\t"
    "v_mfma_f32_16x16x32_bf16 v[100:103], a[40:43], a[112:115], v[100:103]\n\t"
    "ds_read_b64_tr_b16 a[12:13], v193 offset:2048\n\t"
    "v_mfma_f32_16x16x32_bf16 v[100:103], a[44:47], a[136:139], v[100:103]\n\t"
    "ds_read_b128 a[68:71], v206 offset:2048\n\t"
    "v_mfma_f32_16x16x32_bf16 v[108:111], a[44:47], a[140:143], v[108:111]\n\t"
    "ds_read_b64_tr_b16 a[14:15], v193 offset:3072\n\t"
    "v_mfma_f32_16x16x32_bf16 v[108:111], a[40:43], a[116:119], v[108:111]\n\t"
    "ds_read_b128 a[92:95], v207 offset:2048\n\t"
    "v_mfma_f32_16x16x32_bf16 v[116:119], a[40:43], a[120:123], v[116:119]\n\t"
    "ds_read_b64_tr_b16 a[16:17], v194\n\t"
    "v_mfma_f32_16x16x32_bf16 v[116:119], a[44:47], a[144:147], v[116:119]\n\t"
    "ds_read_b128 a[72:75], v206 offset:4096\n\t"
    "v_mfma_f32_16x16x32_bf16 v[124:127], a[44:47], a[148:151], v[124:127]\n\t"
    "ds_read_b64_tr_b16 a[18:19], v194 offset:1024\n\t"
    "v_mfma_f32_16x16x32_bf16 v[124:127], a[40:43], a[124:127], v[124:127]\n\t"
    "ds_read_b128 a[96:99], v207 offset:4096\n\t"
    "v_mfma_f32_16x16x32_bf16 v[132:135], a[40:43], a[128:131], v[132:135]\n\t"
    "ds_read_b64_tr_b16 a[20:21], v194 offset:2048\n\t"
    "v_mfma_f32_16x16x32_bf16 v[132:135], a[44:47], a[152:155], v[132:135]\n\t"
    "ds_read_b128 a[76:79], v206 offset:6144\n\t"
    "v_mfma_f32_16x16x32_bf16 v[140:143], a[44:47], a[156:159], v[140:143]\n\t"
    "ds_read_b64_tr_b16 a[22:23], v194 offset:3072\n\t"
    "v_mfma_f32_16x16x32_bf16 v[140:143], a[40:43], a[132:135], v[140:143]\n\t"
    "ds_read_b128 a[100:103], v207 offset:6144\n\t"
    "v_mfma_f32_16x16x32_bf16 v[144:147], a[48:51], a[112:115], v[144:147]\n\t"
    "ds_read_b64_tr_b16 a[24:25], v195\n\t"
    "v_mfma_f32_16x16x32_bf16 v[144:147], a[52:55], a[136:139], v[144:147]\n\t"
    "ds_read_b128 a[80:83], v206 offset:8192\n\t"
    "v_mfma_f32_16x16x32_bf16 v[152:155], a[52:55], a[140:143], v[152:155]\n\t"
    "ds_read_b64_tr_b16 a[26:27], v195 offset:1024\n\t"
    "v_mfma_f32_16x16x32_bf16 v[152:155], a[48:51], a[116:119], v[152:155]\n\t"
    "ds_read_b128 a[104:107], v207 offset:8192\n\t"
    "v_mfma_f32_16x16x32_bf16 v[160:163], a[48:51], a[120:123], v[160:163]\n\t"
    "ds_read_b64_tr_b16 a[28:29], v195 offset:2048\n\t"
    "v_mfma_f32_16x16x32_bf16 v[160:163], a[52:55], a[144:147], v[160:163]\n\t"
    "ds_read_b128 a[84:87], v206 offset:10240\n\t"
    "v_mfma_f32_16x16x32_bf16 v[168:171], a[52:55], a[148:151], v[168:171]\n\t"
    "ds_read_b64_tr_b16 a[30:31], v195 offset:3072\n\t"
    "v_mfma_f32_16x16x32_bf16 v[168:171], a[48:51], a[124:127], v[168:171]\n\t"
    "ds_read_b128 a[108:111], v207 offset:10240\n\t"
    "v_mfma_f32_16x16x32_bf16 v[176:179], a[48:51], a[128:131], v[176:179]\n\t"
    "s_add_u32 m0, s56, 49152\n\t"
    "v_mfma_f32_16x16x32_bf16 v[176:179], a[52:55], a[152:155], v[176:179]\n\t"
    "buffer_load_dwordx4 v208, s[40:43], s61 offen lds\n\t"
    "v_mfma_f32_16x16x32_bf16 v[184:187], a[52:55], a[156:159], v[184:187]\n\t"
    "v_mfma_f32_16x16x32_bf16 v[184:187], a[48:51], a[132:135], v[184:187]\n\t"
    "buffer_load_dwordx4 v209, s[40:43], s61 offen offset:1024 lds\n\t"
    "v_mfma_f32_16x16x32_bf16 v[148:151], a[56:59], a[112:115], v[148:151]\n\t"
    "v_mfma_f32_16x16x32_bf16 v[148:151], a[60:63], a[136:139], v[148:151]\n\t"
    "buffer_load_dwordx4 v210, s[40:43], s61 offen offset:2048 lds\n\t"
    "v_mfma_f32_16x16x32_bf16 v[156:159], a[60:63], a[140:143], v[156:159]\n\t"
    "v_mfma_f32_16x16x32_bf16 v[156:159], a[56:59], a[116:119], v[156:159]\n\t"
    "buffer_load_dwordx4 v211, s[40:43], s61 offen offset:3072 lds\n\t"
    "s_xor_b32 s61, s61, 3670144\n\t"
    "v_mfma_f32_16x16x32_bf16 v[164:167], a[56:59], a[120:123], v[164:167]\n\t"
    "v_mfma_f32_16x16x32_bf16 v[164:167], a[60:63], a[144:147], v[164:167]\n\t"
    "v_mfma_f32_16x16x32_bf16 v[172:175], a[60:63], a[148:151], v[172:175]\n\t"
    "v_mfma_f32_16x16x32_bf16 v[172:175], a[56:59], a[124:127], v[172:175]\n\t"
    "v_mfma_f32_16x16x32_bf16 v[180:183], a[56:59], a[128:131], v[180:183]\n\t"
    "v_mfma_f32_16x16x32_bf16 v[180:183], a[60:63], a[152:155], v[180:183]\n\t"
    "v_mfma_f32_16x16x32_bf16 v[188:191], a[60:63], a[156:159], v[188:191]\n\t"
    "v_mfma_f32_16x16x32_bf16 v[188:191], a[56:59], a[132:135], v[188:191]\n\t"
    "s_waitcnt vmcnt(18) lgkmcnt(0)\n\t"
    "s_barrier\n\t"
    "v_mfma_f32_16x16x32_bf16 v[0:3], a[0:3], a[64:67], v[0:3]\n\t"
    "ds_read_b64_tr_b16 a[32:33], v192 offset:16384\n\t"
    "v_mfma_f32_16x16x32_bf16 v[0:3], a[4:7], a[88:91], v[0:3]\n\t"
    "v_mfma_f32_16x16x32_bf16 v[8:11], a[4:7], a[92:95], v[8:11]\n\t"
    "ds_read_b64_tr_b16 a[34:35], v192 offset:17408\n\t"
    "s_add_u32 m0, s57, 0\n\t"
    "s_nop 0\n\t"
    "v_mfma_f32_16x16x32_bf16 v[8:11], a[0:3], a[68:71], v[8:11]\n\t"
    "buffer_load_dwordx4 v200, s[36:39], s60 offen lds\n\t"
    "v_mfma_f32_16x16x32_bf16 v[16:19], a[0:3], a[72:75], v[16:19]\n\t"
    "ds_read_b64_tr_b16 a[36:37], v192 offset:18432\n\t"
    "v_mfma_f32_16x16x32_bf16 v[16:19], a[4:7], a[96:99], v[16:19]\n\t"
    "v_mfma_f32_16x16x32_bf16 v[24:27], a[4:7], a[100:103], v[24:27]\n\t"
    "ds_read_b64_tr_b16 a[38:39], v192 offset:19456\n\t"
    "v_mfma_f32_16x16x32_bf16 v[24:27], a[0:3], a[76:79], v[24:27]\n\t"
    "buffer_load_dwordx4 v201, s[36:39], s60 offen offset:1024 lds\n\t"
    "v_mfma_f32_16x16x32_bf16 v[32:35], a[0:3], a[80:83], v[32:35]\n\t"
    "ds_read_b64_tr_b16 a[40:41], v193 offset:16384\n\t"
    "v_mfma_f32_16x16x32_bf16 v[32:35], a[4:7], a[104:107], v[32:35]\n\t"
    "v_mfma_f32_16x16x32_bf16 v[40:43], a[4:7], a[108:111], v[40:43]\n\t"
    "ds_read_b64_tr_b16 a[42:43], v193 offset:17408\n\t"
    "v_mfma_f32_16x16x32_bf16 v[40:43], a[0:3], a[84:87], v[40:43]\n\t"
    "buffer_load_dwordx4 v202, s[36:39], s60 offen offset:2048 lds\n\t"
    "v_mfma_f32_16x16x32_bf16 v[4:7], a[8:11], a[64:67], v[4:7]\n\t"
    "ds_read_b64_tr_b16 a[44:45], v193 offset:18432\n\t"
    "v_mfma_f32_16x16x32_bf16 v[4:7], a[12:15], a[88:91], v[4:7]\n\t"
    "v_mfma_f32_16x16x32_bf16 v[12:15], a[12:15], a[92:95], v[12:15]\n\t"
    "ds_read_b64_tr_b16 a[46:47], v193 offset:19456\n\t"
    "v_mfma_f32_16x16x32_bf16 v[12:15], a[8:11], a[68:71], v[12:15]\n\t"
    "buffer_load_dwordx4 v203, s[36:39], s60 offen offset:3072 lds\n\t"
    "v_mfma_f32_16x16x32_bf16 v[20:23], a[8:11], a[72:75], v[20:23]\n\t"
    "ds_read_b64_tr_b16 a[48:49], v194 offset:16384\n\t"
    "v_mfma_f32_16x16x32_bf16 v[20:23], a[12:15], a[96:99], v[20:23]\n\t"
    "v_mfma_f32_16x16x32_bf16 v[28:31], a[12:15], a[100:103], v[28:31]\n\t"
    "ds_read_b64_tr_b16 a[50:51], v194 offset:17408\n\t"
    "s_add_u32 m0, s57, 4096\n\t"
    "v_mfma_f32_16x16x32_bf16 v[28:31], a[8:11], a[76:79], v[28:31]\n\t"
    "buffer_load_dwordx4 v204, s[36:39], s60 offen lds\n\t"
    "v_mfma_f32_16x16x32_bf16 v[36:39], a[8:11], a[80:83], v[36:39]\n\t"
    "ds_read_b64_tr_b16 a[52:53], v194 offset:18432\n\t"
    "v_mfma_f32_16x16x32_bf16 v[36:39], a[12:15], a[104:107], v[36:39]\n\t"
    "v_mfma_f32_16x16x32_bf16 v[44:47], a[12:15], a[108:111], v[44:47]\n\t"
    "ds_read_b64_tr_b16 a[54:55], v194 offset:19456\n\t"
    "v_mfma_f32_16x16x32_bf16 v[44:47], a[8:11], a[84:87], v[44:47]\n\t"
    "buffer_load_dwordx4 v205, s[36:39], s60 offen offset:1024 lds\n\t"
    "s_xor_b32 s60, s60, 128\n\t"
    "v_mfma_f32_16x16x32_bf16 v[48:51], a[16:19], a[64:67], v[48:51]\n\t"
    "ds_read_b64_tr_b16 a[56:57], v195 offset:16384\n\t"
    "v_mfma_f32_16x16x32_bf16 v[48:51], a[20:23], a[88:91], v[48:51]\n\t"
    "v_mfma_f32_16x16x32_bf16 v[56:59], a[20:23], a[92:95], v[56:59]\n\t"
    "ds_read_b64_tr_b16 a[58:59], v195 offset:17408\n\t"
    "s_add_u32 m0, s56, 0\n\t"
    "s_nop 0\n\t"
    "v_mfma_f32_16x16x32_bf16 v[56:59], a[16:19], a[68:71], v[56:59]\n\t"
    "buffer_load_dwordx4 v208, s[40:43], s61 offen lds\n\t"
    "v_mfma_f32_16x16x32_bf16 v[64:67], a[16:19], a[72:75], v[64:67]\n\t"
    "ds_read_b64_tr_b16 a[60:61], v195 offset:18432\n\t"
    "v_mfma_f32_16x16x32_bf16 v[64:67], a[20:23], a[96:99], v[64:67]\n\t"
    "v_mfma_f32_16x16x32_bf16 v[72:75], a[20:23], a[100:103], v[72:75]\n\t"
    "ds_read_b64_tr_b16 a[62:63], v195 offset:19456\n\t"
    "v_mfma_f32_16x16x32_bf16 v[72:75], a[16:19], a[76:79], v[72:75]\n\t"
    "buffer_load_dwordx4 v209, s[40:43], s61 offen offset:1024 lds\n\t"
    "v_mfma_f32_16x16x32_bf16 v[80:83], a[16:19], a[80:83], v[80:83]\n\t"
    "v_mfma_f32_16x16x32_bf16 v[80:83], a[20:23], a[104:107], v[80:83]\n\t"
    "v_mfma_f32_16x16x32_bf16 v[88:91], a[20:23], a[108:111], v[88:91]\n\t"
    "v_mfma_f32_16x16x32_bf16 v[88:91], a[16:19], a[84:87], v[88:91]\n\t"
    "buffer_load_dwordx4 v210, s[40:43], s61 offen offset:2048 lds\n\t"
    "v_mfma_f32_16x16x32_bf16 v[52:55], a[24:27], a[64:67], v[52:55]\n\t"
    "v_mfma_f32_16x16x32_bf16 v[52:55], a[28:31], a[88:91], v[52:55]\n\t"
    "v_mfma_f32_16x16x32_bf16 v[60:63], a[28:31], a[92:95], v[60:63]\n\t"
    "v_mfma_f32_16x16x32_bf16 v[60:63], a[24:27], a[68:71], v[60:63]\n\t"
    "buffer_load_dwordx4 v211, s[40:43], s61 offen offset:3072 lds\n\t"
    "s_xor_b32 s61, s61, 128\n\t"
    "v_mfma_f32_16x16x32_bf16 v[68:71], a[24:27], a[72:75], v[68:71]\n\t"
    "v_mfma_f32_16x16x32_bf16 v[68:71], a[28:31], a[96:99], v[68:71]\n\t"
    "v_mfma_f32_16x16x32_bf16 v[76:79], a[28:31], a[100:103], v[76:79]\n\t"
    "v_mfma_f32_16x16x32_bf16 v[76:79], a[24:27], a[76:79], v[76:79]\n\t"
    "v_mfma_f32_16x16x32_bf16 v[84:87], a[24:27], a[80:83], v[84:87]\n\t"
    "v_mfma_f32_16x16x32_bf16 v[84:87], a[28:31], a[104:107], v[84:87]\n\t"
    "v_mfma_f32_16x16x32_bf16 v[92:95], a[28:31], a[108:111], v[92:95]\n\t"
    "v_mfma_f32_16x16x32_bf16 v[92:95], a[24:27], a[84:87], v[92:95]\n\t"
    "s_waitcnt vmcnt(18) lgkmcnt(0)\n\t"
    "s_barrier\n\t"
    "v_mfma_f32_16x16x32_bf16 v[96:99], a[32:35], a[64:67], v[96:99]\n\t"
    "ds_read_b64_tr_b16 a[0:1], v192 offset:32768\n\t"
    "v_mfma_f32_16x16x32_bf16 v[96:99], a[36:39], a[88:91], v[96:99]\n\t"
    "buffer_load_dword v239, v246, s[64:67], s76 offen\n\t"
    "s_add_u32 s76, s76, 524288\n\t"
    "v_mfma_f32_16x16x32_bf16 v[104:107], a[36:39], a[92:95], v[104:107]\n\t"
    "ds_read_b64_tr_b16 a[2:3], v192 offset:33792\n\t"
    "v_mfma_f32_16x16x32_bf16 v[104:107], a[32:35], a[68:71], v[104:107]\n\t"
    "ds_read_b128 a[112:115], v206 offset:24576\n\t"
    "v_mfma_f32_16x16x32_bf16 v[112:115], a[32:35], a[72:75], v[112:115]\n\t"
    "ds_read_b64_tr_b16 a[4:5], v192 offset:34816\n\t"
    "v_mfma_f32_16x16x32_bf16 v[112:115], a[36:39], a[96:99], v[112:115]\n\t"
    "ds_read_b128 a[136:139], v207 offset:24576\n\t"
    "v_mfma_f32_16x16x32_bf16 v[120:123], a[36:39], a[100:103], v[120:123]\n\t"
    "ds_read_b64_tr_b16 a[6:7], v192 offset:35840\n\t"
    "v_mfma_f32_16x16x32_bf16 v[120:123], a[32:35], a[76:79], v[120:123]\n\t"
    "ds_read_b128 a[116:119], v206 offset:26624\n\t"
    "v_mfma_f32_16x16x32_bf16 v[128:131], a[32:35], a[80:83], v[128:131]\n\t"
    "ds_read_b64_tr_b16 a[8:9], v193 offset:32768\n\t"
    "v_mfma_f32_16x16x32_bf16 v[128:131], a[36:39], a[104:107], v[128:131]\n\t"
    "ds_read_b128 a[140:143], v207 offset:26624\n\t"
    "v_mfma_f32_16x16x32_bf16 v[136:139], a[36:39], a[108:111], v[136:139]\n\t"
    "ds_read_b64_tr_b16 a[10:11], v193 offset:33792\n\t"
    "v_mfma_f32_16x16x32_bf16 v[136:139], a[32:35], a[84:87], v[136:139]\n\t"
    "ds_read_b128 a[120:123], v206 offset:28672\n\t"
    "v_mfma_f32_16x16x32_bf16 v[100:103], a[40:43], a[64:67], v[100:103]\n\t"
    "ds_read_b64_tr_b16 a[12:13], v193 offset:34816\n\t"
    "v_mfma_f32_16x16x32_bf16 v[100:103], a[44:47], a[88:91], v[100:103]\n\t"
    "ds_read_b128 a[144:147], v207 offset:28672\n\t"
    "v_mfma_f32_16x16x32_bf16 v[108:111], a[44:47], a[92:95], v[108:111]\n\t"
    "ds_read_b64_tr_b16 a[14:15], v193 offset:35840\n\t"
    "v_mfma_f32_16x16x32_bf16 v[108:111], a[40:43], a[68:71], v[108:111]\n\t"
    "ds_read_b128 a[124:127], v206 offset:30720\n\t"
    "v_mfma_f32_16x16x32_bf16 v[116:119], a[40:43], a[72:75], v[116:119]\n\t"
    "ds_read_b64_tr_b16 a[16:17], v194 offset:32768\n\t"
    "v_mfma_f32_16x16x32_bf16 v[116:119], a[44:47], a[96:99], v[116:119]\n\t"
    "ds_read_b128 a[148:151], v207 offset:30720\n\t"
    "v_mfma_f32_16x16x32_bf16 v[124:127], a[44:47], a[100:103], v[124:127]\n\t"
    "ds_read_b64_tr_b16 a[18:19], v194 offset:33792\n\t"
    "v_mfma_f32_16x16x32_bf16 v[124:127], a[40:43], a[76:79], v[124:127]\n\t"
    "ds_read_b128 a[128:131], v206 offset:32768\n\t"
    "v_mfma_f32_16x16x32_bf16 v[132:135], a[40:43], a[80:83], v[132:135]\n\t"
    "ds_read_b64_tr_b16 a[20:21], v194 offset:34816\n\t"
    "v_mfma_f32_16x16x32_bf16 v[132:135], a[44:47], a[104:107], v[132:135]\n\t"
    "ds_read_b128 a[152:155], v207 offset:32768\n\t"
    "v_mfma_f32_16x16x32_bf16 v[140:143], a[44:47], a[108:111], v[140:143]\n\t"
    "ds_read_b64_tr_b16 a[22:23], v194 offset:35840\n\t"
    "v_mfma_f32_16x16x32_bf16 v[140:143], a[40:43], a[84:87], v[140:143]\n\t"
    "ds_read_b128 a[132:135], v206 offset:34816\n\t"
    "v_mfma_f32_16x16x32_bf16 v[144:147], a[48:51], a[64:67], v[144:147]\n\t"
    "ds_read_b64_tr_b16 a[24:25], v195 offset:32768\n\t"
    "v_mfma_f32_16x16x32_bf16 v[144:147], a[52:55], a[88:91], v[144:147]\n\t"
    "ds_read_b128 a[156:159], v207 offset:34816\n\t"
    "v_mfma_f32_16x16x32_bf16 v[152:155], a[52:55], a[92:95], v[152:155]\n\t"
    "ds_read_b64_tr_b16 a[26:27], v195 offset:33792\n\t"
    "v_mfma_f32_16x16x32_bf16 v[152:155], a[48:51], a[68:71], v[152:155]\n\t"
    "v_mfma_f32_16x16x32_bf16 v[160:163], a[48:51], a[72:75], v[160:163]\n\t"
    "ds_read_b64_tr_b16 a[28:29], v195 offset:34816\n\t"
    "s_add_u32 m0, s56, 16384\n\t"
    "v_mfma_f32_16x16x32_bf16 v[160:163], a[52:55], a[96:99], v[160:163]\n\t"
    "buffer_load_dwordx4 v208, s[40:43], s61 offen lds\n\t"
    "v_mfma_f32_16x16x32_bf16 v[168:171], a[52:55], a[100:103], v[168:171]\n\t"
    "ds_read_b64_tr_b16 a[30:31], v195 offset:35840\n\t"
    "v_mfma_f32_16x16x32_bf16 v[168:171], a[48:51], a[76:79], v[168:171]\n\t"
    "v_mfma_f32_16x16x32_bf16 v[176:179], a[48:51], a[80:83], v[176:179]\n\t"
    "v_mfma_f32_16x16x32_bf16 v[176:179], a[52:55], a[104:107], v[176:179]\n\t"
    "buffer_load_dwordx4 v209, s[40:43], s61 offen offset:1024 lds\n\t"
    "v_mfma_f32_16x16x32_bf16 v[184:187], a[52:55], a[108:111], v[184:187]\n\t"
    "v_mfma_f32_16x16x32_bf16 v[184:187], a[48:51], a[84:87], v[184:187]\n\t"
    "v_mfma_f32_16x16x32_bf16 v[148:151], a[56:59], a[64:67], v[148:151]\n\t"
    "v_mfma_f32_16x16x32_bf16 v[148:151], a[60:63], a[88:91], v[148:151]\n\t"
    "buffer_load_dwordx4 v210, s[40:43], s61 offen offset:2048 lds\n\t"
    "v_mfma_f32_16x16x32_bf16 v[156:159], a[60:63], a[92:95], v[156:159]\n\t"
    "v_mfma_f32_16x16x32_bf16 v[156:159], a[56:59], a[68:71], v[156:159]\n\t"
    "v_mfma_f32_16x16x32_bf16 v[164:167], a[56:59], a[72:75], v[164:167]\n\t"
    "v_mfma_f32_16x16x32_bf16 v[164:167], a[60:63], a[96:99], v[164:167]\n\t"
    "buffer_load_dwordx4 v211, s[40:43], s61 offen offset:3072 lds\n\t"
    "s_xor_b32 s61, s61, 524416\n\t"
    "v_mfma_f32_16x16x32_bf16 v[172:175], a[60:63], a[100:103], v[172:175]\n\t"
    "v_mfma_f32_16x16x32_bf16 v[172:175], a[56:59], a[76:79], v[172:175]\n\t"
    "v_mfma_f32_16x16x32_bf16 v[180:183], a[56:59], a[80:83], v[180:183]\n\t"
    "v_mfma_f32_16x16x32_bf16 v[180:183], a[60:63], a[104:107], v[180:183]\n\t"
    "v_mfma_f32_16x16x32_bf16 v[188:191], a[60:63], a[108:111], v[188:191]\n\t"
    "v_mfma_f32_16x16x32_bf16 v[188:191], a[56:59], a[84:87], v[188:191]\n\t"
    "s_waitcnt vmcnt(15) lgkmcnt(0)\n\t"
    "s_barrier\n\t"
    "v_mfma_f32_16x16x32_bf16 v[0:3], a[0:3], a[112:115], v[0:3]\n\t"
    "ds_read_b64_tr_b16 a[32:33], v192 offset:49152\n\t"
    "v_mfma_f32_16x16x32_bf16 v[0:3], a[4:7], a[136:139], v[0:3]\n\t"
    "v_mfma_f32_16x16x32_bf16 v[8:11], a[4:7], a[140:143], v[8:11]\n\t"
    "ds_read_b64_tr_b16 a[34:35], v192 offset:50176\n\t"
    "s_add_u32 m0, s57, 24576\n\t"
    "v_mfma_f32_16x16x32_bf16 v[8:11], a[0:3], a[116:119], v[8:11]\n\t"
    "buffer_load_dwordx4 v200, s[36:39], s60 offen lds\n\t"
    "v_mfma_f32_16x16x32_bf16 v[16:19], a[0:3], a[120:123], v[16:19]\n\t"
    "ds_read_b64_tr_b16 a[36:37], v192 offset:51200\n\t"
    "v_mfma_f32_16x16x32_bf16 v[16:19], a[4:7], a[144:147], v[16:19]\n\t"
    "v_mfma_f32_16x16x32_bf16 v[24:27], a[4:7], a[148:151], v[24:27]\n\t"
    "ds_read_b64_tr_b16 a[38:39], v192 offset:52224\n\t"
    "v_mfma_f32_16x16x32_bf16 v[24:27], a[0:3], a[124:127], v[24:27]\n\t"
    "buffer_load_dwordx4 v201, s[36:39], s60 offen offset:1024 lds\n\t"
    "v_mfma_f32_16x16x32_bf16 v[32:35], a[0:3], a[128:131], v[32:35]\n\t"
    "ds_read_b64_tr_b16 a[40:41], v193 offset:49152\n\t"
    "v_mfma_f32_16x16x32_bf16 v[32:35], a[4:7], a[152:155], v[32:35]\n\t"
    "v_mfma_f32_16x16x32_bf16 v[40:43], a[4:7], a[156:159], v[40:43]\n\t"
    "ds_read_b64_tr_b16 a[42:43], v193 offset:50176\n\t"
    "v_mfma_f32_16x16x32_bf16 v[40:43], a[0:3], a[132:135], v[40:43]\n\t"
    "buffer_load_dwordx4 v202, s[36:39], s60 offen offset:2048 lds\n\t"
    "v_mfma_f32_16x16x32_bf16 v[4:7], a[8:11], a[112:115], v[4:7]\n\t"
    "ds_read_b64_tr_b16 a[44:45], v193 offset:51200\n\t"
    "v_mfma_f32_16x16x32_bf16 v[4:7], a[12:15], a[136:139], v[4:7]\n\t"
    "v_mfma_f32_16x16x32_bf16 v[12:15], a[12:15], a[140:143], v[12:15]\n\t"
    "ds_read_b64_tr_b16 a[46:47], v193 offset:52224\n\t"
    "v_mfma_f32_16x16x32_bf16 v[12:15], a[8:11], a[116:119], v[12:15]\n\t"
    "buffer_load_dwordx4 v203, s[36:39], s60 offen offset:3072 lds\n\t"
    "v_mfma_f32_16x16x32_bf16 v[20:23], a[8:11], a[120:123], v[20:23]\n\t"
    "ds_read_b64_tr_b16 a[48:49], v194 offset:49152\n\t"
    "v_mfma_f32_16x16x32_bf16 v[20:23], a[12:15], a[144:147], v[20:23]\n\t"
    "v_mfma_f32_16x16x32_bf16 v[28:31], a[12:15], a[148:151], v[28:31]\n\t"
    "ds_read_b64_tr_b16 a[50:51], v194 offset:50176\n\t"
    "s_add_u32 m0, s57, 28672\n\t"
    "v_mfma_f32_16x16x32_bf16 v[28:31], a[8:11], a[124:127], v[28:31]\n\t"
    "buffer_load_dwordx4 v204, s[36:39], s60 offen lds\n\t"
    "v_mfma_f32_16x16x32_bf16 v[36:39], a[8:11], a[128:131], v[36:39]\n\t"
    "ds_read_b64_tr_b16 a[52:53], v194 offset:51200\n\t"
    "v_mfma_f32_16x16x32_bf16 v[36:39], a[12:15], a[152:155], v[36:39]\n\t"
    "v_mfma_f32_16x16x32_bf16 v[44:47], a[12:15], a[156:159], v[44:47]\n\t"
    "ds_read_b64_tr_b16 a[54:55], v194 offset:52224\n\t"
    "v_mfma_f32_16x16x32_bf16 v[44:47], a[8:11], a[132:135], v[44:47]\n\t"
    "buffer_load_dwordx4 v205, s[36:39], s60 offen offset:1024 lds\n\t"
    "s_xor_b32 s60, s60, 384\n\t"
    "v_mfma_f32_16x16x32_bf16 v[48:51], a[16:19], a[112:115], v[48:51]\n\t"
    "ds_read_b64_tr_b16 a[56:57], v195 offset:49152\n\t"
    "v_mfma_f32_16x16x32_bf16 v[48:51], a[20:23], a[136:139], v[48:51]\n\t"
    "v_mfma_f32_16x16x32_bf16 v[56:59], a[20:23], a[140:143], v[56:59]\n\t"
    "ds_read_b64_tr_b16 a[58:59], v195 offset:50176\n\t"
    "s_add_u32 m0, s56, 32768\n\t"
    "v_mfma_f32_16x16x32_bf16 v[56:59], a[16:19], a[116:119], v[56:59]\n\t"
    "buffer_load_dwordx4 v208, s[40:43], s61 offen lds\n\t"
    "v_mfma_f32_16x16x32_bf16 v[64:67], a[16:19], a[120:123], v[64:67]\n\t"
    "ds_read_b64_tr_b16 a[60:61], v195 offset:51200\n\t"
    "v_mfma_f32_16x16x32_bf16 v[64:67], a[20:23], a[144:147], v[64:67]\n\t"
    "v_mfma_f32_16x16x32_bf16 v[72:75], a[20:23], a[148:151], v[72:75]\n\t"
    "ds_read_b64_tr_b16 a[62:63], v195 offset:52224\n\t"
    "v_mfma_f32_16x16x32_bf16 v[72:75], a[16:19], a[124:127], v[72:75]\n\t"
    "buffer_load_dwordx4 v209, s[40:43], s61 offen offset:1024 lds\n\t"
    "v_mfma_f32_16x16x32_bf16 v[80:83], a[16:19], a[128:131], v[80:83]\n\t"
    "v_mfma_f32_16x16x32_bf16 v[80:83], a[20:23], a[152:155], v[80:83]\n\t"
    "v_mfma_f32_16x16x32_bf16 v[88:91], a[20:23], a[156:159], v[88:91]\n\t"
    "v_mfma_f32_16x16x32_bf16 v[88:91], a[16:19], a[132:135], v[88:91]\n\t"
    "buffer_load_dwordx4 v210, s[40:43], s61 offen offset:2048 lds\n\t"
    "v_mfma_f32_16x16x32_bf16 v[52:55], a[24:27], a[112:115], v[52:55]\n\t"
    "v_mfma_f32_16x16x32_bf16 v[52:55], a[28:31], a[136:139], v[52:55]\n\t"
    "v_mfma_f32_16x16x32_bf16 v[60:63], a[28:31], a[140:143], v[60:63]\n\t"
    "v_mfma_f32_16x16x32_bf16 v[60:63], a[24:27], a[116:119], v[60:63]\n\t"
    "buffer_load_dwordx4 v211, s[40:43], s61 offen offset:3072 lds\n\t"
    "s_xor_b32 s61, s61, 128\n\t"
    "v_mfma_f32_16x16x32_bf16 v[68:71], a[24:27], a[120:123], v[68:71]\n\t"
    "v_mfma_f32_16x16x32_bf16 v[68:71], a[28:31], a[144:147], v[68:71]\n\t"
    "v_mfma_f32_16x16x32_bf16 v[76:79], a[28:31], a[148:151], v[76:79]\n\t"
    "v_mfma_f32_16x16x32_bf16 v[76:79], a[24:27], a[124:127], v[76:79]\n\t"
    "v_mfma_f32_16x16x32_bf16 v[84:87], a[24:27], a[128:131], v[84:87]\n\t"
    "v_mfma_f32_16x16x32_bf16 v[84:87], a[28:31], a[152:155], v[84:87]\n\t"
    "v_mfma_f32_16x16x32_bf16 v[92:95], a[28:31], a[156:159], v[92:95]\n\t"
    "v_mfma_f32_16x16x32_bf16 v[92:95], a[24:27], a[132:135], v[92:95]\n\t"
    "s_waitcnt vmcnt(15) lgkmcnt(0)\n\t"
    "s_barrier\n\t"
    "v_mfma_f32_16x16x32_bf16 v[96:99], a[32:35], a[112:115], v[96:99]\n\t"
    "ds_read_b64_tr_b16 a[0:1], v192\n\t"
    "s_add_u32 m0, s58, 35840\n\t"
    "v_mfma_f32_16x16x32_bf16 v[96:99], a[36:39], a[136:139], v[96:99]\n\t"
    "buffer_load_dwordx4 v228, s[48:51], s18 offen offset:1024 nt lds\n\t"
    "v_mfma_f32_16x16x32_bf16 v[104:107], a[36:39], a[140:143], v[104:107]\n\t"
    "ds_read_b64_tr_b16 a[2:3], v192 offset:1024\n\t"
    "s_add_u32 m0, s58, 36608\n\t"
    "v_mfma_f32_16x16x32_bf16 v[104:107], a[32:35], a[116:119], v[104:107]\n\t"
    "buffer_load_dwordx4 v238, s[48:51], s18 offen offset:1280 nt lds\n\t"
    "v_mfma_f32_16x16x32_bf16 v[112:115], a[32:35], a[120:123], v[112:115]\n\t"
    "ds_read_b64_tr_b16 a[4:5], v192 offset:2048\n\t"
    "s_add_u32 m0, s58, 37376\n\t"
    "v_mfma_f32_16x16x32_bf16 v[112:115], a[36:39], a[144:147], v[112:115]\n\t"
    "buffer_load_dwordx4 v228, s[48:51], s18 offen offset:1536 nt lds\n\t"
    "v_mfma_f32_16x16x32_bf16 v[120:123], a[36:39], a[148:151], v[120:123]\n\t"
    "ds_read_b64_tr_b16 a[6:7], v192 offset:3072\n\t"
    "s_add_u32 m0, s58, 38144\n\t"
    "v_mfma_f32_16x16x32_bf16 v[120:123], a[32:35], a[124:127], v[120:123]\n\t"
    "buffer_load_dwordx4 v238, s[48:51], s18 offen offset:1792 nt lds\n\t"
    "s_or_b32 s18, s18, 12288\n\t"
    "s_add_u32 s18, s18, 2048\n\t"
    "s_and_b32 s18, s18, 116736\n\t"
    "v_mfma_f32_16x16x32_bf16 v[128:131], a[32:35], a[128:131], v[128:131]\n\t"
    "ds_read_b64_tr_b16 a[8:9], v193\n\t"
    "v_mfma_f32_16x16x32_bf16 v[128:131], a[36:39], a[152:155], v[128:131]\n\t"
    "ds_read_b128 a[64:67], v206\n\t"
    "v_mfma_f32_16x16x32_bf16 v[136:139], a[36:39], a[156:159], v[136:139]\n\t"
    "ds_read_b64_tr_b16 a[10:11], v193 offset:1024\n\t"
    "v_mfma_f32_16x16x32_bf16 v[136:139], a[32:35], a[132:135], v[136:139]\n\t"
    "ds_read_b128 a[88:91], v207\n\t"
    "v_mfma_f32_16x16x32_bf16 v[100:103], a[40:43], a[112:115], v[100:103]\n\t"
    "ds_read_b64_tr_b16 a[12:13], v193 offset:2048\n\t"
    "v_mfma_f32_16x16x32_bf16 v[100:103], a[44:47], a[136:139], v[100:103]\n\t"
    "ds_read_b128 a[68:71], v206 offset:2048\n\t"
    "v_mfma_f32_16x16x32_bf16 v[108:111], a[44:47], a[140:143], v[108:111]\n\t"
    "ds_read_b64_tr_b16 a[14:15], v193 offset:3072\n\t"
    "v_mfma_f32_16x16x32_bf16 v[108:111], a[40:43], a[116:119], v[108:111]\n\t"
    "ds_read_b128 a[92:95], v207 offset:2048\n\t"
    "v_mfma_f32_16x16x32_bf16 v[116:119], a[40:43], a[120:123], v[116:119]\n\t"
    "ds_read_b64_tr_b16 a[16:17], v194\n\t"
    "v_mfma_f32_16x16x32_bf16 v[116:119], a[44:47], a[144:147], v[116:119]\n\t"
    "ds_read_b128 a[72:75], v206 offset:4096\n\t"
    "v_mfma_f32_16x16x32_bf16 v[124:127], a[44:47], a[148:151], v[124:127]\n\t"
    "ds_read_b64_tr_b16 a[18:19], v194 offset:1024\n\t"
    "v_mfma_f32_16x16x32_bf16 v[124:127], a[40:43], a[124:127], v[124:127]\n\t"
    "ds_read_b128 a[96:99], v207 offset:4096\n\t"
    "v_mfma_f32_16x16x32_bf16 v[132:135], a[40:43], a[128:131], v[132:135]\n\t"
    "ds_read_b64_tr_b16 a[20:21], v194 offset:2048\n\t"
    "v_mfma_f32_16x16x32_bf16 v[132:135], a[44:47], a[152:155], v[132:135]\n\t"
    "ds_read_b128 a[76:79], v206 offset:6144\n\t"
    "v_mfma_f32_16x16x32_bf16 v[140:143], a[44:47], a[156:159], v[140:143]\n\t"
    "ds_read_b64_tr_b16 a[22:23], v194 offset:3072\n\t"
    "v_mfma_f32_16x16x32_bf16 v[140:143], a[40:43], a[132:135], v[140:143]\n\t"
    "ds_read_b128 a[100:103], v207 offset:6144\n\t"
    "v_mfma_f32_16x16x32_bf16 v[144:147], a[48:51], a[112:115], v[144:147]\n\t"
    "ds_read_b64_tr_b16 a[24:25], v195\n\t"
    "v_mfma_f32_16x16x32_bf16 v[144:147], a[52:55], a[136:139], v[144:147]\n\t"
    "ds_read_b128 a[80:83], v206 offset:8192\n\t"
    "v_mfma_f32_16x16x32_bf16 v[152:155], a[52:55], a[140:143], v[152:155]\n\t"
    "ds_read_b64_tr_b16 a[26:27], v195 offset:1024\n\t"
    "v_mfma_f32_16x16x32_bf16 v[152:155], a[48:51], a[116:119], v[152:155]\n\t"
    "ds_read_b128 a[104:107], v207 offset:8192\n\t"
    "v_mfma_f32_16x16x32_bf16 v[160:163], a[48:51], a[120:123], v[160:163]\n\t"
    "ds_read_b64_tr_b16 a[28:29], v195 offset:2048\n\t"
    "v_mfma_f32_16x16x32_bf16 v[160:163], a[52:55], a[144:147], v[160:163]\n\t"
    "ds_read_b128 a[84:87], v206 offset:10240\n\t"
    "v_mfma_f32_16x16x32_bf16 v[168:171], a[52:55], a[148:151], v[168:171]\n\t"
    "ds_read_b64_tr_b16 a[30:31], v195 offset:3072\n\t"
    "v_mfma_f32_16x16x32_bf16 v[168:171], a[48:51], a[124:127], v[168:171]\n\t"
    "ds_read_b128 a[108:111], v207 offset:10240\n\t"
    "v_mfma_f32_16x16x32_bf16 v[176:179], a[48:51], a[128:131], v[176:179]\n\t"
    "s_add_u32 m0, s56, 49152\n\t"
    "v_mfma_f32_16x16x32_bf16 v[176:179], a[52:55], a[152:155], v[176:179]\n\t"
    "buffer_load_dwordx4 v208, s[40:43], s61 offen lds\n\t"
    "v_mfma_f32_16x16x32_bf16 v[184:187], a[52:55], a[156:159], v[184:187]\n\t"
    "v_mfma_f32_16x16x32_bf16 v[184:187], a[48:51], a[132:135], v[184:187]\n\t"
    "buffer_load_dwordx4 v209, s[40:43], s61 offen offset:1024 lds\n\t"
    "v_mfma_f32_16x16x32_bf16 v[148:151], a[56:59], a[112:115], v[148:151]\n\t"
    "v_mfma_f32_16x16x32_bf16 v[148:151], a[60:63], a[136:139], v[148:151]\n\t"
    "buffer_load_dwordx4 v210, s[40:43], s61 offen offset:2048 lds\n\t"
    "v_mfma_f32_16x16x32_bf16 v[156:159], a[60:63], a[140:143], v[156:159]\n\t"
    "v_mfma_f32_16x16x32_bf16 v[156:159], a[56:59], a[116:119], v[156:159]\n\t"
    "buffer_load_dwordx4 v211, s[40:43], s61 offen offset:3072 lds\n\t"
    "s_xor_b32 s61, s61, 1572992\n\t"
    "v_mfma_f32_16x16x32_bf16 v[164:167], a[56:59], a[120:123], v[164:167]\n\t"
    "v_mfma_f32_16x16x32_bf16 v[164:167], a[60:63], a[144:147], v[164:167]\n\t"
    "v_mfma_f32_16x16x32_bf16 v[172:175], a[60:63], a[148:151], v[172:175]\n\t"
    "v_mfma_f32_16x16x32_bf16 v[172:175], a[56:59], a[124:127], v[172:175]\n\t"
    "v_mfma_f32_16x16x32_bf16 v[180:183], a[56:59], a[128:131], v[180:183]\n\t"
    "v_mfma_f32_16x16x32_bf16 v[180:183], a[60:63], a[152:155], v[180:183]\n\t"
    "v_mfma_f32_16x16x32_bf16 v[188:191], a[60:63], a[156:159], v[188:191]\n\t"
    "v_mfma_f32_16x16x32_bf16 v[188:191], a[56:59], a[132:135], v[188:191]\n\t"
    "s_waitcnt vmcnt(18) lgkmcnt(0)\n\t"
    "s_barrier\n\t"
    "v_mfma_f32_16x16x32_bf16 v[0:3], a[0:3], a[64:67], v[0:3]\n\t"
    "ds_read_b64_tr_b16 a[32:33], v192 offset:16384\n\t"
    "v_mfma_f32_16x16x32_bf16 v[0:3], a[4:7], a[88:91], v[0:3]\n\t"
    "v_mfma_f32_16x16x32_bf16 v[8:11], a[4:7], a[92:95], v[8:11]\n\t"
    "ds_read_b64_tr_b16 a[34:35], v192 offset:17408\n\t"
    "s_add_u32 m0, s57, 0\n\t"
    "s_nop 0\n\t"
    "v_mfma_f32_16x16x32_bf16 v[8:11], a[0:3], a[68:71], v[8:11]\n\t"
    "buffer_load_dwordx4 v200, s[36:39], s60 offen lds\n\t"
    "v_mfma_f32_16x16x32_bf16 v[16:19], a[0:3], a[72:75], v[16:19]\n\t"
    "ds_read_b64_tr_b16 a[36:37], v192 offset:18432\n\t"
    "v_mfma_f32_16x16x32_bf16 v[16:19], a[4:7], a[96:99], v[16:19]\n\t"
    "v_mfma_f32_16x16x32_bf16 v[24:27], a[4:7], a[100:103], v[24:27]\n\t"
    "ds_read_b64_tr_b16 a[38:39], v192 offset:19456\n\t"
    "v_mfma_f32_16x16x32_bf16 v[24:27], a[0:3], a[76:79], v[24:27]\n\t"
    "buffer_load_dwordx4 v201, s[36:39], s60 offen offset:1024 lds\n\t"
    "v_mfma_f32_16x16x32_bf16 v[32:35], a[0:3], a[80:83], v[32:35]\n\t"
    "ds_read_b64_tr_b16 a[40:41], v193 offset:16384\n\t"
    "v_mfma_f32_16x16x32_bf16 v[32:35], a[4:7], a[104:107], v[32:35]\n\t"
    "v_mfma_f32_16x16x32_bf16 v[40:43], a[4:7], a[108:111], v[40:43]\n\t"
    "ds_read_b64_tr_b16 a[42:43], v193 offset:17408\n\t"
    "v_mfma_f32_16x16x32_bf16 v[40:43], a[0:3], a[84:87], v[40:43]\n\t"
    "buffer_load_dwordx4 v202, s[36:39], s60 offen offset:2048 lds\n\t"
    "v_mfma_f32_16x16x32_bf16 v[4:7], a[8:11], a[64:67], v[4:7]\n\t"
    "ds_read_b64_tr_b16 a[44:45], v193 offset:18432\n\t"
    "v_mfma_f32_16x16x32_bf16 v[4:7], a[12:15], a[88:91], v[4:7]\n\t"
    "v_mfma_f32_16x16x32_bf16 v[12:15], a[12:15], a[92:95], v[12:15]\n\t"
    "ds_read_b64_tr_b16 a[46:47], v193 offset:19456\n\t"
    "v_mfma_f32_16x16x32_bf16 v[12:15], a[8:11], a[68:71], v[12:15]\n\t"
    "buffer_load_dwordx4 v203, s[36:39], s60 offen offset:3072 lds\n\t"
    "v_mfma_f32_16x16x32_bf16 v[20:23], a[8:11], a[72:75], v[20:23]\n\t"
    "ds_read_b64_tr_b16 a[48:49], v194 offset:16384\n\t"
    "v_mfma_f32_16x16x32_bf16 v[20:23], a[12:15], a[96:99], v[20:23]\n\t"
    "v_mfma_f32_16x16x32_bf16 v[28:31], a[12:15], a[100:103], v[28:31]\n\t"
    "ds_read_b64_tr_b16 a[50:51], v194 offset:17408\n\t"
    "s_add_u32 m0, s57, 4096\n\t"
    "v_mfma_f32_16x16x32_bf16 v[28:31], a[8:11], a[76:79], v[28:31]\n\t"
    "buffer_load_dwordx4 v204, s[36:39], s60 offen lds\n\t"
    "v_mfma_f32_16x16x32_bf16 v[36:39], a[8:11], a[80:83], v[36:39]\n\t"
    "ds_read_b64_tr_b16 a[52:53], v194 offset:18432\n\t"
    "v_mfma_f32_16x16x32_bf16 v[36:39], a[12:15], a[104:107], v[36:39]\n\t"
    "v_mfma_f32_16x16x32_bf16 v[44:47], a[12:15], a[108:111], v[44:47]\n\t"
    "ds_read_b64_tr_b16 a[54:55], v194 offset:19456\n\t"
    "v_mfma_f32_16x16x32_bf16 v[44:47], a[8:11], a[84:87], v[44:47]\n\t"
    "buffer_load_dwordx4 v205, s[36:39], s60 offen offset:1024 lds\n\t"
    "s_xor_b32 s60, s60, 128\n\t"
    "v_mfma_f32_16x16x32_bf16 v[48:51], a[16:19], a[64:67], v[48:51]\n\t"
    "ds_read_b64_tr_b16 a[56:57], v195 offset:16384\n\t"
    "v_mfma_f32_16x16x32_bf16 v[48:51], a[20:23], a[88:91], v[48:51]\n\t"
    "v_mfma_f32_16x16x32_bf16 v[56:59], a[20:23], a[92:95], v[56:59]\n\t"
    "ds_read_b64_tr_b16 a[58:59], v195 offset:17408\n\t"
    "s_add_u32 m0, s56, 0\n\t"
    "s_nop 0\n\t"
    "v_mfma_f32_16x16x32_bf16 v[56:59], a[16:19], a[68:71], v[56:59]\n\t"
    "buffer_load_dwordx4 v208, s[40:43], s61 offen lds\n\t"
    "v_mfma_f32_16x16x32_bf16 v[64:67], a[16:19], a[72:75], v[64:67]\n\t"
    "ds_read_b64_tr_b16 a[60:61], v195 offset:18432\n\t"
    "v_mfma_f32_16x16x32_bf16 v[64:67], a[20:23], a[96:99], v[64:67]\n\t"
    "v_mfma_f32_16x16x32_bf16 v[72:75], a[20:23], a[100:103], v[72:75]\n\t"
    "ds_read_b64_tr_b16 a[62:63], v195 offset:19456\n\t"
    "v_mfma_f32_16x16x32_bf16 v[72:75], a[16:19], a[76:79], v[72:75]\n\t"
    "buffer_load_dwordx4 v209, s[40:43], s61 offen offset:1024 lds\n\t"
    "v_mfma_f32_16x16x32_bf16 v[80:83], a[16:19], a[80:83], v[80:83]\n\t"
    "v_mfma_f32_16x16x32_bf16 v[80:83], a[20:23], a[104:107], v[80:83]\n\t"
    "v_mfma_f32_16x16x32_bf16 v[88:91], a[20:23], a[108:111], v[88:91]\n\t"
    "v_mfma_f32_16x16x32_bf16 v[88:91], a[16:19], a[84:87], v[88:91]\n\t"
    "buffer_load_dwordx4 v210, s[40:43], s61 offen offset:2048 lds\n\t"
    "v_mfma_f32_16x16x32_bf16 v[52:55], a[24:27], a[64:67], v[52:55]\n\t"
    "v_mfma_f32_16x16x32_bf16 v[52:55], a[28:31], a[88:91], v[52:55]\n\t"
    "v_mfma_f32_16x16x32_bf16 v[60:63], a[28:31], a[92:95], v[60:63]\n\t"
    "v_mfma_f32_16x16x32_bf16 v[60:63], a[24:27], a[68:71], v[60:63]\n\t"
    "buffer_load_dwordx4 v211, s[40:43], s61 offen offset:3072 lds\n\t"
    "s_xor_b32 s61, s61, 128\n\t"
    "v_mfma_f32_16x16x32_bf16 v[68:71], a[24:27], a[72:75], v[68:71]\n\t"
    "v_mfma_f32_16x16x32_bf16 v[68:71], a[28:31], a[96:99], v[68:71]\n\t"
    "v_mfma_f32_16x16x32_bf16 v[76:79], a[28:31], a[100:103], v[76:79]\n\t"
    "v_mfma_f32_16x16x32_bf16 v[76:79], a[24:27], a[76:79], v[76:79]\n\t"
    "v_mfma_f32_16x16x32_bf16 v[84:87], a[24:27], a[80:83], v[84:87]\n\t"
    "v_mfma_f32_16x16x32_bf16 v[84:87], a[28:31], a[104:107], v[84:87]\n\t"
    "v_mfma_f32_16x16x32_bf16 v[92:95], a[28:31], a[108:111], v[92:95]\n\t"
    "v_mfma_f32_16x16x32_bf16 v[92:95], a[24:27], a[84:87], v[92:95]\n\t"
    "s_waitcnt vmcnt(18) lgkmcnt(0)\n\t"
    "s_barrier\n\t"
    "v_mfma_f32_16x16x32_bf16 v[96:99], a[32:35], a[64:67], v[96:99]\n\t"
    "ds_read_b64_tr_b16 a[0:1], v192 offset:32768\n\t"
    "v_mfma_f32_16x16x32_bf16 v[96:99], a[36:39], a[88:91], v[96:99]\n\t"
    "ds_read_b128 a[112:115], v206 offset:24576\n\t"
    "v_mfma_f32_16x16x32_bf16 v[104:107], a[36:39], a[92:95], v[104:107]\n\t"
    "ds_read_b64_tr_b16 a[2:3], v192 offset:33792\n\t"
    "v_mfma_f32_16x16x32_bf16 v[104:107], a[32:35], a[68:71], v[104:107]\n\t"
    "ds_read_b128 a[136:139], v207 offset:24576\n\t"
    "v_mfma_f32_16x16x32_bf16 v[112:115], a[32:35], a[72:75], v[112:115]\n\t"
    "ds_read_b64_tr_b16 a[4:5], v192 offset:34816\n\t"
    "v_mfma_f32_16x16x32_bf16 v[112:115], a[36:39], a[96:99], v[112:115]\n\t"
    "ds_read_b128 a[116:119], v206 offset:26624\n\t"
    "v_mfma_f32_16x16x32_bf16 v[120:123], a[36:39], a[100:103], v[120:123]\n\t"
    "ds_read_b64_tr_b16 a[6:7], v192 offset:35840\n\t"
    "v_mfma_f32_16x16x32_bf16 v[120:123], a[32:35], a[76:79], v[120:123]\n\t"
    "ds_read_b128 a[140:143], v207 offset:26624\n\t"
    "v_mfma_f32_16x16x32_bf16 v[128:131], a[32:35], a[80:83], v[128:131]\n\t"
    "ds_read_b64_tr_b16 a[8:9], v193 offset:32768\n\t"
    "v_mfma_f32_16x16x32_bf16 v[128:131], a[36:39], a[104:107], v[128:131]\n\t"
    "ds_read_b128 a[120:123], v206 offset:28672\n\t"
    "v_mfma_f32_16x16x32_bf16 v[136:139], a[36:39], a[108:111], v[136:139]\n\t"
    "ds_read_b64_tr_b16 a[10:11], v193 offset:33792\n\t"
    "v_mfma_f32_16x16x32_bf16 v[136:139], a[32:35], a[84:87], v[136:139]\n\t"
    "ds_read_b128 a[144:147], v207 offset:28672\n\t"
    "v_mfma_f32_16x16x32_bf16 v[100:103], a[40:43], a[64:67], v[100:103]\n\t"
    "ds_read_b64_tr_b16 a[12:13], v193 offset:34816\n\t"
    "v_mfma_f32_16x16x32_bf16 v[100:103], a[44:47], a[88:91], v[100:103]\n\t"
    "ds_read_b128 a[124:127], v206 offset:30720\n\t"
    "v_mfma_f32_16x16x32_bf16 v[108:111], a[44:47], a[92:95], v[108:111]\n\t"
    "ds_read_b64_tr_b16 a[14:15], v193 offset:35840\n\t"
    "v_mfma_f32_16x16x32_bf16 v[108:111], a[40:43], a[68:71], v[108:111]\n\t"
    "ds_read_b128 a[148:151], v207 offset:30720\n\t"
    "v_mfma_f32_16x16x32_bf16 v[116:119], a[40:43], a[72:75], v[116:119]\n\t"
    "ds_read_b64_tr_b16 a[16:17], v194 offset:32768\n\t"
    "v_mfma_f32_16x16x32_bf16 v[116:119], a[44:47], a[96:99], v[116:119]\n\t"
    "ds_read_b128 a[128:131], v206 offset:32768\n\t"
    "v_mfma_f32_16x16x32_bf16 v[124:127], a[44:47], a[100:103], v[124:127]\n\t"
    "ds_read_b64_tr_b16 a[18:19], v194 offset:33792\n\t"
    "v_mfma_f32_16x16x32_bf16 v[124:127], a[40:43], a[76:79], v[124:127]\n\t"
    "ds_read_b128 a[152:155], v207 offset:32768\n\t"
    "v_mfma_f32_16x16x32_bf16 v[132:135], a[40:43], a[80:83], v[132:135]\n\t"
    "ds_read_b64_tr_b16 a[20:21], v194 offset:34816\n\t"
    "v_mfma_f32_16x16x32_bf16 v[132:135], a[44:47], a[104:107], v[132:135]\n\t"
    "ds_read_b128 a[132:135], v206 offset:34816\n\t"
    "v_mfma_f32_16x16x32_bf16 v[140:143], a[44:47], a[108:111], v[140:143]\n\t"
    "ds_read_b64_tr_b16 a[22:23], v194 offset:35840\n\t"
    "v_mfma_f32_16x16x32_bf16 v[140:143], a[40:43], a[84:87], v[140:143]\n\t"
    "ds_read_b128 a[156:159], v207 offset:34816\n\t"
    "v_mfma_f32_16x16x32_bf16 v[144:147], a[48:51], a[64:67], v[144:147]\n\t"
    "ds_read_b64_tr_b16 a[24:25], v195 offset:32768\n\t"
    "v_mfma_f32_16x16x32_bf16 v[144:147], a[52:55], a[88:91], v[144:147]\n\t"
    "v_mfma_f32_16x16x32_bf16 v[152:155], a[52:55], a[92:95], v[152:155]\n\t"
    "ds_read_b64_tr_b16 a[26:27], v195 offset:33792\n\t"
    "s_add_u32 m0, s56, 16384\n\t"
    "v_mfma_f32_16x16x32_bf16 v[152:155], a[48:51], a[68:71], v[152:155]\n\t"
    "buffer_load_dwordx4 v208, s[40:43], s61 offen lds\n\t"
    "v_mfma_f32_16x16x32_bf16 v[160:163], a[48:51], a[72:75], v[160:163]\n\t"
    "ds_read_b64_tr_b16 a[28:29], v195 offset:34816\n\t"
    "v_mfma_f32_16x16x32_bf16 v[160:163], a[52:55], a[96:99], v[160:163]\n\t"
    "v_mfma_f32_16x16x32_bf16 v[168:171], a[52:55], a[100:103], v[168:171]\n\t"
    "ds_read_b64_tr_b16 a[30:31], v195 offset:35840\n\t"
    "v_mfma_f32_16x16x32_bf16 v[168:171], a[48:51], a[76:79], v[168:171]\n\t"
    "buffer_load_dwordx4 v209, s[40:43], s61 offen offset:1024 lds\n\t"
    "v_mfma_f32_16x16x32_bf16 v[176:179], a[48:51], a[80:83], v[176:179]\n\t"
    "v_mfma_f32_16x16x32_bf16 v[176:179], a[52:55], a[104:107], v[176:179]\n\t"
    "v_mfma_f32_16x16x32_bf16 v[184:187], a[52:55], a[108:111], v[184:187]\n\t"
    "v_add_u32 v216, 131072, v216\n\t"
    "v_mfma_f32_16x16x32_bf16 v[184:187], a[48:51], a[84:87], v[184:187]\n\t"
    "buffer_load_dwordx4 v210, s[40:43], s61 offen offset:2048 lds\n\t"
    "v_add_u32 v217, 131072, v217\n\t"
    "v_mfma_f32_16x16x32_bf16 v[148:151], a[56:59], a[64:67], v[148:151]\n\t"
    "v_add_u32 v218, 131072, v218\n\t"
    "v_mfma_f32_16x16x32_bf16 v[148:151], a[60:63], a[88:91], v[148:151]\n\t"
    "v_add_u32 v219, 131072, v219\n\t"
    "v_mfma_f32_16x16x32_bf16 v[156:159], a[60:63], a[92:95], v[156:159]\n\t"
    "v_add_u32 v220, 131072, v220\n\t"
    "v_mfma_f32_16x16x32_bf16 v[156:159], a[56:59], a[68:71], v[156:159]\n\t"
    "buffer_load_dwordx4 v211, s[40:43], s61 offen offset:3072 lds\n\t"
    "s_xor_b32 s61, s61, 524416\n\t"
    "v_add_u32 v221, 131072, v221\n\t"
    "v_mfma_f32_16x16x32_bf16 v[164:167], a[56:59], a[72:75], v[164:167]\n\t"
    "v_add_u32 v222, 131072, v222\n\t"
    "v_mfma_f32_16x16x32_bf16 v[164:167], a[60:63], a[96:99], v[164:167]\n\t"
    "buffer_load_dword v254, v230, s[68:71], 0 offen sc1\n\t"
    "v_add_u32 v223, 131072, v223\n\t"
    "v_mfma_f32_16x16x32_bf16 v[172:175], a[60:63], a[100:103], v[172:175]\n\t"
    "v_mfma_f32_16x16x32_bf16 v[172:175], a[56:59], a[76:79], v[172:175]\n\t"
    "v_mfma_f32_16x16x32_bf16 v[180:183], a[56:59], a[80:83], v[180:183]\n\t"
    "v_mfma_f32_16x16x32_bf16 v[180:183], a[60:63], a[104:107], v[180:183]\n\t"
    "v_mfma_f32_16x16x32_bf16 v[188:191], a[60:63], a[108:111], v[188:191]\n\t"
    "v_mfma_f32_16x16x32_bf16 v[188:191], a[56:59], a[84:87], v[188:191]\n\t"
    "s_waitcnt vmcnt(15) lgkmcnt(0)\n\t"
    "s_barrier\n\t"
    "v_mfma_f32_16x16x32_bf16 v[0:3], a[0:3], a[112:115], v[0:3]\n\t"
    "ds_read_b64_tr_b16 a[32:33], v192 offset:49152\n\t"
    "v_mfma_f32_16x16x32_bf16 v[0:3], a[4:7], a[136:139], v[0:3]\n\t"
    "ds_read_b64_tr_b16 a[224:225], v216\n\t"
    "v_mfma_f32_16x16x32_bf16 v[8:11], a[4:7], a[140:143], v[8:11]\n\t"
    "ds_read_b64_tr_b16 a[34:35], v192 offset:50176\n\t"
    "s_add_u32 m0, s57, 24576\n\t"
    "v_mfma_f32_16x16x32_bf16 v[8:11], a[0:3], a[116:119], v[8:11]\n\t"
    "buffer_load_dwordx4 v200, s[36:39], s60 offen lds\n\t"
    "v_mfma_f32_16x16x32_bf16 v[16:19], a[0:3], a[120:123], v[16:19]\n\t"
    "ds_read_b64_tr_b16 a[36:37], v192 offset:51200\n\t"
    "v_mfma_f32_16x16x32_bf16 v[16:19], a[4:7], a[144:147], v[16:19]\n\t"
    "ds_read_b64_tr_b16 a[226:227], v216 offset:4096\n\t"
    "v_mfma_f32_16x16x32_bf16 v[24:27], a[4:7], a[148:151], v[24:27]\n\t"
    "ds_read_b64_tr_b16 a[38:39], v192 offset:52224\n\t"
    "v_mfma_f32_16x16x32_bf16 v[24:27], a[0:3], a[124:127], v[24:27]\n\t"
    "buffer_load_dwordx4 v201, s[36:39], s60 offen offset:1024 lds\n\t"
    "v_mfma_f32_16x16x32_bf16 v[32:35], a[0:3], a[128:131], v[32:35]\n\t"
    "ds_read_b64_tr_b16 a[40:41], v193 offset:49152\n\t"
    "v_mfma_f32_16x16x32_bf16 v[32:35], a[4:7], a[152:155], v[32:35]\n\t"
    "ds_read_b64_tr_b16 a[228:229], v217\n\t"
    "v_mfma_f32_16x16x32_bf16 v[40:43], a[4:7], a[156:159], v[40:43]\n\t"
    "ds_read_b64_tr_b16 a[42:43], v193 offset:50176\n\t"
    "v_mfma_f32_16x16x32_bf16 v[40:43], a[0:3], a[132:135], v[40:43]\n\t"
    "buffer_load_dwordx4 v202, s[36:39], s60 offen offset:2048 lds\n\t"
    "v_mfma_f32_16x16x32_bf16 v[4:7], a[8:11], a[112:115], v[4:7]\n\t"
    "ds_read_b64_tr_b16 a[44:45], v193 offset:51200\n\t"
    "v_mfma_f32_16x16x32_bf16 v[4:7], a[12:15], a[136:139], v[4:7]\n\t"
    "ds_read_b64_tr_b16 a[230:231], v217 offset:4096\n\t"
    "v_mfma_f32_16x16x32_bf16 v[12:15], a[12:15], a[140:143], v[12:15]\n\t"
    "ds_read_b64_tr_b16 a[46:47], v193 offset:52224\n\t"
    "v_mfma_f32_16x16x32_bf16 v[12:15], a[8:11], a[116:119], v[12:15]\n\t"
    "buffer_load_dwordx4 v203, s[36:39], s60 offen offset:3072 lds\n\t"
    "v_mfma_f32_16x16x32_bf16 v[20:23], a[8:11], a[120:123], v[20:23]\n\t"
    "ds_read_b64_tr_b16 a[48:49], v194 offset:49152\n\t"
    "v_mfma_f32_16x16x32_bf16 v[20:23], a[12:15], a[144:147], v[20:23]\n\t"
    "ds_read_b64_tr_b16 a[232:233], v218\n\t"
    "v_mfma_f32_16x16x32_bf16 v[28:31], a[12:15], a[148:151], v[28:31]\n\t"
    "ds_read_b64_tr_b16 a[50:51], v194 offset:50176\n\t"
    "s_add_u32 m0, s57, 28672\n\t"
    "v_mfma_f32_16x16x32_bf16 v[28:31], a[8:11], a[124:127], v[28:31]\n\t"
    "buffer_load_dwordx4 v204, s[36:39], s60 offen lds\n\t"
    "v_mfma_f32_16x16x32_bf16 v[36:39], a[8:11], a[128:131], v[36:39]\n\t"
    "ds_read_b64_tr_b16 a[52:53], v194 offset:51200\n\t"
    "v_mfma_f32_16x16x32_bf16 v[36:39], a[12:15], a[152:155], v[36:39]\n\t"
    "ds_read_b64_tr_b16 a[234:235], v218 offset:4096\n\t"
    "v_mfma_f32_16x16x32_bf16 v[44:47], a[12:15], a[156:159], v[44:47]\n\t"
    "ds_read_b64_tr_b16 a[54:55], v194 offset:52224\n\t"
    "v_mfma_f32_16x16x32_bf16 v[44:47], a[8:11], a[132:135], v[44:47]\n\t"
    "buffer_load_dwordx4 v205, s[36:39], s60 offen offset:1024 lds\n\t"
    "s_xor_b32 s60, s60, 3968\n\t"
    "v_mfma_f32_16x16x32_bf16 v[48:51], a[16:19], a[112:115], v[48:51]\n\t"
    "ds_read_b64_tr_b16 a[56:57], v195 offset:49152\n\t"
    "v_mfma_f32_16x16x32_bf16 v[48:51], a[20:23], a[136:139], v[48:51]\n\t"
    "ds_read_b64_tr_b16 a[236:237], v219\n\t"
    "v_mfma_f32_16x16x32_bf16 v[56:59], a[20:23], a[140:143], v[56:59]\n\t"
    "ds_read_b64_tr_b16 a[58:59], v195 offset:50176\n\t"
    "s_add_u32 m0, s56, 32768\n\t"
    "v_mfma_f32_16x16x32_bf16 v[56:59], a[16:19], a[116:119], v[56:59]\n\t"
    "buffer_load_dwordx4 v208, s[40:43], s61 offen lds\n\t"
    "v_mfma_f32_16x16x32_bf16 v[64:67], a[16:19], a[120:123], v[64:67]\n\t"
    "ds_read_b64_tr_b16 a[60:61], v195 offset:51200\n\t"
    "v_mfma_f32_16x16x32_bf16 v[64:67], a[20:23], a[144:147], v[64:67]\n\t"
    "ds_read_b64_tr_b16 a[238:239], v219 offset:4096\n\t"
    "v_mfma_f32_16x16x32_bf16 v[72:75], a[20:23], a[148:151], v[72:75]\n\t"
    "ds_read_b64_tr_b16 a[62:63], v195 offset:52224\n\t"
    "v_mfma_f32_16x16x32_bf16 v[72:75], a[16:19], a[124:127], v[72:75]\n\t"
    "buffer_load_dwordx4 v209, s[40:43], s61 offen offset:1024 lds\n\t"
    "v_mfma_f32_16x16x32_bf16 v[80:83], a[16:19], a[128:131], v[80:83]\n\t"
    "ds_read_b64_tr_b16 a[240:241], v220\n\t"
    "v_mfma_f32_16x16x32_bf16 v[80:83], a[20:23], a[152:155], v[80:83]\n\t"
    "ds_read_b64_tr_b16 a[242:243], v220 offset:4096\n\t"
    "v_mfma_f32_16x16x32_bf16 v[88:91], a[20:23], a[156:159], v[88:91]\n\t"
    "ds_read_b64_tr_b16 a[244:245], v221\n\t"
    "v_mfma_f32_16x16x32_bf16 v[88:91], a[16:19], a[132:135], v[88:91]\n\t"
    "buffer_load_dwordx4 v210, s[40:43], s61 offen offset:2048 lds\n\t"
    "v_mfma_f32_16x16x32_bf16 v[52:55], a[24:27], a[112:115], v[52:55]\n\t"
    "ds_read_b64_tr_b16 a[246:247], v221 offset:4096\n\t"
    "v_mfma_f32_16x16x32_bf16 v[52:55], a[28:31], a[136:139], v[52:55]\n\t"
    "ds_read_b64_tr_b16 a[248:249], v222\n\t"
    "v_mfma_f32_16x16x32_bf16 v[60:63], a[28:31], a[140:143], v[60:63]\n\t"
    "ds_read_b64_tr_b16 a[250:251], v222 offset:4096\n\t"
    "v_mfma_f32_16x16x32_bf16 v[60:63], a[24:27], a[116:119], v[60:63]\n\t"
    "buffer_load_dwordx4 v211, s[40:43], s61 offen offset:3072 lds\n\t"
    "s_xor_b32 s61, s61, 128\n\t"
    "v_mfma_f32_16x16x32_bf16 v[68:71], a[24:27], a[120:123], v[68:71]\n\t"
    "ds_read_b64_tr_b16 a[252:253], v223\n\t"
    "v_mfma_f32_16x16x32_bf16 v[68:71], a[28:31], a[144:147], v[68:71]\n\t"
    "ds_read_b64_tr_b16 a[254:255], v223 offset:4096\n\t"
    "v_mfma_f32_16x16x32_bf16 v[76:79], a[28:31], a[148:151], v[76:79]\n\t"
    "v_mfma_f32_16x16x32_bf16 v[76:79], a[24:27], a[124:127], v[76:79]\n\t"
    "v_mfma_f32_16x16x32_bf16 v[84:87], a[24:27], a[128:131], v[84:87]\n\t"
    "v_mfma_f32_16x16x32_bf16 v[84:87], a[28:31], a[152:155], v[84:87]\n\t"
    "v_mfma_f32_16x16x32_bf16 v[92:95], a[28:31], a[156:159], v[92:95]\n\t"
    "v_mfma_f32_16x16x32_bf16 v[92:95], a[24:27], a[132:135], v[92:95]\n\t"
    "s_waitcnt vmcnt(15) lgkmcnt(0)\n\t"
    "s_barrier\n\t"
    "v_mfma_f32_16x16x32_bf16 v[96:99], a[32:35], a[112:115], v[96:99]\n\t"
    "ds_read_b64_tr_b16 a[0:1], v192\n\t"
    "s_add_u32 m0, s58, 32768\n\t"
    "v_mfma_f32_16x16x32_bf16 v[96:99], a[36:39], a[136:139], v[96:99]\n\t"
    "buffer_load_dwordx4 v212, s[44:47], s62 offen nt lds\n\t"
    "v_mfma_f32_16x16x32_bf16 v[104:107], a[36:39], a[140:143], v[104:107]\n\t"
    "ds_read_b64_tr_b16 a[2:3], v192 offset:1024\n\t"
    "s_add_u32 m0, s58, 33536\n\t"
    "v_mfma_f32_16x16x32_bf16 v[104:107], a[32:35], a[116:119], v[104:107]\n\t"
    "buffer_load_dwordx4 v213, s[44:47], s62 offen offset:256 nt lds\n\t"
    "v_mfma_f32_16x16x32_bf16 v[112:115], a[32:35], a[120:123], v[112:115]\n\t"
    "ds_read_b64_tr_b16 a[4:5], v192 offset:2048\n\t"
    "s_add_u32 m0, s58, 34304\n\t"
    "v_mfma_f32_16x16x32_bf16 v[112:115], a[36:39], a[144:147], v[112:115]\n\t"
    "buffer_load_dwordx4 v214, s[44:47], s62 offen offset:512 nt lds\n\t"
    "v_mfma_f32_16x16x32_bf16 v[120:123], a[36:39], a[148:151], v[120:123]\n\t"
    "ds_read_b64_tr_b16 a[6:7], v192 offset:3072\n\t"
    "s_add_u32 m0, s58, 35072\n\t"
    "v_mfma_f32_16x16x32_bf16 v[120:123], a[32:35], a[124:127], v[120:123]\n\t"
    "buffer_load_dwordx4 v215, s[44:47], s62 offen offset:768 nt lds\n\t"
    "v_mfma_f32_16x16x32_bf16 v[128:131], a[32:35], a[128:131], v[128:131]\n\t"
    "ds_read_b64_tr_b16 a[8:9], v193\n\t"
    "v_mfma_f32_16x16x32_bf16 v[128:131], a[36:39], a[152:155], v[128:131]\n\t"
    "ds_read_b128 a[64:67], v206\n\t"
    "v_mfma_f32_16x16x32_bf16 v[136:139], a[36:39], a[156:159], v[136:139]\n\t"
    "ds_read_b64_tr_b16 a[10:11], v193 offset:1024\n\t"
    "v_mfma_f32_16x16x32_bf16 v[136:139], a[32:35], a[132:135], v[136:139]\n\t"
    "ds_read_b128 a[88:91], v207\n\t"
    "v_mfma_f32_16x16x32_bf16 v[100:103], a[40:43], a[112:115], v[100:103]\n\t"
    "ds_read_b64_tr_b16 a[12:13], v193 offset:2048\n\t"
    "v_mfma_f32_16x16x32_bf16 v[100:103], a[44:47], a[136:139], v[100:103]\n\t"
    "ds_read_b128 a[68:71], v206 offset:2048\n\t"
    "v_mfma_f32_16x16x32_bf16 v[108:111], a[44:47], a[140:143], v[108:111]\n\t"
    "ds_read_b64_tr_b16 a[14:15], v193 offset:3072\n\t"
    "v_mfma_f32_16x16x32_bf16 v[108:111], a[40:43], a[116:119], v[108:111]\n\t"
    "ds_read_b128 a[92:95], v207 offset:2048\n\t"
    "v_mfma_f32_16x16x32_bf16 v[116:119], a[40:43], a[120:123], v[116:119]\n\t"
    "ds_read_b64_tr_b16 a[16:17], v194\n\t"
    "v_mfma_f32_16x16x32_bf16 v[116:119], a[44:47], a[144:147], v[116:119]\n\t"
    "ds_read_b128 a[72:75], v206 offset:4096\n\t"
    "v_mfma_f32_16x16x32_bf16 v[124:127], a[44:47], a[148:151], v[124:127]\n\t"
    "ds_read_b64_tr_b16 a[18:19], v194 offset:1024\n\t"
    "v_mfma_f32_16x16x32_bf16 v[124:127], a[40:43], a[124:127], v[124:127]\n\t"
    "ds_read_b128 a[96:99], v207 offset:4096\n\t"
    "v_mfma_f32_16x16x32_bf16 v[132:135], a[40:43], a[128:131], v[132:135]\n\t"
    "ds_read_b64_tr_b16 a[20:21], v194 offset:2048\n\t"
    "v_mfma_f32_16x16x32_bf16 v[132:135], a[44:47], a[152:155], v[132:135]\n\t"
    "ds_read_b128 a[76:79], v206 offset:6144\n\t"
    "v_mfma_f32_16x16x32_bf16 v[140:143], a[44:47], a[156:159], v[140:143]\n\t"
    "ds_read_b64_tr_b16 a[22:23], v194 offset:3072\n\t"
    "v_mfma_f32_16x16x32_bf16 v[140:143], a[40:43], a[132:135], v[140:143]\n\t"
    "ds_read_b128 a[100:103], v207 offset:6144\n\t"
    "v_mfma_f32_16x16x32_bf16 v[144:147], a[48:51], a[112:115], v[144:147]\n\t"
    "ds_read_b64_tr_b16 a[24:25], v195\n\t"
    "v_mfma_f32_16x16x32_bf16 v[144:147], a[52:55], a[136:139], v[144:147]\n\t"
    "ds_read_b128 a[80:83], v206 offset:8192\n\t"
    "v_mfma_f32_16x16x32_bf16 v[152:155], a[52:55], a[140:143], v[152:155]\n\t"
    "ds_read_b64_tr_b16 a[26:27], v195 offset:1024\n\t"
    "v_mfma_f32_16x16x32_bf16 v[152:155], a[48:51], a[116:119], v[152:155]\n\t"
    "ds_read_b128 a[104:107], v207 offset:8192\n\t"
    "v_mfma_f32_16x16x32_bf16 v[160:163], a[48:51], a[120:123], v[160:163]\n\t"
    "ds_read_b64_tr_b16 a[28:29], v195 offset:2048\n\t"
    "v_mfma_f32_16x16x32_bf16 v[160:163], a[52:55], a[144:147], v[160:163]\n\t"
    "ds_read_b128 a[84:87], v206 offset:10240\n\t"
    "v_mfma_f32_16x16x32_bf16 v[168:171], a[52:55], a[148:151], v[168:171]\n\t"
    "ds_read_b64_tr_b16 a[30:31], v195 offset:3072\n\t"
    "v_mfma_f32_16x16x32_bf16 v[168:171], a[48:51], a[124:127], v[168:171]\n\t"
    "ds_read_b128 a[108:111], v207 offset:10240\n\t"
    "v_mfma_f32_16x16x32_bf16 v[176:179], a[48:51], a[128:131], v[176:179]\n\t"
    "s_add_u32 m0, s56, 49152\n\t"
    "v_mfma_f32_16x16x32_bf16 v[176:179], a[52:55], a[152:155], v[176:179]\n\t"
    "buffer_load_dwordx4 v208, s[40:43], s61 offen lds\n\t"
    "v_mfma_f32_16x16x32_bf16 v[184:187], a[52:55], a[156:159], v[184:187]\n\t"
    "v_subrev_u32 v216, 131072, v216\n\t"
    "v_mfma_f32_16x16x32_bf16 v[184:187], a[48:51], a[132:135], v[184:187]\n\t"
    "buffer_load_dwordx4 v209, s[40:43], s61 offen offset:1024 lds\n\t"
    "v_subrev_u32 v217, 131072, v217\n\t"
    "v_mfma_f32_16x16x32_bf16 v[148:151], a[56:59], a[112:115], v[148:151]\n\t"
    "v_subrev_u32 v218, 131072, v218\n\t"
    "v_mfma_f32_16x16x32_bf16 v[148:151], a[60:63], a[136:139], v[148:151]\n\t"
    "buffer_load_dwordx4 v210, s[40:43], s61 offen offset:2048 lds\n\t"
    "v_subrev_u32 v219, 131072, v219\n\t"
    "v_mfma_f32_16x16x32_bf16 v[156:159], a[60:63], a[140:143], v[156:159]\n\t"
    "v_subrev_u32 v220, 131072, v220\n\t"
    "v_mfma_f32_16x16x32_bf16 v[156:159], a[56:59], a[116:119], v[156:159]\n\t"
    "buffer_load_dwordx4 v211, s[40:43], s61 offen offset:3072 lds\n\t"
    "s_xor_b32 s61, s61, 16253056\n\t"
    "v_subrev_u32 v221, 131072, v221\n\t"
    "v_mfma_f32_16x16x32_bf16 v[164:167], a[56:59], a[120:123], v[164:167]\n\t"
    "v_subrev_u32 v222, 131072, v222\n\t"
    "v_mfma_f32_16x16x32_bf16 v[164:167], a[60:63], a[144:147], v[164:167]\n\t"
    "v_subrev_u32 v223, 131072, v223\n\t"
    "v_mfma_f32_16x16x32_bf16 v[172:175], a[60:63], a[148:151], v[172:175]\n\t"
    "v_mfma_f32_16x16x32_bf16 v[172:175], a[56:59], a[124:127], v[172:175]\n\t"
    "v_mfma_f32_16x16x32_bf16 v[180:183], a[56:59], a[128:131], v[180:183]\n\t"
    "v_mfma_f32_16x16x32_bf16 v[180:183], a[60:63], a[152:155], v[180:183]\n\t"
    "v_mfma_f32_16x16x32_bf16 v[188:191], a[60:63], a[156:159], v[188:191]\n\t"
    "v_mfma_f32_16x16x32_bf16 v[188:191], a[56:59], a[132:135], v[188:191]\n\t"
    "s_waitcnt vmcnt(19) lgkmcnt(0)\n\t"
    "s_barrier\n\t"
    "v_mfma_f32_16x16x32_bf16 v[0:3], a[0:3], a[64:67], v[0:3]\n\t"
    "ds_read_b64_tr_b16 a[32:33], v192 offset:16384\n\t"
    "s_add_u32 m0, s58, 35840\n\t"
    "v_mfma_f32_16x16x32_bf16 v[0:3], a[4:7], a[88:91], v[0:3]\n\t"
    "buffer_load_dwordx4 v212, s[44:47], s62 offen offset:1024 nt lds\n\t"
    "v_mfma_f32_16x16x32_bf16 v[8:11], a[4:7], a[92:95], v[8:11]\n\t"
    "ds_read_b64_tr_b16 a[34:35], v192 offset:17408\n\t"
    "s_add_u32 m0, s58, 36608\n\t"
    "v_mfma_f32_16x16x32_bf16 v[8:11], a[0:3], a[68:71], v[8:11]\n\t"
    "buffer_load_dwordx4 v213, s[44:47], s62 offen offset:1280 nt lds\n\t"
    "v_mfma_f32_16x16x32_bf16 v[16:19], a[0:3], a[72:75], v[16:19]\n\t"
    "ds_read_b64_tr_b16 a[36:37], v192 offset:18432\n\t"
    "s_add_u32 m0, s58, 37376\n\t"
    "v_mfma_f32_16x16x32_bf16 v[16:19], a[4:7], a[96:99], v[16:19]\n\t"
    "buffer_load_dwordx4 v214, s[44:47], s62 offen offset:1536 nt lds\n\t"
    "v_mfma_f32_16x16x32_bf16 v[24:27], a[4:7], a[100:103], v[24:27]\n\t"
    "ds_read_b64_tr_b16 a[38:39], v192 offset:19456\n\t"
    "s_add_u32 m0, s58, 38144\n\t"
    "v_mfma_f32_16x16x32_bf16 v[24:27], a[0:3], a[76:79], v[24:27]\n\t"
    "buffer_load_dwordx4 v215, s[44:47], s62 offen offset:1792 nt lds\n\t"
    "s_or_b32 s62, s62, 12288\n\t"
    "s_add_u32 s62, s62, 2048\n\t"
    "s_and_b32 s62, s62, 116736\n\t"
    "v_mfma_f32_16x16x32_bf16 v[32:35], a[0:3], a[80:83], v[32:35]\n\t"
    "ds_read_b64_tr_b16 a[40:41], v193 offset:16384\n\t"
    "v_mfma_f32_16x16x32_bf16 v[32:35], a[4:7], a[104:107], v[32:35]\n\t"
    "v_mfma_f32_16x16x32_bf16 v[40:43], a[4:7], a[108:111], v[40:43]\n\t"
    "ds_read_b64_tr_b16 a[42:43], v193 offset:17408\n\t"
    "v_mfma_f32_16x16x32_bf16 v[40:43], a[0:3], a[84:87], v[40:43]\n\t"
    "v_mfma_f32_16x16x32_bf16 v[4:7], a[8:11], a[64:67], v[4:7]\n\t"
    "ds_read_b64_tr_b16 a[44:45], v193 offset:18432\n\t"
    "v_mfma_f32_16x16x32_bf16 v[4:7], a[12:15], a[88:91], v[4:7]\n\t"
    "v_mfma_f32_16x16x32_bf16 v[12:15], a[12:15], a[92:95], v[12:15]\n\t"
    "ds_read_b64_tr_b16 a[46:47], v193 offset:19456\n\t"
    "v_mfma_f32_16x16x32_bf16 v[12:15], a[8:11], a[68:71], v[12:15]\n\t"
    "v_mfma_f32_16x16x32_bf16 v[20:23], a[8:11], a[72:75], v[20:23]\n\t"
    "ds_read_b64_tr_b16 a[48:49], v194 offset:16384\n\t"
    "v_mfma_f32_16x16x32_bf16 v[20:23], a[12:15], a[96:99], v[20:23]\n\t"
    "v_mfma_f32_16x16x32_bf16 v[28:31], a[12:15], a[100:103], v[28:31]\n\t"
    "ds_read_b64_tr_b16 a[50:51], v194 offset:17408\n\t"
    "v_mfma_f32_16x16x32_bf16 v[28:31], a[8:11], a[76:79], v[28:31]\n\t"
    "v_mfma_f32_16x16x32_bf16 v[36:39], a[8:11], a[80:83], v[36:39]\n\t"
    "ds_read_b64_tr_b16 a[52:53], v194 offset:18432\n\t"
    "v_mfma_f32_16x16x32_bf16 v[36:39], a[12:15], a[104:107], v[36:39]\n\t"
    "v_mfma_f32_16x16x32_bf16 v[44:47], a[12:15], a[108:111], v[44:47]\n\t"
    "ds_read_b64_tr_b16 a[54:55], v194 offset:19456\n\t"
    "v_mfma_f32_16x16x32_bf16 v[44:47], a[8:11], a[84:87], v[44:47]\n\t"
    "v_mfma_f32_16x16x32_bf16 v[48:51], a[16:19], a[64:67], v[48:51]\n\t"
    "ds_read_b64_tr_b16 a[56:57], v195 offset:16384\n\t"
    "v_mfma_f32_16x16x32_bf16 v[48:51], a[20:23], a[88:91], v[48:51]\n\t"
    "v_mfma_f32_16x16x32_bf16 v[56:59], a[20:23], a[92:95], v[56:59]\n\t"
    "ds_read_b64_tr_b16 a[58:59], v195 offset:17408\n\t"
    "v_mfma_f32_16x16x32_bf16 v[56:59], a[16:19], a[68:71], v[56:59]\n\t"
    "v_mfma_f32_16x16x32_bf16 v[64:67], a[16:19], a[72:75], v[64:67]\n\t"
    "ds_read_b64_tr_b16 a[60:61], v195 offset:18432\n\t"
    "v_mfma_f32_16x16x32_bf16 v[64:67], a[20:23], a[96:99], v[64:67]\n\t"
    "v_mfma_f32_16x16x32_bf16 v[72:75], a[20:23], a[100:103], v[72:75]\n\t"
    "ds_read_b64_tr_b16 a[62:63], v195 offset:19456\n\t"
    "v_mfma_f32_16x16x32_bf16 v[72:75], a[16:19], a[76:79], v[72:75]\n\t"
    "v_mfma_f32_16x16x32_bf16 v[80:83], a[16:19], a[80:83], v[80:83]\n\t"
    "v_mfma_f32_16x16x32_bf16 v[80:83], a[20:23], a[104:107], v[80:83]\n\t"
    "v_mfma_f32_16x16x32_bf16 v[88:91], a[20:23], a[108:111], v[88:91]\n\t"
    "v_mfma_f32_16x16x32_bf16 v[88:91], a[16:19], a[84:87], v[88:91]\n\t"
    "v_mfma_f32_16x16x32_bf16 v[52:55], a[24:27], a[64:67], v[52:55]\n\t"
    "v_mfma_f32_16x16x32_bf16 v[52:55], a[28:31], a[88:91], v[52:55]\n\t"
    "v_mfma_f32_16x16x32_bf16 v[60:63], a[28:31], a[92:95], v[60:63]\n\t"
    "v_mfma_f32_16x16x32_bf16 v[60:63], a[24:27], a[68:71], v[60:63]\n\t"
    "v_mfma_f32_16x16x32_bf16 v[68:71], a[24:27], a[72:75], v[68:71]\n\t"
    "s_mov_b32 s25, 33423360\n\t"
    "s_mov_b32 s30, 1\n\t"
    "s_mov_b32 s23, 0\n\t"
    "s_waitcnt vmcnt(22)\n\t"
    "v_sub_u32 v254, v254, v231\n\t"
    "v_cmp_gt_u32 vcc, s30, v254\n\t"
    "s_nop 1\n\t"
    "s_and_b32 s26, vcc_lo, s25\n\t"
    "s_cbranch_scc1 .Lk_spin\n\t"
    ".Lk_spinret_0:\n\t"
    "s_add_u32 s24, s78, 131072\n\t"
    "s_mov_b64 exec, 1\n\t"
    "s_nop 0\n\t"
    "buffer_atomic_add v247, off, s[68:71], s24 sc1\n\t"
    "s_mov_b64 exec, -1\n\t"
    "s_nop 0\n\t"
    "v_mfma_f32_16x16x32_bf16 v[68:71], a[28:31], a[96:99], v[68:71]\n\t"
    "v_mfma_f32_16x16x32_bf16 v[76:79], a[28:31], a[100:103], v[76:79]\n\t"
    "v_mfma_f32_16x16x32_bf16 v[76:79], a[24:27], a[76:79], v[76:79]\n\t"
    "v_mfma_f32_16x16x32_bf16 v[84:87], a[24:27], a[80:83], v[84:87]\n\t"
    "v_mfma_f32_16x16x32_bf16 v[84:87], a[28:31], a[104:107], v[84:87]\n\t"
    "v_mfma_f32_16x16x32_bf16 v[92:95], a[28:31], a[108:111], v[92:95]\n\t"
    "v_mfma_f32_16x16x32_bf16 v[92:95], a[24:27], a[84:87], v[92:95]\n\t"
    "s_waitcnt vmcnt(13) lgkmcnt(0)\n\t"
    "s_barrier\n\t"
    "v_mfma_f32_16x16x32_bf16 v[96:99], a[32:35], a[64:67], v[96:99]\n\t"
    "ds_read_b64_tr_b16 a[0:1], v192 offset:32768\n\t"
    "s_add_u32 m0, s59, 0\n\t"
    "s_nop 0\n\t"
    "v_mfma_f32_16x16x32_bf16 v[96:99], a[36:39], a[88:91], v[96:99]\n\t"
    "buffer_load_dwordx4 v228, s[48:51], s18 offen nt lds\n\t"
    "v_mfma_f32_16x16x32_bf16 v[104:107], a[36:39], a[92:95], v[104:107]\n\t"
    "ds_read_b64_tr_b16 a[2:3], v192 offset:33792\n\t"
    "s_add_u32 m0, s59, 768\n\t"
    "v_mfma_f32_16x16x32_bf16 v[104:107], a[32:35], a[68:71], v[104:107]\n\t"
    "buffer_load_dwordx4 v238, s[48:51], s18 offen offset:256 nt lds\n\t"
    "v_mfma_f32_16x16x32_bf16 v[112:115], a[32:35], a[72:75], v[112:115]\n\t"
    "ds_read_b64_tr_b16 a[4:5], v192 offset:34816\n\t"
    "s_add_u32 m0, s59, 1536\n\t"
    "v_mfma_f32_16x16x32_bf16 v[112:115], a[36:39], a[96:99], v[112:115]\n\t"
    "buffer_load_dwordx4 v228, s[48:51], s18 offen offset:512 nt lds\n\t"
    "v_mfma_f32_16x16x32_bf16 v[120:123], a[36:39], a[100:103], v[120:123]\n\t"
    "ds_read_b64_tr_b16 a[6:7], v192 offset:35840\n\t"
    "s_add_u32 m0, s59, 2304\n\t"
    "v_mfma_f32_16x16x32_bf16 v[120:123], a[32:35], a[76:79], v[120:123]\n\t"
    "buffer_load_dwordx4 v238, s[48:51], s18 offen offset:768 nt lds\n\t"
    "v_mfma_f32_16x16x32_bf16 v[128:131], a[32:35], a[80:83], v[128:131]\n\t"
    "ds_read_b64_tr_b16 a[8:9], v193 offset:32768\n\t"
    "v_mfma_f32_16x16x32_bf16 v[128:131], a[36:39], a[104:107], v[128:131]\n\t"
    "ds_read_b128 a[112:115], v206 offset:24576\n\t"
    "v_mfma_f32_16x16x32_bf16 v[136:139], a[36:39], a[108:111], v[136:139]\n\t"
    "ds_read_b64_tr_b16 a[10:11], v193 offset:33792\n\t"
    "v_mfma_f32_16x16x32_bf16 v[136:139], a[32:35], a[84:87], v[136:139]\n\t"
    "ds_read_b128 a[136:139], v207 offset:24576\n\t"
    "v_mfma_f32_16x16x32_bf16 v[100:103], a[40:43], a[64:67], v[100:103]\n\t"
    "ds_read_b64_tr_b16 a[12:13], v193 offset:34816\n\t"
    "v_mfma_f32_16x16x32_bf16 v[100:103], a[44:47], a[88:91], v[100:103]\n\t"
    "ds_read_b128 a[116:119], v206 offset:26624\n\t"
    "v_mfma_f32_16x16x32_bf16 v[108:111], a[44:47], a[92:95], v[108:111]\n\t"
    "ds_read_b64_tr_b16 a[14:15], v193 offset:35840\n\t"
    "v_mfma_f32_16x16x32_bf16 v[108:111], a[40:43], a[68:71], v[108:111]\n\t"
    "ds_read_b128 a[140:143], v207 offset:26624\n\t"
    "v_mfma_f32_16x16x32_bf16 v[116:119], a[40:43], a[72:75], v[116:119]\n\t"
    "ds_read_b64_tr_b16 a[16:17], v194 offset:32768\n\t"
    "v_mfma_f32_16x16x32_bf16 v[116:119], a[44:47], a[96:99], v[116:119]\n\t"
    "ds_read_b128 a[120:123], v206 offset:28672\n\t"
    "v_mfma_f32_16x16x32_bf16 v[124:127], a[44:47], a[100:103], v[124:127]\n\t"
    "ds_read_b64_tr_b16 a[18:19], v194 offset:33792\n\t"
    "v_mfma_f32_16x16x32_bf16 v[124:127], a[40:43], a[76:79], v[124:127]\n\t"
    "ds_read_b128 a[144:147], v207 offset:28672\n\t"
    "v_mfma_f32_16x16x32_bf16 v[132:135], a[40:43], a[80:83], v[132:135]\n\t"
    "ds_read_b64_tr_b16 a[20:21], v194 offset:34816\n\t"
    "v_mfma_f32_16x16x32_bf16 v[132:135], a[44:47], a[104:107], v[132:135]\n\t"
    "ds_read_b128 a[124:127], v206 offset:30720\n\t"
    "v_mfma_f32_16x16x32_bf16 v[140:143], a[44:47], a[108:111], v[140:143]\n\t"
    "ds_read_b64_tr_b16 a[22:23], v194 offset:35840\n\t"
    "v_mfma_f32_16x16x32_bf16 v[140:143], a[40:43], a[84:87], v[140:143]\n\t"
    "ds_read_b128 a[148:151], v207 offset:30720\n\t"
    "v_mfma_f32_16x16x32_bf16 v[144:147], a[48:51], a[64:67], v[144:147]\n\t"
    "ds_read_b64_tr_b16 a[24:25], v195 offset:32768\n\t"
    "v_mfma_f32_16x16x32_bf16 v[144:147], a[52:55], a[88:91], v[144:147]\n\t"
    "ds_read_b128 a[128:131], v206 offset:32768\n\t"
    "v_mfma_f32_16x16x32_bf16 v[152:155], a[52:55], a[92:95], v[152:155]\n\t"
    "ds_read_b64_tr_b16 a[26:27], v195 offset:33792\n\t"
    "v_mfma_f32_16x16x32_bf16 v[152:155], a[48:51], a[68:71], v[152:155]\n\t"
    "ds_read_b128 a[152:155], v207 offset:32768\n\t"
    "v_mfma_f32_16x16x32_bf16 v[160:163], a[48:51], a[72:75], v[160:163]\n\t"
    "ds_read_b64_tr_b16 a[28:29], v195 offset:34816\n\t"
    "v_mfma_f32_16x16x32_bf16 v[160:163], a[52:55], a[96:99], v[160:163]\n\t"
    "ds_read_b128 a[132:135], v206 offset:34816\n\t"
    "v_mfma_f32_16x16x32_bf16 v[168:171], a[52:55], a[100:103], v[168:171]\n\t"
    "ds_read_b64_tr_b16 a[30:31], v195 offset:35840\n\t"
    "v_mfma_f32_16x16x32_bf16 v[168:171], a[48:51], a[76:79], v[168:171]\n\t"
    "ds_read_b128 a[156:159], v207 offset:34816\n\t"
    "v_mfma_f32_16x16x32_bf16 v[176:179], a[48:51], a[80:83], v[176:179]\n\t"
    "v_mfma_f32_16x16x32_bf16 v[176:179], a[52:55], a[104:107], v[176:179]\n\t"
    "v_mfma_f32_16x16x32_bf16 v[184:187], a[52:55], a[108:111], v[184:187]\n\t"
    "v_mfma_f32_16x16x32_bf16 v[184:187], a[48:51], a[84:87], v[184:187]\n\t"
    "v_mfma_f32_16x16x32_bf16 v[148:151], a[56:59], a[64:67], v[148:151]\n\t"
    "v_mfma_f32_16x16x32_bf16 v[148:151], a[60:63], a[88:91], v[148:151]\n\t"
    "v_mfma_f32_16x16x32_bf16 v[156:159], a[60:63], a[92:95], v[156:159]\n\t"
    "v_mfma_f32_16x16x32_bf16 v[156:159], a[56:59], a[68:71], v[156:159]\n\t"
    "v_mfma_f32_16x16x32_bf16 v[164:167], a[56:59], a[72:75], v[164:167]\n\t"
    "v_mfma_f32_16x16x32_bf16 v[164:167], a[60:63], a[96:99], v[164:167]\n\t"
    "v_mfma_f32_16x16x32_bf16 v[172:175], a[60:63], a[100:103], v[172:175]\n\t"
    "v_mfma_f32_16x16x32_bf16 v[172:175], a[56:59], a[76:79], v[172:175]\n\t"
    "v_mfma_f32_16x16x32_bf16 v[180:183], a[56:59], a[80:83], v[180:183]\n\t"
    "v_mfma_f32_16x16x32_bf16 v[180:183], a[60:63], a[104:107], v[180:183]\n\t"
    "v_mfma_f32_16x16x32_bf16 v[188:191], a[60:63], a[108:111], v[188:191]\n\t"
    "v_mfma_f32_16x16x32_bf16 v[188:191], a[56:59], a[84:87], v[188:191]\n\t"
    "s_waitcnt vmcnt(9) lgkmcnt(0)\n\t"
    "s_barrier\n\t"
    "v_mfma_f32_16x16x32_bf16 v[0:3], a[0:3], a[112:115], v[0:3]\n\t"
    "ds_read_b64_tr_b16 a[32:33], v192 offset:49152\n\t"
    "s_add_u32 m0, s59, 3072\n\t"
    "v_mfma_f32_16x16x32_bf16 v[0:3], a[4:7], a[136:139], v[0:3]\n\t"
    "buffer_load_dwordx4 v228, s[48:51], s18 offen offset:1024 nt lds\n\t"
    "v_mfma_f32_16x16x32_bf16 v[8:11], a[4:7], a[140:143], v[8:11]\n\t"
    "ds_read_b64_tr_b16 a[34:35], v192 offset:50176\n\t"
    "s_add_u32 m0, s59, 3840\n\t"
    "v_mfma_f32_16x16x32_bf16 v[8:11], a[0:3], a[116:119], v[8:11]\n\t"
    "buffer_load_dwordx4 v238, s[48:51], s18 offen offset:1280 nt lds\n\t"
    "v_mfma_f32_16x16x32_bf16 v[16:19], a[0:3], a[120:123], v[16:19]\n\t"
    "ds_read_b64_tr_b16 a[36:37], v192 offset:51200\n\t"
    "s_add_u32 m0, s59, 4608\n\t"
    "v_mfma_f32_16x16x32_bf16 v[16:19], a[4:7], a[144:147], v[16:19]\n\t"
    "buffer_load_dwordx4 v228, s[48:51], s18 offen offset:1536 nt lds\n\t"
    "v_mfma_f32_16x16x32_bf16 v[24:27], a[4:7], a[148:151], v[24:27]\n\t"
    "ds_read_b64_tr_b16 a[38:39], v192 offset:52224\n\t"
    "s_add_u32 m0, s59, 5376\n\t"
    "v_mfma_f32_16x16x32_bf16 v[24:27], a[0:3], a[124:127], v[24:27]\n\t"
    "buffer_load_dwordx4 v238, s[48:51], s18 offen offset:1792 nt lds\n\t"
    "s_or_b32 s18, s18, 12288\n\t"
    "s_add_u32 s18, s18, 2048\n\t"
    "s_and_b32 s18, s18, 116736\n\t"
    "v_mfma_f32_16x16x32_bf16 v[32:35], a[0:3], a[128:131], v[32:35]\n\t"
    "ds_read_b64_tr_b16 a[40:41], v193 offset:49152\n\t"
    "v_mfma_f32_16x16x32_bf16 v[32:35], a[4:7], a[152:155], v[32:35]\n\t"
    "v_mfma_f32_16x16x32_bf16 v[40:43], a[4:7], a[156:159], v[40:43]\n\t"
    "ds_read_b64_tr_b16 a[42:43], v193 offset:50176\n\t"
    "v_mfma_f32_16x16x32_bf16 v[40:43], a[0:3], a[132:135], v[40:43]\n\t"
    "v_mfma_f32_16x16x32_bf16 v[4:7], a[8:11], a[112:115], v[4:7]\n\t"
    "ds_read_b64_tr_b16 a[44:45], v193 offset:51200\n\t"
    "v_mfma_f32_16x16x32_bf16 v[4:7], a[12:15], a[136:139], v[4:7]\n\t"
    "v_mfma_f32_16x16x32_bf16 v[12:15], a[12:15], a[140:143], v[12:15]\n\t"
    "ds_read_b64_tr_b16 a[46:47], v193 offset:52224\n\t"
    "v_mfma_f32_16x16x32_bf16 v[12:15], a[8:11], a[116:119], v[12:15]\n\t"
    "v_mfma_f32_16x16x32_bf16 v[20:23], a[8:11], a[120:123], v[20:23]\n\t"
    "ds_read_b64_tr_b16 a[48:49], v194 offset:49152\n\t"
    "v_mfma_f32_16x16x32_bf16 v[20:23], a[12:15], a[144:147], v[20:23]\n\t"
    "v_mfma_f32_16x16x32_bf16 v[28:31], a[12:15], a[148:151], v[28:31]\n\t"
    "ds_read_b64_tr_b16 a[50:51], v194 offset:50176\n\t"
    "v_mfma_f32_16x16x32_bf16 v[28:31], a[8:11], a[124:127], v[28:31]\n\t"
    "v_mfma_f32_16x16x32_bf16 v[36:39], a[8:11], a[128:131], v[36:39]\n\t"
    "ds_read_b64_tr_b16 a[52:53], v194 offset:51200\n\t"
    "v_mfma_f32_16x16x32_bf16 v[36:39], a[12:15], a[152:155], v[36:39]\n\t"
    "v_mfma_f32_16x16x32_bf16 v[44:47], a[12:15], a[156:159], v[44:47]\n\t"
    "ds_read_b64_tr_b16 a[54:55], v194 offset:52224\n\t"
    "v_mfma_f32_16x16x32_bf16 v[44:47], a[8:11], a[132:135], v[44:47]\n\t"
    "v_mfma_f32_16x16x32_bf16 v[48:51], a[16:19], a[112:115], v[48:51]\n\t"
    "ds_read_b64_tr_b16 a[56:57], v195 offset:49152\n\t"
    "v_mfma_f32_16x16x32_bf16 v[48:51], a[20:23], a[136:139], v[48:51]\n\t"
    "v_mfma_f32_16x16x32_bf16 v[56:59], a[20:23], a[140:143], v[56:59]\n\t"
    "ds_read_b64_tr_b16 a[58:59], v195 offset:50176\n\t"
    "v_mfma_f32_16x16x32_bf16 v[56:59], a[16:19], a[116:119], v[56:59]\n\t"
    "v_mfma_f32_16x16x32_bf16 v[64:67], a[16:19], a[120:123], v[64:67]\n\t"
    "ds_read_b64_tr_b16 a[60:61], v195 offset:51200\n\t"
    "v_mfma_f32_16x16x32_bf16 v[64:67], a[20:23], a[144:147], v[64:67]\n\t"
    "v_mfma_f32_16x16x32_bf16 v[72:75], a[20:23], a[148:151], v[72:75]\n\t"
    "ds_read_b64_tr_b16 a[62:63], v195 offset:52224\n\t"
    "v_mfma_f32_16x16x32_bf16 v[72:75], a[16:19], a[124:127], v[72:75]\n\t"
    "v_mfma_f32_16x16x32_bf16 v[80:83], a[16:19], a[128:131], v[80:83]\n\t"
    "v_mfma_f32_16x16x32_bf16 v[80:83], a[20:23], a[152:155], v[80:83]\n\t"
    "v_mfma_f32_16x16x32_bf16 v[88:91], a[20:23], a[156:159], v[88:91]\n\t"
    "v_mfma_f32_16x16x32_bf16 v[88:91], a[16:19], a[132:135], v[88:91]\n\t"
    "v_mfma_f32_16x16x32_bf16 v[52:55], a[24:27], a[112:115], v[52:55]\n\t"
    "v_mfma_f32_16x16x32_bf16 v[52:55], a[28:31], a[136:139], v[52:55]\n\t"
    "v_mfma_f32_16x16x32_bf16 v[60:63], a[28:31], a[140:143], v[60:63]\n\t"
    "v_mfma_f32_16x16x32_bf16 v[60:63], a[24:27], a[116:119], v[60:63]\n\t"
    "v_mfma_f32_16x16x32_bf16 v[68:71], a[24:27], a[120:123], v[68:71]\n\t"
    "v_mfma_f32_16x16x32_bf16 v[68:71], a[28:31], a[144:147], v[68:71]\n\t"
    "v_mfma_f32_16x16x32_bf16 v[76:79], a[28:31], a[148:151], v[76:79]\n\t"
    "v_mfma_f32_16x16x32_bf16 v[76:79], a[24:27], a[124:127], v[76:79]\n\t"
    "v_mfma_f32_16x16x32_bf16 v[84:87], a[24:27], a[128:131], v[84:87]\n\t"
    "v_mfma_f32_16x16x32_bf16 v[84:87], a[28:31], a[152:155], v[84:87]\n\t"
    "v_mfma_f32_16x16x32_bf16 v[92:95], a[28:31], a[156:159], v[92:95]\n\t"
    "v_mfma_f32_16x16x32_bf16 v[92:95], a[24:27], a[132:135], v[92:95]\n\t"
    "s_waitcnt lgkmcnt(0)\n\t"
    "s_barrier\n\t"
    "v_mfma_f32_16x16x32_bf16 v[96:99], a[32:35], a[112:115], v[96:99]\n\t"
    "s_add_u32 m0, s58, 0\n\t"
    "s_nop 0\n\t"
    "v_mfma_f32_16x16x32_bf16 v[96:99], a[36:39], a[136:139], v[96:99]\n\t"
    "buffer_load_dwordx4 v212, s[44:47], s62 offen nt lds\n\t"
    "v_mfma_f32_16x16x32_bf16 v[104:107], a[36:39], a[140:143], v[104:107]\n\t"
    "s_add_u32 m0, s58, 768\n\t"
    "v_mfma_f32_16x16x32_bf16 v[104:107], a[32:35], a[116:119], v[104:107]\n\t"
    "buffer_load_dwordx4 v213, s[44:47], s62 offen offset:256 nt lds\n\t"
    "v_mfma_f32_16x16x32_bf16 v[112:115], a[32:35], a[120:123], v[112:115]\n\t"
    "s_add_u32 m0, s58, 1536\n\t"
    "v_mfma_f32_16x16x32_bf16 v[112:115], a[36:39], a[144:147], v[112:115]\n\t"
    "buffer_load_dwordx4 v214, s[44:47], s62 offen offset:512 nt lds\n\t"
    "v_mfma_f32_16x16x32_bf16 v[120:123], a[36:39], a[148:151], v[120:123]\n\t"
    "s_add_u32 m0, s58, 2304\n\t"
    "v_mfma_f32_16x16x32_bf16 v[120:123], a[32:35], a[124:127], v[120:123]\n\t"
    "buffer_load_dwordx4 v215, s[44:47], s62 offen offset:768 nt lds\n\t"
    "v_mfma_f32_16x16x32_bf16 v[128:131], a[32:35], a[128:131], v[128:131]\n\t"
    "v_mfma_f32_16x16x32_bf16 v[128:131], a[36:39], a[152:155], v[128:131]\n\t"
    "v_mfma_f32_16x16x32_bf16 v[136:139], a[36:39], a[156:159], v[136:139]\n\t"
    "v_mfma_f32_16x16x32_bf16 v[136:139], a[32:35], a[132:135], v[136:139]\n\t"
    "v_mfma_f32_16x16x32_bf16 v[100:103], a[40:43], a[112:115], v[100:103]\n\t"
    "v_mfma_f32_16x16x32_bf16 v[100:103], a[44:47], a[136:139], v[100:103]\n\t"
    "v_mfma_f32_16x16x32_bf16 v[108:111], a[44:47], a[140:143], v[108:111]\n\t"
    "v_mfma_f32_16x16x32_bf16 v[108:111], a[40:43], a[116:119], v[108:111]\n\t"
    "v_mfma_f32_16x16x32_bf16 v[116:119], a[40:43], a[120:123], v[116:119]\n\t"
    "v_mfma_f32_16x16x32_bf16 v[116:119], a[44:47], a[144:147], v[116:119]\n\t"
    "v_mfma_f32_16x16x32_bf16 v[124:127], a[44:47], a[148:151], v[124:127]\n\t"
    "v_mfma_f32_16x16x32_bf16 v[124:127], a[40:43], a[124:127], v[124:127]\n\t"
    "v_mfma_f32_16x16x32_bf16 v[132:135], a[40:43], a[128:131], v[132:135]\n\t"
    "v_mfma_f32_16x16x32_bf16 v[132:135], a[44:47], a[152:155], v[132:135]\n\t"
    "v_mfma_f32_16x16x32_bf16 v[140:143], a[44:47], a[156:159], v[140:143]\n\t"
    "v_mfma_f32_16x16x32_bf16 v[140:143], a[40:43], a[132:135], v[140:143]\n\t"
    "v_mfma_f32_16x16x32_bf16 v[144:147], a[48:51], a[112:115], v[144:147]\n\t"
    "v_mfma_f32_16x16x32_bf16 v[144:147], a[52:55], a[136:139], v[144:147]\n\t"
    "v_mfma_f32_16x16x32_bf16 v[152:155], a[52:55], a[140:143], v[152:155]\n\t"
    "v_mfma_f32_16x16x32_bf16 v[152:155], a[48:51], a[116:119], v[152:155]\n\t"
    "v_mfma_f32_16x16x32_bf16 v[160:163], a[48:51], a[120:123], v[160:163]\n\t"
    "v_mfma_f32_16x16x32_bf16 v[160:163], a[52:55], a[144:147], v[160:163]\n\t"
    "v_mfma_f32_16x16x32_bf16 v[168:171], a[52:55], a[148:151], v[168:171]\n\t"
    "v_mfma_f32_16x16x32_bf16 v[168:171], a[48:51], a[124:127], v[168:171]\n\t"
    "v_mfma_f32_16x16x32_bf16 v[176:179], a[48:51], a[128:131], v[176:179]\n\t"
    "v_mfma_f32_16x16x32_bf16 v[176:179], a[52:55], a[152:155], v[176:179]\n\t"
    "v_mfma_f32_16x16x32_bf16 v[184:187], a[52:55], a[156:159], v[184:187]\n\t"
    "v_mfma_f32_16x16x32_bf16 v[184:187], a[48:51], a[132:135], v[184:187]\n\t"
    "v_mfma_f32_16x16x32_bf16 v[148:151], a[56:59], a[112:115], v[148:151]\n\t"
    "v_mfma_f32_16x16x32_bf16 v[148:151], a[60:63], a[136:139], v[148:151]\n\t"
    "v_mfma_f32_16x16x32_bf16 v[156:159], a[60:63], a[140:143], v[156:159]\n\t"
    "v_mfma_f32_16x16x32_bf16 v[156:159], a[56:59], a[116:119], v[156:159]\n\t"
    "v_mfma_f32_16x16x32_bf16 v[164:167], a[56:59], a[120:123], v[164:167]\n\t"
    "v_mfma_f32_16x16x32_bf16 v[164:167], a[60:63], a[144:147], v[164:167]\n\t"
    "v_mfma_f32_16x16x32_bf16 v[172:175], a[60:63], a[148:151], v[172:175]\n\t"
    "v_mfma_f32_16x16x32_bf16 v[172:175], a[56:59], a[124:127], v[172:175]\n\t"
    "v_mfma_f32_16x16x32_bf16 v[180:183], a[56:59], a[128:131], v[180:183]\n\t"
    "v_mfma_f32_16x16x32_bf16 v[180:183], a[60:63], a[152:155], v[180:183]\n\t"
    "v_mfma_f32_16x16x32_bf16 v[188:191], a[60:63], a[156:159], v[188:191]\n\t"
    "v_mfma_f32_16x16x32_bf16 v[188:191], a[56:59], a[132:135], v[188:191]\n\t"
    "v_mov_b32 v232, s20\n\t"
    "v_mov_b32 v233, s20\n\t"
    "v_pk_mul_f32 v[0:1], v[0:1], v[232:233]\n\t"
    "v_pk_mul_f32 v[2:3], v[2:3], v[232:233]\n\t"
    "v_pk_mul_f32 v[4:5], v[4:5], v[232:233]\n\t"
    "v_pk_mul_f32 v[6:7], v[6:7], v[232:233]\n\t"
    "v_cvt_pk_bf16_f32 v0, v0, v1\n\t"
    "v_cvt_pk_bf16_f32 v1, v2, v3\n\t"
    "v_cvt_pk_bf16_f32 v2, v4, v5\n\t"
    "v_cvt_pk_bf16_f32 v3, v6, v7\n\t"
    "v_pk_mul_f32 v[8:9], v[8:9], v[232:233]\n\t"
    "v_pk_mul_f32 v[10:11], v[10:11], v[232:233]\n\t"
    "v_pk_mul_f32 v[12:13], v[12:13], v[232:233]\n\t"
    "v_pk_mul_f32 v[14:15], v[14:15], v[232:233]\n\t"
    "v_cvt_pk_bf16_f32 v4, v8, v9\n\t"
    "v_cvt_pk_bf16_f32 v5, v10, v11\n\t"
    "v_cvt_pk_bf16_f32 v6, v12, v13\n\t"
    "v_cvt_pk_bf16_f32 v7, v14, v15\n\t"
    "v_pk_mul_f32 v[16:17], v[16:17], v[232:233]\n\t"
    "v_pk_mul_f32 v[18:19], v[18:19], v[232:233]\n\t"
    "v_pk_mul_f32 v[20:21], v[20:21], v[232:233]\n\t"
    "v_pk_mul_f32 v[22:23], v[22:23], v[232:233]\n\t"
    "v_cvt_pk_bf16_f32 v8, v16, v17\n\t"
    "v_cvt_pk_bf16_f32 v9, v18, v19\n\t"
    "v_cvt_pk_bf16_f32 v10, v20, v21\n\t"
    "v_cvt_pk_bf16_f32 v11, v22, v23\n\t"
    "v_pk_mul_f32 v[24:25], v[24:25], v[232:233]\n\t"
    "v_pk_mul_f32 v[26:27], v[26:27], v[232:233]\n\t"
    "v_pk_mul_f32 v[28:29], v[28:29], v[232:233]\n\t"
    "v_pk_mul_f32 v[30:31], v[30:31], v[232:233]\n\t"
    "v_cvt_pk_bf16_f32 v12, v24, v25\n\t"
    "v_cvt_pk_bf16_f32 v13, v26, v27\n\t"
    "v_cvt_pk_bf16_f32 v14, v28, v29\n\t"
    "v_cvt_pk_bf16_f32 v15, v30, v31\n\t"
    "v_pk_mul_f32 v[32:33], v[32:33], v[232:233]\n\t"
    "v_pk_mul_f32 v[34:35], v[34:35], v[232:233]\n\t"
    "v_pk_mul_f32 v[36:37], v[36:37], v[232:233]\n\t"
    "v_pk_mul_f32 v[38:39], v[38:39], v[232:233]\n\t"
    "v_cvt_pk_bf16_f32 v16, v32, v33\n\t"
    "v_cvt_pk_bf16_f32 v17, v34, v35\n\t"
    "v_cvt_pk_bf16_f32 v18, v36, v37\n\t"
    "v_cvt_pk_bf16_f32 v19, v38, v39\n\t"
    "v_pk_mul_f32 v[40:41], v[40:41], v[232:233]\n\t"
    "v_pk_mul_f32 v[42:43], v[42:43], v[232:233]\n\t"
    "v_pk_mul_f32 v[44:45], v[44:45], v[232:233]\n\t"
    "v_pk_mul_f32 v[46:47], v[46:47], v[232:233]\n\t"
    "v_cvt_pk_bf16_f32 v20, v40, v41\n\t"
    "v_cvt_pk_bf16_f32 v21, v42, v43\n\t"
    "v_cvt_pk_bf16_f32 v22, v44, v45\n\t"
    "v_cvt_pk_bf16_f32 v23, v46, v47\n\t"
    "v_pk_mul_f32 v[48:49], v[48:49], v[232:233]\n\t"
    "v_pk_mul_f32 v[50:51], v[50:51], v[232:233]\n\t"
    "v_pk_mul_f32 v[52:53], v[52:53], v[232:233]\n\t"
    "v_pk_mul_f32 v[54:55], v[54:55], v[232:233]\n\t"
    "v_cvt_pk_bf16_f32 v24, v48, v49\n\t"
    "v_cvt_pk_bf16_f32 v25, v50, v51\n\t"
    "v_cvt_pk_bf16_f32 v26, v52, v53\n\t"
    "v_cvt_pk_bf16_f32 v27, v54, v55\n\t"
    "v_pk_mul_f32 v[56:57], v[56:57], v[232:233]\n\t"
    "v_pk_mul_f32 v[58:59], v[58:59], v[232:233]\n\t"
    "v_pk_mul_f32 v[60:61], v[60:61], v[232:233]\n\t"
    "v_pk_mul_f32 v[62:63], v[62:63], v[232:233]\n\t"
    "v_cvt_pk_bf16_f32 v28, v56, v57\n\t"
    "v_cvt_pk_bf16_f32 v29, v58, v59\n\t"
    "v_cvt_pk_bf16_f32 v30, v60, v61\n\t"
    "v_cvt_pk_bf16_f32 v31, v62, v63\n\t"
    "v_pk_mul_f32 v[64:65], v[64:65], v[232:233]\n\t"
    "v_pk_mul_f32 v[66:67], v[66:67], v[232:233]\n\t"
    "v_pk_mul_f32 v[68:69], v[68:69], v[232:233]\n\t"
    "v_pk_mul_f32 v[70:71], v[70:71], v[232:233]\n\t"
    "v_cvt_pk_bf16_f32 v32, v64, v65\n\t"
    "v_cvt_pk_bf16_f32 v33, v66, v67\n\t"
    "v_cvt_pk_bf16_f32 v34, v68, v69\n\t"
    "v_cvt_pk_bf16_f32 v35, v70, v71\n\t"
    "v_pk_mul_f32 v[72:73], v[72:73], v[232:233]\n\t"
    "v_pk_mul_f32 v[74:75], v[74:75], v[232:233]\n\t"
    "v_pk_mul_f32 v[76:77], v[76:77], v[232:233]\n\t"
    "v_pk_mul_f32 v[78:79], v[78:79], v[232:233]\n\t"
    "v_cvt_pk_bf16_f32 v36, v72, v73\n\t"
    "v_cvt_pk_bf16_f32 v37, v74, v75\n\t"
    "v_cvt_pk_bf16_f32 v38, v76, v77\n\t"
    "v_cvt_pk_bf16_f32 v39, v78, v79\n\t"
    "v_pk_mul_f32 v[80:81], v[80:81], v[232:233]\n\t"
    "v_pk_mul_f32 v[82:83], v[82:83], v[232:233]\n\t"
    "v_pk_mul_f32 v[84:85], v[84:85], v[232:233]\n\t"
    "v_pk_mul_f32 v[86:87], v[86:87], v[232:233]\n\t"
    "v_cvt_pk_bf16_f32 v40, v80, v81\n\t"
    "v_cvt_pk_bf16_f32 v41, v82, v83\n\t"
    "v_cvt_pk_bf16_f32 v42, v84, v85\n\t"
    "v_cvt_pk_bf16_f32 v43, v86, v87\n\t"
    "v_pk_mul_f32 v[88:89], v[88:89], v[232:233]\n\t"
    "v_pk_mul_f32 v[90:91], v[90:91], v[232:233]\n\t"
    "v_pk_mul_f32 v[92:93], v[92:93], v[232:233]\n\t"
    "v_pk_mul_f32 v[94:95], v[94:95], v[232:233]\n\t"
    "v_cvt_pk_bf16_f32 v44, v88, v89\n\t"
    "v_cvt_pk_bf16_f32 v45, v90, v91\n\t"
    "v_cvt_pk_bf16_f32 v46, v92, v93\n\t"
    "v_cvt_pk_bf16_f32 v47, v94, v95\n\t"
    "v_pk_mul_f32 v[96:97], v[96:97], v[232:233]\n\t"
    "v_pk_mul_f32 v[98:99], v[98:99], v[232:233]\n\t"
    "v_pk_mul_f32 v[100:101], v[100:101], v[232:233]\n\t"
    "v_pk_mul_f32 v[102:103], v[102:103], v[232:233]\n\t"
    "v_cvt_pk_bf16_f32 v48, v96, v97\n\t"
    "v_cvt_pk_bf16_f32 v49, v98, v99\n\t"
    "v_cvt_pk_bf16_f32 v50, v100, v101\n\t"
    "v_cvt_pk_bf16_f32 v51, v102, v103\n\t"
    "v_pk_mul_f32 v[104:105], v[104:105], v[232:233]\n\t"
    "v_pk_mul_f32 v[106:107], v[106:107], v[232:233]\n\t"
    "v_pk_mul_f32 v[108:109], v[108:109], v[232:233]\n\t"
    "v_pk_mul_f32 v[110:111], v[110:111], v[232:233]\n\t"
    "v_cvt_pk_bf16_f32 v52, v104, v105\n\t"
    "v_cvt_pk_bf16_f32 v53, v106, v107\n\t"
    "v_cvt_pk_bf16_f32 v54, v108, v109\n\t"
    "v_cvt_pk_bf16_f32 v55, v110, v111\n\t"
    "v_pk_mul_f32 v[112:113], v[112:113], v[232:233]\n\t"
    "v_pk_mul_f32 v[114:115], v[114:115], v[232:233]\n\t"
    "v_pk_mul_f32 v[116:117], v[116:117], v[232:233]\n\t"
    "v_pk_mul_f32 v[118:119], v[118:119], v[232:233]\n\t"
    "v_cvt_pk_bf16_f32 v56, v112, v113\n\t"
    "v_cvt_pk_bf16_f32 v57, v114, v115\n\t"
    "v_cvt_pk_bf16_f32 v58, v116, v117\n\t"
    "v_cvt_pk_bf16_f32 v59, v118, v119\n\t"
    "v_pk_mul_f32 v[120:121], v[120:121], v[232:233]\n\t"
    "v_pk_mul_f32 v[122:123], v[122:123], v[232:233]\n\t"
    "v_pk_mul_f32 v[124:125], v[124:125], v[232:233]\n\t"
    "v_pk_mul_f32 v[126:127], v[126:127], v[232:233]\n\t"
    "v_cvt_pk_bf16_f32 v60, v120, v121\n\t"
    "v_cvt_pk_bf16_f32 v61, v122, v123\n\t"
    "v_cvt_pk_bf16_f32 v62, v124, v125\n\t"
    "v_cvt_pk_bf16_f32 v63, v126, v127\n\t"
    "v_pk_mul_f32 v[128:129], v[128:129], v[232:233]\n\t"
    "v_pk_mul_f32 v[130:131], v[130:131], v[232:233]\n\t"
    "v_pk_mul_f32 v[132:133], v[132:133], v[232:233]\n\t"
    "v_pk_mul_f32 v[134:135], v[134:135], v[232:233]\n\t"
    "v_cvt_pk_bf16_f32 v64, v128, v129\n\t"
    "v_cvt_pk_bf16_f32 v65, v130, v131\n\t"
    "v_cvt_pk_bf16_f32 v66, v132, v133\n\t"
    "v_cvt_pk_bf16_f32 v67, v134, v135\n\t"
    "v_pk_mul_f32 v[136:137], v[136:137], v[232:233]\n\t"
    "v_pk_mul_f32 v[138:139], v[138:139], v[232:233]\n\t"
    "v_pk_mul_f32 v[140:141], v[140:141], v[232:233]\n\t"
    "v_pk_mul_f32 v[142:143], v[142:143], v[232:233]\n\t"
    "v_cvt_pk_bf16_f32 v68, v136, v137\n\t"
    "v_cvt_pk_bf16_f32 v69, v138, v139\n\t"
    "v_cvt_pk_bf16_f32 v70, v140, v141\n\t"
    "v_cvt_pk_bf16_f32 v71, v142, v143\n\t"
    "v_pk_mul_f32 v[144:145], v[144:145], v[232:233]\n\t"
    "v_pk_mul_f32 v[146:147], v[146:147], v[232:233]\n\t"
    "v_pk_mul_f32 v[148:149], v[148:149], v[232:233]\n\t"
    "v_pk_mul_f32 v[150:151], v[150:151], v[232:233]\n\t"
    "v_cvt_pk_bf16_f32 v72, v144, v145\n\t"
    "v_cvt_pk_bf16_f32 v73, v146, v147\n\t"
    "v_cvt_pk_bf16_f32 v74, v148, v149\n\t"
    "v_cvt_pk_bf16_f32 v75, v150, v151\n\t"
    "v_pk_mul_f32 v[152:153], v[152:153], v[232:233]\n\t"
    "v_pk_mul_f32 v[154:155], v[154:155], v[232:233]\n\t"
    "v_pk_mul_f32 v[156:157], v[156:157], v[232:233]\n\t"
    "v_pk_mul_f32 v[158:159], v[158:159], v[232:233]\n\t"
    "v_cvt_pk_bf16_f32 v76, v152, v153\n\t"
    "v_cvt_pk_bf16_f32 v77, v154, v155\n\t"
    "v_cvt_pk_bf16_f32 v78, v156, v157\n\t"
    "v_cvt_pk_bf16_f32 v79, v158, v159\n\t"
    "v_pk_mul_f32 v[160:161], v[160:161], v[232:233]\n\t"
    "v_pk_mul_f32 v[162:163], v[162:163], v[232:233]\n\t"
    "v_pk_mul_f32 v[164:165], v[164:165], v[232:233]\n\t"
    "v_pk_mul_f32 v[166:167], v[166:167], v[232:233]\n\t"
    "v_cvt_pk_bf16_f32 v80, v160, v161\n\t"
    "v_cvt_pk_bf16_f32 v81, v162, v163\n\t"
    "v_cvt_pk_bf16_f32 v82, v164, v165\n\t"
    "v_cvt_pk_bf16_f32 v83, v166, v167\n\t"
    "v_pk_mul_f32 v[168:169], v[168:169], v[232:233]\n\t"
    "v_pk_mul_f32 v[170:171], v[170:171], v[232:233]\n\t"
    "v_pk_mul_f32 v[172:173], v[172:173], v[232:233]\n\t"
    "v_pk_mul_f32 v[174:175], v[174:175], v[232:233]\n\t"
    "v_cvt_pk_bf16_f32 v84, v168, v169\n\t"
    "v_cvt_pk_bf16_f32 v85, v170, v171\n\t"
    "v_cvt_pk_bf16_f32 v86, v172, v173\n\t"
    "v_cvt_pk_bf16_f32 v87, v174, v175\n\t"
    "v_pk_mul_f32 v[176:177], v[176:177], v[232:233]\n\t"
    "v_pk_mul_f32 v[178:179], v[178:179], v[232:233]\n\t"
    "v_pk_mul_f32 v[180:181], v[180:181], v[232:233]\n\t"
    "v_pk_mul_f32 v[182:183], v[182:183], v[232:233]\n\t"
    "v_cvt_pk_bf16_f32 v88, v176, v177\n\t"
    "v_cvt_pk_bf16_f32 v89, v178, v179\n\t"
    "v_cvt_pk_bf16_f32 v90, v180, v181\n\t"
    "v_cvt_pk_bf16_f32 v91, v182, v183\n\t"
    "v_pk_mul_f32 v[184:185], v[184:185], v[232:233]\n\t"
    "v_pk_mul_f32 v[186:187], v[186:187], v[232:233]\n\t"
    "v_pk_mul_f32 v[188:189], v[188:189], v[232:233]\n\t"
    "v_pk_mul_f32 v[190:191], v[190:191], v[232:233]\n\t"
    "v_cvt_pk_bf16_f32 v92, v184, v185\n\t"
    "v_cvt_pk_bf16_f32 v93, v186, v187\n\t"
    "v_cvt_pk_bf16_f32 v94, v188, v189\n\t"
    "v_cvt_pk_bf16_f32 v95, v190, v191\n\t"
    "s_add_u32 m0, s58, 3072\n\t"
    "s_nop 0\n\t"
    "s_nop 0\n\t"
    "buffer_load_dwordx4 v212, s[44:47], s62 offen offset:1024 nt lds\n\t"
    "s_add_u32 m0, s58, 3840\n\t"
    "s_nop 0\n\t"
    "s_nop 0\n\t"
    "buffer_load_dwordx4 v213, s[44:47], s62 offen offset:1280 nt lds\n\t"
    "s_add_u32 m0, s58, 4608\n\t"
    "s_nop 0\n\t"
    "s_nop 0\n\t"
    "buffer_load_dwordx4 v214, s[44:47], s62 offen offset:1536 nt lds\n\t"
    "s_add_u32 m0, s58, 5376\n\t"
    "s_nop 0\n\t"
    "s_nop 0\n\t"
    "buffer_load_dwordx4 v215, s[44:47], s62 offen offset:1792 nt lds\n\t"
    "s_or_b32 s62, s62, 12288\n\t"
    "s_add_u32 s62, s62, 2048\n\t"
    "s_and_b32 s62, s62, 116736\n\t"
    "v_permlane16_swap_b32 v0, v2\n\t"
    "v_permlane16_swap_b32 v1, v3\n\t"
    "v_permlane16_swap_b32 v4, v6\n\t"
    "v_permlane16_swap_b32 v5, v7\n\t"
    "v_permlane16_swap_b32 v8, v10\n\t"
    "v_permlane16_swap_b32 v9, v11\n\t"
    "v_permlane16_swap_b32 v12, v14\n\t"
    "v_permlane16_swap_b32 v13, v15\n\t"
    "v_permlane16_swap_b32 v16, v18\n\t"
    "v_permlane16_swap_b32 v17, v19\n\t"
    "v_permlane16_swap_b32 v20, v22\n\t"
    "v_permlane16_swap_b32 v21, v23\n\t"
    "v_permlane16_swap_b32 v24, v26\n\t"
    "v_permlane16_swap_b32 v25, v27\n\t"
    "v_permlane16_swap_b32 v28, v30\n\t"
    "v_permlane16_swap_b32 v29, v31\n\t"
    "v_permlane16_swap_b32 v32, v34\n\t"
    "v_permlane16_swap_b32 v33, v35\n\t"
    "v_permlane16_swap_b32 v36, v38\n\t"
    "v_permlane16_swap_b32 v37, v39\n\t"
    "v_permlane16_swap_b32 v40, v42\n\t"
    "v_permlane16_swap_b32 v41, v43\n\t"
    "v_permlane16_swap_b32 v44, v46\n\t"
    "v_permlane16_swap_b32 v45, v47\n\t"
    "v_permlane16_swap_b32 v48, v50\n\t"
    "v_permlane16_swap_b32 v49, v51\n\t"
    "v_permlane16_swap_b32 v52, v54\n\t"
    "v_permlane16_swap_b32 v53, v55\n\t"
    "v_permlane16_swap_b32 v56, v58\n\t"
    "v_permlane16_swap_b32 v57, v59\n\t"
    "v_permlane16_swap_b32 v60, v62\n\t"
    "v_permlane16_swap_b32 v61, v63\n\t"
    "v_permlane16_swap_b32 v64, v66\n\t"
    "v_permlane16_swap_b32 v65, v67\n\t"
    "v_permlane16_swap_b32 v68, v70\n\t"
    "v_permlane16_swap_b32 v69, v71\n\t"
    "v_permlane16_swap_b32 v72, v74\n\t"
    "v_permlane16_swap_b32 v73, v75\n\t"
    "v_permlane16_swap_b32 v76, v78\n\t"
    "v_permlane16_swap_b32 v77, v79\n\t"
    "v_permlane16_swap_b32 v80, v82\n\t"
    "v_permlane16_swap_b32 v81, v83\n\t"
    "v_permlane16_swap_b32 v84, v86\n\t"
    "v_permlane16_swap_b32 v85, v87\n\t"
    "v_permlane16_swap_b32 v88, v90\n\t"
    "v_permlane16_swap_b32 v89, v91\n\t"
    "v_permlane16_swap_b32 v92, v94\n\t"
    "v_permlane16_swap_b32 v93, v95\n\t"
    "s_mov_b32 s22, 0\n\t"
    "s_mov_b32 s16, 5\n\t"
    "v_mfma_f32_16x16x32_bf16 v[96:99], a[192:195], v[0:3], 0\n\t"
    "v_mfma_f32_16x16x32_bf16 v[96:99], a[196:199], v[24:27], v[96:99]\n\t"
    "v_xor_b32_e64 v192, 0, v240\n\t"
    "v_mfma_f32_16x16x32_bf16 v[96:99], a[200:203], v[48:51], v[96:99]\n\t"
    "v_mfma_f32_16x16x32_bf16 v[96:99], a[204:207], v[72:75], v[96:99]\n\t"
    "v_xor_b32_e64 v193, 32, v240\n\t"
    "v_mfma_f32_16x16x32_bf16 v[100:103], a[220:223], v[72:75], 0\n\t"
    "v_mfma_f32_16x16x32_bf16 v[100:103], a[216:219], v[48:51], v[100:103]\n\t"
    "v_xor_b32_e64 v194, 64, v240\n\t"
    "v_mfma_f32_16x16x32_bf16 v[100:103], a[212:215], v[24:27], v[100:103]\n\t"
    "v_mfma_f32_16x16x32_bf16 v[100:103], a[208:211], v[0:3], v[100:103]\n\t"
    "v_xor_b32 v195, 96, v240\n\t"
    "v_mfma_f32_16x16x32_bf16 v[104:107], a[192:195], v[4:7], 0\n\t"
    "v_mfma_f32_16x16x32_bf16 v[104:107], a[196:199], v[28:31], v[104:107]\n\t"
    "v_mfma_f32_16x16x32_bf16 v[104:107], a[200:203], v[52:55], v[104:107]\n\t"
    "v_mfma_f32_16x16x32_bf16 v[104:107], a[204:207], v[76:79], v[104:107]\n\t"
    "v_mfma_f32_16x16x32_bf16 v[108:111], a[220:223], v[76:79], 0\n\t"
    "v_mfma_f32_16x16x32_bf16 v[108:111], a[216:219], v[52:55], v[108:111]\n\t"
    "v_mfma_f32_16x16x32_bf16 v[108:111], a[212:215], v[28:31], v[108:111]\n\t"
    "v_mfma_f32_16x16x32_bf16 v[108:111], a[208:211], v[4:7], v[108:111]\n\t"
    "s_add_u32 m0, s59, 32768\n\t"
    "v_mfma_f32_16x16x32_bf16 v[112:115], a[192:195], v[8:11], 0\n\t"
    "buffer_load_dwordx4 v228, s[48:51], s18 offen nt lds\n\t"
    "v_mfma_f32_16x16x32_bf16 v[112:115], a[196:199], v[32:35], v[112:115]\n\t"
    "v_mfma_f32_16x16x32_bf16 v[112:115], a[200:203], v[56:59], v[112:115]\n\t"
    "s_add_u32 m0, s59, 33536\n\t"
    "v_mfma_f32_16x16x32_bf16 v[112:115], a[204:207], v[80:83], v[112:115]\n\t"
    "buffer_load_dwordx4 v238, s[48:51], s18 offen offset:256 nt lds\n\t"
    "v_mfma_f32_16x16x32_bf16 v[116:119], a[220:223], v[80:83], 0\n\t"
    "v_mfma_f32_16x16x32_bf16 v[116:119], a[216:219], v[56:59], v[116:119]\n\t"
    "v_mfma_f32_16x16x32_bf16 v[116:119], a[212:215], v[32:35], v[116:119]\n\t"
    "s_add_u32 m0, s59, 34304\n\t"
    "v_mfma_f32_16x16x32_bf16 v[116:119], a[208:211], v[8:11], v[116:119]\n\t"
    "buffer_load_dwordx4 v228, s[48:51], s18 offen offset:512 nt lds\n\t"
    "v_mfma_f32_16x16x32_bf16 v[120:123], a[192:195], v[12:15], 0\n\t"
    "v_mfma_f32_16x16x32_bf16 v[120:123], a[196:199], v[36:39], v[120:123]\n\t"
    "v_mfma_f32_16x16x32_bf16 v[120:123], a[200:203], v[60:63], v[120:123]\n\t"
    "s_add_u32 m0, s59, 35072\n\t"
    "v_mfma_f32_16x16x32_bf16 v[120:123], a[204:207], v[84:87], v[120:123]\n\t"
    "buffer_load_dwordx4 v238, s[48:51], s18 offen offset:768 nt lds\n\t"
    "v_mfma_f32_16x16x32_bf16 v[124:127], a[220:223], v[84:87], 0\n\t"
    "v_mfma_f32_16x16x32_bf16 v[124:127], a[216:219], v[60:63], v[124:127]\n\t"
    "v_mfma_f32_16x16x32_bf16 v[124:127], a[212:215], v[36:39], v[124:127]\n\t"
    "s_add_u32 m0, s59, 35840\n\t"
    "v_mfma_f32_16x16x32_bf16 v[124:127], a[208:211], v[12:15], v[124:127]\n\t"
    "buffer_load_dwordx4 v228, s[48:51], s18 offen offset:1024 nt lds\n\t"
    "v_mfma_f32_16x16x32_bf16 v[128:131], a[192:195], v[16:19], 0\n\t"
    "v_mfma_f32_16x16x32_bf16 v[128:131], a[196:199], v[40:43], v[128:131]\n\t"
    "v_mfma_f32_16x16x32_bf16 v[128:131], a[200:203], v[64:67], v[128:131]\n\t"
    "s_add_u32 m0, s59, 36608\n\t"
    "v_mfma_f32_16x16x32_bf16 v[128:131], a[204:207], v[88:91], v[128:131]\n\t"
    "buffer_load_dwordx4 v238, s[48:51], s18 offen offset:1280 nt lds\n\t"
    "v_mfma_f32_16x16x32_bf16 v[132:135], a[220:223], v[88:91], 0\n\t"
    "v_mfma_f32_16x16x32_bf16 v[132:135], a[216:219], v[64:67], v[132:135]\n\t"
    "v_mfma_f32_16x16x32_bf16 v[132:135], a[212:215], v[40:43], v[132:135]\n\t"
    "s_add_u32 m0, s59, 37376\n\t"
    "v_mfma_f32_16x16x32_bf16 v[132:135], a[208:211], v[16:19], v[132:135]\n\t"
    "buffer_load_dwordx4 v228, s[48:51], s18 offen offset:1536 nt lds\n\t"
    "v_mfma_f32_16x16x32_bf16 v[136:139], a[192:195], v[20:23], 0\n\t"
    "v_mfma_f32_16x16x32_bf16 v[136:139], a[196:199], v[44:47], v[136:139]\n\t"
    "v_mfma_f32_16x16x32_bf16 v[136:139], a[200:203], v[68:71], v[136:139]\n\t"
    "s_add_u32 m0, s59, 38144\n\t"
    "v_mfma_f32_16x16x32_bf16 v[136:139], a[204:207], v[92:95], v[136:139]\n\t"
    "buffer_load_dwordx4 v238, s[48:51], s18 offen offset:1792 nt lds\n\t"
    "s_or_b32 s18, s18, 12288\n\t"
    "s_add_u32 s18, s18, 2048\n\t"
    "s_and_b32 s18, s18, 116736\n\t"
    "v_mfma_f32_16x16x32_bf16 v[140:143], a[220:223], v[92:95], 0\n\t"
    "v_mfma_f32_16x16x32_bf16 v[140:143], a[216:219], v[68:71], v[140:143]\n\t"
    "v_mfma_f32_16x16x32_bf16 v[140:143], a[212:215], v[44:47], v[140:143]\n\t"
    "v_mfma_f32_16x16x32_bf16 v[140:143], a[208:211], v[20:23], v[140:143]\n\t"
    "s_waitcnt vmcnt(25)\n\t"
    "s_nop 0\n\t"
    "ds_read_b128 a[192:195], v224 offset:32768\n\t"
    "ds_read_b128 a[196:199], v225 offset:32768\n\t"
    "ds_read_b128 a[200:203], v226 offset:32768\n\t"
    "ds_read_b128 a[204:207], v227 offset:32768\n\t"
    "ds_read_b128 a[208:211], v224 offset:36864\n\t"
    "ds_read_b128 a[212:215], v225 offset:36864\n\t"
    "ds_read_b128 a[216:219], v226 offset:36864\n\t"
    "ds_read_b128 a[220:223], v227 offset:36864\n\t"
    "v_max_f32 v206, v96, v97\n\t"
    "v_max3_f32 v206, v206, v98, v99\n\t"
    "v_max3_f32 v206, v206, v100, v101\n\t"
    "v_max3_f32 v206, v206, v102, v103\n\t"
    "v_max_f32 v207, v104, v105\n\t"
    "v_max3_f32 v207, v207, v106, v107\n\t"
    "v_max3_f32 v207, v207, v108, v109\n\t"
    "v_max3_f32 v207, v207, v110, v111\n\t"
    "v_max_f32 v208, v112, v113\n\t"
    "v_max3_f32 v208, v208, v114, v115\n\t"
    "v_max3_f32 v208, v208, v116, v117\n\t"
    "v_max3_f32 v208, v208, v118, v119\n\t"
    "v_max_f32 v209, v120, v121\n\t"
    "v_max3_f32 v209, v209, v122, v123\n\t"
    "v_max3_f32 v209, v209, v124, v125\n\t"
    "v_max3_f32 v209, v209, v126, v127\n\t"
    "v_max_f32 v210, v128, v129\n\t"
    "v_max3_f32 v210, v210, v130, v131\n\t"
    "v_max3_f32 v210, v210, v132, v133\n\t"
    "v_max3_f32 v210, v210, v134, v135\n\t"
    "v_max_f32 v211, v136, v137\n\t"
    "v_max3_f32 v211, v211, v138, v139\n\t"
    "v_max3_f32 v211, v211, v140, v141\n\t"
    "v_max3_f32 v211, v211, v142, v143\n\t"
    "v_mov_b32 v200, v206\n\t"
    "v_mov_b32 v201, v207\n\t"
    "v_mov_b32 v202, v208\n\t"
    "v_mov_b32 v203, v209\n\t"
    "v_mov_b32 v204, v210\n\t"
    "v_mov_b32 v205, v211\n\t"
    "v_permlane32_swap_b32 v206, v200\n\t"
    "v_permlane32_swap_b32 v207, v201\n\t"
    "v_permlane32_swap_b32 v208, v202\n\t"
    "v_permlane32_swap_b32 v209, v203\n\t"
    "v_permlane32_swap_b32 v210, v204\n\t"
    "v_permlane32_swap_b32 v211, v205\n\t"
    "v_max_f32 v206, v206, v200\n\t"
    "v_max_f32 v207, v207, v201\n\t"
    "v_max_f32 v208, v208, v202\n\t"
    "v_max_f32 v209, v209, v203\n\t"
    "v_max_f32 v210, v210, v204\n\t"
    "v_max_f32 v211, v211, v205\n\t"
    "v_mov_b32 v200, v206\n\t"
    "v_mov_b32 v201, v207\n\t"
    "v_mov_b32 v202, v208\n\t"
    "v_mov_b32 v203, v209\n\t"
    "v_mov_b32 v204, v210\n\t"
    "v_mov_b32 v205, v211\n\t"
    "v_permlane16_swap_b32 v206, v200\n\t"
    "v_permlane16_swap_b32 v207, v201\n\t"
    "v_permlane16_swap_b32 v208, v202\n\t"
    "v_permlane16_swap_b32 v209, v203\n\t"
    "v_permlane16_swap_b32 v210, v204\n\t"
    "v_permlane16_swap_b32 v211, v205\n\t"
    "v_max_f32 v206, v206, v200\n\t"
    "v_max_f32 v207, v207, v201\n\t"
    "v_max_f32 v208, v208, v202\n\t"
    "v_max_f32 v209, v209, v203\n\t"
    "v_max_f32 v210, v210, v204\n\t"
    "v_max_f32 v211, v211, v205\n\t"
    "v_sub_f32 v168, 0, v206\n\t"
    "v_sub_f32 v169, 0, v206\n\t"
    "v_sub_f32 v170, 0, v206\n\t"
    "v_sub_f32 v171, 0, v206\n\t"
    "v_mov_b32 v248, 0\n\t"
    "v_sub_f32 v172, 0, v207\n\t"
    "v_sub_f32 v173, 0, v207\n\t"
    "v_sub_f32 v174, 0, v207\n\t"
    "v_sub_f32 v175, 0, v207\n\t"
    "v_mov_b32 v249, 0\n\t"
    "v_sub_f32 v176, 0, v208\n\t"
    "v_sub_f32 v177, 0, v208\n\t"
    "v_sub_f32 v178, 0, v208\n\t"
    "v_sub_f32 v179, 0, v208\n\t"
    "v_mov_b32 v250, 0\n\t"
    "v_sub_f32 v180, 0, v209\n\t"
    "v_sub_f32 v181, 0, v209\n\t"
    "v_sub_f32 v182, 0, v209\n\t"
    "v_sub_f32 v183, 0, v209\n\t"
    "v_mov_b32 v251, 0\n\t"
    "v_sub_f32 v184, 0, v210\n\t"
    "v_sub_f32 v185, 0, v210\n\t"
    "v_sub_f32 v186, 0, v210\n\t"
    "v_sub_f32 v187, 0, v210\n\t"
    "v_mov_b32 v252, 0\n\t"
    "v_sub_f32 v188, 0, v211\n\t"
    "v_sub_f32 v189, 0, v211\n\t"
    "v_sub_f32 v190, 0, v211\n\t"
    "v_sub_f32 v191, 0, v211\n\t"
    "v_mov_b32 v253, 0\n\t"
    "v_sub_f32 v96, v96, v206\n\t"
    "v_sub_f32 v97, v97, v206\n\t"
    "v_sub_f32 v98, v98, v206\n\t"
    "v_sub_f32 v99, v99, v206\n\t"
    "v_sub_f32 v100, v100, v206\n\t"
    "v_sub_f32 v101, v101, v206\n\t"
    "v_sub_f32 v102, v102, v206\n\t"
    "v_sub_f32 v103, v103, v206\n\t"
    "v_sub_f32 v104, v104, v207\n\t"
    "v_sub_f32 v105, v105, v207\n\t"
    "v_sub_f32 v106, v106, v207\n\t"
    "v_sub_f32 v107, v107, v207\n\t"
    "v_sub_f32 v108, v108, v207\n\t"
    "v_sub_f32 v109, v109, v207\n\t"
    "v_sub_f32 v110, v110, v207\n\t"
    "v_sub_f32 v111, v111, v207\n\t"
    "v_sub_f32 v112, v112, v208\n\t"
    "v_sub_f32 v113, v113, v208\n\t"
    "v_sub_f32 v114, v114, v208\n\t"
    "v_sub_f32 v115, v115, v208\n\t"
    "v_sub_f32 v116, v116, v208\n\t"
    "v_sub_f32 v117, v117, v208\n\t"
    "v_sub_f32 v118, v118, v208\n\t"
    "v_sub_f32 v119, v119, v208\n\t"
    "v_sub_f32 v120, v120, v209\n\t"
    "v_sub_f32 v121, v121, v209\n\t"
    "v_sub_f32 v122, v122, v209\n\t"
    "v_sub_f32 v123, v123, v209\n\t"
    "v_sub_f32 v124, v124, v209\n\t"
    "v_sub_f32 v125, v125, v209\n\t"
    "v_sub_f32 v126, v126, v209\n\t"
    "v_sub_f32 v127, v127, v209\n\t"
    "v_sub_f32 v128, v128, v210\n\t"
    "v_sub_f32 v129, v129, v210\n\t"
    "v_sub_f32 v130, v130, v210\n\t"
    "v_sub_f32 v131, v131, v210\n\t"
    "v_sub_f32 v132, v132, v210\n\t"
    "v_sub_f32 v133, v133, v210\n\t"
    "v_sub_f32 v134, v134, v210\n\t"
    "v_sub_f32 v135, v135, v210\n\t"
    "v_sub_f32 v136, v136, v211\n\t"
    "v_sub_f32 v137, v137, v211\n\t"
    "v_sub_f32 v138, v138, v211\n\t"
    "v_sub_f32 v139, v139, v211\n\t"
    "v_sub_f32 v140, v140, v211\n\t"
    "v_sub_f32 v141, v141, v211\n\t"
    "v_sub_f32 v142, v142, v211\n\t"
    "v_sub_f32 v143, v143, v211\n\t"
    "v_exp_f32 v96, v96\n\t"
    "v_exp_f32 v97, v97\n\t"
    "v_add_f32 v248, v248, v96\n\t"
    "v_exp_f32 v98, v98\n\t"
    "v_add_f32 v248, v248, v97\n\t"
    "v_cvt_pk_bf16_f32 v144, v96, v97\n\t"
    "v_exp_f32 v99, v99\n\t"
    "v_add_f32 v248, v248, v98\n\t"
    "v_exp_f32 v100, v100\n\t"
    "v_add_f32 v248, v248, v99\n\t"
    "v_cvt_pk_bf16_f32 v145, v98, v99\n\t"
    "v_exp_f32 v101, v101\n\t"
    "v_add_f32 v248, v248, v100\n\t"
    "v_exp_f32 v102, v102\n\t"
    "v_add_f32 v248, v248, v101\n\t"
    "v_cvt_pk_bf16_f32 v146, v100, v101\n\t"
    "v_exp_f32 v103, v103\n\t"
    "v_add_f32 v248, v248, v102\n\t"
    "v_exp_f32 v104, v104\n\t"
    "v_exp_f32 v105, v105\n\t"
    "v_add_f32 v248, v248, v103\n\t"
    "v_cvt_pk_bf16_f32 v147, v102, v103\n\t"
    "v_add_f32 v249, v249, v104\n\t"
    "v_exp_f32 v106, v106\n\t"
    "v_add_f32 v249, v249, v105\n\t"
    "v_cvt_pk_bf16_f32 v148, v104, v105\n\t"
    "v_exp_f32 v107, v107\n\t"
    "v_add_f32 v249, v249, v106\n\t"
    "v_exp_f32 v108, v108\n\t"
    "v_add_f32 v249, v249, v107\n\t"
    "v_cvt_pk_bf16_f32 v149, v106, v107\n\t"
    "v_exp_f32 v109, v109\n\t"
    "v_add_f32 v249, v249, v108\n\t"
    "v_exp_f32 v110, v110\n\t"
    "v_add_f32 v249, v249, v109\n\t"
    "v_cvt_pk_bf16_f32 v150, v108, v109\n\t"
    "v_exp_f32 v111, v111\n\t"
    "v_add_f32 v249, v249, v110\n\t"
    "v_exp_f32 v112, v112\n\t"
    "v_exp_f32 v113, v113\n\t"
    "v_add_f32 v249, v249, v111\n\t"
    "v_cvt_pk_bf16_f32 v151, v110, v111\n\t"
    "v_add_f32 v250, v250, v112\n\t"
    "v_exp_f32 v114, v114\n\t"
    "v_add_f32 v250, v250, v113\n\t"
    "v_cvt_pk_bf16_f32 v152, v112, v113\n\t"
    "v_exp_f32 v115, v115\n\t"
    "v_add_f32 v250, v250, v114\n\t"
    "v_exp_f32 v116, v116\n\t"
    "v_add_f32 v250, v250, v115\n\t"
    "v_cvt_pk_bf16_f32 v153, v114, v115\n\t"
    "v_exp_f32 v117, v117\n\t"
    "v_add_f32 v250, v250, v116\n\t"
    "v_exp_f32 v118, v118\n\t"
    "v_add_f32 v250, v250, v117\n\t"
    "v_cvt_pk_bf16_f32 v154, v116, v117\n\t"
    "v_exp_f32 v119, v119\n\t"
    "v_add_f32 v250, v250, v118\n\t"
    "v_add_f32 v250, v250, v119\n\t"
    "v_cvt_pk_bf16_f32 v155, v118, v119\n\t"
    "s_waitcnt lgkmcnt(0)\n\t"
    "s_nop 0\n\t"
    "v_mfma_f32_16x16x32_bf16 v[96:99], a[160:163], v[0:3], v[168:171]\n\t"
    "v_exp_f32_e64 v120, v120\n\t"
    "v_mfma_f32_16x16x32_bf16 v[96:99], a[164:167], v[24:27], v[96:99]\n\t"
    "v_exp_f32_e64 v121, v121\n\t"
    "v_mfma_f32_16x16x32_bf16 v[96:99], a[168:171], v[48:51], v[96:99]\n\t"
    "v_add_f32 v251, v251, v120\n\t"
    "v_exp_f32 v122, v122\n\t"
    "v_mfma_f32_16x16x32_bf16 v[96:99], a[172:175], v[72:75], v[96:99]\n\t"
    "v_add_f32_e64 v251, v251, v121\n\t"
    "v_cvt_pk_bf16_f32 v156, v120, v121\n\t"
    "v_mfma_f32_16x16x32_bf16 v[100:103], a[188:191], v[72:75], v[168:171]\n\t"
    "v_exp_f32_e64 v123, v123\n\t"
    "v_mfma_f32_16x16x32_bf16 v[100:103], a[184:187], v[48:51], v[100:103]\n\t"
    "v_add_f32 v251, v251, v122\n\t"
    "v_exp_f32 v124, v124\n\t"
    "v_mfma_f32_16x16x32_bf16 v[100:103], a[180:183], v[24:27], v[100:103]\n\t"
    "v_add_f32_e64 v251, v251, v123\n\t"
    "v_cvt_pk_bf16_f32 v157, v122, v123\n\t"
    "v_mfma_f32_16x16x32_bf16 v[100:103], a[176:179], v[0:3], v[100:103]\n\t"
    "v_exp_f32_e64 v125, v125\n\t"
    "v_mfma_f32_16x16x32_bf16 v[104:107], a[160:163], v[4:7], v[172:175]\n\t"
    "v_add_f32 v251, v251, v124\n\t"
    "v_exp_f32 v126, v126\n\t"
    "v_mfma_f32_16x16x32_bf16 v[104:107], a[164:167], v[28:31], v[104:107]\n\t"
    "v_add_f32_e64 v251, v251, v125\n\t"
    "v_cvt_pk_bf16_f32 v158, v124, v125\n\t"
    "v_mfma_f32_16x16x32_bf16 v[104:107], a[168:171], v[52:55], v[104:107]\n\t"
    "v_exp_f32_e64 v127, v127\n\t"
    "v_mfma_f32_16x16x32_bf16 v[104:107], a[172:175], v[76:79], v[104:107]\n\t"
    "v_add_f32 v251, v251, v126\n\t"
    "v_add_f32 v251, v251, v127\n\t"
    "v_cvt_pk_bf16_f32 v159, v126, v127\n\t"
    "v_mfma_f32_16x16x32_bf16 v[108:111], a[188:191], v[76:79], v[172:175]\n\t"
    "v_max_f32_e64 v255, v96, v97\n\t"
    "v_mfma_f32_16x16x32_bf16 v[108:111], a[184:187], v[52:55], v[108:111]\n\t"
    "v_max3_f32 v255, v255, v98, v99\n\t"
    "v_exp_f32_e64 v128, v128\n\t"
    "v_mfma_f32_16x16x32_bf16 v[108:111], a[180:183], v[28:31], v[108:111]\n\t"
    "v_max3_f32 v255, v255, v100, v101\n\t"
    "v_exp_f32 v129, v129\n\t"
    "v_add_f32 v252, v252, v128\n\t"
    "v_mfma_f32_16x16x32_bf16 v[108:111], a[176:179], v[4:7], v[108:111]\n\t"
    "v_max3_f32 v255, v255, v102, v103\n\t"
    "v_exp_f32_e64 v130, v130\n\t"
    "v_mfma_f32_16x16x32_bf16 v[112:115], a[160:163], v[8:11], v[176:179]\n\t"
    "v_add_f32_e64 v252, v252, v129\n\t"
    "v_cvt_pk_bf16_f32 v160, v128, v129\n\t"
    "v_mfma_f32_16x16x32_bf16 v[112:115], a[164:167], v[32:35], v[112:115]\n\t"
    "v_exp_f32 v131, v131\n\t"
    "v_add_f32 v252, v252, v130\n\t"
    "v_mfma_f32_16x16x32_bf16 v[112:115], a[168:171], v[56:59], v[112:115]\n\t"
    "v_exp_f32 v132, v132\n\t"
    "v_add_f32 v252, v252, v131\n\t"
    "v_mfma_f32_16x16x32_bf16 v[112:115], a[172:175], v[80:83], v[112:115]\n\t"
    "v_cvt_pk_bf16_f32 v161, v130, v131\n\t"
    "v_exp_f32_e64 v133, v133\n\t"
    "v_mfma_f32_16x16x32_bf16 v[116:119], a[188:191], v[80:83], v[176:179]\n\t"
    "v_max3_f32 v255, v255, v104, v105\n\t"
    "v_add_f32_e64 v252, v252, v132\n\t"
    "v_mfma_f32_16x16x32_bf16 v[116:119], a[184:187], v[56:59], v[116:119]\n\t"
    "v_max3_f32 v255, v255, v106, v107\n\t"
    "v_exp_f32_e64 v134, v134\n\t"
    "v_mfma_f32_16x16x32_bf16 v[116:119], a[180:183], v[32:35], v[116:119]\n\t"
    "v_max3_f32 v255, v255, v108, v109\n\t"
    "v_add_f32_e64 v252, v252, v133\n\t"
    "v_cvt_pk_bf16_f32 v162, v132, v133\n\t"
    "v_mfma_f32_16x16x32_bf16 v[116:119], a[176:179], v[8:11], v[116:119]\n\t"
    "v_max3_f32 v255, v255, v110, v111\n\t"
    "v_exp_f32_e64 v135, v135\n\t"
    "v_mfma_f32_16x16x32_bf16 v[120:123], a[160:163], v[12:15], v[180:183]\n\t"
    "v_add_f32 v252, v252, v134\n\t"
    "v_add_f32 v252, v252, v135\n\t"
    "v_cvt_pk_bf16_f32 v163, v134, v135\n\t"
    "v_mfma_f32_16x16x32_bf16 v[120:123], a[164:167], v[36:39], v[120:123]\n\t"
    "v_exp_f32_e64 v136, v136\n\t"
    "v_mfma_f32_16x16x32_bf16 v[120:123], a[168:171], v[60:63], v[120:123]\n\t"
    "v_exp_f32 v137, v137\n\t"
    "v_add_f32 v253, v253, v136\n\t"
    "v_mfma_f32_16x16x32_bf16 v[120:123], a[172:175], v[84:87], v[120:123]\n\t"
    "v_exp_f32_e64 v138, v138\n\t"
    "v_mfma_f32_16x16x32_bf16 v[124:127], a[188:191], v[84:87], v[180:183]\n\t"
    "v_max3_f32 v255, v255, v112, v113\n\t"
    "v_add_f32_e64 v253, v253, v137\n\t"
    "v_cvt_pk_bf16_f32 v164, v136, v137\n\t"
    "v_mfma_f32_16x16x32_bf16 v[124:127], a[184:187], v[60:63], v[124:127]\n\t"
    "v_max3_f32 v255, v255, v114, v115\n\t"
    "v_exp_f32_e64 v139, v139\n\t"
    "v_mfma_f32_16x16x32_bf16 v[124:127], a[180:183], v[36:39], v[124:127]\n\t"
    "v_max3_f32 v255, v255, v116, v117\n\t"
    "v_add_f32_e64 v253, v253, v138\n\t"
    "v_mfma_f32_16x16x32_bf16 v[124:127], a[176:179], v[12:15], v[124:127]\n\t"
    "v_max3_f32 v255, v255, v118, v119\n\t"
    "v_exp_f32_e64 v140, v140\n\t"
    "v_mfma_f32_16x16x32_bf16 v[128:131], a[160:163], v[16:19], v[184:187]\n\t"
    "v_add_f32_e64 v253, v253, v139\n\t"
    "v_cvt_pk_bf16_f32 v165, v138, v139\n\t"
    "v_mfma_f32_16x16x32_bf16 v[128:131], a[164:167], v[40:43], v[128:131]\n\t"
    "v_exp_f32 v141, v141\n\t"
    "v_add_f32 v253, v253, v140\n\t"
    "v_mfma_f32_16x16x32_bf16 v[128:131], a[168:171], v[64:67], v[128:131]\n\t"
    "v_exp_f32 v142, v142\n\t"
    "v_add_f32 v253, v253, v141\n\t"
    "v_mfma_f32_16x16x32_bf16 v[128:131], a[172:175], v[88:91], v[128:131]\n\t"
    "v_cvt_pk_bf16_f32 v166, v140, v141\n\t"
    "v_mfma_f32_16x16x32_bf16 v[132:135], a[188:191], v[88:91], v[184:187]\n\t"
    "v_max3_f32 v255, v255, v120, v121\n\t"
    "v_exp_f32 v143, v143\n\t"
    "v_add_f32 v253, v253, v142\n\t"
    "v_mfma_f32_16x16x32_bf16 v[132:135], a[184:187], v[64:67], v[132:135]\n\t"
    "v_max3_f32 v255, v255, v122, v123\n\t"
    "v_add_f32_e64 v253, v253, v143\n\t"
    "v_cvt_pk_bf16_f32 v167, v142, v143\n\t"
    "v_mfma_f32_16x16x32_bf16 v[132:135], a[180:183], v[40:43], v[132:135]\n\t"
    "v_max3_f32 v255, v255, v124, v125\n\t"
    "v_mfma_f32_16x16x32_bf16 v[132:135], a[176:179], v[16:19], v[132:135]\n\t"
    "v_max3_f32 v255, v255, v126, v127\n\t"
    "v_mfma_f32_16x16x32_bf16 v[136:139], a[160:163], v[20:23], v[188:191]\n\t"
    "v_mfma_f32_16x16x32_bf16 v[136:139], a[164:167], v[44:47], v[136:139]\n\t"
    "v_mfma_f32_16x16x32_bf16 v[136:139], a[168:171], v[68:71], v[136:139]\n\t"
    "v_mfma_f32_16x16x32_bf16 v[136:139], a[172:175], v[92:95], v[136:139]\n\t"
    "v_mfma_f32_16x16x32_bf16 v[140:143], a[188:191], v[92:95], v[188:191]\n\t"
    "v_max3_f32 v255, v255, v128, v129\n\t"
    "v_mfma_f32_16x16x32_bf16 v[140:143], a[184:187], v[68:71], v[140:143]\n\t"
    "v_max3_f32 v255, v255, v130, v131\n\t"
    "v_mfma_f32_16x16x32_bf16 v[140:143], a[180:183], v[44:47], v[140:143]\n\t"
    "v_max3_f32 v255, v255, v132, v133\n\t"
    "v_mfma_f32_16x16x32_bf16 v[140:143], a[176:179], v[20:23], v[140:143]\n\t"
    "v_max3_f32 v255, v255, v134, v135\n\t"
    "v_mfma_f32_16x16x32_bf16 a[0:3], a[224:227], v[144:147], 0\n\t"
    "s_add_u32 m0, s58, 32768\n\t"
    "s_waitcnt vmcnt(5)\n\t"
    "s_nop 0\n\t"
    "v_mfma_f32_16x16x32_bf16 a[24:27], a[228:231], v[144:147], 0\n\t"
    "buffer_load_dwordx4 v212, s[44:47], s62 offen nt lds\n\t"
    "v_mfma_f32_16x16x32_bf16 a[48:51], a[232:235], v[144:147], 0\n\t"
    "v_mfma_f32_16x16x32_bf16 a[72:75], a[236:239], v[144:147], 0\n\t"
    "v_mfma_f32_16x16x32_bf16 a[96:99], a[240:243], v[144:147], 0\n\t"
    "v_max3_f32 v255, v255, v136, v137\n\t"
    "v_max3_f32 v255, v255, v138, v139\n\t"
    "v_mfma_f32_16x16x32_bf16 a[120:123], a[244:247], v[144:147], 0\n\t"
    "v_max3_f32 v255, v255, v140, v141\n\t"
    "v_max3_f32 v255, v255, v142, v143\n\t"
    "v_mfma_f32_16x16x32_bf16 a[144:147], a[248:251], v[144:147], 0\n\t"
    "v_cmp_lt_f32 vcc, s21, v255\n\t"
    "s_mov_b32 s23, 2\n\t"
    "s_nop 0\n\t"
    "s_cbranch_vccnz .Lk_adj\n\t"
    ".Lk_adjret_p:\n\t"
    "v_mfma_f32_16x16x32_bf16 a[168:171], a[252:255], v[144:147], 0\n\t"
    "v_exp_f32_e64 v96, v96\n\t"
    "v_mfma_f32_16x16x32_bf16 a[172:175], a[252:255], v[148:151], 0\n\t"
    "v_exp_f32_e64 v97, v97\n\t"
    "v_mfma_f32_16x16x32_bf16 a[148:151], a[248:251], v[148:151], 0\n\t"
    "v_add_f32_e64 v248, v248, v96\n\t"
    "v_mfma_f32_16x16x32_bf16 a[124:127], a[244:247], v[148:151], 0\n\t"
    "v_exp_f32 v98, v98\n\t"
    "v_add_f32 v248, v248, v97\n\t"
    "v_cvt_pk_bf16_f32 v144, v96, v97\n\t"
    "v_mfma_f32_16x16x32_bf16 a[100:103], a[240:243], v[148:151], 0\n\t"
    "s_add_u32 m0, s58, 33536\n\t"
    "s_waitcnt vmcnt(5)\n\t"
    "s_nop 0\n\t"
    "v_mfma_f32_16x16x32_bf16 a[76:79], a[236:239], v[148:151], 0\n\t"
    "buffer_load_dwordx4 v213, s[44:47], s62 offen offset:256 nt lds\n\t"
    "v_exp_f32_e64 v99, v99\n\t"
    "v_mfma_f32_16x16x32_bf16 a[52:55], a[232:235], v[148:151], 0\n\t"
    "v_add_f32 v248, v248, v98\n\t"
    "v_exp_f32 v100, v100\n\t"
    "v_mfma_f32_16x16x32_bf16 a[28:31], a[228:231], v[148:151], 0\n\t"
    "v_add_f32_e64 v248, v248, v99\n\t"
    "v_cvt_pk_bf16_f32 v145, v98, v99\n\t"
    "v_mfma_f32_16x16x32_bf16 a[4:7], a[224:227], v[148:151], 0\n\t"
    "v_exp_f32_e64 v101, v101\n\t"
    "v_mfma_f32_16x16x32_bf16 a[8:11], a[224:227], v[152:155], 0\n\t"
    "v_add_f32 v248, v248, v100\n\t"
    "v_exp_f32 v102, v102\n\t"
    "v_mfma_f32_16x16x32_bf16 a[32:35], a[228:231], v[152:155], 0\n\t"
    "v_add_f32_e64 v248, v248, v101\n\t"
    "v_cvt_pk_bf16_f32 v146, v100, v101\n\t"
    "v_mfma_f32_16x16x32_bf16 a[56:59], a[232:235], v[152:155], 0\n\t"
    "v_exp_f32_e64 v103, v103\n\t"
    "v_mfma_f32_16x16x32_bf16 a[80:83], a[236:239], v[152:155], 0\n\t"
    "v_add_f32 v248, v248, v102\n\t"
    "v_add_f32 v248, v248, v103\n\t"
    "v_cvt_pk_bf16_f32 v147, v102, v103\n\t"
    "v_mfma_f32_16x16x32_bf16 a[104:107], a[240:243], v[152:155], 0\n\t"
    "v_exp_f32_e64 v104, v104\n\t"
    "v_mfma_f32_16x16x32_bf16 a[128:131], a[244:247], v[152:155], 0\n\t"
    "v_exp_f32_e64 v105, v105\n\t"
    "v_mfma_f32_16x16x32_bf16 a[152:155], a[248:251], v[152:155], 0\n\t"
    "v_add_f32_e64 v249, v249, v104\n\t"
    "v_mfma_f32_16x16x32_bf16 a[176:179], a[252:255], v[152:155], 0\n\t"
    "s_add_u32 m0, s58, 34304\n\t"
    "s_waitcnt vmcnt(5)\n\t"
    "v_exp_f32 v106, v106\n\t"
    "v_mfma_f32_16x16x32_bf16 a[180:183], a[252:255], v[156:159], 0\n\t"
    "buffer_load_dwordx4 v214, s[44:47], s62 offen offset:512 nt lds\n\t"
    "v_add_f32_e64 v249, v249, v105\n\t"
    "v_mfma_f32_16x16x32_bf16 a[156:159], a[248:251], v[156:159], 0\n\t"
    "v_cvt_pk_bf16_f32 v148, v104, v105\n\t"
    "v_mfma_f32_16x16x32_bf16 a[132:135], a[244:247], v[156:159], 0\n\t"
    "v_exp_f32 v107, v107\n\t"
    "v_add_f32 v249, v249, v106\n\t"
    "v_mfma_f32_16x16x32_bf16 a[108:111], a[240:243], v[156:159], 0\n\t"
    "v_exp_f32 v108, v108\n\t"
    "v_add_f32 v249, v249, v107\n\t"
    "v_mfma_f32_16x16x32_bf16 a[84:87], a[236:239], v[156:159], 0\n\t"
    "v_cvt_pk_bf16_f32 v149, v106, v107\n\t"
    "v_mfma_f32_16x16x32_bf16 a[60:63], a[232:235], v[156:159], 0\n\t"
    "v_exp_f32 v109, v109\n\t"
    "v_add_f32 v249, v249, v108\n\t"
    "v_mfma_f32_16x16x32_bf16 a[36:39], a[228:231], v[156:159], 0\n\t"
    "v_exp_f32_e64 v110, v110\n\t"
    "v_mfma_f32_16x16x32_bf16 a[12:15], a[224:227], v[156:159], 0\n\t"
    "v_add_f32_e64 v249, v249, v109\n\t"
    "v_cvt_pk_bf16_f32 v150, v108, v109\n\t"
    "v_mfma_f32_16x16x32_bf16 a[16:19], a[224:227], v[160:163], 0\n\t"
    "v_exp_f32_e64 v111, v111\n\t"
    "v_mfma_f32_16x16x32_bf16 a[40:43], a[228:231], v[160:163], 0\n\t"
    "v_add_f32 v249, v249, v110\n\t"
    "v_add_f32 v249, v249, v111\n\t"
    "v_cvt_pk_bf16_f32 v151, v110, v111\n\t"
    "v_mfma_f32_16x16x32_bf16 a[64:67], a[232:235], v[160:163], 0\n\t"
    "v_exp_f32_e64 v112, v112\n\t"
    "v_mfma_f32_16x16x32_bf16 a[88:91], a[236:239], v[160:163], 0\n\t"
    "s_add_u32 m0, s58, 35072\n\t"
    "s_waitcnt vmcnt(5)\n\t"
    "s_nop 0\n\t"
    "v_mfma_f32_16x16x32_bf16 a[112:115], a[240:243], v[160:163], 0\n\t"
    "buffer_load_dwordx4 v215, s[44:47], s62 offen offset:768 nt lds\n\t"
    "v_exp_f32_e64 v113, v113\n\t"
    "v_mfma_f32_16x16x32_bf16 a[136:139], a[244:247], v[160:163], 0\n\t"
    "v_add_f32_e64 v250, v250, v112\n\t"
    "v_mfma_f32_16x16x32_bf16 a[160:163], a[248:251], v[160:163], 0\n\t"
    "v_exp_f32 v114, v114\n\t"
    "v_add_f32 v250, v250, v113\n\t"
    "v_mfma_f32_16x16x32_bf16 a[184:187], a[252:255], v[160:163], 0\n\t"
    "v_cvt_pk_bf16_f32 v152, v112, v113\n\t"
    "v_mfma_f32_16x16x32_bf16 a[188:191], a[252:255], v[164:167], 0\n\t"
    "v_exp_f32 v115, v115\n\t"
    "v_add_f32 v250, v250, v114\n\t"
    "v_mfma_f32_16x16x32_bf16 a[164:167], a[248:251], v[164:167], 0\n\t"
    "v_exp_f32 v116, v116\n\t"
    "v_add_f32 v250, v250, v115\n\t"
    "v_mfma_f32_16x16x32_bf16 a[140:143], a[244:247], v[164:167], 0\n\t"
    "v_cvt_pk_bf16_f32 v153, v114, v115\n\t"
    "v_mfma_f32_16x16x32_bf16 a[116:119], a[240:243], v[164:167], 0\n\t"
    "v_exp_f32 v117, v117\n\t"
    "v_add_f32 v250, v250, v116\n\t"
    "v_mfma_f32_16x16x32_bf16 a[92:95], a[236:239], v[164:167], 0\n\t"
    "v_exp_f32_e64 v118, v118\n\t"
    "v_mfma_f32_16x16x32_bf16 a[68:71], a[232:235], v[164:167], 0\n\t"
    "v_add_f32_e64 v250, v250, v117\n\t"
    "v_cvt_pk_bf16_f32 v154, v116, v117\n\t"
    "v_mfma_f32_16x16x32_bf16 a[44:47], a[228:231], v[164:167], 0\n\t"
    "v_exp_f32_e64 v119, v119\n\t"
    "v_mfma_f32_16x16x32_bf16 a[20:23], a[224:227], v[164:167], 0\n\t"
    "v_add_f32 v250, v250, v118\n\t"
    "v_add_f32 v250, v250, v119\n\t"
    "v_cvt_pk_bf16_f32 v155, v118, v119\n\t"
    "s_cmp_lg_u32 s22, 0\n\t"
    "s_cbranch_scc1 .Lk_resc\n\t"
    ".Lk_rescret_p:\n\t"
    "s_waitcnt vmcnt(20)\n\t"
    "s_nop 0\n\t"
    "v_mfma_f32_16x16x32_bf16 v[96:99], a[192:195], v[0:3], v[168:171]\n\t"
    "ds_read_b64_tr_b16 a[224:225], v216\n\t"
    "s_add_u32 m0, s58, 35840\n\t"
    "s_waitcnt vmcnt(5)\n\t"
    "s_nop 0\n\t"
    "v_mfma_f32_16x16x32_bf16 v[96:99], a[196:199], v[24:27], v[96:99]\n\t"
    "buffer_load_dwordx4 v212, s[44:47], s62 offen offset:1024 nt lds\n\t"
    "v_exp_f32_e64 v120, v120\n\t"
    "v_mfma_f32_16x16x32_bf16 v[96:99], a[200:203], v[48:51], v[96:99]\n\t"
    "ds_read_b64_tr_b16 a[226:227], v216 offset:4096\n\t"
    "v_exp_f32 v121, v121\n\t"
    "v_add_f32 v251, v251, v120\n\t"
    "v_mfma_f32_16x16x32_bf16 v[96:99], a[204:207], v[72:75], v[96:99]\n\t"
    "buffer_load_dword v239, v242, s[12:15], s11 offen\n\t"
    "v_exp_f32_e64 v122, v122\n\t"
    "v_mfma_f32_16x16x32_bf16 v[100:103], a[220:223], v[72:75], v[168:171]\n\t"
    "v_add_f32_e64 v251, v251, v121\n\t"
    "v_cvt_pk_bf16_f32 v156, v120, v121\n\t"
    "v_mfma_f32_16x16x32_bf16 v[100:103], a[216:219], v[48:51], v[100:103]\n\t"
    "ds_read_b64_tr_b16 a[228:229], v217\n\t"
    "v_exp_f32 v123, v123\n\t"
    "v_add_f32 v251, v251, v122\n\t"
    "v_mfma_f32_16x16x32_bf16 v[100:103], a[212:215], v[24:27], v[100:103]\n\t"
    "v_exp_f32 v124, v124\n\t"
    "v_add_f32 v251, v251, v123\n\t"
    "v_mfma_f32_16x16x32_bf16 v[100:103], a[208:211], v[0:3], v[100:103]\n\t"
    "ds_read_b64_tr_b16 a[230:231], v217 offset:4096\n\t"
    "v_cvt_pk_bf16_f32 v157, v122, v123\n\t"
    "v_mfma_f32_16x16x32_bf16 v[104:107], a[192:195], v[4:7], v[172:175]\n\t"
    "v_exp_f32 v125, v125\n\t"
    "v_add_f32 v251, v251, v124\n\t"
    "v_mfma_f32_16x16x32_bf16 v[104:107], a[196:199], v[28:31], v[104:107]\n\t"
    "v_exp_f32 v126, v126\n\t"
    "v_add_f32 v251, v251, v125\n\t"
    "v_cvt_pk_bf16_f32 v158, v124, v125\n\t"
    "v_mfma_f32_16x16x32_bf16 v[104:107], a[200:203], v[52:55], v[104:107]\n\t"
    "ds_read_b64_tr_b16 a[232:233], v218\n\t"
    "v_exp_f32_e64 v127, v127\n\t"
    "v_mfma_f32_16x16x32_bf16 v[104:107], a[204:207], v[76:79], v[104:107]\n\t"
    "s_add_u32 m0, s58, 36608\n\t"
    "s_waitcnt vmcnt(5)\n\t"
    "v_add_f32 v251, v251, v126\n\t"
    "v_add_f32_e64 v251, v251, v127\n\t"
    "v_cvt_pk_bf16_f32 v159, v126, v127\n\t"
    "v_mfma_f32_16x16x32_bf16 v[108:111], a[220:223], v[76:79], v[172:175]\n\t"
    "buffer_load_dwordx4 v213, s[44:47], s62 offen offset:1280 nt lds\n\t"
    "v_exp_f32_e64 v128, v128\n\t"
    "v_mfma_f32_16x16x32_bf16 v[108:111], a[216:219], v[52:55], v[108:111]\n\t"
    "ds_read_b64_tr_b16 a[234:235], v218 offset:4096\n\t"
    "v_max_f32_e64 v255, v96, v97\n\t"
    "v_mfma_f32_16x16x32_bf16 v[108:111], a[212:215], v[28:31], v[108:111]\n\t"
    "v_max3_f32 v255, v255, v98, v99\n\t"
    "v_max3_f32 v255, v255, v100, v101\n\t"
    "v_exp_f32 v129, v129\n\t"
    "v_add_f32 v252, v252, v128\n\t"
    "v_mfma_f32_16x16x32_bf16 v[108:111], a[208:211], v[4:7], v[108:111]\n\t"
    "ds_read_b64_tr_b16 a[236:237], v219\n\t"
    "v_max3_f32 v255, v255, v102, v103\n\t"
    "v_mfma_f32_16x16x32_bf16 v[112:115], a[192:195], v[8:11], v[176:179]\n\t"
    "v_exp_f32 v130, v130\n\t"
    "v_add_f32 v252, v252, v129\n\t"
    "v_cvt_pk_bf16_f32 v160, v128, v129\n\t"
    "v_mfma_f32_16x16x32_bf16 v[112:115], a[196:199], v[32:35], v[112:115]\n\t"
    "ds_read_b64_tr_b16 a[238:239], v219 offset:4096\n\t"
    "v_exp_f32 v131, v131\n\t"
    "v_add_f32 v252, v252, v130\n\t"
    "v_mfma_f32_16x16x32_bf16 v[112:115], a[200:203], v[56:59], v[112:115]\n\t"
    "v_exp_f32 v132, v132\n\t"
    "v_add_f32 v252, v252, v131\n\t"
    "v_mfma_f32_16x16x32_bf16 v[112:115], a[204:207], v[80:83], v[112:115]\n\t"
    "v_cvt_pk_bf16_f32 v161, v130, v131\n\t"
    "v_exp_f32 v133, v133\n\t"
    "v_add_f32 v252, v252, v132\n\t"
    "v_mfma_f32_16x16x32_bf16 v[116:119], a[220:223], v[80:83], v[176:179]\n\t"
    "ds_read_b64_tr_b16 a[240:241], v220\n\t"
    "v_exp_f32_e64 v134, v134\n\t"
    "v_mfma_f32_16x16x32_bf16 v[116:119], a[216:219], v[56:59], v[116:119]\n\t"
    "v_max3_f32 v255, v255, v104, v105\n\t"
    "v_max3_f32 v255, v255, v106, v107\n\t"
    "v_add_f32_e64 v252, v252, v133\n\t"
    "v_mfma_f32_16x16x32_bf16 v[116:119], a[212:215], v[32:35], v[116:119]\n\t"
    "ds_read_b64_tr_b16 a[242:243], v220 offset:4096\n\t"
    "v_max3_f32 v255, v255, v108, v109\n\t"
    "v_cvt_pk_bf16_f32 v162, v132, v133\n\t"
    "v_mfma_f32_16x16x32_bf16 v[116:119], a[208:211], v[8:11], v[116:119]\n\t"
    "s_add_u32 m0, s58, 37376\n\t"
    "s_waitcnt vmcnt(5)\n\t"
    "v_max3_f32 v255, v255, v110, v111\n\t"
    "v_exp_f32 v135, v135\n\t"
    "v_mfma_f32_16x16x32_bf16 v[120:123], a[192:195], v[12:15], v[180:183]\n\t"
    "buffer_load_dwordx4 v214, s[44:47], s62 offen offset:1536 nt lds\n\t"
    "v_add_f32 v252, v252, v134\n\t"
    "v_add_f32 v252, v252, v135\n\t"
    "v_cvt_pk_bf16_f32 v163, v134, v135\n\t"
    "v_mfma_f32_16x16x32_bf16 v[120:123], a[196:199], v[36:39], v[120:123]\n\t"
    "ds_read_b64_tr_b16 a[244:245], v221\n\t"
    "v_exp_f32_e64 v136, v136\n\t"
    "v_mfma_f32_16x16x32_bf16 v[120:123], a[200:203], v[60:63], v[120:123]\n\t"
    "v_exp_f32 v137, v137\n\t"
    "v_add_f32 v253, v253, v136\n\t"
    "v_mfma_f32_16x16x32_bf16 v[120:123], a[204:207], v[84:87], v[120:123]\n\t"
    "ds_read_b64_tr_b16 a[246:247], v221 offset:4096\n\t"
    "v_exp_f32_e64 v138, v138\n\t"
    "v_mfma_f32_16x16x32_bf16 v[124:127], a[220:223], v[84:87], v[180:183]\n\t"
    "v_max3_f32 v255, v255, v112, v113\n\t"
    "v_add_f32_e64 v253, v253, v137\n\t"
    "v_cvt_pk_bf16_f32 v164, v136, v137\n\t"
    "v_mfma_f32_16x16x32_bf16 v[124:127], a[216:219], v[60:63], v[124:127]\n\t"
    "v_max3_f32 v255, v255, v114, v115\n\t"
    "v_exp_f32_e64 v139, v139\n\t"
    "v_mfma_f32_16x16x32_bf16 v[124:127], a[212:215], v[36:39], v[124:127]\n\t"
    "ds_read_b64_tr_b16 a[248:249], v222\n\t"
    "v_add_f32_e64 v253, v253, v138\n\t"
    "v_mfma_f32_16x16x32_bf16 v[124:127], a[208:211], v[12:15], v[124:127]\n\t"
    "v_max3_f32 v255, v255, v116, v117\n\t"
    "v_max3_f32 v255, v255, v118, v119\n\t"
    "v_exp_f32 v140, v140\n\t"
    "v_add_f32 v253, v253, v139\n\t"
    "v_mfma_f32_16x16x32_bf16 v[128:131], a[192:195], v[16:19], v[184:187]\n\t"
    "ds_read_b64_tr_b16 a[250:251], v222 offset:4096\n\t"
    "v_cvt_pk_bf16_f32 v165, v138, v139\n\t"
    "v_mfma_f32_16x16x32_bf16 v[128:131], a[196:199], v[40:43], v[128:131]\n\t"
    "v_exp_f32 v141, v141\n\t"
    "v_add_f32 v253, v253, v140\n\t"
    "v_mfma_f32_16x16x32_bf16 v[128:131], a[200:203], v[64:67], v[128:131]\n\t"
    "v_exp_f32 v142, v142\n\t"
    "v_add_f32 v253, v253, v141\n\t"
    "v_cvt_pk_bf16_f32 v166, v140, v141\n\t"
    "v_mfma_f32_16x16x32_bf16 v[128:131], a[204:207], v[88:91], v[128:131]\n\t"
    "ds_read_b64_tr_b16 a[252:253], v223\n\t"
    "s_add_u32 m0, s58, 38144\n\t"
    "s_waitcnt vmcnt(5)\n\t"
    "s_nop 0\n\t"
    "v_mfma_f32_16x16x32_bf16 v[132:135], a[220:223], v[88:91], v[184:187]\n\t"
    "buffer_load_dwordx4 v215, s[44:47], s62 offen offset:1792 nt lds\n\t"
    "s_or_b32 s62, s62, 12288\n\t"
    "s_add_u32 s62, s62, 2048\n\t"
    "s_and_b32 s62, s62, 116736\n\t"
    "v_exp_f32 v143, v143\n\t"
    "v_add_f32 v253, v253, v142\n\t"
    "v_mfma_f32_16x16x32_bf16 v[132:135], a[216:219], v[64:67], v[132:135]\n\t"
    "ds_read_b64_tr_b16 a[254:255], v223 offset:4096\n\t"
    "v_max3_f32 v255, v255, v120, v121\n\t"
    "v_add_f32_e64 v253, v253, v143\n\t"
    "v_cvt_pk_bf16_f32 v167, v142, v143\n\t"
    "v_mfma_f32_16x16x32_bf16 v[132:135], a[212:215], v[40:43], v[132:135]\n\t"
    "v_max3_f32 v255, v255, v122, v123\n\t"
    "v_mfma_f32_16x16x32_bf16 v[132:135], a[208:211], v[16:19], v[132:135]\n\t"
    "v_max3_f32 v255, v255, v124, v125\n\t"
    "v_max3_f32 v255, v255, v126, v127\n\t"
    "v_mfma_f32_16x16x32_bf16 v[136:139], a[192:195], v[20:23], v[188:191]\n\t"
    "v_mfma_f32_16x16x32_bf16 v[136:139], a[196:199], v[44:47], v[136:139]\n\t"
    "v_mfma_f32_16x16x32_bf16 v[136:139], a[200:203], v[68:71], v[136:139]\n\t"
    "v_mfma_f32_16x16x32_bf16 v[136:139], a[204:207], v[92:95], v[136:139]\n\t"
    "v_mfma_f32_16x16x32_bf16 v[140:143], a[220:223], v[92:95], v[188:191]\n\t"
    "v_max3_f32 v255, v255, v128, v129\n\t"
    "v_mfma_f32_16x16x32_bf16 v[140:143], a[216:219], v[68:71], v[140:143]\n\t"
    "v_max3_f32 v255, v255, v130, v131\n\t"
    "v_mfma_f32_16x16x32_bf16 v[140:143], a[212:215], v[44:47], v[140:143]\n\t"
    "v_max3_f32 v255, v255, v132, v133\n\t"
    "v_mfma_f32_16x16x32_bf16 v[140:143], a[208:211], v[20:23], v[140:143]\n\t"
    "v_max3_f32 v255, v255, v134, v135\n\t"
    "s_waitcnt vmcnt(16) lgkmcnt(0)\n\t"
    "s_nop 0\n\t"
    "v_mfma_f32_16x16x32_bf16 a[0:3], a[224:227], v[144:147], a[0:3]\n\t"
    "ds_read_b128 a[192:195], v224\n\t"
    "s_add_u32 m0, s59, 0\n\t"
    "s_waitcnt vmcnt(4)\n\t"
    "v_mfma_f32_16x16x32_bf16 a[24:27], a[228:231], v[144:147], a[24:27]\n\t"
    "buffer_load_dwordx4 v228, s[48:51], s18 offen nt lds\n\t"
    "v_mfma_f32_16x16x32_bf16 a[48:51], a[232:235], v[144:147], a[48:51]\n\t"
    "v_mfma_f32_16x16x32_bf16 a[72:75], a[236:239], v[144:147], a[72:75]\n\t"
    "v_mfma_f32_16x16x32_bf16 a[96:99], a[240:243], v[144:147], a[96:99]\n\t"
    "ds_read_b128 a[196:199], v225\n\t"
    "v_max3_f32 v255, v255, v136, v137\n\t"
    "v_max3_f32 v255, v255, v138, v139\n\t"
    "v_mfma_f32_16x16x32_bf16 a[120:123], a[244:247], v[144:147], a[120:123]\n\t"
    "s_add_u32 m0, s59, 768\n\t"
    "s_waitcnt vmcnt(5)\n\t"
    "s_nop 0\n\t"
    "v_max3_f32 v255, v255, v140, v141\n\t"
    "v_max3_f32 v255, v255, v142, v143\n\t"
    "v_mfma_f32_16x16x32_bf16 a[144:147], a[248:251], v[144:147], a[144:147]\n\t"
    "buffer_load_dwordx4 v238, s[48:51], s18 offen offset:256 nt lds\n\t"
    "v_cmp_lt_f32 vcc, s21, v255\n\t"
    "s_mov_b32 s23, 5\n\t"
    "s_nop 0\n\t"
    "s_cbranch_vccnz .Lk_adj\n\t"
    ".Lk_adjret_s2:\n\t"
    "v_mfma_f32_16x16x32_bf16 a[168:171], a[252:255], v[144:147], a[168:171]\n\t"
    "v_exp_f32_e64 v96, v96\n\t"
    "v_mfma_f32_16x16x32_bf16 a[172:175], a[252:255], v[148:151], a[172:175]\n\t"
    "v_exp_f32 v97, v97\n\t"
    "v_add_f32 v248, v248, v96\n\t"
    "v_mfma_f32_16x16x32_bf16 a[148:151], a[248:251], v[148:151], a[148:151]\n\t"
    "ds_read_b128 a[200:203], v226\n\t"
    "v_exp_f32_e64 v98, v98\n\t"
    "v_mfma_f32_16x16x32_bf16 a[124:127], a[244:247], v[148:151], a[124:127]\n\t"
    "v_add_f32_e64 v248, v248, v97\n\t"
    "v_cvt_pk_bf16_f32 v144, v96, v97\n\t"
    "v_mfma_f32_16x16x32_bf16 a[100:103], a[240:243], v[148:151], a[100:103]\n\t"
    "s_add_u32 m0, s59, 1536\n\t"
    "s_waitcnt vmcnt(4)\n\t"
    "v_exp_f32 v99, v99\n\t"
    "v_mfma_f32_16x16x32_bf16 a[76:79], a[236:239], v[148:151], a[76:79]\n\t"
    "buffer_load_dwordx4 v228, s[48:51], s18 offen offset:512 nt lds\n\t"
    "v_add_f32_e64 v248, v248, v98\n\t"
    "v_mfma_f32_16x16x32_bf16 a[52:55], a[232:235], v[148:151], a[52:55]\n\t"
    "ds_read_b128 a[204:207], v227\n\t"
    "v_exp_f32_e64 v100, v100\n\t"
    "v_mfma_f32_16x16x32_bf16 a[28:31], a[228:231], v[148:151], a[28:31]\n\t"
    "v_add_f32_e64 v248, v248, v99\n\t"
    "v_cvt_pk_bf16_f32 v145, v98, v99\n\t"
    "v_mfma_f32_16x16x32_bf16 a[4:7], a[224:227], v[148:151], a[4:7]\n\t"
    "v_exp_f32 v101, v101\n\t"
    "v_add_f32 v248, v248, v100\n\t"
    "v_mfma_f32_16x16x32_bf16 a[8:11], a[224:227], v[152:155], a[8:11]\n\t"
    "v_exp_f32 v102, v102\n\t"
    "v_add_f32 v248, v248, v101\n\t"
    "v_mfma_f32_16x16x32_bf16 a[32:35], a[228:231], v[152:155], a[32:35]\n\t"
    "s_add_u32 m0, s59, 2304\n\t"
    "s_waitcnt vmcnt(5)\n\t"
    "s_nop 0\n\t"
    "v_cvt_pk_bf16_f32 v146, v100, v101\n\t"
    "v_mfma_f32_16x16x32_bf16 a[56:59], a[232:235], v[152:155], a[56:59]\n\t"
    "buffer_load_dwordx4 v238, s[48:51], s18 offen offset:768 nt lds\n\t"
    "v_exp_f32 v103, v103\n\t"
    "v_add_f32 v248, v248, v102\n\t"
    "v_mfma_f32_16x16x32_bf16 a[80:83], a[236:239], v[152:155], a[80:83]\n\t"
    "ds_read_b128 a[208:211], v224 offset:4096\n\t"
    "v_add_f32_e64 v248, v248, v103\n\t"
    "v_cvt_pk_bf16_f32 v147, v102, v103\n\t"
    "v_mfma_f32_16x16x32_bf16 a[104:107], a[240:243], v[152:155], a[104:107]\n\t"
    "v_exp_f32_e64 v104, v104\n\t"
    "v_mfma_f32_16x16x32_bf16 a[128:131], a[244:247], v[152:155], a[128:131]\n\t"
    "v_exp_f32 v105, v105\n\t"
    "v_add_f32 v249, v249, v104\n\t"
    "v_mfma_f32_16x16x32_bf16 a[152:155], a[248:251], v[152:155], a[152:155]\n\t"
    "ds_read_b128 a[212:215], v225 offset:4096\n\t"
    "v_mfma_f32_16x16x32_bf16 a[176:179], a[252:255], v[152:155], a[176:179]\n\t"
    "s_add_u32 m0, s59, 3072\n\t"
    "s_waitcnt vmcnt(4)\n\t"
    "v_exp_f32 v106, v106\n\t"
    "v_add_f32_e64 v249, v249, v105\n\t"
    "v_mfma_f32_16x16x32_bf16 a[180:183], a[252:255], v[156:159], a[180:183]\n\t"
    "buffer_load_dwordx4 v228, s[48:51], s18 offen offset:1024 nt lds\n\t"
    "v_cvt_pk_bf16_f32 v148, v104, v105\n\t"
    "v_mfma_f32_16x16x32_bf16 a[156:159], a[248:251], v[156:159], a[156:159]\n\t"
    "v_exp_f32 v107, v107\n\t"
    "v_add_f32 v249, v249, v106\n\t"
    "v_mfma_f32_16x16x32_bf16 a[132:135], a[244:247], v[156:159], a[132:135]\n\t"
    "v_exp_f32_e64 v108, v108\n\t"
    "v_mfma_f32_16x16x32_bf16 a[108:111], a[240:243], v[156:159], a[108:111]\n\t"
    "ds_read_b128 a[216:219], v226 offset:4096\n\t"
    "v_add_f32_e64 v249, v249, v107\n\t"
    "v_cvt_pk_bf16_f32 v149, v106, v107\n\t"
    "v_mfma_f32_16x16x32_bf16 a[84:87], a[236:239], v[156:159], a[84:87]\n\t"
    "v_exp_f32_e64 v109, v109\n\t"
    "v_mfma_f32_16x16x32_bf16 a[60:63], a[232:235], v[156:159], a[60:63]\n\t"
    "s_add_u32 m0, s59, 3840\n\t"
    "s_waitcnt vmcnt(5)\n\t"
    "v_add_f32 v249, v249, v108\n\t"
    "v_mfma_f32_16x16x32_bf16 a[36:39], a[228:231], v[156:159], a[36:39]\n\t"
    "buffer_load_dwordx4 v238, s[48:51], s18 offen offset:1280 nt lds\n\t"
    "v_exp_f32_e64 v110, v110\n\t"
    "v_mfma_f32_16x16x32_bf16 a[12:15], a[224:227], v[156:159], a[12:15]\n\t"
    "ds_read_b128 a[220:223], v227 offset:4096\n\t"
    "v_add_f32_e64 v249, v249, v109\n\t"
    "v_cvt_pk_bf16_f32 v150, v108, v109\n\t"
    "v_mfma_f32_16x16x32_bf16 a[16:19], a[224:227], v[160:163], a[16:19]\n\t"
    "v_exp_f32_e64 v111, v111\n\t"
    "v_mfma_f32_16x16x32_bf16 a[40:43], a[228:231], v[160:163], a[40:43]\n\t"
    "v_add_f32 v249, v249, v110\n\t"
    "v_add_f32 v249, v249, v111\n\t"
    "v_cvt_pk_bf16_f32 v151, v110, v111\n\t"
    "v_mfma_f32_16x16x32_bf16 a[64:67], a[232:235], v[160:163], a[64:67]\n\t"
    "v_exp_f32_e64 v112, v112\n\t"
    "v_mfma_f32_16x16x32_bf16 a[88:91], a[236:239], v[160:163], a[88:91]\n\t"
    "s_add_u32 m0, s59, 4608\n\t"
    "s_waitcnt vmcnt(4)\n\t"
    "s_nop 0\n\t"
    "v_mfma_f32_16x16x32_bf16 a[112:115], a[240:243], v[160:163], a[112:115]\n\t"
    "buffer_load_dwordx4 v228, s[48:51], s18 offen offset:1536 nt lds\n\t"
    "v_exp_f32_e64 v113, v113\n\t"
    "v_mfma_f32_16x16x32_bf16 a[136:139], a[244:247], v[160:163], a[136:139]\n\t"
    "v_add_f32 v250, v250, v112\n\t"
    "v_exp_f32 v114, v114\n\t"
    "v_mfma_f32_16x16x32_bf16 a[160:163], a[248:251], v[160:163], a[160:163]\n\t"
    "v_add_f32_e64 v250, v250, v113\n\t"
    "v_cvt_pk_bf16_f32 v152, v112, v113\n\t"
    "v_mfma_f32_16x16x32_bf16 a[184:187], a[252:255], v[160:163], a[184:187]\n\t"
    "v_exp_f32_e64 v115, v115\n\t"
    "v_mfma_f32_16x16x32_bf16 a[188:191], a[252:255], v[164:167], a[188:191]\n\t"
    "v_add_f32 v250, v250, v114\n\t"
    "v_exp_f32 v116, v116\n\t"
    "v_mfma_f32_16x16x32_bf16 a[164:167], a[248:251], v[164:167], a[164:167]\n\t"
    "s_add_u32 m0, s59, 5376\n\t"
    "s_waitcnt vmcnt(5)\n\t"
    "v_add_f32 v250, v250, v115\n\t"
    "v_mfma_f32_16x16x32_bf16 a[140:143], a[244:247], v[164:167], a[140:143]\n\t"
    "buffer_load_dwordx4 v238, s[48:51], s18 offen offset:1792 nt lds\n\t"
    "s_or_b32 s18, s18, 12288\n\t"
    "s_add_u32 s18, s18, 2048\n\t"
    "s_and_b32 s18, s18, 116736\n\t"
    "v_cvt_pk_bf16_f32 v153, v114, v115\n\t"
    "v_mfma_f32_16x16x32_bf16 a[116:119], a[240:243], v[164:167], a[116:119]\n\t"
    "v_exp_f32_e64 v117, v117\n\t"
    "v_mfma_f32_16x16x32_bf16 a[92:95], a[236:239], v[164:167], a[92:95]\n\t"
    "v_add_f32 v250, v250, v116\n\t"
    "v_exp_f32 v118, v118\n\t"
    "v_mfma_f32_16x16x32_bf16 a[68:71], a[232:235], v[164:167], a[68:71]\n\t"
    "v_add_f32_e64 v250, v250, v117\n\t"
    "v_cvt_pk_bf16_f32 v154, v116, v117\n\t"
    "v_mfma_f32_16x16x32_bf16 a[44:47], a[228:231], v[164:167], a[44:47]\n\t"
    "v_exp_f32_e64 v119, v119\n\t"
    "v_mfma_f32_16x16x32_bf16 a[20:23], a[224:227], v[164:167], a[20:23]\n\t"
    "v_add_f32 v250, v250, v118\n\t"
    "v_add_f32 v250, v250, v119\n\t"
    "v_cvt_pk_bf16_f32 v155, v118, v119\n\t"
    "s_cmp_lg_u32 s22, 0\n\t"
    "s_cbranch_scc1 .Lk_resc\n\t"
    ".Lk_rescret_s2:\n\t"
    "s_waitcnt vmcnt(16) lgkmcnt(0)\n\t"
    "s_nop 0\n\t"
    "v_mfma_f32_16x16x32_bf16 v[96:99], a[192:195], v[0:3], v[168:171]\n\t"
    "ds_read_b64_tr_b16 a[224:225], v216 offset:32768\n\t"
    "s_add_u32 m0, s58, 0\n\t"
    "s_waitcnt vmcnt(4)\n\t"
    "v_mfma_f32_16x16x32_bf16 v[96:99], a[196:199], v[24:27], v[96:99]\n\t"
    "buffer_load_dwordx4 v212, s[44:47], s62 offen nt lds\n\t"
    "v_exp_f32_e64 v120, v120\n\t"
    "v_mfma_f32_16x16x32_bf16 v[96:99], a[200:203], v[48:51], v[96:99]\n\t"
    "ds_read_b64_tr_b16 a[226:227], v216 offset:36864\n\t"
    "v_exp_f32 v121, v121\n\t"
    "v_add_f32 v251, v251, v120\n\t"
    "v_mfma_f32_16x16x32_bf16 v[96:99], a[204:207], v[72:75], v[96:99]\n\t"
    "v_exp_f32 v122, v122\n\t"
    "v_add_f32 v251, v251, v121\n\t"
    "v_mfma_f32_16x16x32_bf16 v[100:103], a[220:223], v[72:75], v[168:171]\n\t"
    "v_cvt_pk_bf16_f32 v156, v120, v121\n\t"
    "v_exp_f32_e64 v123, v123\n\t"
    "v_mfma_f32_16x16x32_bf16 v[100:103], a[216:219], v[48:51], v[100:103]\n\t"
    "ds_read_b64_tr_b16 a[228:229], v217 offset:32768\n\t"
    "s_add_u32 m0, s58, 768\n\t"
    "s_waitcnt vmcnt(5)\n\t"
    "v_add_f32 v251, v251, v122\n\t"
    "v_mfma_f32_16x16x32_bf16 v[100:103], a[212:215], v[24:27], v[100:103]\n\t"
    "buffer_load_dwordx4 v213, s[44:47], s62 offen offset:256 nt lds\n\t"
    "v_exp_f32 v124, v124\n\t"
    "v_add_f32 v251, v251, v123\n\t"
    "v_mfma_f32_16x16x32_bf16 v[100:103], a[208:211], v[0:3], v[100:103]\n\t"
    "ds_read_b64_tr_b16 a[230:231], v217 offset:36864\n\t"
    "v_cvt_pk_bf16_f32 v157, v122, v123\n\t"
    "v_mfma_f32_16x16x32_bf16 v[104:107], a[192:195], v[4:7], v[172:175]\n\t"
    "v_exp_f32 v125, v125\n\t"
    "v_add_f32 v251, v251, v124\n\t"
    "v_mfma_f32_16x16x32_bf16 v[104:107], a[196:199], v[28:31], v[104:107]\n\t"
    "v_exp_f32 v126, v126\n\t"
    "v_add_f32 v251, v251, v125\n\t"
    "v_cvt_pk_bf16_f32 v158, v124, v125\n\t"
    "v_mfma_f32_16x16x32_bf16 v[104:107], a[200:203], v[52:55], v[104:107]\n\t"
    "ds_read_b64_tr_b16 a[232:233], v218 offset:32768\n\t"
    "v_exp_f32_e64 v127, v127\n\t"
    "v_mfma_f32_16x16x32_bf16 v[104:107], a[204:207], v[76:79], v[104:107]\n\t"
    "s_add_u32 m0, s58, 1536\n\t"
    "s_waitcnt vmcnt(4)\n\t"
    "v_add_f32 v251, v251, v126\n\t"
    "v_add_f32_e64 v251, v251, v127\n\t"
    "v_cvt_pk_bf16_f32 v159, v126, v127\n\t"
    "v_mfma_f32_16x16x32_bf16 v[108:111], a[220:223], v[76:79], v[172:175]\n\t"
    "buffer_load_dwordx4 v214, s[44:47], s62 offen offset:512 nt lds\n\t"
    "v_exp_f32_e64 v128, v128\n\t"
    "v_mfma_f32_16x16x32_bf16 v[108:111], a[216:219], v[52:55], v[108:111]\n\t"
    "ds_read_b64_tr_b16 a[234:235], v218 offset:36864\n\t"
    "v_max_f32 v255, v96, v97\n\t"
    "v_exp_f32 v129, v129\n\t"
    "v_mfma_f32_16x16x32_bf16 v[108:111], a[212:215], v[28:31], v[108:111]\n\t"
    "v_max3_f32 v255, v255, v98, v99\n\t"
    "v_max3_f32 v255, v255, v100, v101\n\t"
    "v_add_f32_e64 v252, v252, v128\n\t"
    "v_mfma_f32_16x16x32_bf16 v[108:111], a[208:211], v[4:7], v[108:111]\n\t"
    "ds_read_b64_tr_b16 a[236:237], v219 offset:32768\n\t"
    "v_max3_f32 v255, v255, v102, v103\n\t"
    "v_exp_f32_e64 v130, v130\n\t"
    "v_mfma_f32_16x16x32_bf16 v[112:115], a[192:195], v[8:11], v[176:179]\n\t"
    "v_add_f32_e64 v252, v252, v129\n\t"
    "v_cvt_pk_bf16_f32 v160, v128, v129\n\t"
    "v_mfma_f32_16x16x32_bf16 v[112:115], a[196:199], v[32:35], v[112:115]\n\t"
    "ds_read_b64_tr_b16 a[238:239], v219 offset:36864\n\t"
    "s_add_u32 m0, s58, 2304\n\t"
    "s_waitcnt vmcnt(5)\n\t"
    "v_exp_f32 v131, v131\n\t"
    "v_add_f32_e64 v252, v252, v130\n\t"
    "v_mfma_f32_16x16x32_bf16 v[112:115], a[200:203], v[56:59], v[112:115]\n\t"
    "buffer_load_dwordx4 v215, s[44:47], s62 offen offset:768 nt lds\n\t"
    "v_exp_f32_e64 v132, v132\n\t"
    "v_mfma_f32_16x16x32_bf16 v[112:115], a[204:207], v[80:83], v[112:115]\n\t"
    "v_add_f32 v252, v252, v131\n\t"
    "v_cvt_pk_bf16_f32 v161, v130, v131\n\t"
    "v_exp_f32 v133, v133\n\t"
    "v_mfma_f32_16x16x32_bf16 v[116:119], a[220:223], v[80:83], v[176:179]\n\t"
    "ds_read_b64_tr_b16 a[240:241], v220 offset:32768\n\t"
    "v_add_f32_e64 v252, v252, v132\n\t"
    "v_mfma_f32_16x16x32_bf16 v[116:119], a[216:219], v[56:59], v[116:119]\n\t"
    "v_max3_f32 v255, v255, v104, v105\n\t"
    "v_max3_f32 v255, v255, v106, v107\n\t"
    "v_exp_f32 v134, v134\n\t"
    "v_add_f32 v252, v252, v133\n\t"
    "v_mfma_f32_16x16x32_bf16 v[116:119], a[212:215], v[32:35], v[116:119]\n\t"
    "ds_read_b64_tr_b16 a[242:243], v220 offset:36864\n\t"
    "v_max3_f32 v255, v255, v108, v109\n\t"
    "v_cvt_pk_bf16_f32 v162, v132, v133\n\t"
    "v_mfma_f32_16x16x32_bf16 v[116:119], a[208:211], v[8:11], v[116:119]\n\t"
    "s_add_u32 m0, s58, 3072\n\t"
    "s_waitcnt vmcnt(4)\n\t"
    "v_max3_f32 v255, v255, v110, v111\n\t"
    "v_exp_f32 v135, v135\n\t"
    "v_mfma_f32_16x16x32_bf16 v[120:123], a[192:195], v[12:15], v[180:183]\n\t"
    "buffer_load_dwordx4 v212, s[44:47], s62 offen offset:1024 nt lds\n\t"
    "v_add_f32 v252, v252, v134\n\t"
    "v_add_f32 v252, v252, v135\n\t"
    "v_cvt_pk_bf16_f32 v163, v134, v135\n\t"
    "v_mfma_f32_16x16x32_bf16 v[120:123], a[196:199], v[36:39], v[120:123]\n\t"
    "ds_read_b64_tr_b16 a[244:245], v221 offset:32768\n\t"
    "v_exp_f32_e64 v136, v136\n\t"
    "v_mfma_f32_16x16x32_bf16 v[120:123], a[200:203], v[60:63], v[120:123]\n\t"
    "v_exp_f32 v137, v137\n\t"
    "v_add_f32 v253, v253, v136\n\t"
    "v_mfma_f32_16x16x32_bf16 v[120:123], a[204:207], v[84:87], v[120:123]\n\t"
    "ds_read_b64_tr_b16 a[246:247], v221 offset:36864\n\t"
    "v_exp_f32_e64 v138, v138\n\t"
    "v_mfma_f32_16x16x32_bf16 v[124:127], a[220:223], v[84:87], v[180:183]\n\t"
    "v_max3_f32 v255, v255, v112, v113\n\t"
    "v_add_f32_e64 v253, v253, v137\n\t"
    "v_cvt_pk_bf16_f32 v164, v136, v137\n\t"
    "v_mfma_f32_16x16x32_bf16 v[124:127], a[216:219], v[60:63], v[124:127]\n\t"
    "s_add_u32 m0, s58, 3840\n\t"
    "s_waitcnt vmcnt(5)\n\t"
    "v_max3_f32 v255, v255, v114, v115\n\t"
    "v_exp_f32 v139, v139\n\t"
    "v_mfma_f32_16x16x32_bf16 v[124:127], a[212:215], v[36:39], v[124:127]\n\t"
    "buffer_load_dwordx4 v213, s[44:47], s62 offen offset:1280 nt lds\n\t"
    "v_max3_f32 v255, v255, v116, v117\n\t"
    "v_add_f32_e64 v253, v253, v138\n\t"
    "v_mfma_f32_16x16x32_bf16 v[124:127], a[208:211], v[12:15], v[124:127]\n\t"
    "ds_read_b64_tr_b16 a[248:249], v222 offset:32768\n\t"
    "v_max3_f32 v255, v255, v118, v119\n\t"
    "v_exp_f32_e64 v140, v140\n\t"
    "v_mfma_f32_16x16x32_bf16 v[128:131], a[192:195], v[16:19], v[184:187]\n\t"
    "ds_read_b64_tr_b16 a[250:251], v222 offset:36864\n\t"
    "v_add_f32_e64 v253, v253, v139\n\t"
    "v_cvt_pk_bf16_f32 v165, v138, v139\n\t"
    "v_mfma_f32_16x16x32_bf16 v[128:131], a[196:199], v[40:43], v[128:131]\n\t"
    "v_exp_f32 v141, v141\n\t"
    "v_add_f32 v253, v253, v140\n\t"
    "v_mfma_f32_16x16x32_bf16 v[128:131], a[200:203], v[64:67], v[128:131]\n\t"
    "v_exp_f32 v142, v142\n\t"
    "v_add_f32 v253, v253, v141\n\t"
    "v_cvt_pk_bf16_f32 v166, v140, v141\n\t"
    "v_mfma_f32_16x16x32_bf16 v[128:131], a[204:207], v[88:91], v[128:131]\n\t"
    "ds_read_b64_tr_b16 a[252:253], v223 offset:32768\n\t"
    "s_add_u32 m0, s58, 4608\n\t"
    "s_waitcnt vmcnt(4)\n\t"
    "s_nop 0\n\t"
    "v_mfma_f32_16x16x32_bf16 v[132:135], a[220:223], v[88:91], v[184:187]\n\t"
    "buffer_load_dwordx4 v214, s[44:47], s62 offen offset:1536 nt lds\n\t"
    "v_exp_f32 v143, v143\n\t"
    "v_add_f32 v253, v253, v142\n\t"
    "v_mfma_f32_16x16x32_bf16 v[132:135], a[216:219], v[64:67], v[132:135]\n\t"
    "ds_read_b64_tr_b16 a[254:255], v223 offset:36864\n\t"
    "v_max3_f32 v255, v255, v120, v121\n\t"
    "v_add_f32_e64 v253, v253, v143\n\t"
    "v_cvt_pk_bf16_f32 v167, v142, v143\n\t"
    "v_mfma_f32_16x16x32_bf16 v[132:135], a[212:215], v[40:43], v[132:135]\n\t"
    "v_max3_f32 v255, v255, v122, v123\n\t"
    "v_mfma_f32_16x16x32_bf16 v[132:135], a[208:211], v[16:19], v[132:135]\n\t"
    "v_max3_f32 v255, v255, v124, v125\n\t"
    "v_max3_f32 v255, v255, v126, v127\n\t"
    "v_mfma_f32_16x16x32_bf16 v[136:139], a[192:195], v[20:23], v[188:191]\n\t"
    "v_mfma_f32_16x16x32_bf16 v[136:139], a[196:199], v[44:47], v[136:139]\n\t"
    "s_add_u32 m0, s58, 5376\n\t"
    "s_waitcnt vmcnt(5)\n\t"
    "s_nop 0\n\t"
    "v_mfma_f32_16x16x32_bf16 v[136:139], a[200:203], v[68:71], v[136:139]\n\t"
    "buffer_load_dwordx4 v215, s[44:47], s62 offen offset:1792 nt lds\n\t"
    "s_or_b32 s62, s62, 12288\n\t"
    "s_add_u32 s62, s62, 2048\n\t"
    "s_and_b32 s62, s62, 116736\n\t"
    "v_mfma_f32_16x16x32_bf16 v[136:139], a[204:207], v[92:95], v[136:139]\n\t"
    "v_mfma_f32_16x16x32_bf16 v[140:143], a[220:223], v[92:95], v[188:191]\n\t"
    "v_max3_f32 v255, v255, v128, v129\n\t"
    "v_mfma_f32_16x16x32_bf16 v[140:143], a[216:219], v[68:71], v[140:143]\n\t"
    "v_max3_f32 v255, v255, v130, v131\n\t"
    "v_mfma_f32_16x16x32_bf16 v[140:143], a[212:215], v[44:47], v[140:143]\n\t"
    "v_max3_f32 v255, v255, v132, v133\n\t"
    "v_mfma_f32_16x16x32_bf16 v[140:143], a[208:211], v[20:23], v[140:143]\n\t"
    "v_max3_f32 v255, v255, v134, v135\n\t"
    "s_waitcnt vmcnt(16) lgkmcnt(0)\n\t"
    "s_nop 0\n\t"
    "v_mfma_f32_16x16x32_bf16 a[0:3], a[224:227], v[144:147], a[0:3]\n\t"
    "ds_read_b128 a[192:195], v224 offset:32768\n\t"
    "s_add_u32 m0, s59, 32768\n\t"
    "s_waitcnt vmcnt(4)\n\t"
    "s_nop 0\n\t"
    "v_mfma_f32_16x16x32_bf16 a[24:27], a[228:231], v[144:147], a[24:27]\n\t"
    "buffer_load_dwordx4 v228, s[48:51], s18 offen nt lds\n\t"
    "v_mfma_f32_16x16x32_bf16 a[48:51], a[232:235], v[144:147], a[48:51]\n\t"
    "v_mfma_f32_16x16x32_bf16 a[72:75], a[236:239], v[144:147], a[72:75]\n\t"
    "v_mfma_f32_16x16x32_bf16 a[96:99], a[240:243], v[144:147], a[96:99]\n\t"
    "ds_read_b128 a[196:199], v225 offset:32768\n\t"
    "v_max3_f32 v255, v255, v136, v137\n\t"
    "v_max3_f32 v255, v255, v138, v139\n\t"
    "v_mfma_f32_16x16x32_bf16 a[120:123], a[244:247], v[144:147], a[120:123]\n\t"
    "s_add_u32 m0, s59, 33536\n\t"
    "s_waitcnt vmcnt(5)\n\t"
    "s_nop 0\n\t"
    "v_max3_f32 v255, v255, v140, v141\n\t"
    "v_max3_f32 v255, v255, v142, v143\n\t"
    "v_mfma_f32_16x16x32_bf16 a[144:147], a[248:251], v[144:147], a[144:147]\n\t"
    "buffer_load_dwordx4 v238, s[48:51], s18 offen offset:256 nt lds\n\t"
    "v_cmp_lt_f32 vcc, s21, v255\n\t"
    "s_mov_b32 s23, 6\n\t"
    "s_nop 0\n\t"
    "s_cbranch_vccnz .Lk_adj\n\t"
    ".Lk_adjret_s3:\n\t"
    "v_mfma_f32_16x16x32_bf16 a[168:171], a[252:255], v[144:147], a[168:171]\n\t"
    "v_exp_f32_e64 v96, v96\n\t"
    "v_mfma_f32_16x16x32_bf16 a[172:175], a[252:255], v[148:151], a[172:175]\n\t"
    "v_exp_f32 v97, v97\n\t"
    "v_add_f32 v248, v248, v96\n\t"
    "v_mfma_f32_16x16x32_bf16 a[148:151], a[248:251], v[148:151], a[148:151]\n\t"
    "ds_read_b128 a[200:203], v226 offset:32768\n\t"
    "v_exp_f32_e64 v98, v98\n\t"
    "v_mfma_f32_16x16x32_bf16 a[124:127], a[244:247], v[148:151], a[124:127]\n\t"
    "v_add_f32_e64 v248, v248, v97\n\t"
    "v_cvt_pk_bf16_f32 v144, v96, v97\n\t"
    "v_mfma_f32_16x16x32_bf16 a[100:103], a[240:243], v[148:151], a[100:103]\n\t"
    "s_add_u32 m0, s59, 34304\n\t"
    "s_waitcnt vmcnt(4)\n\t"
    "v_exp_f32 v99, v99\n\t"
    "v_mfma_f32_16x16x32_bf16 a[76:79], a[236:239], v[148:151], a[76:79]\n\t"
    "buffer_load_dwordx4 v228, s[48:51], s18 offen offset:512 nt lds\n\t"
    "v_add_f32_e64 v248, v248, v98\n\t"
    "v_mfma_f32_16x16x32_bf16 a[52:55], a[232:235], v[148:151], a[52:55]\n\t"
    "ds_read_b128 a[204:207], v227 offset:32768\n\t"
    "v_exp_f32_e64 v100, v100\n\t"
    "v_mfma_f32_16x16x32_bf16 a[28:31], a[228:231], v[148:151], a[28:31]\n\t"
    "v_add_f32_e64 v248, v248, v99\n\t"
    "v_cvt_pk_bf16_f32 v145, v98, v99\n\t"
    "v_mfma_f32_16x16x32_bf16 a[4:7], a[224:227], v[148:151], a[4:7]\n\t"
    "v_exp_f32 v101, v101\n\t"
    "v_add_f32 v248, v248, v100\n\t"
    "v_mfma_f32_16x16x32_bf16 a[8:11], a[224:227], v[152:155], a[8:11]\n\t"
    "v_exp_f32 v102, v102\n\t"
    "v_add_f32 v248, v248, v101\n\t"
    "v_mfma_f32_16x16x32_bf16 a[32:35], a[228:231], v[152:155], a[32:35]\n\t"
    "s_add_u32 m0, s59, 35072\n\t"
    "s_waitcnt vmcnt(5)\n\t"
    "s_nop 0\n\t"
    "v_cvt_pk_bf16_f32 v146, v100, v101\n\t"
    "v_mfma_f32_16x16x32_bf16 a[56:59], a[232:235], v[152:155], a[56:59]\n\t"
    "buffer_load_dwordx4 v238, s[48:51], s18 offen offset:768 nt lds\n\t"
    "v_exp_f32 v103, v103\n\t"
    "v_add_f32 v248, v248, v102\n\t"
    "v_mfma_f32_16x16x32_bf16 a[80:83], a[236:239], v[152:155], a[80:83]\n\t"
    "ds_read_b128 a[208:211], v224 offset:36864\n\t"
    "v_add_f32_e64 v248, v248, v103\n\t"
    "v_cvt_pk_bf16_f32 v147, v102, v103\n\t"
    "v_mfma_f32_16x16x32_bf16 a[104:107], a[240:243], v[152:155], a[104:107]\n\t"
    "v_exp_f32_e64 v104, v104\n\t"
    "v_mfma_f32_16x16x32_bf16 a[128:131], a[244:247], v[152:155], a[128:131]\n\t"
    "v_exp_f32 v105, v105\n\t"
    "v_add_f32 v249, v249, v104\n\t"
    "v_mfma_f32_16x16x32_bf16 a[152:155], a[248:251], v[152:155], a[152:155]\n\t"
    "ds_read_b128 a[212:215], v225 offset:36864\n\t"
    "v_mfma_f32_16x16x32_bf16 a[176:179], a[252:255], v[152:155], a[176:179]\n\t"
    "s_add_u32 m0, s59, 35840\n\t"
    "s_waitcnt vmcnt(4)\n\t"
    "v_exp_f32 v106, v106\n\t"
    "v_add_f32_e64 v249, v249, v105\n\t"
    "v_mfma_f32_16x16x32_bf16 a[180:183], a[252:255], v[156:159], a[180:183]\n\t"
    "buffer_load_dwordx4 v228, s[48:51], s18 offen offset:1024 nt lds\n\t"
    "v_cvt_pk_bf16_f32 v148, v104, v105\n\t"
    "v_mfma_f32_16x16x32_bf16 a[156:159], a[248:251], v[156:159], a[156:159]\n\t"
    "v_exp_f32 v107, v107\n\t"
    "v_add_f32 v249, v249, v106\n\t"
    "v_mfma_f32_16x16x32_bf16 a[132:135], a[244:247], v[156:159], a[132:135]\n\t"
    "v_exp_f32_e64 v108, v108\n\t"
    "v_mfma_f32_16x16x32_bf16 a[108:111], a[240:243], v[156:159], a[108:111]\n\t"
    "ds_read_b128 a[216:219], v226 offset:36864\n\t"
    "v_add_f32_e64 v249, v249, v107\n\t"
    "v_cvt_pk_bf16_f32 v149, v106, v107\n\t"
    "v_mfma_f32_16x16x32_bf16 a[84:87], a[236:239], v[156:159], a[84:87]\n\t"
    "v_exp_f32_e64 v109, v109\n\t"
    "v_mfma_f32_16x16x32_bf16 a[60:63], a[232:235], v[156:159], a[60:63]\n\t"
    "s_add_u32 m0, s59, 36608\n\t"
    "s_waitcnt vmcnt(5)\n\t"
    "v_add_f32 v249, v249, v108\n\t"
    "v_mfma_f32_16x16x32_bf16 a[36:39], a[228:231], v[156:159], a[36:39]\n\t"
    "buffer_load_dwordx4 v238, s[48:51], s18 offen offset:1280 nt lds\n\t"
    "v_exp_f32_e64 v110, v110\n\t"
    "v_mfma_f32_16x16x32_bf16 a[12:15], a[224:227], v[156:159], a[12:15]\n\t"
    "ds_read_b128 a[220:223], v227 offset:36864\n\t"
    "v_add_f32_e64 v249, v249, v109\n\t"
    "v_cvt_pk_bf16_f32 v150, v108, v109\n\t"
    "v_mfma_f32_16x16x32_bf16 a[16:19], a[224:227], v[160:163], a[16:19]\n\t"
    "v_exp_f32_e64 v111, v111\n\t"
    "v_mfma_f32_16x16x32_bf16 a[40:43], a[228:231], v[160:163], a[40:43]\n\t"
    "v_add_f32 v249, v249, v110\n\t"
    "v_add_f32 v249, v249, v111\n\t"
    "v_cvt_pk_bf16_f32 v151, v110, v111\n\t"
    "v_mfma_f32_16x16x32_bf16 a[64:67], a[232:235], v[160:163], a[64:67]\n\t"
    "v_exp_f32_e64 v112, v112\n\t"
    "v_mfma_f32_16x16x32_bf16 a[88:91], a[236:239], v[160:163], a[88:91]\n\t"
    "s_add_u32 m0, s59, 37376\n\t"
    "s_waitcnt vmcnt(4)\n\t"
    "s_nop 0\n\t"
    "v_mfma_f32_16x16x32_bf16 a[112:115], a[240:243], v[160:163], a[112:115]\n\t"
    "buffer_load_dwordx4 v228, s[48:51], s18 offen offset:1536 nt lds\n\t"
    "v_exp_f32_e64 v113, v113\n\t"
    "v_mfma_f32_16x16x32_bf16 a[136:139], a[244:247], v[160:163], a[136:139]\n\t"
    "v_add_f32 v250, v250, v112\n\t"
    "v_exp_f32 v114, v114\n\t"
    "v_mfma_f32_16x16x32_bf16 a[160:163], a[248:251], v[160:163], a[160:163]\n\t"
    "v_add_f32_e64 v250, v250, v113\n\t"
    "v_cvt_pk_bf16_f32 v152, v112, v113\n\t"
    "v_mfma_f32_16x16x32_bf16 a[184:187], a[252:255], v[160:163], a[184:187]\n\t"
    "v_exp_f32_e64 v115, v115\n\t"
    "v_mfma_f32_16x16x32_bf16 a[188:191], a[252:255], v[164:167], a[188:191]\n\t"
    "v_add_f32 v250, v250, v114\n\t"
    "v_exp_f32 v116, v116\n\t"
    "v_mfma_f32_16x16x32_bf16 a[164:167], a[248:251], v[164:167], a[164:167]\n\t"
    "s_add_u32 m0, s59, 38144\n\t"
    "s_waitcnt vmcnt(5)\n\t"
    "v_add_f32 v250, v250, v115\n\t"
    "v_mfma_f32_16x16x32_bf16 a[140:143], a[244:247], v[164:167], a[140:143]\n\t"
    "buffer_load_dwordx4 v238, s[48:51], s18 offen offset:1792 nt lds\n\t"
    "s_or_b32 s18, s18, 12288\n\t"
    "s_add_u32 s18, s18, 2048\n\t"
    "s_and_b32 s18, s18, 116736\n\t"
    "v_cvt_pk_bf16_f32 v153, v114, v115\n\t"
    "v_mfma_f32_16x16x32_bf16 a[116:119], a[240:243], v[164:167], a[116:119]\n\t"
    "v_exp_f32_e64 v117, v117\n\t"
    "v_mfma_f32_16x16x32_bf16 a[92:95], a[236:239], v[164:167], a[92:95]\n\t"
    "v_add_f32 v250, v250, v116\n\t"
    "v_exp_f32 v118, v118\n\t"
    "v_mfma_f32_16x16x32_bf16 a[68:71], a[232:235], v[164:167], a[68:71]\n\t"
    "v_add_f32_e64 v250, v250, v117\n\t"
    "v_cvt_pk_bf16_f32 v154, v116, v117\n\t"
    "v_mfma_f32_16x16x32_bf16 a[44:47], a[228:231], v[164:167], a[44:47]\n\t"
    "v_exp_f32_e64 v119, v119\n\t"
    "v_mfma_f32_16x16x32_bf16 a[20:23], a[224:227], v[164:167], a[20:23]\n\t"
    "v_add_f32 v250, v250, v118\n\t"
    "v_add_f32 v250, v250, v119\n\t"
    "v_cvt_pk_bf16_f32 v155, v118, v119\n\t"
    "s_cmp_lg_u32 s22, 0\n\t"
    "s_cbranch_scc1 .Lk_resc\n\t"
    ".Lk_rescret_s3:\n\t"
    ".Lk_aloop:\n\t"
    "s_waitcnt vmcnt(16) lgkmcnt(0)\n\t"
    "s_nop 0\n\t"
    "v_mfma_f32_16x16x32_bf16 v[96:99], a[192:195], v[0:3], v[168:171]\n\t"
    "ds_read_b64_tr_b16 a[224:225], v216\n\t"
    "s_add_u32 m0, s58, 32768\n\t"
    "s_waitcnt vmcnt(4)\n\t"
    "s_nop 0\n\t"
    "v_mfma_f32_16x16x32_bf16 v[96:99], a[196:199], v[24:27], v[96:99]\n\t"
    "buffer_load_dwordx4 v212, s[44:47], s62 offen nt lds\n\t"
    "v_exp_f32_e64 v120, v120\n\t"
    "v_mfma_f32_16x16x32_bf16 v[96:99], a[200:203], v[48:51], v[96:99]\n\t"
    "ds_read_b64_tr_b16 a[226:227], v216 offset:4096\n\t"
    "v_exp_f32 v121, v121\n\t"
    "v_add_f32 v251, v251, v120\n\t"
    "v_mfma_f32_16x16x32_bf16 v[96:99], a[204:207], v[72:75], v[96:99]\n\t"
    "buffer_load_dword v239, v242, s[12:15], s11 offen\n\t"
    "v_exp_f32 v122, v122\n\t"
    "v_add_f32 v251, v251, v121\n\t"
    "v_mfma_f32_16x16x32_bf16 v[100:103], a[220:223], v[72:75], v[168:171]\n\t"
    "v_cvt_pk_bf16_f32 v156, v120, v121\n\t"
    "v_exp_f32_e64 v123, v123\n\t"
    "v_mfma_f32_16x16x32_bf16 v[100:103], a[216:219], v[48:51], v[100:103]\n\t"
    "ds_read_b64_tr_b16 a[228:229], v217\n\t"
    "s_add_u32 m0, s58, 33536\n\t"
    "s_waitcnt vmcnt(5)\n\t"
    "v_add_f32 v251, v251, v122\n\t"
    "v_mfma_f32_16x16x32_bf16 v[100:103], a[212:215], v[24:27], v[100:103]\n\t"
    "buffer_load_dwordx4 v213, s[44:47], s62 offen offset:256 nt lds\n\t"
    "v_exp_f32_e64 v124, v124\n\t"
    "v_mfma_f32_16x16x32_bf16 v[100:103], a[208:211], v[0:3], v[100:103]\n\t"
    "ds_read_b64_tr_b16 a[230:231], v217 offset:4096\n\t"
    "v_add_f32_e64 v251, v251, v123\n\t"
    "v_cvt_pk_bf16_f32 v157, v122, v123\n\t"
    "v_mfma_f32_16x16x32_bf16 v[104:107], a[192:195], v[4:7], v[172:175]\n\t"
    "v_exp_f32 v125, v125\n\t"
    "v_add_f32 v251, v251, v124\n\t"
    "v_mfma_f32_16x16x32_bf16 v[104:107], a[196:199], v[28:31], v[104:107]\n\t"
    "v_exp_f32 v126, v126\n\t"
    "v_add_f32 v251, v251, v125\n\t"
    "v_cvt_pk_bf16_f32 v158, v124, v125\n\t"
    "v_mfma_f32_16x16x32_bf16 v[104:107], a[200:203], v[52:55], v[104:107]\n\t"
    "ds_read_b64_tr_b16 a[232:233], v218\n\t"
    "v_exp_f32_e64 v127, v127\n\t"
    "v_mfma_f32_16x16x32_bf16 v[104:107], a[204:207], v[76:79], v[104:107]\n\t"
    "s_add_u32 m0, s58, 34304\n\t"
    "s_waitcnt vmcnt(4)\n\t"
    "v_add_f32 v251, v251, v126\n\t"
    "v_add_f32_e64 v251, v251, v127\n\t"
    "v_cvt_pk_bf16_f32 v159, v126, v127\n\t"
    "v_mfma_f32_16x16x32_bf16 v[108:111], a[220:223], v[76:79], v[172:175]\n\t"
    "buffer_load_dwordx4 v214, s[44:47], s62 offen offset:512 nt lds\n\t"
    "v_exp_f32_e64 v128, v128\n\t"
    "v_mfma_f32_16x16x32_bf16 v[108:111], a[216:219], v[52:55], v[108:111]\n\t"
    "ds_read_b64_tr_b16 a[234:235], v218 offset:4096\n\t"
    "v_max_f32 v255, v96, v97\n\t"
    "v_exp_f32 v129, v129\n\t"
    "v_mfma_f32_16x16x32_bf16 v[108:111], a[212:215], v[28:31], v[108:111]\n\t"
    "v_max3_f32 v255, v255, v98, v99\n\t"
    "v_max3_f32 v255, v255, v100, v101\n\t"
    "v_add_f32_e64 v252, v252, v128\n\t"
    "v_mfma_f32_16x16x32_bf16 v[108:111], a[208:211], v[4:7], v[108:111]\n\t"
    "ds_read_b64_tr_b16 a[236:237], v219\n\t"
    "v_max3_f32 v255, v255, v102, v103\n\t"
    "v_exp_f32_e64 v130, v130\n\t"
    "v_mfma_f32_16x16x32_bf16 v[112:115], a[192:195], v[8:11], v[176:179]\n\t"
    "v_add_f32_e64 v252, v252, v129\n\t"
    "v_cvt_pk_bf16_f32 v160, v128, v129\n\t"
    "v_mfma_f32_16x16x32_bf16 v[112:115], a[196:199], v[32:35], v[112:115]\n\t"
    "ds_read_b64_tr_b16 a[238:239], v219 offset:4096\n\t"
    "s_add_u32 m0, s58, 35072\n\t"
    "s_waitcnt vmcnt(5)\n\t"
    "v_exp_f32 v131, v131\n\t"
    "v_add_f32_e64 v252, v252, v130\n\t"
    "v_mfma_f32_16x16x32_bf16 v[112:115], a[200:203], v[56:59], v[112:115]\n\t"
    "buffer_load_dwordx4 v215, s[44:47], s62 offen offset:768 nt lds\n\t"
    "v_exp_f32_e64 v132, v132\n\t"
    "v_mfma_f32_16x16x32_bf16 v[112:115], a[204:207], v[80:83], v[112:115]\n\t"
    "v_add_f32 v252, v252, v131\n\t"
    "v_cvt_pk_bf16_f32 v161, v130, v131\n\t"
    "v_exp_f32 v133, v133\n\t"
    "v_mfma_f32_16x16x32_bf16 v[116:119], a[220:223], v[80:83], v[176:179]\n\t"
    "ds_read_b64_tr_b16 a[240:241], v220\n\t"
    "v_add_f32_e64 v252, v252, v132\n\t"
    "v_mfma_f32_16x16x32_bf16 v[116:119], a[216:219], v[56:59], v[116:119]\n\t"
    "v_max3_f32 v255, v255, v104, v105\n\t"
    "v_max3_f32 v255, v255, v106, v107\n\t"
    "v_exp_f32 v134, v134\n\t"
    "v_add_f32 v252, v252, v133\n\t"
    "v_mfma_f32_16x16x32_bf16 v[116:119], a[212:215], v[32:35], v[116:119]\n\t"
    "ds_read_b64_tr_b16 a[242:243], v220 offset:4096\n\t"
    "v_max3_f32 v255, v255, v108, v109\n\t"
    "v_cvt_pk_bf16_f32 v162, v132, v133\n\t"
    "v_mfma_f32_16x16x32_bf16 v[116:119], a[208:211], v[8:11], v[116:119]\n\t"
    "s_add_u32 m0, s58, 35840\n\t"
    "s_waitcnt vmcnt(4)\n\t"
    "v_max3_f32 v255, v255, v110, v111\n\t"
    "v_exp_f32 v135, v135\n\t"
    "v_mfma_f32_16x16x32_bf16 v[120:123], a[192:195], v[12:15], v[180:183]\n\t"
    "buffer_load_dwordx4 v212, s[44:47], s62 offen offset:1024 nt lds\n\t"
    "v_add_f32 v252, v252, v134\n\t"
    "v_add_f32 v252, v252, v135\n\t"
    "v_cvt_pk_bf16_f32 v163, v134, v135\n\t"
    "v_mfma_f32_16x16x32_bf16 v[120:123], a[196:199], v[36:39], v[120:123]\n\t"
    "ds_read_b64_tr_b16 a[244:245], v221\n\t"
    "v_exp_f32_e64 v136, v136\n\t"
    "v_mfma_f32_16x16x32_bf16 v[120:123], a[200:203], v[60:63], v[120:123]\n\t"
    "v_exp_f32 v137, v137\n\t"
    "v_add_f32 v253, v253, v136\n\t"
    "v_mfma_f32_16x16x32_bf16 v[120:123], a[204:207], v[84:87], v[120:123]\n\t"
    "ds_read_b64_tr_b16 a[246:247], v221 offset:4096\n\t"
    "v_exp_f32_e64 v138, v138\n\t"
    "v_mfma_f32_16x16x32_bf16 v[124:127], a[220:223], v[84:87], v[180:183]\n\t"
    "v_max3_f32 v255, v255, v112, v113\n\t"
    "v_add_f32_e64 v253, v253, v137\n\t"
    "v_cvt_pk_bf16_f32 v164, v136, v137\n\t"
    "v_mfma_f32_16x16x32_bf16 v[124:127], a[216:219], v[60:63], v[124:127]\n\t"
    "s_add_u32 m0, s58, 36608\n\t"
    "s_waitcnt vmcnt(5)\n\t"
    "v_max3_f32 v255, v255, v114, v115\n\t"
    "v_exp_f32 v139, v139\n\t"
    "v_mfma_f32_16x16x32_bf16 v[124:127], a[212:215], v[36:39], v[124:127]\n\t"
    "buffer_load_dwordx4 v213, s[44:47], s62 offen offset:1280 nt lds\n\t"
    "v_max3_f32 v255, v255, v116, v117\n\t"
    "v_add_f32_e64 v253, v253, v138\n\t"
    "v_mfma_f32_16x16x32_bf16 v[124:127], a[208:211], v[12:15], v[124:127]\n\t"
    "ds_read_b64_tr_b16 a[248:249], v222\n\t"
    "v_max3_f32 v255, v255, v118, v119\n\t"
    "v_exp_f32_e64 v140, v140\n\t"
    "v_mfma_f32_16x16x32_bf16 v[128:131], a[192:195], v[16:19], v[184:187]\n\t"
    "ds_read_b64_tr_b16 a[250:251], v222 offset:4096\n\t"
    "v_add_f32_e64 v253, v253, v139\n\t"
    "v_cvt_pk_bf16_f32 v165, v138, v139\n\t"
    "v_mfma_f32_16x16x32_bf16 v[128:131], a[196:199], v[40:43], v[128:131]\n\t"
    "v_exp_f32 v141, v141\n\t"
    "v_add_f32 v253, v253, v140\n\t"
    "v_mfma_f32_16x16x32_bf16 v[128:131], a[200:203], v[64:67], v[128:131]\n\t"
    "v_exp_f32 v142, v142\n\t"
    "v_add_f32 v253, v253, v141\n\t"
    "v_cvt_pk_bf16_f32 v166, v140, v141\n\t"
    "v_mfma_f32_16x16x32_bf16 v[128:131], a[204:207], v[88:91], v[128:131]\n\t"
    "ds_read_b64_tr_b16 a[252:253], v223\n\t"
    "s_add_u32 m0, s58, 37376\n\t"
    "s_waitcnt vmcnt(4)\n\t"
    "s_nop 0\n\t"
    "v_mfma_f32_16x16x32_bf16 v[132:135], a[220:223], v[88:91], v[184:187]\n\t"
    "buffer_load_dwordx4 v214, s[44:47], s62 offen offset:1536 nt lds\n\t"
    "v_exp_f32 v143, v143\n\t"
    "v_add_f32 v253, v253, v142\n\t"
    "v_mfma_f32_16x16x32_bf16 v[132:135], a[216:219], v[64:67], v[132:135]\n\t"
    "ds_read_b64_tr_b16 a[254:255], v223 offset:4096\n\t"
    "v_max3_f32 v255, v255, v120, v121\n\t"
    "v_add_f32_e64 v253, v253, v143\n\t"
    "v_cvt_pk_bf16_f32 v167, v142, v143\n\t"
    "v_mfma_f32_16x16x32_bf16 v[132:135], a[212:215], v[40:43], v[132:135]\n\t"
    "v_max3_f32 v255, v255, v122, v123\n\t"
    "v_mfma_f32_16x16x32_bf16 v[132:135], a[208:211], v[16:19], v[132:135]\n\t"
    "v_max3_f32 v255, v255, v124, v125\n\t"
    "v_max3_f32 v255, v255, v126, v127\n\t"
    "v_mfma_f32_16x16x32_bf16 v[136:139], a[192:195], v[20:23], v[188:191]\n\t"
    "v_mfma_f32_16x16x32_bf16 v[136:139], a[196:199], v[44:47], v[136:139]\n\t"
    "s_add_u32 m0, s58, 38144\n\t"
    "s_waitcnt vmcnt(5)\n\t"
    "s_nop 0\n\t"
    "v_mfma_f32_16x16x32_bf16 v[136:139], a[200:203], v[68:71], v[136:139]\n\t"
    "buffer_load_dwordx4 v215, s[44:47], s62 offen offset:1792 nt lds\n\t"
    "s_or_b32 s62, s62, 12288\n\t"
    "s_add_u32 s62, s62, 2048\n\t"
    "s_and_b32 s62, s62, 116736\n\t"
    "v_mfma_f32_16x16x32_bf16 v[136:139], a[204:207], v[92:95], v[136:139]\n\t"
    "v_mfma_f32_16x16x32_bf16 v[140:143], a[220:223], v[92:95], v[188:191]\n\t"
    "v_max3_f32 v255, v255, v128, v129\n\t"
    "v_mfma_f32_16x16x32_bf16 v[140:143], a[216:219], v[68:71], v[140:143]\n\t"
    "v_max3_f32 v255, v255, v130, v131\n\t"
    "v_mfma_f32_16x16x32_bf16 v[140:143], a[212:215], v[44:47], v[140:143]\n\t"
    "v_max3_f32 v255, v255, v132, v133\n\t"
    "v_mfma_f32_16x16x32_bf16 v[140:143], a[208:211], v[20:23], v[140:143]\n\t"
    "v_max3_f32 v255, v255, v134, v135\n\t"
    "s_waitcnt vmcnt(16) lgkmcnt(0)\n\t"
    "s_nop 0\n\t"
    "v_mfma_f32_16x16x32_bf16 a[0:3], a[224:227], v[144:147], a[0:3]\n\t"
    "ds_read_b128 a[192:195], v224\n\t"
    "s_add_u32 m0, s59, 0\n\t"
    "s_waitcnt vmcnt(4)\n\t"
    "v_mfma_f32_16x16x32_bf16 a[24:27], a[228:231], v[144:147], a[24:27]\n\t"
    "buffer_load_dwordx4 v228, s[48:51], s18 offen nt lds\n\t"
    "v_mfma_f32_16x16x32_bf16 a[48:51], a[232:235], v[144:147], a[48:51]\n\t"
    "v_mfma_f32_16x16x32_bf16 a[72:75], a[236:239], v[144:147], a[72:75]\n\t"
    "v_mfma_f32_16x16x32_bf16 a[96:99], a[240:243], v[144:147], a[96:99]\n\t"
    "ds_read_b128 a[196:199], v225\n\t"
    "v_max3_f32 v255, v255, v136, v137\n\t"
    "v_max3_f32 v255, v255, v138, v139\n\t"
    "v_mfma_f32_16x16x32_bf16 a[120:123], a[244:247], v[144:147], a[120:123]\n\t"
    "s_add_u32 m0, s59, 768\n\t"
    "s_waitcnt vmcnt(5)\n\t"
    "s_nop 0\n\t"
    "v_max3_f32 v255, v255, v140, v141\n\t"
    "v_max3_f32 v255, v255, v142, v143\n\t"
    "v_mfma_f32_16x16x32_bf16 a[144:147], a[248:251], v[144:147], a[144:147]\n\t"
    "buffer_load_dwordx4 v238, s[48:51], s18 offen offset:256 nt lds\n\t"
    "v_cmp_lt_f32 vcc, s21, v255\n\t"
    "s_mov_b32 s23, 0\n\t"
    "s_nop 0\n\t"
    "s_cbranch_vccnz .Lk_adj\n\t"
    ".Lk_adjret_e:\n\t"
    "v_mfma_f32_16x16x32_bf16 a[168:171], a[252:255], v[144:147], a[168:171]\n\t"
    "v_exp_f32_e64 v96, v96\n\t"
    "v_mfma_f32_16x16x32_bf16 a[172:175], a[252:255], v[148:151], a[172:175]\n\t"
    "v_exp_f32 v97, v97\n\t"
    "v_add_f32 v248, v248, v96\n\t"
    "v_mfma_f32_16x16x32_bf16 a[148:151], a[248:251], v[148:151], a[148:151]\n\t"
    "ds_read_b128 a[200:203], v226\n\t"
    "v_exp_f32_e64 v98, v98\n\t"
    "v_mfma_f32_16x16x32_bf16 a[124:127], a[244:247], v[148:151], a[124:127]\n\t"
    "v_add_f32_e64 v248, v248, v97\n\t"
    "v_cvt_pk_bf16_f32 v144, v96, v97\n\t"
    "v_mfma_f32_16x16x32_bf16 a[100:103], a[240:243], v[148:151], a[100:103]\n\t"
    "s_add_u32 m0, s59, 1536\n\t"
    "s_waitcnt vmcnt(4)\n\t"
    "v_exp_f32 v99, v99\n\t"
    "v_mfma_f32_16x16x32_bf16 a[76:79], a[236:239], v[148:151], a[76:79]\n\t"
    "buffer_load_dwordx4 v228, s[48:51], s18 offen offset:512 nt lds\n\t"
    "v_add_f32_e64 v248, v248, v98\n\t"
    "v_mfma_f32_16x16x32_bf16 a[52:55], a[232:235], v[148:151], a[52:55]\n\t"
    "ds_read_b128 a[204:207], v227\n\t"
    "v_exp_f32_e64 v100, v100\n\t"
    "v_mfma_f32_16x16x32_bf16 a[28:31], a[228:231], v[148:151], a[28:31]\n\t"
    "v_add_f32_e64 v248, v248, v99\n\t"
    "v_cvt_pk_bf16_f32 v145, v98, v99\n\t"
    "v_mfma_f32_16x16x32_bf16 a[4:7], a[224:227], v[148:151], a[4:7]\n\t"
    "v_exp_f32 v101, v101\n\t"
    "v_add_f32 v248, v248, v100\n\t"
    "v_mfma_f32_16x16x32_bf16 a[8:11], a[224:227], v[152:155], a[8:11]\n\t"
    "v_exp_f32 v102, v102\n\t"
    "v_add_f32 v248, v248, v101\n\t"
    "v_mfma_f32_16x16x32_bf16 a[32:35], a[228:231], v[152:155], a[32:35]\n\t"
    "s_add_u32 m0, s59, 2304\n\t"
    "s_waitcnt vmcnt(5)\n\t"
    "s_nop 0\n\t"
    "v_cvt_pk_bf16_f32 v146, v100, v101\n\t"
    "v_mfma_f32_16x16x32_bf16 a[56:59], a[232:235], v[152:155], a[56:59]\n\t"
    "buffer_load_dwordx4 v238, s[48:51], s18 offen offset:768 nt lds\n\t"
    "v_exp_f32 v103, v103\n\t"
    "v_add_f32 v248, v248, v102\n\t"
    "v_mfma_f32_16x16x32_bf16 a[80:83], a[236:239], v[152:155], a[80:83]\n\t"
    "ds_read_b128 a[208:211], v224 offset:4096\n\t"
    "v_add_f32_e64 v248, v248, v103\n\t"
    "v_cvt_pk_bf16_f32 v147, v102, v103\n\t"
    "v_mfma_f32_16x16x32_bf16 a[104:107], a[240:243], v[152:155], a[104:107]\n\t"
    "v_exp_f32_e64 v104, v104\n\t"
    "v_mfma_f32_16x16x32_bf16 a[128:131], a[244:247], v[152:155], a[128:131]\n\t"
    "v_exp_f32 v105, v105\n\t"
    "v_add_f32 v249, v249, v104\n\t"
    "v_mfma_f32_16x16x32_bf16 a[152:155], a[248:251], v[152:155], a[152:155]\n\t"
    "ds_read_b128 a[212:215], v225 offset:4096\n\t"
    "v_mfma_f32_16x16x32_bf16 a[176:179], a[252:255], v[152:155], a[176:179]\n\t"
    "s_add_u32 m0, s59, 3072\n\t"
    "s_waitcnt vmcnt(4)\n\t"
    "v_exp_f32 v106, v106\n\t"
    "v_add_f32_e64 v249, v249, v105\n\t"
    "v_mfma_f32_16x16x32_bf16 a[180:183], a[252:255], v[156:159], a[180:183]\n\t"
    "buffer_load_dwordx4 v228, s[48:51], s18 offen offset:1024 nt lds\n\t"
    "v_cvt_pk_bf16_f32 v148, v104, v105\n\t"
    "v_mfma_f32_16x16x32_bf16 a[156:159], a[248:251], v[156:159], a[156:159]\n\t"
    "v_exp_f32 v107, v107\n\t"
    "v_add_f32 v249, v249, v106\n\t"
    "v_mfma_f32_16x16x32_bf16 a[132:135], a[244:247], v[156:159], a[132:135]\n\t"
    "v_exp_f32_e64 v108, v108\n\t"
    "v_mfma_f32_16x16x32_bf16 a[108:111], a[240:243], v[156:159], a[108:111]\n\t"
    "ds_read_b128 a[216:219], v226 offset:4096\n\t"
    "v_add_f32_e64 v249, v249, v107\n\t"
    "v_cvt_pk_bf16_f32 v149, v106, v107\n\t"
    "v_mfma_f32_16x16x32_bf16 a[84:87], a[236:239], v[156:159], a[84:87]\n\t"
    "v_exp_f32_e64 v109, v109\n\t"
    "v_mfma_f32_16x16x32_bf16 a[60:63], a[232:235], v[156:159], a[60:63]\n\t"
    "s_add_u32 m0, s59, 3840\n\t"
    "s_waitcnt vmcnt(5)\n\t"
    "v_add_f32 v249, v249, v108\n\t"
    "v_mfma_f32_16x16x32_bf16 a[36:39], a[228:231], v[156:159], a[36:39]\n\t"
    "buffer_load_dwordx4 v238, s[48:51], s18 offen offset:1280 nt lds\n\t"
    "v_exp_f32_e64 v110, v110\n\t"
    "v_mfma_f32_16x16x32_bf16 a[12:15], a[224:227], v[156:159], a[12:15]\n\t"
    "ds_read_b128 a[220:223], v227 offset:4096\n\t"
    "v_add_f32_e64 v249, v249, v109\n\t"
    "v_cvt_pk_bf16_f32 v150, v108, v109\n\t"
    "v_mfma_f32_16x16x32_bf16 a[16:19], a[224:227], v[160:163], a[16:19]\n\t"
    "v_exp_f32_e64 v111, v111\n\t"
    "v_mfma_f32_16x16x32_bf16 a[40:43], a[228:231], v[160:163], a[40:43]\n\t"
    "v_add_f32 v249, v249, v110\n\t"
    "v_add_f32 v249, v249, v111\n\t"
    "v_cvt_pk_bf16_f32 v151, v110, v111\n\t"
    "v_mfma_f32_16x16x32_bf16 a[64:67], a[232:235], v[160:163], a[64:67]\n\t"
    "v_exp_f32_e64 v112, v112\n\t"
    "v_mfma_f32_16x16x32_bf16 a[88:91], a[236:239], v[160:163], a[88:91]\n\t"
    "s_add_u32 m0, s59, 4608\n\t"
    "s_waitcnt vmcnt(4)\n\t"
    "s_nop 0\n\t"
    "v_mfma_f32_16x16x32_bf16 a[112:115], a[240:243], v[160:163], a[112:115]\n\t"
    "buffer_load_dwordx4 v228, s[48:51], s18 offen offset:1536 nt lds\n\t"
    "v_exp_f32_e64 v113, v113\n\t"
    "v_mfma_f32_16x16x32_bf16 a[136:139], a[244:247], v[160:163], a[136:139]\n\t"
    "v_add_f32 v250, v250, v112\n\t"
    "v_exp_f32 v114, v114\n\t"
    "v_mfma_f32_16x16x32_bf16 a[160:163], a[248:251], v[160:163], a[160:163]\n\t"
    "v_add_f32_e64 v250, v250, v113\n\t"
    "v_cvt_pk_bf16_f32 v152, v112, v113\n\t"
    "v_mfma_f32_16x16x32_bf16 a[184:187], a[252:255], v[160:163], a[184:187]\n\t"
    "v_exp_f32_e64 v115, v115\n\t"
    "v_mfma_f32_16x16x32_bf16 a[188:191], a[252:255], v[164:167], a[188:191]\n\t"
    "v_add_f32 v250, v250, v114\n\t"
    "v_exp_f32 v116, v116\n\t"
    "v_mfma_f32_16x16x32_bf16 a[164:167], a[248:251], v[164:167], a[164:167]\n\t"
    "s_add_u32 m0, s59, 5376\n\t"
    "s_waitcnt vmcnt(5)\n\t"
    "v_add_f32 v250, v250, v115\n\t"
    "v_mfma_f32_16x16x32_bf16 a[140:143], a[244:247], v[164:167], a[140:143]\n\t"
    "buffer_load_dwordx4 v238, s[48:51], s18 offen offset:1792 nt lds\n\t"
    "s_or_b32 s18, s18, 12288\n\t"
    "s_add_u32 s18, s18, 2048\n\t"
    "s_and_b32 s18, s18, 116736\n\t"
    "v_cvt_pk_bf16_f32 v153, v114, v115\n\t"
    "v_mfma_f32_16x16x32_bf16 a[116:119], a[240:243], v[164:167], a[116:119]\n\t"
    "v_exp_f32_e64 v117, v117\n\t"
    "v_mfma_f32_16x16x32_bf16 a[92:95], a[236:239], v[164:167], a[92:95]\n\t"
    "v_add_f32 v250, v250, v116\n\t"
    "v_exp_f32 v118, v118\n\t"
    "v_mfma_f32_16x16x32_bf16 a[68:71], a[232:235], v[164:167], a[68:71]\n\t"
    "v_add_f32_e64 v250, v250, v117\n\t"
    "v_cvt_pk_bf16_f32 v154, v116, v117\n\t"
    "v_mfma_f32_16x16x32_bf16 a[44:47], a[228:231], v[164:167], a[44:47]\n\t"
    "v_exp_f32_e64 v119, v119\n\t"
    "v_mfma_f32_16x16x32_bf16 a[20:23], a[224:227], v[164:167], a[20:23]\n\t"
    "v_add_f32 v250, v250, v118\n\t"
    "v_add_f32 v250, v250, v119\n\t"
    "v_cvt_pk_bf16_f32 v155, v118, v119\n\t"
    "s_cmp_lg_u32 s22, 0\n\t"
    "s_cbranch_scc1 .Lk_resc\n\t"
    ".Lk_rescret_e:\n\t"
    "s_waitcnt vmcnt(16) lgkmcnt(0)\n\t"
    "s_nop 0\n\t"
    "v_mfma_f32_16x16x32_bf16 v[96:99], a[192:195], v[0:3], v[168:171]\n\t"
    "ds_read_b64_tr_b16 a[224:225], v216 offset:32768\n\t"
    "s_add_u32 m0, s58, 0\n\t"
    "s_waitcnt vmcnt(4)\n\t"
    "v_mfma_f32_16x16x32_bf16 v[96:99], a[196:199], v[24:27], v[96:99]\n\t"
    "buffer_load_dwordx4 v212, s[44:47], s62 offen nt lds\n\t"
    "v_exp_f32_e64 v120, v120\n\t"
    "v_mfma_f32_16x16x32_bf16 v[96:99], a[200:203], v[48:51], v[96:99]\n\t"
    "ds_read_b64_tr_b16 a[226:227], v216 offset:36864\n\t"
    "v_exp_f32 v121, v121\n\t"
    "v_add_f32 v251, v251, v120\n\t"
    "v_mfma_f32_16x16x32_bf16 v[96:99], a[204:207], v[72:75], v[96:99]\n\t"
    "v_exp_f32 v122, v122\n\t"
    "v_add_f32 v251, v251, v121\n\t"
    "v_mfma_f32_16x16x32_bf16 v[100:103], a[220:223], v[72:75], v[168:171]\n\t"
    "v_cvt_pk_bf16_f32 v156, v120, v121\n\t"
    "v_exp_f32_e64 v123, v123\n\t"
    "v_mfma_f32_16x16x32_bf16 v[100:103], a[216:219], v[48:51], v[100:103]\n\t"
    "ds_read_b64_tr_b16 a[228:229], v217 offset:32768\n\t"
    "s_add_u32 m0, s58, 768\n\t"
    "s_waitcnt vmcnt(5)\n\t"
    "v_add_f32 v251, v251, v122\n\t"
    "v_mfma_f32_16x16x32_bf16 v[100:103], a[212:215], v[24:27], v[100:103]\n\t"
    "buffer_load_dwordx4 v213, s[44:47], s62 offen offset:256 nt lds\n\t"
    "v_exp_f32 v124, v124\n\t"
    "v_add_f32 v251, v251, v123\n\t"
    "v_mfma_f32_16x16x32_bf16 v[100:103], a[208:211], v[0:3], v[100:103]\n\t"
    "ds_read_b64_tr_b16 a[230:231], v217 offset:36864\n\t"
    "v_cvt_pk_bf16_f32 v157, v122, v123\n\t"
    "v_mfma_f32_16x16x32_bf16 v[104:107], a[192:195], v[4:7], v[172:175]\n\t"
    "v_exp_f32 v125, v125\n\t"
    "v_add_f32 v251, v251, v124\n\t"
    "v_mfma_f32_16x16x32_bf16 v[104:107], a[196:199], v[28:31], v[104:107]\n\t"
    "v_exp_f32 v126, v126\n\t"
    "v_add_f32 v251, v251, v125\n\t"
    "v_cvt_pk_bf16_f32 v158, v124, v125\n\t"
    "v_mfma_f32_16x16x32_bf16 v[104:107], a[200:203], v[52:55], v[104:107]\n\t"
    "ds_read_b64_tr_b16 a[232:233], v218 offset:32768\n\t"
    "v_exp_f32_e64 v127, v127\n\t"
    "v_mfma_f32_16x16x32_bf16 v[104:107], a[204:207], v[76:79], v[104:107]\n\t"
    "s_add_u32 m0, s58, 1536\n\t"
    "s_waitcnt vmcnt(4)\n\t"
    "v_add_f32 v251, v251, v126\n\t"
    "v_add_f32_e64 v251, v251, v127\n\t"
    "v_cvt_pk_bf16_f32 v159, v126, v127\n\t"
    "v_mfma_f32_16x16x32_bf16 v[108:111], a[220:223], v[76:79], v[172:175]\n\t"
    "buffer_load_dwordx4 v214, s[44:47], s62 offen offset:512 nt lds\n\t"
    "v_exp_f32_e64 v128, v128\n\t"
    "v_mfma_f32_16x16x32_bf16 v[108:111], a[216:219], v[52:55], v[108:111]\n\t"
    "ds_read_b64_tr_b16 a[234:235], v218 offset:36864\n\t"
    "v_max_f32 v255, v96, v97\n\t"
    "v_exp_f32 v129, v129\n\t"
    "v_mfma_f32_16x16x32_bf16 v[108:111], a[212:215], v[28:31], v[108:111]\n\t"
    "v_max3_f32 v255, v255, v98, v99\n\t"
    "v_max3_f32 v255, v255, v100, v101\n\t"
    "v_add_f32_e64 v252, v252, v128\n\t"
    "v_mfma_f32_16x16x32_bf16 v[108:111], a[208:211], v[4:7], v[108:111]\n\t"
    "ds_read_b64_tr_b16 a[236:237], v219 offset:32768\n\t"
    "v_max3_f32 v255, v255, v102, v103\n\t"
    "v_exp_f32_e64 v130, v130\n\t"
    "v_mfma_f32_16x16x32_bf16 v[112:115], a[192:195], v[8:11], v[176:179]\n\t"
    "v_add_f32_e64 v252, v252, v129\n\t"
    "v_cvt_pk_bf16_f32 v160, v128, v129\n\t"
    "v_mfma_f32_16x16x32_bf16 v[112:115], a[196:199], v[32:35], v[112:115]\n\t"
    "ds_read_b64_tr_b16 a[238:239], v219 offset:36864\n\t"
    "s_add_u32 m0, s58, 2304\n\t"
    "s_waitcnt vmcnt(5)\n\t"
    "v_exp_f32 v131, v131\n\t"
    "v_add_f32_e64 v252, v252, v130\n\t"
    "v_mfma_f32_16x16x32_bf16 v[112:115], a[200:203], v[56:59], v[112:115]\n\t"
    "buffer_load_dwordx4 v215, s[44:47], s62 offen offset:768 nt lds\n\t"
    "v_exp_f32_e64 v132, v132\n\t"
    "v_mfma_f32_16x16x32_bf16 v[112:115], a[204:207], v[80:83], v[112:115]\n\t"
    "v_add_f32 v252, v252, v131\n\t"
    "v_cvt_pk_bf16_f32 v161, v130, v131\n\t"
    "v_exp_f32 v133, v133\n\t"
    "v_mfma_f32_16x16x32_bf16 v[116:119], a[220:223], v[80:83], v[176:179]\n\t"
    "ds_read_b64_tr_b16 a[240:241], v220 offset:32768\n\t"
    "v_add_f32_e64 v252, v252, v132\n\t"
    "v_mfma_f32_16x16x32_bf16 v[116:119], a[216:219], v[56:59], v[116:119]\n\t"
    "v_max3_f32 v255, v255, v104, v105\n\t"
    "v_max3_f32 v255, v255, v106, v107\n\t"
    "v_exp_f32 v134, v134\n\t"
    "v_add_f32 v252, v252, v133\n\t"
    "v_mfma_f32_16x16x32_bf16 v[116:119], a[212:215], v[32:35], v[116:119]\n\t"
    "ds_read_b64_tr_b16 a[242:243], v220 offset:36864\n\t"
    "v_max3_f32 v255, v255, v108, v109\n\t"
    "v_cvt_pk_bf16_f32 v162, v132, v133\n\t"
    "v_mfma_f32_16x16x32_bf16 v[116:119], a[208:211], v[8:11], v[116:119]\n\t"
    "s_add_u32 m0, s58, 3072\n\t"
    "s_waitcnt vmcnt(4)\n\t"
    "v_max3_f32 v255, v255, v110, v111\n\t"
    "v_exp_f32 v135, v135\n\t"
    "v_mfma_f32_16x16x32_bf16 v[120:123], a[192:195], v[12:15], v[180:183]\n\t"
    "buffer_load_dwordx4 v212, s[44:47], s62 offen offset:1024 nt lds\n\t"
    "v_add_f32 v252, v252, v134\n\t"
    "v_add_f32 v252, v252, v135\n\t"
    "v_cvt_pk_bf16_f32 v163, v134, v135\n\t"
    "v_mfma_f32_16x16x32_bf16 v[120:123], a[196:199], v[36:39], v[120:123]\n\t"
    "ds_read_b64_tr_b16 a[244:245], v221 offset:32768\n\t"
    "v_exp_f32_e64 v136, v136\n\t"
    "v_mfma_f32_16x16x32_bf16 v[120:123], a[200:203], v[60:63], v[120:123]\n\t"
    "v_exp_f32 v137, v137\n\t"
    "v_add_f32 v253, v253, v136\n\t"
    "v_mfma_f32_16x16x32_bf16 v[120:123], a[204:207], v[84:87], v[120:123]\n\t"
    "ds_read_b64_tr_b16 a[246:247], v221 offset:36864\n\t"
    "v_exp_f32_e64 v138, v138\n\t"
    "v_mfma_f32_16x16x32_bf16 v[124:127], a[220:223], v[84:87], v[180:183]\n\t"
    "v_max3_f32 v255, v255, v112, v113\n\t"
    "v_add_f32_e64 v253, v253, v137\n\t"
    "v_cvt_pk_bf16_f32 v164, v136, v137\n\t"
    "v_mfma_f32_16x16x32_bf16 v[124:127], a[216:219], v[60:63], v[124:127]\n\t"
    "s_add_u32 m0, s58, 3840\n\t"
    "s_waitcnt vmcnt(5)\n\t"
    "v_max3_f32 v255, v255, v114, v115\n\t"
    "v_exp_f32 v139, v139\n\t"
    "v_mfma_f32_16x16x32_bf16 v[124:127], a[212:215], v[36:39], v[124:127]\n\t"
    "buffer_load_dwordx4 v213, s[44:47], s62 offen offset:1280 nt lds\n\t"
    "v_max3_f32 v255, v255, v116, v117\n\t"
    "v_add_f32_e64 v253, v253, v138\n\t"
    "v_mfma_f32_16x16x32_bf16 v[124:127], a[208:211], v[12:15], v[124:127]\n\t"
    "ds_read_b64_tr_b16 a[248:249], v222 offset:32768\n\t"
    "v_max3_f32 v255, v255, v118, v119\n\t"
    "v_exp_f32_e64 v140, v140\n\t"
    "v_mfma_f32_16x16x32_bf16 v[128:131], a[192:195], v[16:19], v[184:187]\n\t"
    "ds_read_b64_tr_b16 a[250:251], v222 offset:36864\n\t"
    "v_add_f32_e64 v253, v253, v139\n\t"
    "v_cvt_pk_bf16_f32 v165, v138, v139\n\t"
    "v_mfma_f32_16x16x32_bf16 v[128:131], a[196:199], v[40:43], v[128:131]\n\t"
    "v_exp_f32 v141, v141\n\t"
    "v_add_f32 v253, v253, v140\n\t"
    "v_mfma_f32_16x16x32_bf16 v[128:131], a[200:203], v[64:67], v[128:131]\n\t"
    "v_exp_f32 v142, v142\n\t"
    "v_add_f32 v253, v253, v141\n\t"
    "v_cvt_pk_bf16_f32 v166, v140, v141\n\t"
    "v_mfma_f32_16x16x32_bf16 v[128:131], a[204:207], v[88:91], v[128:131]\n\t"
    "ds_read_b64_tr_b16 a[252:253], v223 offset:32768\n\t"
    "s_add_u32 m0, s58, 4608\n\t"
    "s_waitcnt vmcnt(4)\n\t"
    "s_nop 0\n\t"
    "v_mfma_f32_16x16x32_bf16 v[132:135], a[220:223], v[88:91], v[184:187]\n\t"
    "buffer_load_dwordx4 v214, s[44:47], s62 offen offset:1536 nt lds\n\t"
    "v_exp_f32 v143, v143\n\t"
    "v_add_f32 v253, v253, v142\n\t"
    "v_mfma_f32_16x16x32_bf16 v[132:135], a[216:219], v[64:67], v[132:135]\n\t"
    "ds_read_b64_tr_b16 a[254:255], v223 offset:36864\n\t"
    "v_max3_f32 v255, v255, v120, v121\n\t"
    "v_add_f32_e64 v253, v253, v143\n\t"
    "v_cvt_pk_bf16_f32 v167, v142, v143\n\t"
    "v_mfma_f32_16x16x32_bf16 v[132:135], a[212:215], v[40:43], v[132:135]\n\t"
    "v_max3_f32 v255, v255, v122, v123\n\t"
    "v_mfma_f32_16x16x32_bf16 v[132:135], a[208:211], v[16:19], v[132:135]\n\t"
    "v_max3_f32 v255, v255, v124, v125\n\t"
    "v_max3_f32 v255, v255, v126, v127\n\t"
    "v_mfma_f32_16x16x32_bf16 v[136:139], a[192:195], v[20:23], v[188:191]\n\t"
    "v_mfma_f32_16x16x32_bf16 v[136:139], a[196:199], v[44:47], v[136:139]\n\t"
    "s_add_u32 m0, s58, 5376\n\t"
    "s_waitcnt vmcnt(5)\n\t"
    "s_nop 0\n\t"
    "v_mfma_f32_16x16x32_bf16 v[136:139], a[200:203], v[68:71], v[136:139]\n\t"
    "buffer_load_dwordx4 v215, s[44:47], s62 offen offset:1792 nt lds\n\t"
    "s_or_b32 s62, s62, 12288\n\t"
    "s_add_u32 s62, s62, 2048\n\t"
    "s_and_b32 s62, s62, 116736\n\t"
    "v_mfma_f32_16x16x32_bf16 v[136:139], a[204:207], v[92:95], v[136:139]\n\t"
    "v_mfma_f32_16x16x32_bf16 v[140:143], a[220:223], v[92:95], v[188:191]\n\t"
    "v_max3_f32 v255, v255, v128, v129\n\t"
    "v_mfma_f32_16x16x32_bf16 v[140:143], a[216:219], v[68:71], v[140:143]\n\t"
    "v_max3_f32 v255, v255, v130, v131\n\t"
    "v_mfma_f32_16x16x32_bf16 v[140:143], a[212:215], v[44:47], v[140:143]\n\t"
    "v_max3_f32 v255, v255, v132, v133\n\t"
    "v_mfma_f32_16x16x32_bf16 v[140:143], a[208:211], v[20:23], v[140:143]\n\t"
    "v_max3_f32 v255, v255, v134, v135\n\t"
    "s_waitcnt vmcnt(16) lgkmcnt(0)\n\t"
    "s_nop 0\n\t"
    "v_mfma_f32_16x16x32_bf16 a[0:3], a[224:227], v[144:147], a[0:3]\n\t"
    "ds_read_b128 a[192:195], v224 offset:32768\n\t"
    "s_add_u32 m0, s59, 32768\n\t"
    "s_waitcnt vmcnt(4)\n\t"
    "s_nop 0\n\t"
    "v_mfma_f32_16x16x32_bf16 a[24:27], a[228:231], v[144:147], a[24:27]\n\t"
    "buffer_load_dwordx4 v228, s[48:51], s18 offen nt lds\n\t"
    "v_mfma_f32_16x16x32_bf16 a[48:51], a[232:235], v[144:147], a[48:51]\n\t"
    "v_mfma_f32_16x16x32_bf16 a[72:75], a[236:239], v[144:147], a[72:75]\n\t"
    "v_mfma_f32_16x16x32_bf16 a[96:99], a[240:243], v[144:147], a[96:99]\n\t"
    "ds_read_b128 a[196:199], v225 offset:32768\n\t"
    "v_max3_f32 v255, v255, v136, v137\n\t"
    "v_max3_f32 v255, v255, v138, v139\n\t"
    "v_mfma_f32_16x16x32_bf16 a[120:123], a[244:247], v[144:147], a[120:123]\n\t"
    "s_add_u32 m0, s59, 33536\n\t"
    "s_waitcnt vmcnt(5)\n\t"
    "s_nop 0\n\t"
    "v_max3_f32 v255, v255, v140, v141\n\t"
    "v_max3_f32 v255, v255, v142, v143\n\t"
    "v_mfma_f32_16x16x32_bf16 a[144:147], a[248:251], v[144:147], a[144:147]\n\t"
    "buffer_load_dwordx4 v238, s[48:51], s18 offen offset:256 nt lds\n\t"
    "v_cmp_lt_f32 vcc, s21, v255\n\t"
    "s_mov_b32 s23, 1\n\t"
    "s_nop 0\n\t"
    "s_cbranch_vccnz .Lk_adj\n\t"
    ".Lk_adjret_o:\n\t"
    "v_mfma_f32_16x16x32_bf16 a[168:171], a[252:255], v[144:147], a[168:171]\n\t"
    "v_exp_f32_e64 v96, v96\n\t"
    "v_mfma_f32_16x16x32_bf16 a[172:175], a[252:255], v[148:151], a[172:175]\n\t"
    "v_exp_f32 v97, v97\n\t"
    "v_add_f32 v248, v248, v96\n\t"
    "v_mfma_f32_16x16x32_bf16 a[148:151], a[248:251], v[148:151], a[148:151]\n\t"
    "ds_read_b128 a[200:203], v226 offset:32768\n\t"
    "v_exp_f32_e64 v98, v98\n\t"
    "v_mfma_f32_16x16x32_bf16 a[124:127], a[244:247], v[148:151], a[124:127]\n\t"
    "v_add_f32_e64 v248, v248, v97\n\t"
    "v_cvt_pk_bf16_f32 v144, v96, v97\n\t"
    "v_mfma_f32_16x16x32_bf16 a[100:103], a[240:243], v[148:151], a[100:103]\n\t"
    "s_add_u32 m0, s59, 34304\n\t"
    "s_waitcnt vmcnt(4)\n\t"
    "v_exp_f32 v99, v99\n\t"
    "v_mfma_f32_16x16x32_bf16 a[76:79], a[236:239], v[148:151], a[76:79]\n\t"
    "buffer_load_dwordx4 v228, s[48:51], s18 offen offset:512 nt lds\n\t"
    "v_add_f32_e64 v248, v248, v98\n\t"
    "v_mfma_f32_16x16x32_bf16 a[52:55], a[232:235], v[148:151], a[52:55]\n\t"
    "ds_read_b128 a[204:207], v227 offset:32768\n\t"
    "v_exp_f32_e64 v100, v100\n\t"
    "v_mfma_f32_16x16x32_bf16 a[28:31], a[228:231], v[148:151], a[28:31]\n\t"
    "v_add_f32_e64 v248, v248, v99\n\t"
    "v_cvt_pk_bf16_f32 v145, v98, v99\n\t"
    "v_mfma_f32_16x16x32_bf16 a[4:7], a[224:227], v[148:151], a[4:7]\n\t"
    "v_exp_f32 v101, v101\n\t"
    "v_add_f32 v248, v248, v100\n\t"
    "v_mfma_f32_16x16x32_bf16 a[8:11], a[224:227], v[152:155], a[8:11]\n\t"
    "v_exp_f32 v102, v102\n\t"
    "v_add_f32 v248, v248, v101\n\t"
    "v_mfma_f32_16x16x32_bf16 a[32:35], a[228:231], v[152:155], a[32:35]\n\t"
    "s_add_u32 m0, s59, 35072\n\t"
    "s_waitcnt vmcnt(5)\n\t"
    "s_nop 0\n\t"
    "v_cvt_pk_bf16_f32 v146, v100, v101\n\t"
    "v_mfma_f32_16x16x32_bf16 a[56:59], a[232:235], v[152:155], a[56:59]\n\t"
    "buffer_load_dwordx4 v238, s[48:51], s18 offen offset:768 nt lds\n\t"
    "v_exp_f32 v103, v103\n\t"
    "v_add_f32 v248, v248, v102\n\t"
    "v_mfma_f32_16x16x32_bf16 a[80:83], a[236:239], v[152:155], a[80:83]\n\t"
    "ds_read_b128 a[208:211], v224 offset:36864\n\t"
    "v_add_f32_e64 v248, v248, v103\n\t"
    "v_cvt_pk_bf16_f32 v147, v102, v103\n\t"
    "v_mfma_f32_16x16x32_bf16 a[104:107], a[240:243], v[152:155], a[104:107]\n\t"
    "v_exp_f32_e64 v104, v104\n\t"
    "v_mfma_f32_16x16x32_bf16 a[128:131], a[244:247], v[152:155], a[128:131]\n\t"
    "v_exp_f32 v105, v105\n\t"
    "v_add_f32 v249, v249, v104\n\t"
    "v_mfma_f32_16x16x32_bf16 a[152:155], a[248:251], v[152:155], a[152:155]\n\t"
    "ds_read_b128 a[212:215], v225 offset:36864\n\t"
    "v_mfma_f32_16x16x32_bf16 a[176:179], a[252:255], v[152:155], a[176:179]\n\t"
    "s_add_u32 m0, s59, 35840\n\t"
    "s_waitcnt vmcnt(4)\n\t"
    "v_exp_f32 v106, v106\n\t"
    "v_add_f32_e64 v249, v249, v105\n\t"
    "v_mfma_f32_16x16x32_bf16 a[180:183], a[252:255], v[156:159], a[180:183]\n\t"
    "buffer_load_dwordx4 v228, s[48:51], s18 offen offset:1024 nt lds\n\t"
    "v_cvt_pk_bf16_f32 v148, v104, v105\n\t"
    "v_mfma_f32_16x16x32_bf16 a[156:159], a[248:251], v[156:159], a[156:159]\n\t"
    "v_exp_f32 v107, v107\n\t"
    "v_add_f32 v249, v249, v106\n\t"
    "v_mfma_f32_16x16x32_bf16 a[132:135], a[244:247], v[156:159], a[132:135]\n\t"
    "v_exp_f32_e64 v108, v108\n\t"
    "v_mfma_f32_16x16x32_bf16 a[108:111], a[240:243], v[156:159], a[108:111]\n\t"
    "ds_read_b128 a[216:219], v226 offset:36864\n\t"
    "v_add_f32_e64 v249, v249, v107\n\t"
    "v_cvt_pk_bf16_f32 v149, v106, v107\n\t"
    "v_mfma_f32_16x16x32_bf16 a[84:87], a[236:239], v[156:159], a[84:87]\n\t"
    "v_exp_f32_e64 v109, v109\n\t"
    "v_mfma_f32_16x16x32_bf16 a[60:63], a[232:235], v[156:159], a[60:63]\n\t"
    "s_add_u32 m0, s59, 36608\n\t"
    "s_waitcnt vmcnt(5)\n\t"
    "v_add_f32 v249, v249, v108\n\t"
    "v_mfma_f32_16x16x32_bf16 a[36:39], a[228:231], v[156:159], a[36:39]\n\t"
    "buffer_load_dwordx4 v238, s[48:51], s18 offen offset:1280 nt lds\n\t"
    "v_exp_f32_e64 v110, v110\n\t"
    "v_mfma_f32_16x16x32_bf16 a[12:15], a[224:227], v[156:159], a[12:15]\n\t"
    "ds_read_b128 a[220:223], v227 offset:36864\n\t"
    "v_add_f32_e64 v249, v249, v109\n\t"
    "v_cvt_pk_bf16_f32 v150, v108, v109\n\t"
    "v_mfma_f32_16x16x32_bf16 a[16:19], a[224:227], v[160:163], a[16:19]\n\t"
    "v_exp_f32_e64 v111, v111\n\t"
    "v_mfma_f32_16x16x32_bf16 a[40:43], a[228:231], v[160:163], a[40:43]\n\t"
    "v_add_f32 v249, v249, v110\n\t"
    "v_add_f32 v249, v249, v111\n\t"
    "v_cvt_pk_bf16_f32 v151, v110, v111\n\t"
    "v_mfma_f32_16x16x32_bf16 a[64:67], a[232:235], v[160:163], a[64:67]\n\t"
    "v_exp_f32_e64 v112, v112\n\t"
    "v_mfma_f32_16x16x32_bf16 a[88:91], a[236:239], v[160:163], a[88:91]\n\t"
    "s_add_u32 m0, s59, 37376\n\t"
    "s_waitcnt vmcnt(4)\n\t"
    "s_nop 0\n\t"
    "v_mfma_f32_16x16x32_bf16 a[112:115], a[240:243], v[160:163], a[112:115]\n\t"
    "buffer_load_dwordx4 v228, s[48:51], s18 offen offset:1536 nt lds\n\t"
    "v_exp_f32_e64 v113, v113\n\t"
    "v_mfma_f32_16x16x32_bf16 a[136:139], a[244:247], v[160:163], a[136:139]\n\t"
    "v_add_f32 v250, v250, v112\n\t"
    "v_exp_f32 v114, v114\n\t"
    "v_mfma_f32_16x16x32_bf16 a[160:163], a[248:251], v[160:163], a[160:163]\n\t"
    "v_add_f32_e64 v250, v250, v113\n\t"
    "v_cvt_pk_bf16_f32 v152, v112, v113\n\t"
    "v_mfma_f32_16x16x32_bf16 a[184:187], a[252:255], v[160:163], a[184:187]\n\t"
    "v_exp_f32_e64 v115, v115\n\t"
    "v_mfma_f32_16x16x32_bf16 a[188:191], a[252:255], v[164:167], a[188:191]\n\t"
    "v_add_f32 v250, v250, v114\n\t"
    "v_exp_f32 v116, v116\n\t"
    "v_mfma_f32_16x16x32_bf16 a[164:167], a[248:251], v[164:167], a[164:167]\n\t"
    "s_add_u32 m0, s59, 38144\n\t"
    "s_waitcnt vmcnt(5)\n\t"
    "v_add_f32 v250, v250, v115\n\t"
    "v_mfma_f32_16x16x32_bf16 a[140:143], a[244:247], v[164:167], a[140:143]\n\t"
    "buffer_load_dwordx4 v238, s[48:51], s18 offen offset:1792 nt lds\n\t"
    "s_or_b32 s18, s18, 12288\n\t"
    "s_add_u32 s18, s18, 2048\n\t"
    "s_and_b32 s18, s18, 116736\n\t"
    "v_cvt_pk_bf16_f32 v153, v114, v115\n\t"
    "v_mfma_f32_16x16x32_bf16 a[116:119], a[240:243], v[164:167], a[116:119]\n\t"
    "v_exp_f32_e64 v117, v117\n\t"
    "v_mfma_f32_16x16x32_bf16 a[92:95], a[236:239], v[164:167], a[92:95]\n\t"
    "v_add_f32 v250, v250, v116\n\t"
    "v_exp_f32 v118, v118\n\t"
    "v_mfma_f32_16x16x32_bf16 a[68:71], a[232:235], v[164:167], a[68:71]\n\t"
    "v_add_f32_e64 v250, v250, v117\n\t"
    "v_cvt_pk_bf16_f32 v154, v116, v117\n\t"
    "v_mfma_f32_16x16x32_bf16 a[44:47], a[228:231], v[164:167], a[44:47]\n\t"
    "v_exp_f32_e64 v119, v119\n\t"
    "v_mfma_f32_16x16x32_bf16 a[20:23], a[224:227], v[164:167], a[20:23]\n\t"
    "v_add_f32 v250, v250, v118\n\t"
    "v_add_f32 v250, v250, v119\n\t"
    "v_cvt_pk_bf16_f32 v155, v118, v119\n\t"
    "s_cmp_lg_u32 s22, 0\n\t"
    "s_cbranch_scc1 .Lk_resc\n\t"
    ".Lk_rescret_o:\n\t"
    "s_sub_u32 s16, s16, 1\n\t"
    "s_cmp_lg_u32 s16, 0\n\t"
    "s_cbranch_scc1 .Lk_aloop\n\t"
    "s_nop 0\n\t"
    "s_waitcnt vmcnt(16) lgkmcnt(0)\n\t"
    "s_nop 0\n\t"
    "s_add_u32 s56, s59, 65536\n\t"
    "v_mfma_f32_16x16x32_bf16 v[96:99], a[192:195], v[0:3], v[168:171]\n\t"
    "ds_read_b64_tr_b16 a[224:225], v216\n\t"
    "s_add_u32 m0, s56, 0\n\t"
    "s_waitcnt vmcnt(4)\n\t"
    "v_mfma_f32_16x16x32_bf16 v[96:99], a[196:199], v[24:27], v[96:99]\n\t"
    "buffer_load_dwordx4 v228, s[48:51], s18 offen nt lds\n\t"
    "v_exp_f32_e64 v120, v120\n\t"
    "v_mfma_f32_16x16x32_bf16 v[96:99], a[200:203], v[48:51], v[96:99]\n\t"
    "ds_read_b64_tr_b16 a[226:227], v216 offset:4096\n\t"
    "v_exp_f32 v121, v121\n\t"
    "v_add_f32 v251, v251, v120\n\t"
    "v_mfma_f32_16x16x32_bf16 v[96:99], a[204:207], v[72:75], v[96:99]\n\t"
    "v_exp_f32 v122, v122\n\t"
    "v_add_f32 v251, v251, v121\n\t"
    "v_mfma_f32_16x16x32_bf16 v[100:103], a[220:223], v[72:75], v[168:171]\n\t"
    "v_cvt_pk_bf16_f32 v156, v120, v121\n\t"
    "v_exp_f32_e64 v123, v123\n\t"
    "v_mfma_f32_16x16x32_bf16 v[100:103], a[216:219], v[48:51], v[100:103]\n\t"
    "ds_read_b64_tr_b16 a[228:229], v217\n\t"
    "s_add_u32 m0, s56, 768\n\t"
    "s_waitcnt vmcnt(4)\n\t"
    "v_add_f32 v251, v251, v122\n\t"
    "v_mfma_f32_16x16x32_bf16 v[100:103], a[212:215], v[24:27], v[100:103]\n\t"
    "buffer_load_dwordx4 v238, s[48:51], s18 offen offset:256 nt lds\n\t"
    "v_exp_f32 v124, v124\n\t"
    "v_add_f32 v251, v251, v123\n\t"
    "v_mfma_f32_16x16x32_bf16 v[100:103], a[208:211], v[0:3], v[100:103]\n\t"
    "ds_read_b64_tr_b16 a[230:231], v217 offset:4096\n\t"
    "v_cvt_pk_bf16_f32 v157, v122, v123\n\t"
    "v_mfma_f32_16x16x32_bf16 v[104:107], a[192:195], v[4:7], v[172:175]\n\t"
    "v_exp_f32 v125, v125\n\t"
    "v_add_f32 v251, v251, v124\n\t"
    "v_mfma_f32_16x16x32_bf16 v[104:107], a[196:199], v[28:31], v[104:107]\n\t"
    "v_exp_f32 v126, v126\n\t"
    "v_add_f32 v251, v251, v125\n\t"
    "v_cvt_pk_bf16_f32 v158, v124, v125\n\t"
    "v_mfma_f32_16x16x32_bf16 v[104:107], a[200:203], v[52:55], v[104:107]\n\t"
    "ds_read_b64_tr_b16 a[232:233], v218\n\t"
    "v_exp_f32_e64 v127, v127\n\t"
    "v_mfma_f32_16x16x32_bf16 v[104:107], a[204:207], v[76:79], v[104:107]\n\t"
    "s_add_u32 m0, s56, 1536\n\t"
    "s_waitcnt vmcnt(4)\n\t"
    "v_add_f32 v251, v251, v126\n\t"
    "v_add_f32_e64 v251, v251, v127\n\t"
    "v_cvt_pk_bf16_f32 v159, v126, v127\n\t"
    "v_mfma_f32_16x16x32_bf16 v[108:111], a[220:223], v[76:79], v[172:175]\n\t"
    "buffer_load_dwordx4 v228, s[48:51], s18 offen offset:512 nt lds\n\t"
    "v_exp_f32_e64 v128, v128\n\t"
    "v_mfma_f32_16x16x32_bf16 v[108:111], a[216:219], v[52:55], v[108:111]\n\t"
    "ds_read_b64_tr_b16 a[234:235], v218 offset:4096\n\t"
    "v_max_f32 v255, v96, v97\n\t"
    "v_exp_f32 v129, v129\n\t"
    "v_mfma_f32_16x16x32_bf16 v[108:111], a[212:215], v[28:31], v[108:111]\n\t"
    "v_max3_f32 v255, v255, v98, v99\n\t"
    "v_max3_f32 v255, v255, v100, v101\n\t"
    "v_add_f32_e64 v252, v252, v128\n\t"
    "v_mfma_f32_16x16x32_bf16 v[108:111], a[208:211], v[4:7], v[108:111]\n\t"
    "ds_read_b64_tr_b16 a[236:237], v219\n\t"
    "v_max3_f32 v255, v255, v102, v103\n\t"
    "v_exp_f32_e64 v130, v130\n\t"
    "v_mfma_f32_16x16x32_bf16 v[112:115], a[192:195], v[8:11], v[176:179]\n\t"
    "v_add_f32_e64 v252, v252, v129\n\t"
    "v_cvt_pk_bf16_f32 v160, v128, v129\n\t"
    "v_mfma_f32_16x16x32_bf16 v[112:115], a[196:199], v[32:35], v[112:115]\n\t"
    "ds_read_b64_tr_b16 a[238:239], v219 offset:4096\n\t"
    "s_add_u32 m0, s56, 2304\n\t"
    "s_waitcnt vmcnt(4)\n\t"
    "v_exp_f32 v131, v131\n\t"
    "v_add_f32_e64 v252, v252, v130\n\t"
    "v_mfma_f32_16x16x32_bf16 v[112:115], a[200:203], v[56:59], v[112:115]\n\t"
    "buffer_load_dwordx4 v238, s[48:51], s18 offen offset:768 nt lds\n\t"
    "v_exp_f32_e64 v132, v132\n\t"
    "v_mfma_f32_16x16x32_bf16 v[112:115], a[204:207], v[80:83], v[112:115]\n\t"
    "v_add_f32 v252, v252, v131\n\t"
    "v_cvt_pk_bf16_f32 v161, v130, v131\n\t"
    "v_exp_f32 v133, v133\n\t"
    "v_mfma_f32_16x16x32_bf16 v[116:119], a[220:223], v[80:83], v[176:179]\n\t"
    "ds_read_b64_tr_b16 a[240:241], v220\n\t"
    "v_add_f32_e64 v252, v252, v132\n\t"
    "v_mfma_f32_16x16x32_bf16 v[116:119], a[216:219], v[56:59], v[116:119]\n\t"
    "v_max3_f32 v255, v255, v104, v105\n\t"
    "v_max3_f32 v255, v255, v106, v107\n\t"
    "v_exp_f32 v134, v134\n\t"
    "v_add_f32 v252, v252, v133\n\t"
    "v_mfma_f32_16x16x32_bf16 v[116:119], a[212:215], v[32:35], v[116:119]\n\t"
    "ds_read_b64_tr_b16 a[242:243], v220 offset:4096\n\t"
    "v_max3_f32 v255, v255, v108, v109\n\t"
    "v_cvt_pk_bf16_f32 v162, v132, v133\n\t"
    "v_mfma_f32_16x16x32_bf16 v[116:119], a[208:211], v[8:11], v[116:119]\n\t"
    "s_add_u32 m0, s56, 3072\n\t"
    "s_waitcnt vmcnt(4)\n\t"
    "v_max3_f32 v255, v255, v110, v111\n\t"
    "v_exp_f32 v135, v135\n\t"
    "v_mfma_f32_16x16x32_bf16 v[120:123], a[192:195], v[12:15], v[180:183]\n\t"
    "buffer_load_dwordx4 v228, s[48:51], s18 offen offset:1024 nt lds\n\t"
    "v_add_f32 v252, v252, v134\n\t"
    "v_add_f32 v252, v252, v135\n\t"
    "v_cvt_pk_bf16_f32 v163, v134, v135\n\t"
    "v_mfma_f32_16x16x32_bf16 v[120:123], a[196:199], v[36:39], v[120:123]\n\t"
    "ds_read_b64_tr_b16 a[244:245], v221\n\t"
    "v_exp_f32_e64 v136, v136\n\t"
    "v_mfma_f32_16x16x32_bf16 v[120:123], a[200:203], v[60:63], v[120:123]\n\t"
    "v_exp_f32 v137, v137\n\t"
    "v_add_f32 v253, v253, v136\n\t"
    "v_mfma_f32_16x16x32_bf16 v[120:123], a[204:207], v[84:87], v[120:123]\n\t"
    "ds_read_b64_tr_b16 a[246:247], v221 offset:4096\n\t"
    "v_exp_f32_e64 v138, v138\n\t"
    "v_mfma_f32_16x16x32_bf16 v[124:127], a[220:223], v[84:87], v[180:183]\n\t"
    "v_max3_f32 v255, v255, v112, v113\n\t"
    "v_add_f32_e64 v253, v253, v137\n\t"
    "v_cvt_pk_bf16_f32 v164, v136, v137\n\t"
    "v_mfma_f32_16x16x32_bf16 v[124:127], a[216:219], v[60:63], v[124:127]\n\t"
    "s_add_u32 m0, s56, 3840\n\t"
    "s_waitcnt vmcnt(4)\n\t"
    "v_max3_f32 v255, v255, v114, v115\n\t"
    "v_exp_f32 v139, v139\n\t"
    "v_mfma_f32_16x16x32_bf16 v[124:127], a[212:215], v[36:39], v[124:127]\n\t"
    "buffer_load_dwordx4 v238, s[48:51], s18 offen offset:1280 nt lds\n\t"
    "v_max3_f32 v255, v255, v116, v117\n\t"
    "v_add_f32_e64 v253, v253, v138\n\t"
    "v_mfma_f32_16x16x32_bf16 v[124:127], a[208:211], v[12:15], v[124:127]\n\t"
    "ds_read_b64_tr_b16 a[248:249], v222\n\t"
    "v_max3_f32 v255, v255, v118, v119\n\t"
    "v_exp_f32_e64 v140, v140\n\t"
    "v_mfma_f32_16x16x32_bf16 v[128:131], a[192:195], v[16:19], v[184:187]\n\t"
    "ds_read_b64_tr_b16 a[250:251], v222 offset:4096\n\t"
    "v_add_f32_e64 v253, v253, v139\n\t"
    "v_cvt_pk_bf16_f32 v165, v138, v139\n\t"
    "v_mfma_f32_16x16x32_bf16 v[128:131], a[196:199], v[40:43], v[128:131]\n\t"
    "v_exp_f32 v141, v141\n\t"
    "v_add_f32 v253, v253, v140\n\t"
    "v_mfma_f32_16x16x32_bf16 v[128:131], a[200:203], v[64:67], v[128:131]\n\t"
    "v_exp_f32 v142, v142\n\t"
    "v_add_f32 v253, v253, v141\n\t"
    "v_cvt_pk_bf16_f32 v166, v140, v141\n\t"
    "v_mfma_f32_16x16x32_bf16 v[128:131], a[204:207], v[88:91], v[128:131]\n\t"
    "ds_read_b64_tr_b16 a[252:253], v223\n\t"
    "s_add_u32 m0, s56, 4608\n\t"
    "s_waitcnt vmcnt(4)\n\t"
    "s_nop 0\n\t"
    "v_mfma_f32_16x16x32_bf16 v[132:135], a[220:223], v[88:91], v[184:187]\n\t"
    "buffer_load_dwordx4 v228, s[48:51], s18 offen offset:1536 nt lds\n\t"
    "v_exp_f32 v143, v143\n\t"
    "v_add_f32 v253, v253, v142\n\t"
    "v_mfma_f32_16x16x32_bf16 v[132:135], a[216:219], v[64:67], v[132:135]\n\t"
    "ds_read_b64_tr_b16 a[254:255], v223 offset:4096\n\t"
    "v_max3_f32 v255, v255, v120, v121\n\t"
    "v_add_f32_e64 v253, v253, v143\n\t"
    "v_cvt_pk_bf16_f32 v167, v142, v143\n\t"
    "v_mfma_f32_16x16x32_bf16 v[132:135], a[212:215], v[40:43], v[132:135]\n\t"
    "v_max3_f32 v255, v255, v122, v123\n\t"
    "v_mfma_f32_16x16x32_bf16 v[132:135], a[208:211], v[16:19], v[132:135]\n\t"
    "v_max3_f32 v255, v255, v124, v125\n\t"
    "v_max3_f32 v255, v255, v126, v127\n\t"
    "v_mfma_f32_16x16x32_bf16 v[136:139], a[192:195], v[20:23], v[188:191]\n\t"
    "v_mfma_f32_16x16x32_bf16 v[136:139], a[196:199], v[44:47], v[136:139]\n\t"
    "s_add_u32 m0, s56, 5376\n\t"
    "s_waitcnt vmcnt(4)\n\t"
    "s_nop 0\n\t"
    "v_mfma_f32_16x16x32_bf16 v[136:139], a[200:203], v[68:71], v[136:139]\n\t"
    "buffer_load_dwordx4 v238, s[48:51], s18 offen offset:1792 nt lds\n\t"
    "s_or_b32 s18, s18, 12288\n\t"
    "s_add_u32 s18, s18, 2048\n\t"
    "s_and_b32 s18, s18, 116736\n\t"
    "v_mfma_f32_16x16x32_bf16 v[136:139], a[204:207], v[92:95], v[136:139]\n\t"
    "v_mfma_f32_16x16x32_bf16 v[140:143], a[220:223], v[92:95], v[188:191]\n\t"
    "v_max3_f32 v255, v255, v128, v129\n\t"
    "v_mfma_f32_16x16x32_bf16 v[140:143], a[216:219], v[68:71], v[140:143]\n\t"
    "v_max3_f32 v255, v255, v130, v131\n\t"
    "v_mfma_f32_16x16x32_bf16 v[140:143], a[212:215], v[44:47], v[140:143]\n\t"
    "v_max3_f32 v255, v255, v132, v133\n\t"
    "v_mfma_f32_16x16x32_bf16 v[140:143], a[208:211], v[20:23], v[140:143]\n\t"
    "v_max3_f32 v255, v255, v134, v135\n\t"
    "s_waitcnt vmcnt(16) lgkmcnt(0)\n\t"
    "s_nop 0\n\t"
    "v_mfma_f32_16x16x32_bf16 a[0:3], a[224:227], v[144:147], a[0:3]\n\t"
    "ds_read_b128 a[192:195], v224\n\t"
    "v_mfma_f32_16x16x32_bf16 a[24:27], a[228:231], v[144:147], a[24:27]\n\t"
    "v_mfma_f32_16x16x32_bf16 a[48:51], a[232:235], v[144:147], a[48:51]\n\t"
    "v_mfma_f32_16x16x32_bf16 a[72:75], a[236:239], v[144:147], a[72:75]\n\t"
    "v_mfma_f32_16x16x32_bf16 a[96:99], a[240:243], v[144:147], a[96:99]\n\t"
    "ds_read_b128 a[196:199], v225\n\t"
    "v_max3_f32 v255, v255, v136, v137\n\t"
    "v_mfma_f32_16x16x32_bf16 a[120:123], a[244:247], v[144:147], a[120:123]\n\t"
    "v_max3_f32 v255, v255, v138, v139\n\t"
    "v_max3_f32 v255, v255, v140, v141\n\t"
    "v_max3_f32 v255, v255, v142, v143\n\t"
    "v_mfma_f32_16x16x32_bf16 a[144:147], a[248:251], v[144:147], a[144:147]\n\t"
    "v_cmp_lt_f32 vcc, s21, v255\n\t"
    "s_mov_b32 s23, 4\n\t"
    "s_nop 0\n\t"
    "s_cbranch_vccnz .Lk_adj\n\t"
    ".Lk_adjret_g:\n\t"
    "v_mfma_f32_16x16x32_bf16 a[168:171], a[252:255], v[144:147], a[168:171]\n\t"
    "v_exp_f32_e64 v96, v96\n\t"
    "v_mfma_f32_16x16x32_bf16 a[172:175], a[252:255], v[148:151], a[172:175]\n\t"
    "v_exp_f32_e64 v97, v97\n\t"
    "v_mfma_f32_16x16x32_bf16 a[148:151], a[248:251], v[148:151], a[148:151]\n\t"
    "ds_read_b128 a[200:203], v226\n\t"
    "v_add_f32_e64 v248, v248, v96\n\t"
    "v_mfma_f32_16x16x32_bf16 a[124:127], a[244:247], v[148:151], a[124:127]\n\t"
    "v_exp_f32 v98, v98\n\t"
    "v_add_f32 v248, v248, v97\n\t"
    "v_mfma_f32_16x16x32_bf16 a[100:103], a[240:243], v[148:151], a[100:103]\n\t"
    "v_cvt_pk_bf16_f32 v144, v96, v97\n\t"
    "v_mfma_f32_16x16x32_bf16 a[76:79], a[236:239], v[148:151], a[76:79]\n\t"
    "v_exp_f32 v99, v99\n\t"
    "v_add_f32 v248, v248, v98\n\t"
    "v_mfma_f32_16x16x32_bf16 a[52:55], a[232:235], v[148:151], a[52:55]\n\t"
    "ds_read_b128 a[204:207], v227\n\t"
    "v_exp_f32_e64 v100, v100\n\t"
    "v_mfma_f32_16x16x32_bf16 a[28:31], a[228:231], v[148:151], a[28:31]\n\t"
    "v_add_f32_e64 v248, v248, v99\n\t"
    "v_cvt_pk_bf16_f32 v145, v98, v99\n\t"
    "v_mfma_f32_16x16x32_bf16 a[4:7], a[224:227], v[148:151], a[4:7]\n\t"
    "v_exp_f32 v101, v101\n\t"
    "v_add_f32 v248, v248, v100\n\t"
    "v_mfma_f32_16x16x32_bf16 a[8:11], a[224:227], v[152:155], a[8:11]\n\t"
    "v_exp_f32_e64 v102, v102\n\t"
    "v_mfma_f32_16x16x32_bf16 a[32:35], a[228:231], v[152:155], a[32:35]\n\t"
    "v_add_f32_e64 v248, v248, v101\n\t"
    "v_cvt_pk_bf16_f32 v146, v100, v101\n\t"
    "v_mfma_f32_16x16x32_bf16 a[56:59], a[232:235], v[152:155], a[56:59]\n\t"
    "ds_read_b128 a[208:211], v224 offset:4096\n\t"
    "v_exp_f32_e64 v103, v103\n\t"
    "v_mfma_f32_16x16x32_bf16 a[80:83], a[236:239], v[152:155], a[80:83]\n\t"
    "v_add_f32 v248, v248, v102\n\t"
    "v_add_f32 v248, v248, v103\n\t"
    "v_cvt_pk_bf16_f32 v147, v102, v103\n\t"
    "v_mfma_f32_16x16x32_bf16 a[104:107], a[240:243], v[152:155], a[104:107]\n\t"
    "v_exp_f32_e64 v104, v104\n\t"
    "v_mfma_f32_16x16x32_bf16 a[128:131], a[244:247], v[152:155], a[128:131]\n\t"
    "v_exp_f32_e64 v105, v105\n\t"
    "v_mfma_f32_16x16x32_bf16 a[152:155], a[248:251], v[152:155], a[152:155]\n\t"
    "ds_read_b128 a[212:215], v225 offset:4096\n\t"
    "v_add_f32_e64 v249, v249, v104\n\t"
    "v_mfma_f32_16x16x32_bf16 a[176:179], a[252:255], v[152:155], a[176:179]\n\t"
    "v_exp_f32_e64 v106, v106\n\t"
    "v_mfma_f32_16x16x32_bf16 a[180:183], a[252:255], v[156:159], a[180:183]\n\t"
    "v_add_f32_e64 v249, v249, v105\n\t"
    "v_cvt_pk_bf16_f32 v148, v104, v105\n\t"
    "v_mfma_f32_16x16x32_bf16 a[156:159], a[248:251], v[156:159], a[156:159]\n\t"
    "v_exp_f32 v107, v107\n\t"
    "v_add_f32 v249, v249, v106\n\t"
    "v_mfma_f32_16x16x32_bf16 a[132:135], a[244:247], v[156:159], a[132:135]\n\t"
    "v_exp_f32_e64 v108, v108\n\t"
    "v_mfma_f32_16x16x32_bf16 a[108:111], a[240:243], v[156:159], a[108:111]\n\t"
    "ds_read_b128 a[216:219], v226 offset:4096\n\t"
    "v_add_f32_e64 v249, v249, v107\n\t"
    "v_mfma_f32_16x16x32_bf16 a[84:87], a[236:239], v[156:159], a[84:87]\n\t"
    "v_cvt_pk_bf16_f32 v149, v106, v107\n\t"
    "v_mfma_f32_16x16x32_bf16 a[60:63], a[232:235], v[156:159], a[60:63]\n\t"
    "v_exp_f32 v109, v109\n\t"
    "v_add_f32 v249, v249, v108\n\t"
    "v_mfma_f32_16x16x32_bf16 a[36:39], a[228:231], v[156:159], a[36:39]\n\t"
    "v_exp_f32 v110, v110\n\t"
    "v_add_f32 v249, v249, v109\n\t"
    "v_mfma_f32_16x16x32_bf16 a[12:15], a[224:227], v[156:159], a[12:15]\n\t"
    "ds_read_b128 a[220:223], v227 offset:4096\n\t"
    "v_cvt_pk_bf16_f32 v150, v108, v109\n\t"
    "v_mfma_f32_16x16x32_bf16 a[16:19], a[224:227], v[160:163], a[16:19]\n\t"
    "v_exp_f32_e64 v111, v111\n\t"
    "v_mfma_f32_16x16x32_bf16 a[40:43], a[228:231], v[160:163], a[40:43]\n\t"
    "v_add_f32 v249, v249, v110\n\t"
    "v_add_f32 v249, v249, v111\n\t"
    "v_cvt_pk_bf16_f32 v151, v110, v111\n\t"
    "v_mfma_f32_16x16x32_bf16 a[64:67], a[232:235], v[160:163], a[64:67]\n\t"
    "v_exp_f32_e64 v112, v112\n\t"
    "v_mfma_f32_16x16x32_bf16 a[88:91], a[236:239], v[160:163], a[88:91]\n\t"
    "v_exp_f32_e64 v113, v113\n\t"
    "v_mfma_f32_16x16x32_bf16 a[112:115], a[240:243], v[160:163], a[112:115]\n\t"
    "v_add_f32_e64 v250, v250, v112\n\t"
    "v_mfma_f32_16x16x32_bf16 a[136:139], a[244:247], v[160:163], a[136:139]\n\t"
    "v_exp_f32 v114, v114\n\t"
    "v_add_f32 v250, v250, v113\n\t"
    "v_mfma_f32_16x16x32_bf16 a[160:163], a[248:251], v[160:163], a[160:163]\n\t"
    "v_cvt_pk_bf16_f32 v152, v112, v113\n\t"
    "v_mfma_f32_16x16x32_bf16 a[184:187], a[252:255], v[160:163], a[184:187]\n\t"
    "v_exp_f32 v115, v115\n\t"
    "v_add_f32 v250, v250, v114\n\t"
    "v_mfma_f32_16x16x32_bf16 a[188:191], a[252:255], v[164:167], a[188:191]\n\t"
    "v_exp_f32_e64 v116, v116\n\t"
    "v_mfma_f32_16x16x32_bf16 a[164:167], a[248:251], v[164:167], a[164:167]\n\t"
    "v_add_f32_e64 v250, v250, v115\n\t"
    "v_cvt_pk_bf16_f32 v153, v114, v115\n\t"
    "v_mfma_f32_16x16x32_bf16 a[140:143], a[244:247], v[164:167], a[140:143]\n\t"
    "v_exp_f32_e64 v117, v117\n\t"
    "v_mfma_f32_16x16x32_bf16 a[116:119], a[240:243], v[164:167], a[116:119]\n\t"
    "v_add_f32_e64 v250, v250, v116\n\t"
    "v_mfma_f32_16x16x32_bf16 a[92:95], a[236:239], v[164:167], a[92:95]\n\t"
    "v_exp_f32 v118, v118\n\t"
    "v_add_f32 v250, v250, v117\n\t"
    "v_mfma_f32_16x16x32_bf16 a[68:71], a[232:235], v[164:167], a[68:71]\n\t"
    "v_cvt_pk_bf16_f32 v154, v116, v117\n\t"
    "v_mfma_f32_16x16x32_bf16 a[44:47], a[228:231], v[164:167], a[44:47]\n\t"
    "v_exp_f32 v119, v119\n\t"
    "v_add_f32 v250, v250, v118\n\t"
    "v_mfma_f32_16x16x32_bf16 a[20:23], a[224:227], v[164:167], a[20:23]\n\t"
    "v_add_f32 v250, v250, v119\n\t"
    "v_cvt_pk_bf16_f32 v155, v118, v119\n\t"
    "s_cmp_lg_u32 s22, 0\n\t"
    "s_cbranch_scc1 .Lk_resc\n\t"
    ".Lk_rescret_g:\n\t"
    "s_waitcnt vmcnt(8) lgkmcnt(0)\n\t"
    "v_mfma_f32_16x16x32_bf16 v[96:99], a[192:195], v[0:3], v[168:171]\n\t"
    "ds_read_b64_tr_b16 a[224:225], v216 offset:32768\n\t"
    "v_mfma_f32_16x16x32_bf16 v[96:99], a[196:199], v[24:27], v[96:99]\n\t"
    "v_exp_f32 v120, v120\n\t"
    "v_exp_f32 v121, v121\n\t"
    "v_mfma_f32_16x16x32_bf16 v[96:99], a[200:203], v[48:51], v[96:99]\n\t"
    "ds_read_b64_tr_b16 a[226:227], v216 offset:36864\n\t"
    "v_add_f32_e64 v251, v251, v120\n\t"
    "v_mfma_f32_16x16x32_bf16 v[96:99], a[204:207], v[72:75], v[96:99]\n\t"
    "v_exp_f32 v122, v122\n\t"
    "v_add_f32 v251, v251, v121\n\t"
    "v_cvt_pk_bf16_f32 v156, v120, v121\n\t"
    "v_mfma_f32_16x16x32_bf16 v[100:103], a[220:223], v[72:75], v[168:171]\n\t"
    "v_exp_f32_e64 v123, v123\n\t"
    "v_mfma_f32_16x16x32_bf16 v[100:103], a[216:219], v[48:51], v[100:103]\n\t"
    "ds_read_b64_tr_b16 a[228:229], v217 offset:32768\n\t"
    "v_add_f32_e64 v251, v251, v122\n\t"
    "v_mfma_f32_16x16x32_bf16 v[100:103], a[212:215], v[24:27], v[100:103]\n\t"
    "v_exp_f32 v124, v124\n\t"
    "v_add_f32 v251, v251, v123\n\t"
    "v_cvt_pk_bf16_f32 v157, v122, v123\n\t"
    "v_mfma_f32_16x16x32_bf16 v[100:103], a[208:211], v[0:3], v[100:103]\n\t"
    "ds_read_b64_tr_b16 a[230:231], v217 offset:36864\n\t"
    "v_exp_f32_e64 v125, v125\n\t"
    "v_mfma_f32_16x16x32_bf16 v[104:107], a[192:195], v[4:7], v[172:175]\n\t"
    "v_add_f32_e64 v251, v251, v124\n\t"
    "v_mfma_f32_16x16x32_bf16 v[104:107], a[196:199], v[28:31], v[104:107]\n\t"
    "v_exp_f32 v126, v126\n\t"
    "v_add_f32 v251, v251, v125\n\t"
    "v_cvt_pk_bf16_f32 v158, v124, v125\n\t"
    "v_mfma_f32_16x16x32_bf16 v[104:107], a[200:203], v[52:55], v[104:107]\n\t"
    "ds_read_b64_tr_b16 a[232:233], v218 offset:32768\n\t"
    "v_exp_f32_e64 v127, v127\n\t"
    "v_mfma_f32_16x16x32_bf16 v[104:107], a[204:207], v[76:79], v[104:107]\n\t"
    "v_add_f32 v251, v251, v126\n\t"
    "v_add_f32 v251, v251, v127\n\t"
    "v_cvt_pk_bf16_f32 v159, v126, v127\n\t"
    "v_mfma_f32_16x16x32_bf16 v[108:111], a[220:223], v[76:79], v[172:175]\n\t"
    "ds_read_b64_tr_b16 a[234:235], v218 offset:36864\n\t"
    "v_exp_f32_e64 v128, v128\n\t"
    "v_mfma_f32_16x16x32_bf16 v[108:111], a[216:219], v[52:55], v[108:111]\n\t"
    "v_max_f32_e64 v255, v96, v97\n\t"
    "v_max3_f32 v255, v255, v98, v99\n\t"
    "v_mfma_f32_16x16x32_bf16 v[108:111], a[212:215], v[28:31], v[108:111]\n\t"
    "v_max3_f32 v255, v255, v100, v101\n\t"
    "v_exp_f32 v129, v129\n\t"
    "v_add_f32 v252, v252, v128\n\t"
    "v_mfma_f32_16x16x32_bf16 v[108:111], a[208:211], v[4:7], v[108:111]\n\t"
    "ds_read_b64_tr_b16 a[236:237], v219 offset:32768\n\t"
    "s_mov_b32 s28, s63\n\t"
    "s_lshl_b32 s28, s28, 19\n\t"
    "s_add_u32 s28, s28, 0\n\t"
    "s_add_u32 m0, s59, 0\n\t"
    "v_max3_f32 v255, v255, v102, v103\n\t"
    "v_exp_f32_e64 v130, v130\n\t"
    "v_mfma_f32_16x16x32_bf16 v[112:115], a[192:195], v[8:11], v[176:179]\n\t"
    "buffer_load_dwordx4 v198, s[64:67], s28 offen lds\n\t"
    "v_add_f32_e64 v252, v252, v129\n\t"
    "v_cvt_pk_bf16_f32 v160, v128, v129\n\t"
    "v_mfma_f32_16x16x32_bf16 v[112:115], a[196:199], v[32:35], v[112:115]\n\t"
    "ds_read_b64_tr_b16 a[238:239], v219 offset:36864\n\t"
    "v_exp_f32 v131, v131\n\t"
    "v_add_f32 v252, v252, v130\n\t"
    "v_mfma_f32_16x16x32_bf16 v[112:115], a[200:203], v[56:59], v[112:115]\n\t"
    "v_exp_f32 v132, v132\n\t"
    "v_add_f32 v252, v252, v131\n\t"
    "v_mfma_f32_16x16x32_bf16 v[112:115], a[204:207], v[80:83], v[112:115]\n\t"
    "buffer_load_dwordx4 v199, s[64:67], s28 offen offset:1024 lds\n\t"
    "v_cvt_pk_bf16_f32 v161, v130, v131\n\t"
    "v_exp_f32_e64 v133, v133\n\t"
    "v_mfma_f32_16x16x32_bf16 v[116:119], a[220:223], v[80:83], v[176:179]\n\t"
    "ds_read_b64_tr_b16 a[240:241], v220 offset:32768\n\t"
    "v_add_f32_e64 v252, v252, v132\n\t"
    "v_mfma_f32_16x16x32_bf16 v[116:119], a[216:219], v[56:59], v[116:119]\n\t"
    "v_max3_f32 v255, v255, v104, v105\n\t"
    "v_max3_f32 v255, v255, v106, v107\n\t"
    "v_exp_f32_e64 v134, v134\n\t"
    "v_mfma_f32_16x16x32_bf16 v[116:119], a[212:215], v[32:35], v[116:119]\n\t"
    "ds_read_b64_tr_b16 a[242:243], v220 offset:36864\n\t"
    "s_mov_b32 s29, s63\n\t"
    "s_lshl_b32 s29, s29, 19\n\t"
    "s_add_u32 s29, s29, 65536\n\t"
    "s_add_u32 m0, s59, 2048\n\t"
    "v_max3_f32 v255, v255, v108, v109\n\t"
    "v_add_f32_e64 v252, v252, v133\n\t"
    "v_cvt_pk_bf16_f32 v162, v132, v133\n\t"
    "v_mfma_f32_16x16x32_bf16 v[116:119], a[208:211], v[8:11], v[116:119]\n\t"
    "buffer_load_dwordx4 v198, s[64:67], s29 offen lds\n\t"
    "v_max3_f32 v255, v255, v110, v111\n\t"
    "v_mfma_f32_16x16x32_bf16 v[120:123], a[192:195], v[12:15], v[180:183]\n\t"
    "v_exp_f32 v135, v135\n\t"
    "v_add_f32 v252, v252, v134\n\t"
    "v_add_f32_e64 v252, v252, v135\n\t"
    "v_cvt_pk_bf16_f32 v163, v134, v135\n\t"
    "v_mfma_f32_16x16x32_bf16 v[120:123], a[196:199], v[36:39], v[120:123]\n\t"
    "ds_read_b64_tr_b16 a[244:245], v221 offset:32768\n\t"
    "v_exp_f32_e64 v136, v136\n\t"
    "v_mfma_f32_16x16x32_bf16 v[120:123], a[200:203], v[60:63], v[120:123]\n\t"
    "v_exp_f32 v137, v137\n\t"
    "v_add_f32 v253, v253, v136\n\t"
    "v_mfma_f32_16x16x32_bf16 v[120:123], a[204:207], v[84:87], v[120:123]\n\t"
    "buffer_load_dwordx4 v199, s[64:67], s29 offen offset:1024 lds\n\t"
    "v_exp_f32_e64 v138, v138\n\t"
    "v_mfma_f32_16x16x32_bf16 v[124:127], a[220:223], v[84:87], v[180:183]\n\t"
    "ds_read_b64_tr_b16 a[246:247], v221 offset:36864\n\t"
    "v_add_f32_e64 v253, v253, v137\n\t"
    "v_cvt_pk_bf16_f32 v164, v136, v137\n\t"
    "v_mfma_f32_16x16x32_bf16 v[124:127], a[216:219], v[60:63], v[124:127]\n\t"
    "v_max3_f32 v255, v255, v112, v113\n\t"
    "v_max3_f32 v255, v255, v114, v115\n\t"
    "v_exp_f32_e64 v139, v139\n\t"
    "v_mfma_f32_16x16x32_bf16 v[124:127], a[212:215], v[36:39], v[124:127]\n\t"
    "ds_read_b64_tr_b16 a[248:249], v222 offset:32768\n\t"
    "s_add_u32 m0, s59, 4096\n\t"
    "v_max3_f32 v255, v255, v116, v117\n\t"
    "v_add_f32_e64 v253, v253, v138\n\t"
    "v_mfma_f32_16x16x32_bf16 v[124:127], a[208:211], v[12:15], v[124:127]\n\t"
    "buffer_load_dwordx4 v196, s[64:67], s28 offen lds\n\t"
    "v_max3_f32 v255, v255, v118, v119\n\t"
    "v_mfma_f32_16x16x32_bf16 v[128:131], a[192:195], v[16:19], v[184:187]\n\t"
    "ds_read_b64_tr_b16 a[250:251], v222 offset:36864\n\t"
    "v_exp_f32 v140, v140\n\t"
    "v_add_f32 v253, v253, v139\n\t"
    "v_mfma_f32_16x16x32_bf16 v[128:131], a[196:199], v[40:43], v[128:131]\n\t"
    "v_cvt_pk_bf16_f32 v165, v138, v139\n\t"
    "v_exp_f32 v141, v141\n\t"
    "v_add_f32 v253, v253, v140\n\t"
    "v_mfma_f32_16x16x32_bf16 v[128:131], a[200:203], v[64:67], v[128:131]\n\t"
    "v_exp_f32 v142, v142\n\t"
    "v_add_f32 v253, v253, v141\n\t"
    "v_mfma_f32_16x16x32_bf16 v[128:131], a[204:207], v[88:91], v[128:131]\n\t"
    "buffer_load_dwordx4 v197, s[64:67], s28 offen offset:1024 lds\n\t"
    "v_cvt_pk_bf16_f32 v166, v140, v141\n\t"
    "v_mfma_f32_16x16x32_bf16 v[132:135], a[220:223], v[88:91], v[184:187]\n\t"
    "ds_read_b64_tr_b16 a[252:253], v223 offset:32768\n\t"
    "v_exp_f32 v143, v143\n\t"
    "v_add_f32 v253, v253, v142\n\t"
    "v_mfma_f32_16x16x32_bf16 v[132:135], a[216:219], v[64:67], v[132:135]\n\t"
    "ds_read_b64_tr_b16 a[254:255], v223 offset:36864\n\t"
    "v_max3_f32 v255, v255, v120, v121\n\t"
    "v_add_f32_e64 v253, v253, v143\n\t"
    "v_cvt_pk_bf16_f32 v167, v142, v143\n\t"
    "v_mfma_f32_16x16x32_bf16 v[132:135], a[212:215], v[40:43], v[132:135]\n\t"
    "s_add_u32 m0, s59, 6144\n\t"
    "v_max3_f32 v255, v255, v122, v123\n\t"
    "v_max3_f32 v255, v255, v124, v125\n\t"
    "v_mfma_f32_16x16x32_bf16 v[132:135], a[208:211], v[16:19], v[132:135]\n\t"
    "buffer_load_dwordx4 v196, s[64:67], s29 offen lds\n\t"
    "v_max3_f32 v255, v255, v126, v127\n\t"
    "v_mfma_f32_16x16x32_bf16 v[136:139], a[192:195], v[20:23], v[188:191]\n\t"
    "v_mfma_f32_16x16x32_bf16 v[136:139], a[196:199], v[44:47], v[136:139]\n\t"
    "v_mfma_f32_16x16x32_bf16 v[136:139], a[200:203], v[68:71], v[136:139]\n\t"
    "v_mfma_f32_16x16x32_bf16 v[136:139], a[204:207], v[92:95], v[136:139]\n\t"
    "buffer_load_dwordx4 v197, s[64:67], s29 offen offset:1024 lds\n\t"
    "v_mfma_f32_16x16x32_bf16 v[140:143], a[220:223], v[92:95], v[188:191]\n\t"
    "v_max3_f32 v255, v255, v128, v129\n\t"
    "v_mfma_f32_16x16x32_bf16 v[140:143], a[216:219], v[68:71], v[140:143]\n\t"
    "v_max3_f32 v255, v255, v130, v131\n\t"
    "v_mfma_f32_16x16x32_bf16 v[140:143], a[212:215], v[44:47], v[140:143]\n\t"
    "v_max3_f32 v255, v255, v132, v133\n\t"
    "v_mfma_f32_16x16x32_bf16 v[140:143], a[208:211], v[20:23], v[140:143]\n\t"
    "v_max3_f32 v255, v255, v134, v135\n\t"
    "s_waitcnt vmcnt(8) lgkmcnt(0)\n\t"
    "s_nop 0\n\t"
    "v_mfma_f32_16x16x32_bf16 a[0:3], a[224:227], v[144:147], a[0:3]\n\t"
    "s_mov_b32 s28, s63\n\t"
    "s_lshl_b32 s28, s28, 19\n\t"
    "s_add_u32 s28, s28, 131072\n\t"
    "s_add_u32 m0, s59, 32768\n\t"
    "v_add_u32 v216, 65536, v216\n\t"
    "v_add_u32 v217, 65536, v217\n\t"
    "v_add_u32 v218, 65536, v218\n\t"
    "v_mfma_f32_16x16x32_bf16 a[24:27], a[228:231], v[144:147], a[24:27]\n\t"
    "buffer_load_dwordx4 v198, s[64:67], s28 offen lds\n\t"
    "v_add_u32 v219, 65536, v219\n\t"
    "v_add_u32 v220, 65536, v220\n\t"
    "v_mfma_f32_16x16x32_bf16 a[48:51], a[232:235], v[144:147], a[48:51]\n\t"
    "v_add_u32 v221, 65536, v221\n\t"
    "v_add_u32 v222, 65536, v222\n\t"
    "v_add_u32 v223, 65536, v223\n\t"
    "v_mfma_f32_16x16x32_bf16 a[72:75], a[236:239], v[144:147], a[72:75]\n\t"
    "v_mfma_f32_16x16x32_bf16 a[96:99], a[240:243], v[144:147], a[96:99]\n\t"
    "ds_read_b64_tr_b16 a[192:193], v216\n\t"
    "v_max3_f32 v255, v255, v136, v137\n\t"
    "v_max3_f32 v255, v255, v138, v139\n\t"
    "v_mfma_f32_16x16x32_bf16 a[120:123], a[244:247], v[144:147], a[120:123]\n\t"
    "buffer_load_dwordx4 v199, s[64:67], s28 offen offset:1024 lds\n\t"
    "v_max3_f32 v255, v255, v140, v141\n\t"
    "v_max3_f32 v255, v255, v142, v143\n\t"
    "v_mfma_f32_16x16x32_bf16 a[144:147], a[248:251], v[144:147], a[144:147]\n\t"
    "ds_read_b64_tr_b16 a[194:195], v216 offset:4096\n\t"
    "v_cmp_lt_f32 vcc, s21, v255\n\t"
    "s_mov_b32 s23, 3\n\t"
    "s_nop 0\n\t"
    "s_cbranch_vccnz .Lk_adj\n\t"
    ".Lk_adjret_f:\n\t"
    "v_mfma_f32_16x16x32_bf16 a[168:171], a[252:255], v[144:147], a[168:171]\n\t"
    "v_exp_f32 v96, v96\n\t"
    "v_exp_f32 v97, v97\n\t"
    "v_mfma_f32_16x16x32_bf16 a[172:175], a[252:255], v[148:151], a[172:175]\n\t"
    "ds_read_b64_tr_b16 a[196:197], v217\n\t"
    "s_mov_b32 s29, s63\n\t"
    "s_lshl_b32 s29, s29, 19\n\t"
    "s_add_u32 s29, s29, 196608\n\t"
    "s_add_u32 m0, s59, 34816\n\t"
    "v_add_f32 v248, v248, v96\n\t"
    "v_exp_f32 v98, v98\n\t"
    "v_add_f32_e64 v248, v248, v97\n\t"
    "v_mfma_f32_16x16x32_bf16 a[148:151], a[248:251], v[148:151], a[148:151]\n\t"
    "buffer_load_dwordx4 v198, s[64:67], s29 offen lds\n\t"
    "v_cvt_pk_bf16_f32 v168, v96, v97\n\t"
    "v_exp_f32_e64 v99, v99\n\t"
    "v_mfma_f32_16x16x32_bf16 a[124:127], a[244:247], v[148:151], a[124:127]\n\t"
    "ds_read_b64_tr_b16 a[198:199], v217 offset:4096\n\t"
    "v_add_f32 v248, v248, v98\n\t"
    "v_exp_f32 v100, v100\n\t"
    "v_add_f32_e64 v248, v248, v99\n\t"
    "v_mfma_f32_16x16x32_bf16 a[100:103], a[240:243], v[148:151], a[100:103]\n\t"
    "v_cvt_pk_bf16_f32 v169, v98, v99\n\t"
    "v_exp_f32 v101, v101\n\t"
    "v_add_f32 v248, v248, v100\n\t"
    "v_mfma_f32_16x16x32_bf16 a[76:79], a[236:239], v[148:151], a[76:79]\n\t"
    "v_exp_f32 v102, v102\n\t"
    "v_add_f32 v248, v248, v101\n\t"
    "v_cvt_pk_bf16_f32 v170, v100, v101\n\t"
    "v_mfma_f32_16x16x32_bf16 a[52:55], a[232:235], v[148:151], a[52:55]\n\t"
    "ds_read_b64_tr_b16 a[200:201], v218\n\t"
    "v_exp_f32 v103, v103\n\t"
    "v_add_f32 v248, v248, v102\n\t"
    "v_exp_f32_e64 v104, v104\n\t"
    "v_mfma_f32_16x16x32_bf16 a[28:31], a[228:231], v[148:151], a[28:31]\n\t"
    "buffer_load_dwordx4 v199, s[64:67], s29 offen offset:1024 lds\n\t"
    "v_exp_f32 v105, v105\n\t"
    "v_add_f32 v248, v248, v103\n\t"
    "v_mfma_f32_16x16x32_bf16 a[4:7], a[224:227], v[148:151], a[4:7]\n\t"
    "ds_read_b64_tr_b16 a[202:203], v218 offset:4096\n\t"
    "v_cvt_pk_bf16_f32 v171, v102, v103\n\t"
    "v_add_f32 v249, v249, v104\n\t"
    "v_exp_f32 v106, v106\n\t"
    "v_mfma_f32_16x16x32_bf16 a[8:11], a[224:227], v[152:155], a[8:11]\n\t"
    "v_add_f32 v249, v249, v105\n\t"
    "v_cvt_pk_bf16_f32 v172, v104, v105\n\t"
    "v_exp_f32 v107, v107\n\t"
    "v_mfma_f32_16x16x32_bf16 a[32:35], a[228:231], v[152:155], a[32:35]\n\t"
    "ds_read_b64_tr_b16 a[204:205], v219\n\t"
    "s_add_u32 m0, s59, 36864\n\t"
    "v_add_f32 v249, v249, v106\n\t"
    "v_exp_f32 v108, v108\n\t"
    "v_add_f32_e64 v249, v249, v107\n\t"
    "v_mfma_f32_16x16x32_bf16 a[56:59], a[232:235], v[152:155], a[56:59]\n\t"
    "buffer_load_dwordx4 v196, s[64:67], s28 offen lds\n\t"
    "v_cvt_pk_bf16_f32 v173, v106, v107\n\t"
    "v_exp_f32 v109, v109\n\t"
    "v_add_f32 v249, v249, v108\n\t"
    "v_mfma_f32_16x16x32_bf16 a[80:83], a[236:239], v[152:155], a[80:83]\n\t"
    "ds_read_b64_tr_b16 a[206:207], v219 offset:4096\n\t"
    "v_exp_f32 v110, v110\n\t"
    "v_add_f32 v249, v249, v109\n\t"
    "v_mfma_f32_16x16x32_bf16 a[104:107], a[240:243], v[152:155], a[104:107]\n\t"
    "v_cvt_pk_bf16_f32 v174, v108, v109\n\t"
    "v_exp_f32 v111, v111\n\t"
    "v_add_f32 v249, v249, v110\n\t"
    "v_mfma_f32_16x16x32_bf16 a[128:131], a[244:247], v[152:155], a[128:131]\n\t"
    "v_exp_f32 v112, v112\n\t"
    "v_exp_f32 v113, v113\n\t"
    "v_add_f32_e64 v249, v249, v111\n\t"
    "v_mfma_f32_16x16x32_bf16 a[152:155], a[248:251], v[152:155], a[152:155]\n\t"
    "buffer_load_dwordx4 v197, s[64:67], s28 offen offset:1024 lds\n\t"
    "v_cvt_pk_bf16_f32 v175, v110, v111\n\t"
    "v_add_f32 v250, v250, v112\n\t"
    "v_exp_f32 v114, v114\n\t"
    "v_mfma_f32_16x16x32_bf16 a[176:179], a[252:255], v[152:155], a[176:179]\n\t"
    "ds_read_b64_tr_b16 a[208:209], v220\n\t"
    "v_add_f32_e64 v250, v250, v113\n\t"
    "v_cvt_pk_bf16_f32 v176, v112, v113\n\t"
    "v_mfma_f32_16x16x32_bf16 a[180:183], a[252:255], v[156:159], a[180:183]\n\t"
    "ds_read_b64_tr_b16 a[210:211], v220 offset:4096\n\t"
    "v_exp_f32 v115, v115\n\t"
    "v_add_f32 v250, v250, v114\n\t"
    "v_exp_f32_e64 v116, v116\n\t"
    "v_mfma_f32_16x16x32_bf16 a[156:159], a[248:251], v[156:159], a[156:159]\n\t"
    "v_add_f32 v250, v250, v115\n\t"
    "v_cvt_pk_bf16_f32 v177, v114, v115\n\t"
    "v_exp_f32 v117, v117\n\t"
    "v_add_f32_e64 v250, v250, v116\n\t"
    "v_mfma_f32_16x16x32_bf16 a[132:135], a[244:247], v[156:159], a[132:135]\n\t"
    "ds_read_b64_tr_b16 a[212:213], v221\n\t"
    "s_add_u32 m0, s59, 38912\n\t"
    "v_exp_f32 v118, v118\n\t"
    "v_add_f32 v250, v250, v117\n\t"
    "v_mfma_f32_16x16x32_bf16 a[108:111], a[240:243], v[156:159], a[108:111]\n\t"
    "buffer_load_dwordx4 v196, s[64:67], s29 offen lds\n\t"
    "v_cvt_pk_bf16_f32 v178, v116, v117\n\t"
    "v_exp_f32 v119, v119\n\t"
    "v_add_f32 v250, v250, v118\n\t"
    "v_mfma_f32_16x16x32_bf16 a[84:87], a[236:239], v[156:159], a[84:87]\n\t"
    "ds_read_b64_tr_b16 a[214:215], v221 offset:4096\n\t"
    "v_exp_f32_e64 v120, v120\n\t"
    "v_mfma_f32_16x16x32_bf16 a[60:63], a[232:235], v[156:159], a[60:63]\n\t"
    "v_exp_f32 v121, v121\n\t"
    "v_add_f32 v250, v250, v119\n\t"
    "v_cvt_pk_bf16_f32 v179, v118, v119\n\t"
    "v_add_f32_e64 v251, v251, v120\n\t"
    "v_mfma_f32_16x16x32_bf16 a[36:39], a[228:231], v[156:159], a[36:39]\n\t"
    "v_exp_f32 v122, v122\n\t"
    "v_add_f32 v251, v251, v121\n\t"
    "v_cvt_pk_bf16_f32 v180, v120, v121\n\t"
    "v_exp_f32_e64 v123, v123\n\t"
    "v_mfma_f32_16x16x32_bf16 a[12:15], a[224:227], v[156:159], a[12:15]\n\t"
    "buffer_load_dwordx4 v197, s[64:67], s29 offen offset:1024 lds\n\t"
    "v_add_f32 v251, v251, v122\n\t"
    "v_exp_f32 v124, v124\n\t"
    "v_mfma_f32_16x16x32_bf16 a[16:19], a[224:227], v[160:163], a[16:19]\n\t"
    "ds_read_b64_tr_b16 a[216:217], v222\n\t"
    "v_add_f32 v251, v251, v123\n\t"
    "v_cvt_pk_bf16_f32 v181, v122, v123\n\t"
    "v_exp_f32 v125, v125\n\t"
    "v_mfma_f32_16x16x32_bf16 a[40:43], a[228:231], v[160:163], a[40:43]\n\t"
    "ds_read_b64_tr_b16 a[218:219], v222 offset:4096\n\t"
    "v_add_f32 v251, v251, v124\n\t"
    "v_exp_f32 v126, v126\n\t"
    "v_mfma_f32_16x16x32_bf16 a[64:67], a[232:235], v[160:163], a[64:67]\n\t"
    "v_add_f32 v251, v251, v125\n\t"
    "v_cvt_pk_bf16_f32 v182, v124, v125\n\t"
    "v_exp_f32 v127, v127\n\t"
    "v_add_f32_e64 v251, v251, v126\n\t"
    "v_mfma_f32_16x16x32_bf16 a[88:91], a[236:239], v[160:163], a[88:91]\n\t"
    "ds_read_b64_tr_b16 a[220:221], v223\n\t"
    "v_exp_f32_e64 v128, v128\n\t"
    "v_mfma_f32_16x16x32_bf16 a[112:115], a[240:243], v[160:163], a[112:115]\n\t"
    "v_exp_f32 v129, v129\n\t"
    "v_add_f32 v251, v251, v127\n\t"
    "v_cvt_pk_bf16_f32 v183, v126, v127\n\t"
    "v_add_f32_e64 v252, v252, v128\n\t"
    "v_mfma_f32_16x16x32_bf16 a[136:139], a[244:247], v[160:163], a[136:139]\n\t"
    "ds_read_b64_tr_b16 a[222:223], v223 offset:4096\n\t"
    "v_exp_f32 v130, v130\n\t"
    "v_add_f32 v252, v252, v129\n\t"
    "v_cvt_pk_bf16_f32 v184, v128, v129\n\t"
    "v_mfma_f32_16x16x32_bf16 a[160:163], a[248:251], v[160:163], a[160:163]\n\t"
    "v_exp_f32 v131, v131\n\t"
    "v_add_f32 v252, v252, v130\n\t"
    "v_exp_f32_e64 v132, v132\n\t"
    "v_mfma_f32_16x16x32_bf16 a[184:187], a[252:255], v[160:163], a[184:187]\n\t"
    "v_add_f32 v252, v252, v131\n\t"
    "v_cvt_pk_bf16_f32 v185, v130, v131\n\t"
    "v_exp_f32 v133, v133\n\t"
    "v_add_f32_e64 v252, v252, v132\n\t"
    "v_mfma_f32_16x16x32_bf16 a[188:191], a[252:255], v[164:167], a[188:191]\n\t"
    "v_exp_f32 v134, v134\n\t"
    "v_add_f32 v252, v252, v133\n\t"
    "v_cvt_pk_bf16_f32 v186, v132, v133\n\t"
    "v_mfma_f32_16x16x32_bf16 a[164:167], a[248:251], v[164:167], a[164:167]\n\t"
    "v_exp_f32 v135, v135\n\t"
    "v_add_f32 v252, v252, v134\n\t"
    "v_exp_f32_e64 v136, v136\n\t"
    "v_mfma_f32_16x16x32_bf16 a[140:143], a[244:247], v[164:167], a[140:143]\n\t"
    "v_exp_f32 v137, v137\n\t"
    "v_add_f32 v252, v252, v135\n\t"
    "v_cvt_pk_bf16_f32 v187, v134, v135\n\t"
    "v_add_f32_e64 v253, v253, v136\n\t"
    "v_mfma_f32_16x16x32_bf16 a[116:119], a[240:243], v[164:167], a[116:119]\n\t"
    "v_exp_f32 v138, v138\n\t"
    "v_add_f32 v253, v253, v137\n\t"
    "v_cvt_pk_bf16_f32 v188, v136, v137\n\t"
    "v_mfma_f32_16x16x32_bf16 a[92:95], a[236:239], v[164:167], a[92:95]\n\t"
    "v_exp_f32 v139, v139\n\t"
    "v_add_f32 v253, v253, v138\n\t"
    "v_exp_f32_e64 v140, v140\n\t"
    "v_mfma_f32_16x16x32_bf16 a[68:71], a[232:235], v[164:167], a[68:71]\n\t"
    "v_add_f32 v253, v253, v139\n\t"
    "v_cvt_pk_bf16_f32 v189, v138, v139\n\t"
    "v_exp_f32 v141, v141\n\t"
    "v_mfma_f32_16x16x32_bf16 a[44:47], a[228:231], v[164:167], a[44:47]\n\t"
    "v_add_f32 v253, v253, v140\n\t"
    "v_exp_f32 v142, v142\n\t"
    "v_add_f32_e64 v253, v253, v141\n\t"
    "v_cvt_pk_bf16_f32 v190, v140, v141\n\t"
    "v_mfma_f32_16x16x32_bf16 a[20:23], a[224:227], v[164:167], a[20:23]\n\t"
    "v_exp_f32 v143, v143\n\t"
    "v_add_f32 v253, v253, v142\n\t"
    "v_add_f32 v253, v253, v143\n\t"
    "v_cvt_pk_bf16_f32 v191, v142, v143\n\t"
    "s_cmp_lg_u32 s22, 0\n\t"
    "s_cbranch_scc1 .Lk_resc\n\t"
    ".Lk_rescret_f:\n\t"
    "s_waitcnt vmcnt(12) lgkmcnt(0)\n\t"
    "s_barrier\n\t"
    "s_nop 0\n\t"
    "v_mfma_f32_16x16x32_bf16 a[0:3], a[192:195], v[168:171], a[0:3]\n\t"
    "ds_read_b64_tr_b16 v[128:129], v192\n\t"
    "v_mov_b32 v200, v248\n\t"
    "v_mov_b32 v201, v249\n\t"
    "v_mov_b32_e64 v202, v250\n\t"
    "v_mfma_f32_16x16x32_bf16 a[4:7], a[192:195], v[172:175], a[4:7]\n\t"
    "v_mov_b32 v203, v251\n\t"
    "v_mov_b32 v204, v252\n\t"
    "v_mov_b32_e64 v205, v253\n\t"
    "v_mfma_f32_16x16x32_bf16 a[8:11], a[192:195], v[176:179], a[8:11]\n\t"
    "ds_read_b64_tr_b16 v[130:131], v192 offset:1024\n\t"
    "v_permlane32_swap_b32 v248, v200\n\t"
    "v_permlane32_swap_b32 v249, v201\n\t"
    "v_mfma_f32_16x16x32_bf16 a[12:15], a[192:195], v[180:183], a[12:15]\n\t"
    "v_permlane32_swap_b32 v250, v202\n\t"
    "v_permlane32_swap_b32 v251, v203\n\t"
    "v_mfma_f32_16x16x32_bf16 a[16:19], a[192:195], v[184:187], a[16:19]\n\t"
    "v_permlane32_swap_b32 v252, v204\n\t"
    "v_permlane32_swap_b32 v253, v205\n\t"
    "v_add_f32 v248, v248, v200\n\t"
    "v_add_f32 v249, v249, v201\n\t"
    "v_mfma_f32_16x16x32_bf16 a[20:23], a[192:195], v[188:191], a[20:23]\n\t"
    "ds_read_b64_tr_b16 v[132:133], v192 offset:2048\n\t"
    "v_add_f32 v250, v250, v202\n\t"
    "v_add_f32 v251, v251, v203\n\t"
    "v_add_f32_e64 v252, v252, v204\n\t"
    "v_mfma_f32_16x16x32_bf16 a[44:47], a[196:199], v[188:191], a[44:47]\n\t"
    "v_add_f32 v253, v253, v205\n\t"
    "v_mov_b32 v200, v248\n\t"
    "v_mov_b32 v201, v249\n\t"
    "v_mov_b32 v202, v250\n\t"
    "v_mfma_f32_16x16x32_bf16 a[40:43], a[196:199], v[184:187], a[40:43]\n\t"
    "ds_read_b64_tr_b16 v[134:135], v192 offset:3072\n\t"
    "v_mov_b32 v203, v251\n\t"
    "v_mov_b32 v204, v252\n\t"
    "v_mov_b32_e64 v205, v253\n\t"
    "v_mfma_f32_16x16x32_bf16 a[36:39], a[196:199], v[180:183], a[36:39]\n\t"
    "v_permlane16_swap_b32 v248, v200\n\t"
    "v_permlane16_swap_b32 v249, v201\n\t"
    "v_mfma_f32_16x16x32_bf16 a[32:35], a[196:199], v[176:179], a[32:35]\n\t"
    "v_permlane16_swap_b32 v250, v202\n\t"
    "v_permlane16_swap_b32 v251, v203\n\t"
    "v_permlane16_swap_b32 v252, v204\n\t"
    "s_nop 0\n\t"
    "v_mfma_f32_16x16x32_bf16 a[28:31], a[196:199], v[172:175], a[28:31]\n\t"
    "ds_read_b64_tr_b16 v[136:137], v193\n\t"
    "v_permlane16_swap_b32 v253, v205\n\t"
    "v_add_f32 v248, v248, v200\n\t"
    "v_mfma_f32_16x16x32_bf16 a[24:27], a[196:199], v[168:171], a[24:27]\n\t"
    "v_add_f32 v249, v249, v201\n\t"
    "v_add_f32 v250, v250, v202\n\t"
    "v_add_f32 v251, v251, v203\n\t"
    "v_add_f32 v252, v252, v204\n\t"
    "v_mfma_f32_16x16x32_bf16 a[48:51], a[200:203], v[168:171], a[48:51]\n\t"
    "ds_read_b64_tr_b16 v[138:139], v193 offset:1024\n\t"
    "v_add_f32 v253, v253, v205\n\t"
    "v_rcp_f32 v248, v248\n\t"
    "v_mfma_f32_16x16x32_bf16 a[52:55], a[200:203], v[172:175], a[52:55]\n\t"
    "v_rcp_f32 v249, v249\n\t"
    "v_rcp_f32 v250, v250\n\t"
    "v_mfma_f32_16x16x32_bf16 a[56:59], a[200:203], v[176:179], a[56:59]\n\t"
    "v_rcp_f32 v251, v251\n\t"
    "v_rcp_f32 v252, v252\n\t"
    "v_rcp_f32_e64 v253, v253\n\t"
    "v_mfma_f32_16x16x32_bf16 a[60:63], a[200:203], v[180:183], a[60:63]\n\t"
    "ds_read_b64_tr_b16 v[140:141], v193 offset:2048\n\t"
    "v_accvgpr_read_b32 v48, a0\n\t"
    "v_accvgpr_read_b32 v49, a1\n\t"
    "v_accvgpr_read_b32 v50, a2\n\t"
    "v_mfma_f32_16x16x32_bf16 a[64:67], a[200:203], v[184:187], a[64:67]\n\t"
    "v_accvgpr_read_b32 v51, a3\n\t"
    "v_accvgpr_read_b32 v52, a24\n\t"
    "v_accvgpr_read_b32 v53, a25\n\t"
    "v_accvgpr_read_b32 v54, a26\n\t"
    "v_mfma_f32_16x16x32_bf16 a[68:71], a[200:203], v[188:191], a[68:71]\n\t"
    "ds_read_b64_tr_b16 v[142:143], v193 offset:3072\n\t"
    "v_accvgpr_read_b32 v55, a27\n\t"
    "v_accvgpr_read_b32 v56, a4\n\t"
    "v_accvgpr_read_b32 v57, a5\n\t"
    "v_mfma_f32_16x16x32_bf16 a[92:95], a[204:207], v[188:191], a[92:95]\n\t"
    "v_accvgpr_read_b32 v58, a6\n\t"
    "v_accvgpr_read_b32 v59, a7\n\t"
    "v_accvgpr_read_b32 v60, a28\n\t"
    "v_accvgpr_read_b32 v61, a29\n\t"
    "v_mfma_f32_16x16x32_bf16 a[88:91], a[204:207], v[184:187], a[88:91]\n\t"
    "v_accvgpr_read_b32 v62, a30\n\t"
    "v_accvgpr_read_b32 v63, a31\n\t"
    "v_mul_f32 v48, v48, v248\n\t"
    "v_mul_f32 v49, v49, v248\n\t"
    "v_mfma_f32_16x16x32_bf16 a[84:87], a[204:207], v[180:183], a[84:87]\n\t"
    "ds_read_b64_tr_b16 v[144:145], v194\n\t"
    "v_mul_f32 v50, v50, v248\n\t"
    "v_mul_f32 v51, v51, v248\n\t"
    "v_mul_f32_e64 v52, v52, v248\n\t"
    "v_mfma_f32_16x16x32_bf16 a[80:83], a[204:207], v[176:179], a[80:83]\n\t"
    "v_mul_f32 v53, v53, v248\n\t"
    "v_mul_f32 v54, v54, v248\n\t"
    "v_mul_f32 v55, v55, v248\n\t"
    "v_mul_f32 v56, v56, v249\n\t"
    "v_mfma_f32_16x16x32_bf16 a[76:79], a[204:207], v[172:175], a[76:79]\n\t"
    "ds_read_b64_tr_b16 v[146:147], v194 offset:1024\n\t"
    "v_mul_f32 v57, v57, v249\n\t"
    "v_mul_f32 v58, v58, v249\n\t"
    "v_mul_f32_e64 v59, v59, v249\n\t"
    "v_mfma_f32_16x16x32_bf16 a[72:75], a[204:207], v[168:171], a[72:75]\n\t"
    "v_mul_f32 v60, v60, v249\n\t"
    "v_mul_f32 v61, v61, v249\n\t"
    "v_mul_f32 v62, v62, v249\n\t"
    "v_mul_f32 v63, v63, v249\n\t"
    "v_cvt_pk_bf16_f32 v0, v48, v49\n\t"
    "v_mfma_f32_16x16x32_bf16 a[96:99], a[208:211], v[168:171], a[96:99]\n\t"
    "v_cvt_pk_bf16_f32 v1, v50, v51\n\t"
    "v_cvt_pk_bf16_f32 v2, v52, v53\n\t"
    "v_cvt_pk_bf16_f32 v3, v54, v55\n\t"
    "v_cvt_pk_bf16_f32 v4, v56, v57\n\t"
    "v_mfma_f32_16x16x32_bf16 a[100:103], a[208:211], v[172:175], a[100:103]\n\t"
    "ds_read_b64_tr_b16 v[148:149], v194 offset:2048\n\t"
    "v_cvt_pk_bf16_f32 v5, v58, v59\n\t"
    "v_cvt_pk_bf16_f32 v6, v60, v61\n\t"
    "v_cvt_pk_bf16_f32 v7, v62, v63\n\t"
    "s_add_u32 s24, s77, 0\n\t"
    "s_nop 0\n\t"
    "v_mfma_f32_16x16x32_bf16 a[104:107], a[208:211], v[176:179], a[104:107]\n\t"
    "buffer_store_dwordx4 v[0:3], v229, s[52:55], s24 offen sc1\n\t"
    "buffer_store_dwordx4 v[4:7], v229, s[52:55], s24 offen offset:1024 sc1\n\t"
    "v_accvgpr_read_b32 v64, a8\n\t"
    "v_accvgpr_read_b32 v65, a9\n\t"
    "v_mfma_f32_16x16x32_bf16 a[108:111], a[208:211], v[180:183], a[108:111]\n\t"
    "ds_read_b64_tr_b16 v[150:151], v194 offset:3072\n\t"
    "v_accvgpr_read_b32 v66, a10\n\t"
    "v_accvgpr_read_b32 v67, a11\n\t"
    "v_accvgpr_read_b32 v68, a32\n\t"
    "v_mfma_f32_16x16x32_bf16 a[112:115], a[208:211], v[184:187], a[112:115]\n\t"
    "v_accvgpr_read_b32 v69, a33\n\t"
    "v_accvgpr_read_b32 v70, a34\n\t"
    "v_accvgpr_read_b32 v71, a35\n\t"
    "v_accvgpr_read_b32 v72, a12\n\t"
    "v_mfma_f32_16x16x32_bf16 a[116:119], a[208:211], v[188:191], a[116:119]\n\t"
    "v_accvgpr_read_b32 v73, a13\n\t"
    "v_accvgpr_read_b32 v74, a14\n\t"
    "v_accvgpr_read_b32 v75, a15\n\t"
    "v_accvgpr_read_b32 v76, a36\n\t"
    "v_mfma_f32_16x16x32_bf16 a[140:143], a[212:215], v[188:191], a[140:143]\n\t"
    "ds_read_b64_tr_b16 v[152:153], v195\n\t"
    "v_accvgpr_read_b32 v77, a37\n\t"
    "v_accvgpr_read_b32 v78, a38\n\t"
    "v_accvgpr_read_b32 v79, a39\n\t"
    "v_mfma_f32_16x16x32_bf16 a[136:139], a[212:215], v[184:187], a[136:139]\n\t"
    "v_mul_f32 v64, v64, v250\n\t"
    "v_mul_f32 v65, v65, v250\n\t"
    "v_mul_f32 v66, v66, v250\n\t"
    "v_mul_f32 v67, v67, v250\n\t"
    "v_mul_f32_e64 v68, v68, v250\n\t"
    "v_mfma_f32_16x16x32_bf16 a[132:135], a[212:215], v[180:183], a[132:135]\n\t"
    "ds_read_b64_tr_b16 v[154:155], v195 offset:1024\n\t"
    "v_mul_f32 v69, v69, v250\n\t"
    "v_mul_f32 v70, v70, v250\n\t"
    "v_mul_f32_e64 v71, v71, v250\n\t"
    "v_mfma_f32_16x16x32_bf16 a[128:131], a[212:215], v[176:179], a[128:131]\n\t"
    "v_mul_f32 v72, v72, v251\n\t"
    "v_mul_f32 v73, v73, v251\n\t"
    "v_mul_f32 v74, v74, v251\n\t"
    "v_mul_f32 v75, v75, v251\n\t"
    "v_mfma_f32_16x16x32_bf16 a[124:127], a[212:215], v[172:175], a[124:127]\n\t"
    "v_mul_f32 v76, v76, v251\n\t"
    "v_mul_f32 v77, v77, v251\n\t"
    "v_mul_f32 v78, v78, v251\n\t"
    "v_mul_f32 v79, v79, v251\n\t"
    "v_mfma_f32_16x16x32_bf16 a[120:123], a[212:215], v[168:171], a[120:123]\n\t"
    "ds_read_b64_tr_b16 v[156:157], v195 offset:2048\n\t"
    "v_cvt_pk_bf16_f32 v8, v64, v65\n\t"
    "v_cvt_pk_bf16_f32 v9, v66, v67\n\t"
    "v_cvt_pk_bf16_f32 v10, v68, v69\n\t"
    "v_mfma_f32_16x16x32_bf16 a[144:147], a[216:219], v[168:171], a[144:147]\n\t"
    "v_cvt_pk_bf16_f32 v11, v70, v71\n\t"
    "v_cvt_pk_bf16_f32 v12, v72, v73\n\t"
    "v_cvt_pk_bf16_f32 v13, v74, v75\n\t"
    "v_cvt_pk_bf16_f32 v14, v76, v77\n\t"
    "v_mfma_f32_16x16x32_bf16 a[148:151], a[216:219], v[172:175], a[148:151]\n\t"
    "ds_read_b64_tr_b16 v[158:159], v195 offset:3072\n\t"
    "v_cvt_pk_bf16_f32 v15, v78, v79\n\t"
    "s_add_u32 s24, s77, 0\n\t"
    "s_nop 0\n\t"
    "buffer_store_dwordx4 v[8:11], v229, s[52:55], s24 offen offset:2048 sc1\n\t"
    "buffer_store_dwordx4 v[12:15], v229, s[52:55], s24 offen offset:3072 sc1\n\t"
    "v_mfma_f32_16x16x32_bf16 a[152:155], a[216:219], v[176:179], a[152:155]\n\t"
    "v_accvgpr_read_b32 v80, a16\n\t"
    "v_accvgpr_read_b32 v81, a17\n\t"
    "v_accvgpr_read_b32 v82, a18\n\t"
    "v_accvgpr_read_b32 v83, a19\n\t"
    "v_mfma_f32_16x16x32_bf16 a[156:159], a[216:219], v[180:183], a[156:159]\n\t"
    "v_accvgpr_read_b32 v84, a40\n\t"
    "v_accvgpr_read_b32 v85, a41\n\t"
    "v_accvgpr_read_b32 v86, a42\n\t"
    "v_accvgpr_read_b32 v87, a43\n\t"
    "v_accvgpr_read_b32 v88, a20\n\t"
    "v_mfma_f32_16x16x32_bf16 a[160:163], a[216:219], v[184:187], a[160:163]\n\t"
    "v_accvgpr_read_b32 v89, a21\n\t"
    "v_accvgpr_read_b32 v90, a22\n\t"
    "v_accvgpr_read_b32 v91, a23\n\t"
    "v_accvgpr_read_b32 v92, a44\n\t"
    "v_mfma_f32_16x16x32_bf16 a[164:167], a[216:219], v[188:191], a[164:167]\n\t"
    "v_accvgpr_read_b32 v93, a45\n\t"
    "v_accvgpr_read_b32 v94, a46\n\t"
    "v_accvgpr_read_b32 v95, a47\n\t"
    "v_mul_f32_e64 v80, v80, v252\n\t"
    "v_mfma_f32_16x16x32_bf16 a[188:191], a[220:223], v[188:191], a[188:191]\n\t"
    "v_mul_f32 v81, v81, v252\n\t"
    "v_mul_f32 v82, v82, v252\n\t"
    "v_mul_f32 v83, v83, v252\n\t"
    "v_mul_f32 v84, v84, v252\n\t"
    "v_mfma_f32_16x16x32_bf16 a[184:187], a[220:223], v[184:187], a[184:187]\n\t"
    "v_mul_f32 v85, v85, v252\n\t"
    "v_mul_f32 v86, v86, v252\n\t"
    "v_mul_f32 v87, v87, v252\n\t"
    "v_mul_f32 v88, v88, v253\n\t"
    "v_mfma_f32_16x16x32_bf16 a[180:183], a[220:223], v[180:183], a[180:183]\n\t"
    "v_mul_f32 v89, v89, v253\n\t"
    "v_mul_f32 v90, v90, v253\n\t"
    "v_mul_f32 v91, v91, v253\n\t"
    "v_mul_f32 v92, v92, v253\n\t"
    "v_mfma_f32_16x16x32_bf16 a[176:179], a[220:223], v[176:179], a[176:179]\n\t"
    "v_mul_f32 v93, v93, v253\n\t"
    "v_mul_f32 v94, v94, v253\n\t"
    "v_mul_f32_e64 v95, v95, v253\n\t"
    "v_cvt_pk_bf16_f32 v16, v80, v81\n\t"
    "v_mfma_f32_16x16x32_bf16 a[172:175], a[220:223], v[172:175], a[172:175]\n\t"
    "v_cvt_pk_bf16_f32 v17, v82, v83\n\t"
    "v_cvt_pk_bf16_f32 v18, v84, v85\n\t"
    "v_cvt_pk_bf16_f32 v19, v86, v87\n\t"
    "v_cvt_pk_bf16_f32 v20, v88, v89\n\t"
    "v_mfma_f32_16x16x32_bf16 a[168:171], a[220:223], v[168:171], a[168:171]\n\t"
    "v_cvt_pk_bf16_f32 v21, v90, v91\n\t"
    "v_cvt_pk_bf16_f32 v22, v92, v93\n\t"
    "v_cvt_pk_bf16_f32 v23, v94, v95\n\t"
    "s_add_u32 s24, s77, 0\n\t"
    "s_nop 0\n\t"
    "buffer_store_dwordx4 v[16:19], v241, s[52:55], s24 offen sc1\n\t"
    "buffer_store_dwordx4 v[20:23], v241, s[52:55], s24 offen offset:1024 sc1\n\t"
    "s_waitcnt vmcnt(14) lgkmcnt(0)\n\t"
    "s_barrier\n\t"
    "v_mfma_f32_16x16x32_bf16 a[192:195], v[128:131], v[0:3], 0\n\t"
    "ds_read_b64_tr_b16 v[160:161], v192 offset:4096\n\t"
    "s_cmp_le_u32 s63, 0\n\t"
    "s_addc_u32 s28, 0, 0\n\t"
    "s_lshl_b32 s28, s28, 19\n\t"
    "s_add_u32 s28, s28, 0\n\t"
    "s_add_u32 m0, s58, 0\n\t"
    "s_nop 0\n\t"
    "v_accvgpr_read_b32 v48, a48\n\t"
    "v_accvgpr_read_b32 v49, a49\n\t"
    "v_mfma_f32_16x16x32_bf16 a[216:219], v[136:139], v[0:3], 0\n\t"
    "buffer_load_dwordx4 v198, s[64:67], s28 offen lds\n\t"
    "v_accvgpr_read_b32 v50, a50\n\t"
    "v_accvgpr_read_b32 v51, a51\n\t"
    "v_accvgpr_read_b32 v52, a72\n\t"
    "v_mfma_f32_16x16x32_bf16 a[240:243], v[144:147], v[0:3], 0\n\t"
    "ds_read_b64_tr_b16 v[162:163], v192 offset:5120\n\t"
    "v_accvgpr_read_b32 v53, a73\n\t"
    "v_accvgpr_read_b32 v54, a74\n\t"
    "v_mfma_f32_16x16x32_bf16 a[8:11], v[152:155], v[0:3], 0\n\t"
    "v_accvgpr_read_b32 v55, a75\n\t"
    "v_accvgpr_read_b32 v56, a52\n\t"
    "v_accvgpr_read_b32 v57, a53\n\t"
    "v_accvgpr_read_b32 v58, a54\n\t"
    "v_mfma_f32_16x16x32_bf16 a[196:199], v[128:131], v[4:7], 0\n\t"
    "ds_read_b64_tr_b16 v[164:165], v192 offset:6144\n\t"
    "v_accvgpr_read_b32 v59, a55\n\t"
    "v_accvgpr_read_b32 v60, a76\n\t"
    "v_accvgpr_read_b32 v61, a77\n\t"
    "v_mfma_f32_16x16x32_bf16 a[220:223], v[136:139], v[4:7], 0\n\t"
    "buffer_load_dwordx4 v199, s[64:67], s28 offen offset:1024 lds\n\t"
    "v_accvgpr_read_b32 v62, a78\n\t"
    "v_accvgpr_read_b32 v63, a79\n\t"
    "v_mfma_f32_16x16x32_bf16 a[244:247], v[144:147], v[4:7], 0\n\t"
    "ds_read_b64_tr_b16 v[166:167], v192 offset:7168\n\t"
    "v_mul_f32 v48, v48, v248\n\t"
    "v_mul_f32 v49, v49, v248\n\t"
    "v_mul_f32_e64 v50, v50, v248\n\t"
    "v_mfma_f32_16x16x32_bf16 a[12:15], v[152:155], v[4:7], 0\n\t"
    "v_mul_f32 v51, v51, v248\n\t"
    "v_mul_f32 v52, v52, v248\n\t"
    "v_mul_f32 v53, v53, v248\n\t"
    "v_mul_f32 v54, v54, v248\n\t"
    "v_mfma_f32_16x16x32_bf16 a[200:203], v[128:131], v[8:11], 0\n\t"
    "ds_read_b64_tr_b16 v[168:169], v193 offset:4096\n\t"
    "s_cmp_le_u32 s63, 1\n\t"
    "s_addc_u32 s29, 1, 0\n\t"
    "s_lshl_b32 s29, s29, 19\n\t"
    "s_add_u32 s29, s29, 0\n\t"
    "s_add_u32 m0, s58, 2048\n\t"
    "v_mul_f32 v55, v55, v248\n\t"
    "v_mul_f32 v56, v56, v249\n\t"
    "v_mfma_f32_16x16x32_bf16 a[224:227], v[136:139], v[8:11], 0\n\t"
    "buffer_load_dwordx4 v198, s[64:67], s29 offen lds\n\t"
    "v_mul_f32 v57, v57, v249\n\t"
    "v_mul_f32 v58, v58, v249\n\t"
    "v_mul_f32_e64 v59, v59, v249\n\t"
    "v_mfma_f32_16x16x32_bf16 a[248:251], v[144:147], v[8:11], 0\n\t"
    "ds_read_b64_tr_b16 v[170:171], v193 offset:5120\n\t"
    "v_mul_f32 v60, v60, v249\n\t"
    "v_mul_f32 v61, v61, v249\n\t"
    "v_mul_f32_e64 v62, v62, v249\n\t"
    "v_mfma_f32_16x16x32_bf16 a[16:19], v[152:155], v[8:11], 0\n\t"
    "v_mul_f32_e64 v63, v63, v249\n\t"
    "v_cvt_pk_bf16_f32 v24, v48, v49\n\t"
    "v_cvt_pk_bf16_f32 v25, v50, v51\n\t"
    "v_mfma_f32_16x16x32_bf16 a[204:207], v[128:131], v[12:15], 0\n\t"
    "ds_read_b64_tr_b16 v[172:173], v193 offset:6144\n\t"
    "v_cvt_pk_bf16_f32 v26, v52, v53\n\t"
    "v_cvt_pk_bf16_f32 v27, v54, v55\n\t"
    "v_cvt_pk_bf16_f32 v28, v56, v57\n\t"
    "v_mfma_f32_16x16x32_bf16 a[228:231], v[136:139], v[12:15], 0\n\t"
    "buffer_load_dwordx4 v199, s[64:67], s29 offen offset:1024 lds\n\t"
    "v_cvt_pk_bf16_f32 v29, v58, v59\n\t"
    "v_cvt_pk_bf16_f32 v30, v60, v61\n\t"
    "v_cvt_pk_bf16_f32 v31, v62, v63\n\t"
    "v_mfma_f32_16x16x32_bf16 a[252:255], v[144:147], v[12:15], 0\n\t"
    "ds_read_b64_tr_b16 v[174:175], v193 offset:7168\n\t"
    "v_accvgpr_read_b32 v64, a56\n\t"
    "v_accvgpr_read_b32 v65, a57\n\t"
    "v_accvgpr_read_b32 v66, a58\n\t"
    "v_mfma_f32_16x16x32_bf16 a[20:23], v[152:155], v[12:15], 0\n\t"
    "v_accvgpr_read_b32 v67, a59\n\t"
    "v_accvgpr_read_b32 v68, a80\n\t"
    "v_accvgpr_read_b32 v69, a81\n\t"
    "v_accvgpr_read_b32 v70, a82\n\t"
    "v_mfma_f32_16x16x32_bf16 a[208:211], v[128:131], v[16:19], 0\n\t"
    "ds_read_b64_tr_b16 v[176:177], v194 offset:4096\n\t"
    "s_add_u32 m0, s58, 4096\n\t"
    "v_accvgpr_read_b32 v71, a83\n\t"
    "v_accvgpr_read_b32 v72, a60\n\t"
    "v_accvgpr_read_b32 v73, a61\n\t"
    "v_mfma_f32_16x16x32_bf16 a[232:235], v[136:139], v[16:19], 0\n\t"
    "buffer_load_dwordx4 v196, s[64:67], s28 offen lds\n\t"
    "v_accvgpr_read_b32 v74, a62\n\t"
    "v_accvgpr_read_b32 v75, a63\n\t"
    "v_accvgpr_read_b32 v76, a84\n\t"
    "v_mfma_f32_16x16x32_bf16 a[0:3], v[144:147], v[16:19], 0\n\t"
    "ds_read_b64_tr_b16 v[178:179], v194 offset:5120\n\t"
    "v_accvgpr_read_b32 v77, a85\n\t"
    "v_accvgpr_read_b32 v78, a86\n\t"
    "v_accvgpr_read_b32 v79, a87\n\t"
    "v_mfma_f32_16x16x32_bf16 a[24:27], v[152:155], v[16:19], 0\n\t"
    "buffer_load_dwordx4 v197, s[64:67], s28 offen offset:1024 lds\n\t"
    "v_mul_f32 v64, v64, v250\n\t"
    "v_mul_f32 v65, v65, v250\n\t"
    "v_mul_f32_e64 v66, v66, v250\n\t"
    "v_mfma_f32_16x16x32_bf16 a[212:215], v[128:131], v[20:23], 0\n\t"
    "ds_read_b64_tr_b16 v[180:181], v194 offset:6144\n\t"
    "v_mul_f32 v67, v67, v250\n\t"
    "v_mul_f32 v68, v68, v250\n\t"
    "v_mul_f32_e64 v69, v69, v250\n\t"
    "v_mfma_f32_16x16x32_bf16 a[236:239], v[136:139], v[20:23], 0\n\t"
    "v_mul_f32 v70, v70, v250\n\t"
    "v_mul_f32 v71, v71, v250\n\t"
    "v_mul_f32 v72, v72, v251\n\t"
    "v_mul_f32 v73, v73, v251\n\t"
    "v_mfma_f32_16x16x32_bf16 a[4:7], v[144:147], v[20:23], 0\n\t"
    "ds_read_b64_tr_b16 v[182:183], v194 offset:7168\n\t"
    "s_add_u32 m0, s58, 6144\n\t"
    "v_mul_f32 v74, v74, v251\n\t"
    "v_mul_f32 v75, v75, v251\n\t"
    "v_mul_f32_e64 v76, v76, v251\n\t"
    "v_mfma_f32_16x16x32_bf16 a[28:31], v[152:155], v[20:23], 0\n\t"
    "buffer_load_dwordx4 v196, s[64:67], s29 offen lds\n\t"
    "v_mul_f32 v77, v77, v251\n\t"
    "v_mul_f32 v78, v78, v251\n\t"
    "v_mul_f32_e64 v79, v79, v251\n\t"
    "v_mfma_f32_16x16x32_bf16 a[192:195], v[132:135], v[24:27], a[192:195]\n\t"
    "ds_read_b64_tr_b16 v[184:185], v195 offset:4096\n\t"
    "v_cvt_pk_bf16_f32 v32, v64, v65\n\t"
    "v_cvt_pk_bf16_f32 v33, v66, v67\n\t"
    "v_cvt_pk_bf16_f32 v34, v68, v69\n\t"
    "v_mfma_f32_16x16x32_bf16 a[216:219], v[140:143], v[24:27], a[216:219]\n\t"
    "v_cvt_pk_bf16_f32 v35, v70, v71\n\t"
    "v_cvt_pk_bf16_f32 v36, v72, v73\n\t"
    "v_cvt_pk_bf16_f32 v37, v74, v75\n\t"
    "v_cvt_pk_bf16_f32 v38, v76, v77\n\t"
    "v_cvt_pk_bf16_f32 v39, v78, v79\n\t"
    "v_mfma_f32_16x16x32_bf16 a[240:243], v[148:151], v[24:27], a[240:243]\n\t"
    "ds_read_b64_tr_b16 v[186:187], v195 offset:5120\n\t"
    "v_accvgpr_read_b32 v80, a64\n\t"
    "v_accvgpr_read_b32 v81, a65\n\t"
    "v_mfma_f32_16x16x32_bf16 a[8:11], v[156:159], v[24:27], a[8:11]\n\t"
    "buffer_load_dwordx4 v197, s[64:67], s29 offen offset:1024 lds\n\t"
    "v_accvgpr_read_b32 v82, a66\n\t"
    "v_accvgpr_read_b32 v83, a67\n\t"
    "v_accvgpr_read_b32 v84, a88\n\t"
    "v_mfma_f32_16x16x32_bf16 a[196:199], v[132:135], v[28:31], a[196:199]\n\t"
    "ds_read_b64_tr_b16 v[188:189], v195 offset:6144\n\t"
    "v_accvgpr_read_b32 v85, a89\n\t"
    "v_accvgpr_read_b32 v86, a90\n\t"
    "v_accvgpr_read_b32 v87, a91\n\t"
    "v_mfma_f32_16x16x32_bf16 a[220:223], v[140:143], v[28:31], a[220:223]\n\t"
    "v_accvgpr_read_b32 v88, a68\n\t"
    "v_accvgpr_read_b32 v89, a69\n\t"
    "v_accvgpr_read_b32 v90, a70\n\t"
    "v_accvgpr_read_b32 v91, a71\n\t"
    "v_mfma_f32_16x16x32_bf16 a[244:247], v[148:151], v[28:31], a[244:247]\n\t"
    "ds_read_b64_tr_b16 v[190:191], v195 offset:7168\n\t"
    "s_cmp_le_u32 s63, 2\n\t"
    "s_addc_u32 s28, 2, 0\n\t"
    "s_lshl_b32 s28, s28, 19\n\t"
    "s_add_u32 s28, s28, 0\n\t"
    "s_add_u32 m0, s58, 32768\n\t"
    "v_accvgpr_read_b32 v92, a92\n\t"
    "v_accvgpr_read_b32 v93, a93\n\t"
    "v_accvgpr_read_b32 v94, a94\n\t"
    "v_mfma_f32_16x16x32_bf16 a[12:15], v[156:159], v[28:31], a[12:15]\n\t"
    "buffer_load_dwordx4 v198, s[64:67], s28 offen lds\n\t"
    "v_accvgpr_read_b32 v95, a95\n\t"
    "v_mul_f32 v80, v80, v252\n\t"
    "v_mul_f32 v81, v81, v252\n\t"
    "v_mfma_f32_16x16x32_bf16 a[200:203], v[132:135], v[32:35], a[200:203]\n\t"
    "v_mul_f32 v82, v82, v252\n\t"
    "v_mul_f32 v83, v83, v252\n\t"
    "v_mul_f32_e64 v84, v84, v252\n\t"
    "v_mfma_f32_16x16x32_bf16 a[224:227], v[140:143], v[32:35], a[224:227]\n\t"
    "v_mul_f32 v85, v85, v252\n\t"
    "v_mul_f32 v86, v86, v252\n\t"
    "v_mul_f32 v87, v87, v252\n\t"
    "v_mul_f32 v88, v88, v253\n\t"
    "v_mfma_f32_16x16x32_bf16 a[248:251], v[148:151], v[32:35], a[248:251]\n\t"
    "buffer_load_dwordx4 v199, s[64:67], s28 offen offset:1024 lds\n\t"
    "v_mul_f32 v89, v89, v253\n\t"
    "v_mul_f32 v90, v90, v253\n\t"
    "v_mul_f32_e64 v91, v91, v253\n\t"
    "v_mfma_f32_16x16x32_bf16 a[16:19], v[156:159], v[32:35], a[16:19]\n\t"
    "v_mul_f32 v92, v92, v253\n\t"
    "v_mul_f32 v93, v93, v253\n\t"
    "v_mul_f32 v94, v94, v253\n\t"
    "v_mul_f32 v95, v95, v253\n\t"
    "v_mfma_f32_16x16x32_bf16 a[204:207], v[132:135], v[36:39], a[204:207]\n\t"
    "v_cvt_pk_bf16_f32 v40, v80, v81\n\t"
    "v_cvt_pk_bf16_f32 v41, v82, v83\n\t"
    "v_cvt_pk_bf16_f32 v42, v84, v85\n\t"
    "v_cvt_pk_bf16_f32 v43, v86, v87\n\t"
    "v_mfma_f32_16x16x32_bf16 a[228:231], v[140:143], v[36:39], a[228:231]\n\t"
    "s_cmp_le_u32 s63, 3\n\t"
    "s_addc_u32 s29, 3, 0\n\t"
    "s_lshl_b32 s29, s29, 19\n\t"
    "s_add_u32 s29, s29, 0\n\t"
    "s_add_u32 m0, s58, 34816\n\t"
    "v_cvt_pk_bf16_f32 v44, v88, v89\n\t"
    "v_cvt_pk_bf16_f32 v45, v90, v91\n\t"
    "v_cvt_pk_bf16_f32 v46, v92, v93\n\t"
    "v_cvt_pk_bf16_f32 v47, v94, v95\n\t"
    "v_mfma_f32_16x16x32_bf16 a[252:255], v[148:151], v[36:39], a[252:255]\n\t"
    "buffer_load_dwordx4 v198, s[64:67], s29 offen lds\n\t"
    "v_mfma_f32_16x16x32_bf16 a[20:23], v[156:159], v[36:39], a[20:23]\n\t"
    "v_mfma_f32_16x16x32_bf16 a[208:211], v[132:135], v[40:43], a[208:211]\n\t"
    "v_mfma_f32_16x16x32_bf16 a[232:235], v[140:143], v[40:43], a[232:235]\n\t"
    "v_mfma_f32_16x16x32_bf16 a[0:3], v[148:151], v[40:43], a[0:3]\n\t"
    "buffer_load_dwordx4 v199, s[64:67], s29 offen offset:1024 lds\n\t"
    "v_mfma_f32_16x16x32_bf16 a[24:27], v[156:159], v[40:43], a[24:27]\n\t"
    "v_mfma_f32_16x16x32_bf16 a[212:215], v[132:135], v[44:47], a[212:215]\n\t"
    "s_waitcnt vmcnt(12)\n\t"
    "s_mov_b64 exec, 1\n\t"
    "buffer_atomic_add v247, off, s[68:71], s78 sc1\n\t"
    "s_mov_b64 exec, -1\n\t"
    "s_nop 0\n\t"
    "v_mfma_f32_16x16x32_bf16 a[236:239], v[140:143], v[44:47], a[236:239]\n\t"
    "v_mfma_f32_16x16x32_bf16 a[4:7], v[148:151], v[44:47], a[4:7]\n\t"
    "v_mfma_f32_16x16x32_bf16 a[28:31], v[156:159], v[44:47], a[28:31]\n\t"
    "s_waitcnt lgkmcnt(0)\n\t"
    "s_barrier\n\t"
    "v_mfma_f32_16x16x32_bf16 a[32:35], v[160:163], v[0:3], 0\n\t"
    "ds_read_b64_tr_b16 v[128:129], v192 offset:32768\n\t"
    "v_accvgpr_read_b32 v48, a96\n\t"
    "v_accvgpr_read_b32 v49, a97\n\t"
    "v_mfma_f32_16x16x32_bf16 a[32:35], v[164:167], v[24:27], a[32:35]\n\t"
    "v_accvgpr_read_b32 v50, a98\n\t"
    "v_accvgpr_read_b32 v51, a99\n\t"
    "v_accvgpr_read_b32 v52, a120\n\t"
    "v_mfma_f32_16x16x32_bf16 a[36:39], v[164:167], v[28:31], 0\n\t"
    "ds_read_b64_tr_b16 v[130:131], v192 offset:33792\n\t"
    "s_add_u32 s24, s77, 98304\n\t"
    "v_accvgpr_read_b32 v53, a121\n\t"
    "v_accvgpr_read_b32 v54, a122\n\t"
    "v_mfma_f32_16x16x32_bf16 a[36:39], v[160:163], v[4:7], a[36:39]\n\t"
    "buffer_store_dwordx4 v[24:27], v229, s[52:55], s24 offen sc1\n\t"
    "v_accvgpr_read_b32 v55, a123\n\t"
    "v_accvgpr_read_b32 v56, a100\n\t"
    "v_mfma_f32_16x16x32_bf16 a[40:43], v[160:163], v[8:11], 0\n\t"
    "ds_read_b64_tr_b16 v[132:133], v192 offset:34816\n\t"
    "v_accvgpr_read_b32 v57, a101\n\t"
    "v_accvgpr_read_b32 v58, a102\n\t"
    "v_accvgpr_read_b32 v59, a103\n\t"
    "v_mfma_f32_16x16x32_bf16 a[40:43], v[164:167], v[32:35], a[40:43]\n\t"
    "v_accvgpr_read_b32 v60, a124\n\t"
    "v_accvgpr_read_b32 v61, a125\n\t"
    "v_accvgpr_read_b32 v62, a126\n\t"
    "v_mfma_f32_16x16x32_bf16 a[44:47], v[164:167], v[36:39], 0\n\t"
    "ds_read_b64_tr_b16 v[134:135], v192 offset:35840\n\t"
    "v_accvgpr_read_b32 v63, a127\n\t"
    "v_accvgpr_read_b32 v64, a104\n\t"
    "v_mfma_f32_16x16x32_bf16 a[44:47], v[160:163], v[12:15], a[44:47]\n\t"
    "buffer_store_dwordx4 v[28:31], v229, s[52:55], s24 offen offset:1024 sc1\n\t"
    "v_accvgpr_read_b32 v65, a105\n\t"
    "v_accvgpr_read_b32 v66, a106\n\t"
    "v_mfma_f32_16x16x32_bf16 a[48:51], v[160:163], v[16:19], 0\n\t"
    "ds_read_b64_tr_b16 v[136:137], v193 offset:32768\n\t"
    "v_accvgpr_read_b32 v67, a107\n\t"
    "v_accvgpr_read_b32 v68, a128\n\t"
    "v_accvgpr_read_b32 v69, a129\n\t"
    "v_mfma_f32_16x16x32_bf16 a[48:51], v[164:167], v[40:43], a[48:51]\n\t"
    "v_accvgpr_read_b32 v70, a130\n\t"
    "v_accvgpr_read_b32 v71, a131\n\t"
    "v_accvgpr_read_b32 v72, a108\n\t"
    "v_mfma_f32_16x16x32_bf16 a[52:55], v[164:167], v[44:47], 0\n\t"
    "ds_read_b64_tr_b16 v[138:139], v193 offset:33792\n\t"
    "v_accvgpr_read_b32 v73, a109\n\t"
    "v_accvgpr_read_b32 v74, a110\n\t"
    "v_mfma_f32_16x16x32_bf16 a[52:55], v[160:163], v[20:23], a[52:55]\n\t"
    "buffer_store_dwordx4 v[32:35], v229, s[52:55], s24 offen offset:2048 sc1\n\t"
    "v_accvgpr_read_b32 v75, a111\n\t"
    "v_accvgpr_read_b32 v76, a132\n\t"
    "v_mfma_f32_16x16x32_bf16 a[56:59], v[168:171], v[0:3], 0\n\t"
    "ds_read_b64_tr_b16 v[140:141], v193 offset:34816\n\t"
    "v_accvgpr_read_b32 v77, a133\n\t"
    "v_accvgpr_read_b32 v78, a134\n\t"
    "v_accvgpr_read_b32 v79, a135\n\t"
    "v_mfma_f32_16x16x32_bf16 a[56:59], v[172:175], v[24:27], a[56:59]\n\t"
    "v_accvgpr_read_b32 v80, a112\n\t"
    "v_accvgpr_read_b32 v81, a113\n\t"
    "v_accvgpr_read_b32 v82, a114\n\t"
    "v_mfma_f32_16x16x32_bf16 a[60:63], v[172:175], v[28:31], 0\n\t"
    "ds_read_b64_tr_b16 v[142:143], v193 offset:35840\n\t"
    "v_accvgpr_read_b32 v83, a115\n\t"
    "v_accvgpr_read_b32 v84, a136\n\t"
    "v_mfma_f32_16x16x32_bf16 a[60:63], v[168:171], v[4:7], a[60:63]\n\t"
    "buffer_store_dwordx4 v[36:39], v229, s[52:55], s24 offen offset:3072 sc1\n\t"
    "v_accvgpr_read_b32 v85, a137\n\t"
    "v_accvgpr_read_b32 v86, a138\n\t"
    "v_mfma_f32_16x16x32_bf16 a[64:67], v[168:171], v[8:11], 0\n\t"
    "ds_read_b64_tr_b16 v[144:145], v194 offset:32768\n\t"
    "v_accvgpr_read_b32 v87, a139\n\t"
    "v_accvgpr_read_b32 v88, a116\n\t"
    "v_accvgpr_read_b32 v89, a117\n\t"
    "v_mfma_f32_16x16x32_bf16 a[64:67], v[172:175], v[32:35], a[64:67]\n\t"
    "v_accvgpr_read_b32 v90, a118\n\t"
    "v_accvgpr_read_b32 v91, a119\n\t"
    "v_accvgpr_read_b32 v92, a140\n\t"
    "v_mfma_f32_16x16x32_bf16 a[68:71], v[172:175], v[36:39], 0\n\t"
    "ds_read_b64_tr_b16 v[146:147], v194 offset:33792\n\t"
    "v_accvgpr_read_b32 v93, a141\n\t"
    "v_accvgpr_read_b32 v94, a142\n\t"
    "v_mfma_f32_16x16x32_bf16 a[68:71], v[168:171], v[12:15], a[68:71]\n\t"
    "buffer_store_dwordx4 v[40:43], v241, s[52:55], s24 offen sc1\n\t"
    "v_accvgpr_read_b32 v95, a143\n\t"
    "v_mul_f32_e64 v48, v48, v248\n\t"
    "v_mfma_f32_16x16x32_bf16 a[72:75], v[168:171], v[16:19], 0\n\t"
    "ds_read_b64_tr_b16 v[148:149], v194 offset:34816\n\t"
    "v_mul_f32 v49, v49, v248\n\t"
    "v_mul_f32 v50, v50, v248\n\t"
    "v_mul_f32_e64 v51, v51, v248\n\t"
    "v_mfma_f32_16x16x32_bf16 a[72:75], v[172:175], v[40:43], a[72:75]\n\t"
    "v_mul_f32 v52, v52, v248\n\t"
    "v_mul_f32 v53, v53, v248\n\t"
    "v_mul_f32_e64 v54, v54, v248\n\t"
    "v_mfma_f32_16x16x32_bf16 a[76:79], v[172:175], v[44:47], 0\n\t"
    "ds_read_b64_tr_b16 v[150:151], v194 offset:35840\n\t"
    "v_mul_f32 v55, v55, v248\n\t"
    "v_mul_f32 v56, v56, v249\n\t"
    "v_mfma_f32_16x16x32_bf16 a[76:79], v[168:171], v[20:23], a[76:79]\n\t"
    "buffer_store_dwordx4 v[44:47], v241, s[52:55], s24 offen offset:1024 sc1\n\t"
    "v_mul_f32 v57, v57, v249\n\t"
    "v_mul_f32 v58, v58, v249\n\t"
    "v_mfma_f32_16x16x32_bf16 a[80:83], v[176:179], v[0:3], 0\n\t"
    "ds_read_b64_tr_b16 v[152:153], v195 offset:32768\n\t"
    "v_mul_f32 v59, v59, v249\n\t"
    "v_mul_f32 v60, v60, v249\n\t"
    "v_mul_f32_e64 v61, v61, v249\n\t"
    "v_mfma_f32_16x16x32_bf16 a[80:83], v[180:183], v[24:27], a[80:83]\n\t"
    "v_mul_f32 v62, v62, v249\n\t"
    "v_mul_f32 v63, v63, v249\n\t"
    "v_cvt_pk_bf16_f32 v96, v48, v49\n\t"
    "v_mfma_f32_16x16x32_bf16 a[84:87], v[180:183], v[28:31], 0\n\t"
    "ds_read_b64_tr_b16 v[154:155], v195 offset:33792\n\t"
    "s_add_u32 m0, s58, 36864\n\t"
    "v_cvt_pk_bf16_f32 v97, v50, v51\n\t"
    "v_cvt_pk_bf16_f32 v98, v52, v53\n\t"
    "v_mfma_f32_16x16x32_bf16 a[84:87], v[176:179], v[4:7], a[84:87]\n\t"
    "buffer_load_dwordx4 v196, s[64:67], s28 offen lds\n\t"
    "v_cvt_pk_bf16_f32 v99, v54, v55\n\t"
    "v_cvt_pk_bf16_f32 v100, v56, v57\n\t"
    "v_mfma_f32_16x16x32_bf16 a[88:91], v[176:179], v[8:11], 0\n\t"
    "ds_read_b64_tr_b16 v[156:157], v195 offset:34816\n\t"
    "v_cvt_pk_bf16_f32 v101, v58, v59\n\t"
    "v_cvt_pk_bf16_f32 v102, v60, v61\n\t"
    "v_cvt_pk_bf16_f32 v103, v62, v63\n\t"
    "v_mfma_f32_16x16x32_bf16 a[88:91], v[180:183], v[32:35], a[88:91]\n\t"
    "v_mul_f32 v64, v64, v250\n\t"
    "v_mul_f32 v65, v65, v250\n\t"
    "v_mul_f32_e64 v66, v66, v250\n\t"
    "v_mfma_f32_16x16x32_bf16 a[92:95], v[180:183], v[36:39], 0\n\t"
    "ds_read_b64_tr_b16 v[158:159], v195 offset:35840\n\t"
    "v_mul_f32 v67, v67, v250\n\t"
    "v_mul_f32 v68, v68, v250\n\t"
    "v_mfma_f32_16x16x32_bf16 a[92:95], v[176:179], v[12:15], a[92:95]\n\t"
    "buffer_load_dwordx4 v197, s[64:67], s28 offen offset:1024 lds\n\t"
    "v_mul_f32 v69, v69, v250\n\t"
    "v_mul_f32 v70, v70, v250\n\t"
    "v_mfma_f32_16x16x32_bf16 a[96:99], v[176:179], v[16:19], 0\n\t"
    "v_mul_f32 v71, v71, v250\n\t"
    "v_mul_f32 v72, v72, v251\n\t"
    "v_mul_f32 v73, v73, v251\n\t"
    "v_mul_f32 v74, v74, v251\n\t"
    "v_mfma_f32_16x16x32_bf16 a[96:99], v[180:183], v[40:43], a[96:99]\n\t"
    "v_mul_f32 v75, v75, v251\n\t"
    "v_mul_f32 v76, v76, v251\n\t"
    "v_mul_f32_e64 v77, v77, v251\n\t"
    "v_mfma_f32_16x16x32_bf16 a[100:103], v[180:183], v[44:47], 0\n\t"
    "s_add_u32 m0, s58, 38912\n\t"
    "v_mul_f32 v78, v78, v251\n\t"
    "v_mul_f32 v79, v79, v251\n\t"
    "v_cvt_pk_bf16_f32 v104, v64, v65\n\t"
    "v_mfma_f32_16x16x32_bf16 a[100:103], v[176:179], v[20:23], a[100:103]\n\t"
    "buffer_load_dwordx4 v196, s[64:67], s29 offen lds\n\t"
    "v_cvt_pk_bf16_f32 v105, v66, v67\n\t"
    "v_cvt_pk_bf16_f32 v106, v68, v69\n\t"
    "v_mfma_f32_16x16x32_bf16 a[104:107], v[184:187], v[0:3], 0\n\t"
    "v_cvt_pk_bf16_f32 v107, v70, v71\n\t"
    "v_cvt_pk_bf16_f32 v108, v72, v73\n\t"
    "v_cvt_pk_bf16_f32 v109, v74, v75\n\t"
    "v_cvt_pk_bf16_f32 v110, v76, v77\n\t"
    "v_mfma_f32_16x16x32_bf16 a[104:107], v[188:191], v[24:27], a[104:107]\n\t"
    "v_cvt_pk_bf16_f32 v111, v78, v79\n\t"
    "v_mul_f32 v80, v80, v252\n\t"
    "v_mul_f32 v81, v81, v252\n\t"
    "v_mfma_f32_16x16x32_bf16 a[108:111], v[188:191], v[28:31], 0\n\t"
    "v_mul_f32 v82, v82, v252\n\t"
    "v_mul_f32 v83, v83, v252\n\t"
    "v_mul_f32_e64 v84, v84, v252\n\t"
    "v_mfma_f32_16x16x32_bf16 a[108:111], v[184:187], v[4:7], a[108:111]\n\t"
    "buffer_load_dwordx4 v197, s[64:67], s29 offen offset:1024 lds\n\t"
    "v_mul_f32 v85, v85, v252\n\t"
    "v_mul_f32 v86, v86, v252\n\t"
    "v_mfma_f32_16x16x32_bf16 a[112:115], v[184:187], v[8:11], 0\n\t"
    "v_mul_f32 v87, v87, v252\n\t"
    "v_mul_f32 v88, v88, v253\n\t"
    "v_mul_f32 v89, v89, v253\n\t"
    "v_mul_f32 v90, v90, v253\n\t"
    "v_mfma_f32_16x16x32_bf16 a[112:115], v[188:191], v[32:35], a[112:115]\n\t"
    "v_mul_f32 v91, v91, v253\n\t"
    "v_mul_f32 v92, v92, v253\n\t"
    "v_mul_f32_e64 v93, v93, v253\n\t"
    "v_mfma_f32_16x16x32_bf16 a[116:119], v[188:191], v[36:39], 0\n\t"
    "v_mul_f32 v94, v94, v253\n\t"
    "v_mul_f32 v95, v95, v253\n\t"
    "v_cvt_pk_bf16_f32 v112, v80, v81\n\t"
    "v_mfma_f32_16x16x32_bf16 a[116:119], v[184:187], v[12:15], a[116:119]\n\t"
    "v_cvt_pk_bf16_f32 v113, v82, v83\n\t"
    "v_cvt_pk_bf16_f32 v114, v84, v85\n\t"
    "v_cvt_pk_bf16_f32 v115, v86, v87\n\t"
    "v_mfma_f32_16x16x32_bf16 a[120:123], v[184:187], v[16:19], 0\n\t"
    "v_cvt_pk_bf16_f32 v116, v88, v89\n\t"
    "v_cvt_pk_bf16_f32 v117, v90, v91\n\t"
    "v_cvt_pk_bf16_f32 v118, v92, v93\n\t"
    "v_cvt_pk_bf16_f32 v119, v94, v95\n\t"
    "v_mfma_f32_16x16x32_bf16 a[120:123], v[188:191], v[40:43], a[120:123]\n\t"
    "v_mfma_f32_16x16x32_bf16 a[124:127], v[188:191], v[44:47], 0\n\t"
    "v_mfma_f32_16x16x32_bf16 a[124:127], v[184:187], v[20:23], a[124:127]\n\t"
    "s_waitcnt lgkmcnt(0)\n\t"
    "s_barrier\n\t"
    "v_mfma_f32_16x16x32_bf16 a[192:195], v[128:131], v[96:99], a[192:195]\n\t"
    "ds_read_b64_tr_b16 v[160:161], v192 offset:36864\n\t"
    "s_cmp_le_u32 s63, 4\n\t"
    "s_addc_u32 s28, 4, 0\n\t"
    "s_lshl_b32 s28, s28, 19\n\t"
    "s_add_u32 s28, s28, 0\n\t"
    "s_add_u32 m0, s59, 0\n\t"
    "s_nop 0\n\t"
    "v_accvgpr_read_b32 v48, a144\n\t"
    "v_accvgpr_read_b32 v49, a145\n\t"
    "v_mfma_f32_16x16x32_bf16 a[216:219], v[136:139], v[96:99], a[216:219]\n\t"
    "buffer_load_dwordx4 v198, s[64:67], s28 offen lds\n\t"
    "v_accvgpr_read_b32 v50, a146\n\t"
    "v_accvgpr_read_b32 v51, a147\n\t"
    "v_accvgpr_read_b32 v52, a168\n\t"
    "v_mfma_f32_16x16x32_bf16 a[240:243], v[144:147], v[96:99], a[240:243]\n\t"
    "ds_read_b64_tr_b16 v[162:163], v192 offset:37888\n\t"
    "v_accvgpr_read_b32 v53, a169\n\t"
    "v_accvgpr_read_b32 v54, a170\n\t"
    "v_mfma_f32_16x16x32_bf16 a[8:11], v[152:155], v[96:99], a[8:11]\n\t"
    "v_accvgpr_read_b32 v55, a171\n\t"
    "v_accvgpr_read_b32 v56, a148\n\t"
    "v_accvgpr_read_b32 v57, a149\n\t"
    "v_accvgpr_read_b32 v58, a150\n\t"
    "v_mfma_f32_16x16x32_bf16 a[196:199], v[128:131], v[100:103], a[196:199]\n\t"
    "ds_read_b64_tr_b16 v[164:165], v192 offset:38912\n\t"
    "v_accvgpr_read_b32 v59, a151\n\t"
    "v_accvgpr_read_b32 v60, a172\n\t"
    "v_accvgpr_read_b32 v61, a173\n\t"
    "v_mfma_f32_16x16x32_bf16 a[220:223], v[136:139], v[100:103], a[220:223]\n\t"
    "buffer_load_dwordx4 v199, s[64:67], s28 offen offset:1024 lds\n\t"
    "v_accvgpr_read_b32 v62, a174\n\t"
    "v_accvgpr_read_b32 v63, a175\n\t"
    "v_mfma_f32_16x16x32_bf16 a[244:247], v[144:147], v[100:103], a[244:247]\n\t"
    "ds_read_b64_tr_b16 v[166:167], v192 offset:39936\n\t"
    "v_mul_f32 v48, v48, v248\n\t"
    "v_mul_f32 v49, v49, v248\n\t"
    "v_mul_f32_e64 v50, v50, v248\n\t"
    "v_mfma_f32_16x16x32_bf16 a[12:15], v[152:155], v[100:103], a[12:15]\n\t"
    "v_mul_f32 v51, v51, v248\n\t"
    "v_mul_f32 v52, v52, v248\n\t"
    "v_mul_f32 v53, v53, v248\n\t"
    "v_mul_f32 v54, v54, v248\n\t"
    "v_mfma_f32_16x16x32_bf16 a[200:203], v[128:131], v[104:107], a[200:203]\n\t"
    "ds_read_b64_tr_b16 v[168:169], v193 offset:36864\n\t"
    "s_cmp_le_u32 s63, 5\n\t"
    "s_addc_u32 s29, 5, 0\n\t"
    "s_lshl_b32 s29, s29, 19\n\t"
    "s_add_u32 s29, s29, 0\n\t"
    "s_add_u32 m0, s59, 2048\n\t"
    "v_mul_f32 v55, v55, v248\n\t"
    "v_mul_f32 v56, v56, v249\n\t"
    "v_mfma_f32_16x16x32_bf16 a[224:227], v[136:139], v[104:107], a[224:227]\n\t"
    "buffer_load_dwordx4 v198, s[64:67], s29 offen lds\n\t"
    "v_mul_f32 v57, v57, v249\n\t"
    "v_mul_f32 v58, v58, v249\n\t"
    "v_mul_f32_e64 v59, v59, v249\n\t"
    "v_mfma_f32_16x16x32_bf16 a[248:251], v[144:147], v[104:107], a[248:251]\n\t"
    "ds_read_b64_tr_b16 v[170:171], v193 offset:37888\n\t"
    "v_mul_f32 v60, v60, v249\n\t"
    "v_mul_f32 v61, v61, v249\n\t"
    "v_mul_f32_e64 v62, v62, v249\n\t"
    "v_mfma_f32_16x16x32_bf16 a[16:19], v[152:155], v[104:107], a[16:19]\n\t"
    "v_mul_f32_e64 v63, v63, v249\n\t"
    "v_cvt_pk_bf16_f32 v120, v48, v49\n\t"
    "v_cvt_pk_bf16_f32 v121, v50, v51\n\t"
    "v_mfma_f32_16x16x32_bf16 a[204:207], v[128:131], v[108:111], a[204:207]\n\t"
    "ds_read_b64_tr_b16 v[172:173], v193 offset:38912\n\t"
    "v_cvt_pk_bf16_f32 v122, v52, v53\n\t"
    "v_cvt_pk_bf16_f32 v123, v54, v55\n\t"
    "v_cvt_pk_bf16_f32 v124, v56, v57\n\t"
    "v_mfma_f32_16x16x32_bf16 a[228:231], v[136:139], v[108:111], a[228:231]\n\t"
    "buffer_load_dwordx4 v199, s[64:67], s29 offen offset:1024 lds\n\t"
    "v_cvt_pk_bf16_f32 v125, v58, v59\n\t"
    "v_cvt_pk_bf16_f32 v126, v60, v61\n\t"
    "v_cvt_pk_bf16_f32 v127, v62, v63\n\t"
    "v_mfma_f32_16x16x32_bf16 a[252:255], v[144:147], v[108:111], a[252:255]\n\t"
    "ds_read_b64_tr_b16 v[174:175], v193 offset:39936\n\t"
    "v_accvgpr_read_b32 v64, a152\n\t"
    "v_accvgpr_read_b32 v65, a153\n\t"
    "v_accvgpr_read_b32 v66, a154\n\t"
    "v_mfma_f32_16x16x32_bf16 a[20:23], v[152:155], v[108:111], a[20:23]\n\t"
    "v_accvgpr_read_b32 v67, a155\n\t"
    "v_accvgpr_read_b32 v68, a176\n\t"
    "v_accvgpr_read_b32 v69, a177\n\t"
    "v_accvgpr_read_b32 v70, a178\n\t"
    "v_mfma_f32_16x16x32_bf16 a[208:211], v[128:131], v[112:115], a[208:211]\n\t"
    "ds_read_b64_tr_b16 v[176:177], v194 offset:36864\n\t"
    "s_add_u32 m0, s59, 4096\n\t"
    "v_accvgpr_read_b32 v71, a179\n\t"
    "v_accvgpr_read_b32 v72, a156\n\t"
    "v_accvgpr_read_b32 v73, a157\n\t"
    "v_mfma_f32_16x16x32_bf16 a[232:235], v[136:139], v[112:115], a[232:235]\n\t"
    "buffer_load_dwordx4 v196, s[64:67], s28 offen lds\n\t"
    "v_accvgpr_read_b32 v74, a158\n\t"
    "v_accvgpr_read_b32 v75, a159\n\t"
    "v_accvgpr_read_b32 v76, a180\n\t"
    "v_mfma_f32_16x16x32_bf16 a[0:3], v[144:147], v[112:115], a[0:3]\n\t"
    "ds_read_b64_tr_b16 v[178:179], v194 offset:37888\n\t"
    "v_accvgpr_read_b32 v77, a181\n\t"
    "v_accvgpr_read_b32 v78, a182\n\t"
    "v_accvgpr_read_b32 v79, a183\n\t"
    "v_mfma_f32_16x16x32_bf16 a[24:27], v[152:155], v[112:115], a[24:27]\n\t"
    "buffer_load_dwordx4 v197, s[64:67], s28 offen offset:1024 lds\n\t"
    "v_mul_f32 v64, v64, v250\n\t"
    "v_mul_f32 v65, v65, v250\n\t"
    "v_mul_f32 v66, v66, v250\n\t"
    "v_mul_f32 v67, v67, v250\n\t"
    "v_mfma_f32_16x16x32_bf16 a[212:215], v[128:131], v[116:119], a[212:215]\n\t"
    "ds_read_b64_tr_b16 v[180:181], v194 offset:38912\n\t"
    "v_mul_f32 v68, v68, v250\n\t"
    "v_mul_f32 v69, v69, v250\n\t"
    "v_mul_f32_e64 v70, v70, v250\n\t"
    "v_mfma_f32_16x16x32_bf16 a[236:239], v[136:139], v[116:119], a[236:239]\n\t"
    "v_mul_f32 v71, v71, v250\n\t"
    "v_mul_f32 v72, v72, v251\n\t"
    "v_mul_f32 v73, v73, v251\n\t"
    "v_mul_f32 v74, v74, v251\n\t"
    "v_mfma_f32_16x16x32_bf16 a[4:7], v[144:147], v[116:119], a[4:7]\n\t"
    "ds_read_b64_tr_b16 v[182:183], v194 offset:39936\n\t"
    "s_add_u32 m0, s59, 6144\n\t"
    "v_mul_f32 v75, v75, v251\n\t"
    "v_mul_f32 v76, v76, v251\n\t"
    "v_mul_f32_e64 v77, v77, v251\n\t"
    "v_mfma_f32_16x16x32_bf16 a[28:31], v[152:155], v[116:119], a[28:31]\n\t"
    "buffer_load_dwordx4 v196, s[64:67], s29 offen lds\n\t"
    "v_mul_f32 v78, v78, v251\n\t"
    "v_mul_f32 v79, v79, v251\n\t"
    "v_cvt_pk_bf16_f32 v208, v64, v65\n\t"
    "v_mfma_f32_16x16x32_bf16 a[192:195], v[132:135], v[120:123], a[192:195]\n\t"
    "ds_read_b64_tr_b16 v[184:185], v195 offset:36864\n\t"
    "v_cvt_pk_bf16_f32 v209, v66, v67\n\t"
    "v_cvt_pk_bf16_f32 v210, v68, v69\n\t"
    "v_cvt_pk_bf16_f32 v211, v70, v71\n\t"
    "v_mfma_f32_16x16x32_bf16 a[216:219], v[140:143], v[120:123], a[216:219]\n\t"
    "buffer_load_dword v254, v230, s[68:71], 0 offen sc1\n\t"
    "v_cvt_pk_bf16_f32 v212, v72, v73\n\t"
    "v_cvt_pk_bf16_f32 v213, v74, v75\n\t"
    "v_cvt_pk_bf16_f32 v214, v76, v77\n\t"
    "v_cvt_pk_bf16_f32 v215, v78, v79\n\t"
    "v_mfma_f32_16x16x32_bf16 a[240:243], v[148:151], v[120:123], a[240:243]\n\t"
    "ds_read_b64_tr_b16 v[186:187], v195 offset:37888\n\t"
    "v_accvgpr_read_b32 v80, a160\n\t"
    "v_accvgpr_read_b32 v81, a161\n\t"
    "v_accvgpr_read_b32 v82, a162\n\t"
    "v_mfma_f32_16x16x32_bf16 a[8:11], v[156:159], v[120:123], a[8:11]\n\t"
    "buffer_load_dwordx4 v197, s[64:67], s29 offen offset:1024 lds\n\t"
    "v_accvgpr_read_b32 v83, a163\n\t"
    "v_accvgpr_read_b32 v84, a184\n\t"
    "v_accvgpr_read_b32 v85, a185\n\t"
    "v_mfma_f32_16x16x32_bf16 a[196:199], v[132:135], v[124:127], a[196:199]\n\t"
    "ds_read_b64_tr_b16 v[188:189], v195 offset:38912\n\t"
    "v_accvgpr_read_b32 v86, a186\n\t"
    "v_accvgpr_read_b32 v87, a187\n\t"
    "v_accvgpr_read_b32 v88, a164\n\t"
    "v_mfma_f32_16x16x32_bf16 a[220:223], v[140:143], v[124:127], a[220:223]\n\t"
    "v_accvgpr_read_b32 v89, a165\n\t"
    "v_accvgpr_read_b32 v90, a166\n\t"
    "v_accvgpr_read_b32 v91, a167\n\t"
    "v_accvgpr_read_b32 v92, a188\n\t"
    "v_mfma_f32_16x16x32_bf16 a[244:247], v[148:151], v[124:127], a[244:247]\n\t"
    "ds_read_b64_tr_b16 v[190:191], v195 offset:39936\n\t"
    "s_cmp_le_u32 s63, 6\n\t"
    "s_addc_u32 s28, 6, 0\n\t"
    "s_lshl_b32 s28, s28, 19\n\t"
    "s_add_u32 s28, s28, 0\n\t"
    "s_add_u32 m0, s59, 32768\n\t"
    "v_accvgpr_read_b32 v93, a189\n\t"
    "v_accvgpr_read_b32 v94, a190\n\t"
    "v_accvgpr_read_b32 v95, a191\n\t"
    "v_mfma_f32_16x16x32_bf16 a[12:15], v[156:159], v[124:127], a[12:15]\n\t"
    "buffer_load_dwordx4 v198, s[64:67], s28 offen lds\n\t"
    "v_mul_f32 v80, v80, v252\n\t"
    "v_mul_f32 v81, v81, v252\n\t"
    "v_mul_f32 v82, v82, v252\n\t"
    "v_mul_f32 v83, v83, v252\n\t"
    "v_mfma_f32_16x16x32_bf16 a[200:203], v[132:135], v[208:211], a[200:203]\n\t"
    "v_mul_f32 v84, v84, v252\n\t"
    "v_mul_f32 v85, v85, v252\n\t"
    "v_mul_f32 v86, v86, v252\n\t"
    "v_mul_f32 v87, v87, v252\n\t"
    "v_mfma_f32_16x16x32_bf16 a[224:227], v[140:143], v[208:211], a[224:227]\n\t"
    "v_add_u32 v192, 98304, v192\n\t"
    "v_mul_f32 v88, v88, v253\n\t"
    "v_mul_f32 v89, v89, v253\n\t"
    "v_mul_f32_e64 v90, v90, v253\n\t"
    "v_mfma_f32_16x16x32_bf16 a[248:251], v[148:151], v[208:211], a[248:251]\n\t"
    "buffer_load_dwordx4 v199, s[64:67], s28 offen offset:1024 lds\n\t"
    "v_add_u32 v193, 98304, v193\n\t"
    "v_mul_f32 v91, v91, v253\n\t"
    "v_mul_f32 v92, v92, v253\n\t"
    "v_mfma_f32_16x16x32_bf16 a[16:19], v[156:159], v[208:211], a[16:19]\n\t"
    "v_add_u32 v194, 98304, v194\n\t"
    "v_mul_f32 v93, v93, v253\n\t"
    "v_mul_f32 v94, v94, v253\n\t"
    "v_mul_f32_e64 v95, v95, v253\n\t"
    "v_mfma_f32_16x16x32_bf16 a[204:207], v[132:135], v[212:215], a[204:207]\n\t"
    "v_add_u32 v195, 98304, v195\n\t"
    "v_cvt_pk_bf16_f32 v216, v80, v81\n\t"
    "v_cvt_pk_bf16_f32 v217, v82, v83\n\t"
    "v_cvt_pk_bf16_f32 v218, v84, v85\n\t"
    "v_mfma_f32_16x16x32_bf16 a[228:231], v[140:143], v[212:215], a[228:231]\n\t"
    "s_cmp_le_u32 s63, 0\n\t"
    "s_addc_u32 s29, 0, 0\n\t"
    "s_lshl_b32 s29, s29, 19\n\t"
    "s_nop 0\n\t"
    "s_add_u32 s29, s29, 65536\n\t"
    "s_add_u32 m0, s59, 34816\n\t"
    "v_cvt_pk_bf16_f32 v219, v86, v87\n\t"
    "v_cvt_pk_bf16_f32 v220, v88, v89\n\t"
    "v_cvt_pk_bf16_f32 v221, v90, v91\n\t"
    "v_cvt_pk_bf16_f32 v222, v92, v93\n\t"
    "v_cvt_pk_bf16_f32 v223, v94, v95\n\t"
    "v_mfma_f32_16x16x32_bf16 a[252:255], v[148:151], v[212:215], a[252:255]\n\t"
    "buffer_load_dwordx4 v198, s[64:67], s29 offen lds\n\t"
    "v_mfma_f32_16x16x32_bf16 a[20:23], v[156:159], v[212:215], a[20:23]\n\t"
    "v_mfma_f32_16x16x32_bf16 a[208:211], v[132:135], v[216:219], a[208:211]\n\t"
    "v_mfma_f32_16x16x32_bf16 a[232:235], v[140:143], v[216:219], a[232:235]\n\t"
    "v_mfma_f32_16x16x32_bf16 a[0:3], v[148:151], v[216:219], a[0:3]\n\t"
    "buffer_load_dwordx4 v199, s[64:67], s29 offen offset:1024 lds\n\t"
    "v_mfma_f32_16x16x32_bf16 a[24:27], v[156:159], v[216:219], a[24:27]\n\t"
    "v_mfma_f32_16x16x32_bf16 a[212:215], v[132:135], v[220:223], a[212:215]\n\t"
    "v_mfma_f32_16x16x32_bf16 a[236:239], v[140:143], v[220:223], a[236:239]\n\t"
    "v_mfma_f32_16x16x32_bf16 a[4:7], v[148:151], v[220:223], a[4:7]\n\t"
    "s_mov_b32 s25, 254\n\t"
    "s_mov_b32 s30, 1\n\t"
    "s_mov_b32 s23, 1\n\t"
    "s_waitcnt vmcnt(5)\n\t"
    "v_sub_u32 v254, v254, v231\n\t"
    "v_cmp_gt_u32 vcc, s30, v254\n\t"
    "s_nop 1\n\t"
    "s_and_b32 s26, vcc_lo, s25\n\t"
    "s_cbranch_scc1 .Lk_spin\n\t"
    ".Lk_spinret_1:\n\t"
    "v_mfma_f32_16x16x32_bf16 a[28:31], v[156:159], v[220:223], a[28:31]\n\t"
    "s_waitcnt lgkmcnt(0)\n\t"
    "s_barrier\n\t"
    "v_mfma_f32_16x16x32_bf16 a[32:35], v[160:163], v[96:99], a[32:35]\n\t"
    "ds_read_b64_tr_b16 v[128:129], v192\n\t"
    "s_cmp_le_u32 s63, 0\n\t"
    "s_addc_u32 s31, 0, 0\n\t"
    "s_mul_i32 s31, s31, 12288\n\t"
    "s_add_u32 s31, s31, 0\n\t"
    "s_nop 0\n\t"
    "v_mfma_f32_16x16x32_bf16 a[32:35], v[164:167], v[120:123], a[32:35]\n\t"
    "buffer_load_dwordx4 v[0:3], v229, s[52:55], s31 offen sc1\n\t"
    "v_mfma_f32_16x16x32_bf16 a[36:39], v[164:167], v[124:127], a[36:39]\n\t"
    "ds_read_b64_tr_b16 v[130:131], v192 offset:1024\n\t"
    "v_mfma_f32_16x16x32_bf16 a[36:39], v[160:163], v[100:103], a[36:39]\n\t"
    "buffer_load_dwordx4 v[4:7], v229, s[52:55], s31 offen offset:1024 sc1\n\t"
    "v_mfma_f32_16x16x32_bf16 a[40:43], v[160:163], v[104:107], a[40:43]\n\t"
    "ds_read_b64_tr_b16 v[132:133], v192 offset:2048\n\t"
    "v_mfma_f32_16x16x32_bf16 a[40:43], v[164:167], v[208:211], a[40:43]\n\t"
    "v_mfma_f32_16x16x32_bf16 a[44:47], v[164:167], v[212:215], a[44:47]\n\t"
    "ds_read_b64_tr_b16 v[134:135], v192 offset:3072\n\t"
    "v_mfma_f32_16x16x32_bf16 a[44:47], v[160:163], v[108:111], a[44:47]\n\t"
    "buffer_load_dwordx4 v[8:11], v229, s[52:55], s31 offen offset:2048 sc1\n\t"
    "v_mfma_f32_16x16x32_bf16 a[48:51], v[160:163], v[112:115], a[48:51]\n\t"
    "ds_read_b64_tr_b16 v[136:137], v193\n\t"
    "v_mfma_f32_16x16x32_bf16 a[48:51], v[164:167], v[216:219], a[48:51]\n\t"
    "buffer_load_dwordx4 v[12:15], v229, s[52:55], s31 offen offset:3072 sc1\n\t"
    "v_mfma_f32_16x16x32_bf16 a[52:55], v[164:167], v[220:223], a[52:55]\n\t"
    "ds_read_b64_tr_b16 v[138:139], v193 offset:1024\n\t"
    "v_mfma_f32_16x16x32_bf16 a[52:55], v[160:163], v[116:119], a[52:55]\n\t"
    "v_mfma_f32_16x16x32_bf16 a[56:59], v[168:171], v[96:99], a[56:59]\n\t"
    "ds_read_b64_tr_b16 v[140:141], v193 offset:2048\n\t"
    "v_mfma_f32_16x16x32_bf16 a[56:59], v[172:175], v[120:123], a[56:59]\n\t"
    "buffer_load_dwordx4 v[16:19], v241, s[52:55], s31 offen sc1\n\t"
    "v_mfma_f32_16x16x32_bf16 a[60:63], v[172:175], v[124:127], a[60:63]\n\t"
    "ds_read_b64_tr_b16 v[142:143], v193 offset:3072\n\t"
    "v_mfma_f32_16x16x32_bf16 a[60:63], v[168:171], v[100:103], a[60:63]\n\t"
    "buffer_load_dwordx4 v[20:23], v241, s[52:55], s31 offen offset:1024 sc1\n\t"
    "v_mfma_f32_16x16x32_bf16 a[64:67], v[168:171], v[104:107], a[64:67]\n\t"
    "ds_read_b64_tr_b16 v[144:145], v194\n\t"
    "s_cmp_le_u32 s63, 1\n\t"
    "s_addc_u32 s31, 1, 0\n\t"
    "s_mul_i32 s31, s31, 12288\n\t"
    "s_add_u32 s31, s31, 0\n\t"
    "s_nop 0\n\t"
    "v_mfma_f32_16x16x32_bf16 a[64:67], v[172:175], v[208:211], a[64:67]\n\t"
    "buffer_load_dwordx4 v[24:27], v229, s[52:55], s31 offen sc1\n\t"
    "v_mfma_f32_16x16x32_bf16 a[68:71], v[172:175], v[212:215], a[68:71]\n\t"
    "ds_read_b64_tr_b16 v[146:147], v194 offset:1024\n\t"
    "v_mfma_f32_16x16x32_bf16 a[68:71], v[168:171], v[108:111], a[68:71]\n\t"
    "v_mfma_f32_16x16x32_bf16 a[72:75], v[168:171], v[112:115], a[72:75]\n\t"
    "ds_read_b64_tr_b16 v[148:149], v194 offset:2048\n\t"
    "v_mfma_f32_16x16x32_bf16 a[72:75], v[172:175], v[216:219], a[72:75]\n\t"
    "buffer_load_dwordx4 v[28:31], v229, s[52:55], s31 offen offset:1024 sc1\n\t"
    "v_mfma_f32_16x16x32_bf16 a[76:79], v[172:175], v[220:223], a[76:79]\n\t"
    "ds_read_b64_tr_b16 v[150:151], v194 offset:3072\n\t"
    "v_mfma_f32_16x16x32_bf16 a[76:79], v[168:171], v[116:119], a[76:79]\n\t"
    "buffer_load_dwordx4 v[32:35], v229, s[52:55], s31 offen offset:2048 sc1\n\t"
    "v_mfma_f32_16x16x32_bf16 a[80:83], v[176:179], v[96:99], a[80:83]\n\t"
    "ds_read_b64_tr_b16 v[152:153], v195\n\t"
    "v_mfma_f32_16x16x32_bf16 a[80:83], v[180:183], v[120:123], a[80:83]\n\t"
    "v_mfma_f32_16x16x32_bf16 a[84:87], v[180:183], v[124:127], a[84:87]\n\t"
    "ds_read_b64_tr_b16 v[154:155], v195 offset:1024\n\t"
    "v_mfma_f32_16x16x32_bf16 a[84:87], v[176:179], v[100:103], a[84:87]\n\t"
    "buffer_load_dwordx4 v[36:39], v229, s[52:55], s31 offen offset:3072 sc1\n\t"
    "v_mfma_f32_16x16x32_bf16 a[88:91], v[176:179], v[104:107], a[88:91]\n\t"
    "ds_read_b64_tr_b16 v[156:157], v195 offset:2048\n\t"
    "v_mfma_f32_16x16x32_bf16 a[88:91], v[180:183], v[208:211], a[88:91]\n\t"
    "buffer_load_dwordx4 v[40:43], v241, s[52:55], s31 offen sc1\n\t"
    "v_mfma_f32_16x16x32_bf16 a[92:95], v[180:183], v[212:215], a[92:95]\n\t"
    "ds_read_b64_tr_b16 v[158:159], v195 offset:3072\n\t"
    "v_mfma_f32_16x16x32_bf16 a[92:95], v[176:179], v[108:111], a[92:95]\n\t"
    "buffer_load_dwordx4 v[44:47], v241, s[52:55], s31 offen offset:1024 sc1\n\t"
    "v_mfma_f32_16x16x32_bf16 a[96:99], v[176:179], v[112:115], a[96:99]\n\t"
    "v_mfma_f32_16x16x32_bf16 a[96:99], v[180:183], v[216:219], a[96:99]\n\t"
    "s_add_u32 m0, s59, 36864\n\t"
    "v_mfma_f32_16x16x32_bf16 a[100:103], v[180:183], v[220:223], a[100:103]\n\t"
    "buffer_load_dwordx4 v196, s[64:67], s28 offen lds\n\t"
    "v_mfma_f32_16x16x32_bf16 a[100:103], v[176:179], v[116:119], a[100:103]\n\t"
    "v_mfma_f32_16x16x32_bf16 a[104:107], v[184:187], v[96:99], a[104:107]\n\t"
    "v_mfma_f32_16x16x32_bf16 a[104:107], v[188:191], v[120:123], a[104:107]\n\t"
    "buffer_load_dwordx4 v197, s[64:67], s28 offen offset:1024 lds\n\t"
    "v_mfma_f32_16x16x32_bf16 a[108:111], v[188:191], v[124:127], a[108:111]\n\t"
    "v_mfma_f32_16x16x32_bf16 a[108:111], v[184:187], v[100:103], a[108:111]\n\t"
    "s_add_u32 m0, s59, 38912\n\t"
    "v_mfma_f32_16x16x32_bf16 a[112:115], v[184:187], v[104:107], a[112:115]\n\t"
    "buffer_load_dwordx4 v196, s[64:67], s29 offen lds\n\t"
    "v_mfma_f32_16x16x32_bf16 a[112:115], v[188:191], v[208:211], a[112:115]\n\t"
    "v_mfma_f32_16x16x32_bf16 a[116:119], v[188:191], v[212:215], a[116:119]\n\t"
    "v_mfma_f32_16x16x32_bf16 a[116:119], v[184:187], v[108:111], a[116:119]\n\t"
    "buffer_load_dwordx4 v197, s[64:67], s29 offen offset:1024 lds\n\t"
    "v_mfma_f32_16x16x32_bf16 a[120:123], v[184:187], v[112:115], a[120:123]\n\t"
    "s_mov_b64 exec, 1\n\t"
    "s_nop 0\n\t"
    "buffer_atomic_add v247, off, s[68:71], s78 sc1\n\t"
    "s_mov_b64 exec, -1\n\t"
    "s_nop 0\n\t"
    "v_mfma_f32_16x16x32_bf16 a[120:123], v[188:191], v[216:219], a[120:123]\n\t"
    "v_mfma_f32_16x16x32_bf16 a[124:127], v[188:191], v[220:223], a[124:127]\n\t"
    "v_mfma_f32_16x16x32_bf16 a[124:127], v[184:187], v[116:119], a[124:127]\n\t"
    "s_waitcnt vmcnt(11) lgkmcnt(0)\n\t"
    "s_barrier\n\t"
    "v_mfma_f32_16x16x32_bf16 a[192:195], v[128:131], v[0:3], a[192:195]\n\t"
    "ds_read_b64_tr_b16 v[160:161], v192 offset:4096\n\t"
    "s_cmp_le_u32 s63, 2\n\t"
    "s_addc_u32 s31, 2, 0\n\t"
    "s_mul_i32 s31, s31, 12288\n\t"
    "s_add_u32 s31, s31, 0\n\t"
    "s_nop 0\n\t"
    "v_mfma_f32_16x16x32_bf16 a[216:219], v[136:139], v[0:3], a[216:219]\n\t"
    "buffer_load_dwordx4 a[128:131], v229, s[52:55], s31 offen sc1\n\t"
    "v_mfma_f32_16x16x32_bf16 a[240:243], v[144:147], v[0:3], a[240:243]\n\t"
    "ds_read_b64_tr_b16 v[162:163], v192 offset:5120\n\t"
    "v_mfma_f32_16x16x32_bf16 a[8:11], v[152:155], v[0:3], a[8:11]\n\t"
    "v_mfma_f32_16x16x32_bf16 a[196:199], v[128:131], v[4:7], a[196:199]\n\t"
    "ds_read_b64_tr_b16 v[164:165], v192 offset:6144\n\t"
    "v_mfma_f32_16x16x32_bf16 a[220:223], v[136:139], v[4:7], a[220:223]\n\t"
    "buffer_load_dwordx4 a[132:135], v229, s[52:55], s31 offen offset:1024 sc1\n\t"
    "v_mfma_f32_16x16x32_bf16 a[244:247], v[144:147], v[4:7], a[244:247]\n\t"
    "ds_read_b64_tr_b16 v[166:167], v192 offset:7168\n\t"
    "v_mfma_f32_16x16x32_bf16 a[12:15], v[152:155], v[4:7], a[12:15]\n\t"
    "v_mfma_f32_16x16x32_bf16 a[200:203], v[128:131], v[8:11], a[200:203]\n\t"
    "ds_read_b64_tr_b16 v[168:169], v193 offset:4096\n\t"
    "v_mfma_f32_16x16x32_bf16 a[224:227], v[136:139], v[8:11], a[224:227]\n\t"
    "buffer_load_dwordx4 a[136:139], v229, s[52:55], s31 offen offset:2048 sc1\n\t"
    "v_mfma_f32_16x16x32_bf16 a[248:251], v[144:147], v[8:11], a[248:251]\n\t"
    "ds_read_b64_tr_b16 v[170:171], v193 offset:5120\n\t"
    "v_mfma_f32_16x16x32_bf16 a[16:19], v[152:155], v[8:11], a[16:19]\n\t"
    "v_mfma_f32_16x16x32_bf16 a[204:207], v[128:131], v[12:15], a[204:207]\n\t"
    "ds_read_b64_tr_b16 v[172:173], v193 offset:6144\n\t"
    "v_mfma_f32_16x16x32_bf16 a[228:231], v[136:139], v[12:15], a[228:231]\n\t"
    "buffer_load_dwordx4 a[140:143], v229, s[52:55], s31 offen offset:3072 sc1\n\t"
    "v_mfma_f32_16x16x32_bf16 a[252:255], v[144:147], v[12:15], a[252:255]\n\t"
    "ds_read_b64_tr_b16 v[174:175], v193 offset:7168\n\t"
    "v_mfma_f32_16x16x32_bf16 a[20:23], v[152:155], v[12:15], a[20:23]\n\t"
    "v_mfma_f32_16x16x32_bf16 a[208:211], v[128:131], v[16:19], a[208:211]\n\t"
    "ds_read_b64_tr_b16 v[176:177], v194 offset:4096\n\t"
    "v_mfma_f32_16x16x32_bf16 a[232:235], v[136:139], v[16:19], a[232:235]\n\t"
    "buffer_load_dwordx4 a[144:147], v241, s[52:55], s31 offen sc1\n\t"
    "v_mfma_f32_16x16x32_bf16 a[0:3], v[144:147], v[16:19], a[0:3]\n\t"
    "ds_read_b64_tr_b16 v[178:179], v194 offset:5120\n\t"
    "v_mfma_f32_16x16x32_bf16 a[24:27], v[152:155], v[16:19], a[24:27]\n\t"
    "buffer_load_dwordx4 a[148:151], v241, s[52:55], s31 offen offset:1024 sc1\n\t"
    "v_mfma_f32_16x16x32_bf16 a[212:215], v[128:131], v[20:23], a[212:215]\n\t"
    "ds_read_b64_tr_b16 v[180:181], v194 offset:6144\n\t"
    "v_mfma_f32_16x16x32_bf16 a[236:239], v[136:139], v[20:23], a[236:239]\n\t"
    "v_mfma_f32_16x16x32_bf16 a[4:7], v[144:147], v[20:23], a[4:7]\n\t"
    "ds_read_b64_tr_b16 v[182:183], v194 offset:7168\n\t"
    "s_cmp_le_u32 s63, 3\n\t"
    "s_addc_u32 s31, 3, 0\n\t"
    "s_mul_i32 s31, s31, 12288\n\t"
    "s_add_u32 s31, s31, 0\n\t"
    "s_nop 0\n\t"
    "v_mfma_f32_16x16x32_bf16 a[28:31], v[152:155], v[20:23], a[28:31]\n\t"
    "buffer_load_dwordx4 a[152:155], v229, s[52:55], s31 offen sc1\n\t"
    "s_waitcnt vmcnt(17)\n\t"
    "s_nop 0\n\t"
    "v_mfma_f32_16x16x32_bf16 a[192:195], v[132:135], v[24:27], a[192:195]\n\t"
    "ds_read_b64_tr_b16 v[184:185], v195 offset:4096\n\t"
    "v_mfma_f32_16x16x32_bf16 a[216:219], v[140:143], v[24:27], a[216:219]\n\t"
    "v_mfma_f32_16x16x32_bf16 a[240:243], v[148:151], v[24:27], a[240:243]\n\t"
    "ds_read_b64_tr_b16 v[186:187], v195 offset:5120\n\t"
    "v_mfma_f32_16x16x32_bf16 a[8:11], v[156:159], v[24:27], a[8:11]\n\t"
    "buffer_load_dwordx4 a[156:159], v229, s[52:55], s31 offen offset:1024 sc1\n\t"
    "s_waitcnt vmcnt(17)\n\t"
    "s_nop 0\n\t"
    "v_mfma_f32_16x16x32_bf16 a[196:199], v[132:135], v[28:31], a[196:199]\n\t"
    "ds_read_b64_tr_b16 v[188:189], v195 offset:6144\n\t"
    "v_mfma_f32_16x16x32_bf16 a[220:223], v[140:143], v[28:31], a[220:223]\n\t"
    "v_mfma_f32_16x16x32_bf16 a[244:247], v[148:151], v[28:31], a[244:247]\n\t"
    "ds_read_b64_tr_b16 v[190:191], v195 offset:7168\n\t"
    "v_mfma_f32_16x16x32_bf16 a[12:15], v[156:159], v[28:31], a[12:15]\n\t"
    "buffer_load_dwordx4 a[160:163], v229, s[52:55], s31 offen offset:2048 sc1\n\t"
    "s_waitcnt vmcnt(17)\n\t"
    "s_nop 0\n\t"
    "v_mfma_f32_16x16x32_bf16 a[200:203], v[132:135], v[32:35], a[200:203]\n\t"
    "v_mfma_f32_16x16x32_bf16 a[224:227], v[140:143], v[32:35], a[224:227]\n\t"
    "v_mfma_f32_16x16x32_bf16 a[248:251], v[148:151], v[32:35], a[248:251]\n\t"
    "buffer_load_dwordx4 a[164:167], v229, s[52:55], s31 offen offset:3072 sc1\n\t"
    "v_mfma_f32_16x16x32_bf16 a[16:19], v[156:159], v[32:35], a[16:19]\n\t"
    "s_waitcnt vmcnt(17)\n\t"
    "s_nop 0\n\t"
    "v_mfma_f32_16x16x32_bf16 a[204:207], v[132:135], v[36:39], a[204:207]\n\t"
    "v_mfma_f32_16x16x32_bf16 a[228:231], v[140:143], v[36:39], a[228:231]\n\t"
    "v_mfma_f32_16x16x32_bf16 a[252:255], v[148:151], v[36:39], a[252:255]\n\t"
    "buffer_load_dwordx4 a[168:171], v241, s[52:55], s31 offen sc1\n\t"
    "v_mfma_f32_16x16x32_bf16 a[20:23], v[156:159], v[36:39], a[20:23]\n\t"
    "s_waitcnt vmcnt(17)\n\t"
    "s_nop 0\n\t"
    "v_mfma_f32_16x16x32_bf16 a[208:211], v[132:135], v[40:43], a[208:211]\n\t"
    "v_mfma_f32_16x16x32_bf16 a[232:235], v[140:143], v[40:43], a[232:235]\n\t"
    "v_mfma_f32_16x16x32_bf16 a[0:3], v[148:151], v[40:43], a[0:3]\n\t"
    "buffer_load_dwordx4 a[172:175], v241, s[52:55], s31 offen offset:1024 sc1\n\t"
    "v_mfma_f32_16x16x32_bf16 a[24:27], v[156:159], v[40:43], a[24:27]\n\t"
    "s_waitcnt vmcnt(17)\n\t"
    "s_nop 0\n\t"
    "v_mfma_f32_16x16x32_bf16 a[212:215], v[132:135], v[44:47], a[212:215]\n\t"
    "v_mfma_f32_16x16x32_bf16 a[236:239], v[140:143], v[44:47], a[236:239]\n\t"
    "v_mfma_f32_16x16x32_bf16 a[4:7], v[148:151], v[44:47], a[4:7]\n\t"
    "v_mfma_f32_16x16x32_bf16 a[28:31], v[156:159], v[44:47], a[28:31]\n\t"
    "s_waitcnt lgkmcnt(0)\n\t"
    "s_barrier\n\t"
    "v_mfma_f32_16x16x32_bf16 a[32:35], v[160:163], v[0:3], a[32:35]\n\t"
    "ds_read_b64_tr_b16 v[128:129], v192 offset:32768\n\t"
    "v_mfma_f32_16x16x32_bf16 a[32:35], v[164:167], v[24:27], a[32:35]\n\t"
    "v_mfma_f32_16x16x32_bf16 a[36:39], v[164:167], v[28:31], a[36:39]\n\t"
    "ds_read_b64_tr_b16 v[130:131], v192 offset:33792\n\t"
    "s_cmp_le_u32 s63, 4\n\t"
    "s_addc_u32 s31, 4, 0\n\t"
    "s_mul_i32 s31, s31, 12288\n\t"
    "s_add_u32 s31, s31, 0\n\t"
    "s_nop 0\n\t"
    "v_mfma_f32_16x16x32_bf16 a[36:39], v[160:163], v[4:7], a[36:39]\n\t"
    "buffer_load_dwordx4 v[48:51], v229, s[52:55], s31 offen sc1\n\t"
    "v_mfma_f32_16x16x32_bf16 a[40:43], v[160:163], v[8:11], a[40:43]\n\t"
    "ds_read_b64_tr_b16 v[132:133], v192 offset:34816\n\t"
    "v_mfma_f32_16x16x32_bf16 a[40:43], v[164:167], v[32:35], a[40:43]\n\t"
    "v_mfma_f32_16x16x32_bf16 a[44:47], v[164:167], v[36:39], a[44:47]\n\t"
    "ds_read_b64_tr_b16 v[134:135], v192 offset:35840\n\t"
    "v_mfma_f32_16x16x32_bf16 a[44:47], v[160:163], v[12:15], a[44:47]\n\t"
    "buffer_load_dwordx4 v[52:55], v229, s[52:55], s31 offen offset:1024 sc1\n\t"
    "v_mfma_f32_16x16x32_bf16 a[48:51], v[160:163], v[16:19], a[48:51]\n\t"
    "ds_read_b64_tr_b16 v[136:137], v193 offset:32768\n\t"
    "v_mfma_f32_16x16x32_bf16 a[48:51], v[164:167], v[40:43], a[48:51]\n\t"
    "v_mfma_f32_16x16x32_bf16 a[52:55], v[164:167], v[44:47], a[52:55]\n\t"
    "ds_read_b64_tr_b16 v[138:139], v193 offset:33792\n\t"
    "v_mfma_f32_16x16x32_bf16 a[52:55], v[160:163], v[20:23], a[52:55]\n\t"
    "buffer_load_dwordx4 v[56:59], v229, s[52:55], s31 offen offset:2048 sc1\n\t"
    "v_mfma_f32_16x16x32_bf16 a[56:59], v[168:171], v[0:3], a[56:59]\n\t"
    "ds_read_b64_tr_b16 v[140:141], v193 offset:34816\n\t"
    "v_mfma_f32_16x16x32_bf16 a[56:59], v[172:175], v[24:27], a[56:59]\n\t"
    "v_mfma_f32_16x16x32_bf16 a[60:63], v[172:175], v[28:31], a[60:63]\n\t"
    "ds_read_b64_tr_b16 v[142:143], v193 offset:35840\n\t"
    "v_mfma_f32_16x16x32_bf16 a[60:63], v[168:171], v[4:7], a[60:63]\n\t"
    "buffer_load_dwordx4 v[60:63], v229, s[52:55], s31 offen offset:3072 sc1\n\t"
    "v_mfma_f32_16x16x32_bf16 a[64:67], v[168:171], v[8:11], a[64:67]\n\t"
    "ds_read_b64_tr_b16 v[144:145], v194 offset:32768\n\t"
    "v_mfma_f32_16x16x32_bf16 a[64:67], v[172:175], v[32:35], a[64:67]\n\t"
    "v_mfma_f32_16x16x32_bf16 a[68:71], v[172:175], v[36:39], a[68:71]\n\t"
    "ds_read_b64_tr_b16 v[146:147], v194 offset:33792\n\t"
    "v_mfma_f32_16x16x32_bf16 a[68:71], v[168:171], v[12:15], a[68:71]\n\t"
    "buffer_load_dwordx4 v[64:67], v241, s[52:55], s31 offen sc1\n\t"
    "v_mfma_f32_16x16x32_bf16 a[72:75], v[168:171], v[16:19], a[72:75]\n\t"
    "ds_read_b64_tr_b16 v[148:149], v194 offset:34816\n\t"
    "v_mfma_f32_16x16x32_bf16 a[72:75], v[172:175], v[40:43], a[72:75]\n\t"
    "v_mfma_f32_16x16x32_bf16 a[76:79], v[172:175], v[44:47], a[76:79]\n\t"
    "ds_read_b64_tr_b16 v[150:151], v194 offset:35840\n\t"
    "v_mfma_f32_16x16x32_bf16 a[76:79], v[168:171], v[20:23], a[76:79]\n\t"
    "buffer_load_dwordx4 v[68:71], v241, s[52:55], s31 offen offset:1024 sc1\n\t"
    "v_mfma_f32_16x16x32_bf16 a[80:83], v[176:179], v[0:3], a[80:83]\n\t"
    "ds_read_b64_tr_b16 v[152:153], v195 offset:32768\n\t"
    "v_mfma_f32_16x16x32_bf16 a[80:83], v[180:183], v[24:27], a[80:83]\n\t"
    "v_mfma_f32_16x16x32_bf16 a[84:87], v[180:183], v[28:31], a[84:87]\n\t"
    "ds_read_b64_tr_b16 v[154:155], v195 offset:33792\n\t"
    "s_cmp_le_u32 s63, 1\n\t"
    "s_addc_u32 s28, 1, 0\n\t"
    "s_lshl_b32 s28, s28, 19\n\t"
    "s_add_u32 s28, s28, 65536\n\t"
    "s_add_u32 m0, s58, 0\n\t"
    "v_mfma_f32_16x16x32_bf16 a[84:87], v[176:179], v[4:7], a[84:87]\n\t"
    "buffer_load_dwordx4 v198, s[64:67], s28 offen lds\n\t"
    "v_mfma_f32_16x16x32_bf16 a[88:91], v[176:179], v[8:11], a[88:91]\n\t"
    "ds_read_b64_tr_b16 v[156:157], v195 offset:34816\n\t"
    "v_mfma_f32_16x16x32_bf16 a[88:91], v[180:183], v[32:35], a[88:91]\n\t"
    "v_mfma_f32_16x16x32_bf16 a[92:95], v[180:183], v[36:39], a[92:95]\n\t"
    "ds_read_b64_tr_b16 v[158:159], v195 offset:35840\n\t"
    "v_mfma_f32_16x16x32_bf16 a[92:95], v[176:179], v[12:15], a[92:95]\n\t"
    "buffer_load_dwordx4 v199, s[64:67], s28 offen offset:1024 lds\n\t"
    "v_mfma_f32_16x16x32_bf16 a[96:99], v[176:179], v[16:19], a[96:99]\n\t"
    "v_mfma_f32_16x16x32_bf16 a[96:99], v[180:183], v[40:43], a[96:99]\n\t"
    "v_mfma_f32_16x16x32_bf16 a[100:103], v[180:183], v[44:47], a[100:103]\n\t"
    "s_cmp_le_u32 s63, 2\n\t"
    "s_addc_u32 s29, 2, 0\n\t"
    "s_lshl_b32 s29, s29, 19\n\t"
    "s_nop 0\n\t"
    "s_add_u32 s29, s29, 65536\n\t"
    "s_add_u32 m0, s58, 2048\n\t"
    "v_mfma_f32_16x16x32_bf16 a[100:103], v[176:179], v[20:23], a[100:103]\n\t"
    "buffer_load_dwordx4 v198, s[64:67], s29 offen lds\n\t"
    "v_mfma_f32_16x16x32_bf16 a[104:107], v[184:187], v[0:3], a[104:107]\n\t"
    "v_mfma_f32_16x16x32_bf16 a[104:107], v[188:191], v[24:27], a[104:107]\n\t"
    "v_mfma_f32_16x16x32_bf16 a[108:111], v[188:191], v[28:31], a[108:111]\n\t"
    "v_mfma_f32_16x16x32_bf16 a[108:111], v[184:187], v[4:7], a[108:111]\n\t"
    "buffer_load_dwordx4 v199, s[64:67], s29 offen offset:1024 lds\n\t"
    "v_mfma_f32_16x16x32_bf16 a[112:115], v[184:187], v[8:11], a[112:115]\n\t"
    "v_mfma_f32_16x16x32_bf16 a[112:115], v[188:191], v[32:35], a[112:115]\n\t"
    "v_mfma_f32_16x16x32_bf16 a[116:119], v[188:191], v[36:39], a[116:119]\n\t"
    "v_mfma_f32_16x16x32_bf16 a[116:119], v[184:187], v[12:15], a[116:119]\n\t"
    "v_mfma_f32_16x16x32_bf16 a[120:123], v[184:187], v[16:19], a[120:123]\n\t"
    "v_mfma_f32_16x16x32_bf16 a[120:123], v[188:191], v[40:43], a[120:123]\n\t"
    "v_mfma_f32_16x16x32_bf16 a[124:127], v[188:191], v[44:47], a[124:127]\n\t"
    "v_mfma_f32_16x16x32_bf16 a[124:127], v[184:187], v[20:23], a[124:127]\n\t"
    "s_waitcnt vmcnt(10) lgkmcnt(0)\n\t"
    "s_barrier\n\t"
    "v_mfma_f32_16x16x32_bf16 a[192:195], v[128:131], a[128:131], a[192:195]\n\t"
    "ds_read_b64_tr_b16 v[160:161], v192 offset:36864\n\t"
    "v_mfma_f32_16x16x32_bf16 a[192:195], v[132:135], a[152:155], a[192:195]\n\t"
    "buffer_load_dword v254, v230, s[68:71], 0 offen sc1\n\t"
    "v_mfma_f32_16x16x32_bf16 a[196:199], v[132:135], a[156:159], a[196:199]\n\t"
    "ds_read_b64_tr_b16 v[162:163], v192 offset:37888\n\t"
    "s_add_u32 s24, s77, 196608\n\t"
    "v_mfma_f32_16x16x32_bf16 a[196:199], v[128:131], a[132:135], a[196:199]\n\t"
    "buffer_store_dwordx4 v[96:99], v229, s[52:55], s24 offen sc1\n\t"
    "v_mfma_f32_16x16x32_bf16 a[200:203], v[128:131], a[136:139], a[200:203]\n\t"
    "ds_read_b64_tr_b16 v[164:165], v192 offset:38912\n\t"
    "v_mfma_f32_16x16x32_bf16 a[200:203], v[132:135], a[160:163], a[200:203]\n\t"
    "v_mfma_f32_16x16x32_bf16 a[204:207], v[132:135], a[164:167], a[204:207]\n\t"
    "ds_read_b64_tr_b16 v[166:167], v192 offset:39936\n\t"
    "v_mfma_f32_16x16x32_bf16 a[204:207], v[128:131], a[140:143], a[204:207]\n\t"
    "buffer_store_dwordx4 v[100:103], v229, s[52:55], s24 offen offset:1024 sc1\n\t"
    "v_mfma_f32_16x16x32_bf16 a[208:211], v[128:131], a[144:147], a[208:211]\n\t"
    "ds_read_b64_tr_b16 v[168:169], v193 offset:36864\n\t"
    "v_mfma_f32_16x16x32_bf16 a[208:211], v[132:135], a[168:171], a[208:211]\n\t"
    "v_mfma_f32_16x16x32_bf16 a[212:215], v[132:135], a[172:175], a[212:215]\n\t"
    "ds_read_b64_tr_b16 v[170:171], v193 offset:37888\n\t"
    "v_mfma_f32_16x16x32_bf16 a[212:215], v[128:131], a[148:151], a[212:215]\n\t"
    "buffer_store_dwordx4 v[104:107], v229, s[52:55], s24 offen offset:2048 sc1\n\t"
    "v_mfma_f32_16x16x32_bf16 a[216:219], v[136:139], a[128:131], a[216:219]\n\t"
    "ds_read_b64_tr_b16 v[172:173], v193 offset:38912\n\t"
    "v_mfma_f32_16x16x32_bf16 a[216:219], v[140:143], a[152:155], a[216:219]\n\t"
    "v_mfma_f32_16x16x32_bf16 a[220:223], v[140:143], a[156:159], a[220:223]\n\t"
    "ds_read_b64_tr_b16 v[174:175], v193 offset:39936\n\t"
    "s_cmp_le_u32 s63, 5\n\t"
    "s_addc_u32 s31, 5, 0\n\t"
    "s_mul_i32 s31, s31, 12288\n\t"
    "s_add_u32 s31, s31, 0\n\t"
    "s_nop 0\n\t"
    "v_mfma_f32_16x16x32_bf16 a[220:223], v[136:139], a[132:135], a[220:223]\n\t"
    "buffer_load_dwordx4 v[72:75], v229, s[52:55], s31 offen sc1\n\t"
    "v_mfma_f32_16x16x32_bf16 a[224:227], v[136:139], a[136:139], a[224:227]\n\t"
    "ds_read_b64_tr_b16 v[176:177], v194 offset:36864\n\t"
    "v_mfma_f32_16x16x32_bf16 a[224:227], v[140:143], a[160:163], a[224:227]\n\t"
    "v_mfma_f32_16x16x32_bf16 a[228:231], v[140:143], a[164:167], a[228:231]\n\t"
    "ds_read_b64_tr_b16 v[178:179], v194 offset:37888\n\t"
    "v_mfma_f32_16x16x32_bf16 a[228:231], v[136:139], a[140:143], a[228:231]\n\t"
    "buffer_load_dwordx4 v[76:79], v229, s[52:55], s31 offen offset:1024 sc1\n\t"
    "v_mfma_f32_16x16x32_bf16 a[232:235], v[136:139], a[144:147], a[232:235]\n\t"
    "ds_read_b64_tr_b16 v[180:181], v194 offset:38912\n\t"
    "v_mfma_f32_16x16x32_bf16 a[232:235], v[140:143], a[168:171], a[232:235]\n\t"
    "v_mfma_f32_16x16x32_bf16 a[236:239], v[140:143], a[172:175], a[236:239]\n\t"
    "ds_read_b64_tr_b16 v[182:183], v194 offset:39936\n\t"
    "v_mfma_f32_16x16x32_bf16 a[236:239], v[136:139], a[148:151], a[236:239]\n\t"
    "buffer_load_dwordx4 v[80:83], v229, s[52:55], s31 offen offset:2048 sc1\n\t"
    "v_mfma_f32_16x16x32_bf16 a[240:243], v[144:147], a[128:131], a[240:243]\n\t"
    "ds_read_b64_tr_b16 v[184:185], v195 offset:36864\n\t"
    "v_mfma_f32_16x16x32_bf16 a[240:243], v[148:151], a[152:155], a[240:243]\n\t"
    "v_mfma_f32_16x16x32_bf16 a[244:247], v[148:151], a[156:159], a[244:247]\n\t"
    "ds_read_b64_tr_b16 v[186:187], v195 offset:37888\n\t"
    "v_mfma_f32_16x16x32_bf16 a[244:247], v[144:147], a[132:135], a[244:247]\n\t"
    "buffer_load_dwordx4 v[84:87], v229, s[52:55], s31 offen offset:3072 sc1\n\t"
    "v_mfma_f32_16x16x32_bf16 a[248:251], v[144:147], a[136:139], a[248:251]\n\t"
    "ds_read_b64_tr_b16 v[188:189], v195 offset:38912\n\t"
    "v_mfma_f32_16x16x32_bf16 a[248:251], v[148:151], a[160:163], a[248:251]\n\t"
    "v_mfma_f32_16x16x32_bf16 a[252:255], v[148:151], a[164:167], a[252:255]\n\t"
    "ds_read_b64_tr_b16 v[190:191], v195 offset:39936\n\t"
    "v_mfma_f32_16x16x32_bf16 a[252:255], v[144:147], a[140:143], a[252:255]\n\t"
    "buffer_load_dwordx4 v[88:91], v241, s[52:55], s31 offen sc1\n\t"
    "v_mfma_f32_16x16x32_bf16 a[0:3], v[144:147], a[144:147], a[0:3]\n\t"
    "v_mfma_f32_16x16x32_bf16 a[0:3], v[148:151], a[168:171], a[0:3]\n\t"
    "v_subrev_u32 v192, 98304, v192\n\t"
    "v_mfma_f32_16x16x32_bf16 a[4:7], v[148:151], a[172:175], a[4:7]\n\t"
    "v_subrev_u32 v193, 98304, v193\n\t"
    "v_mfma_f32_16x16x32_bf16 a[4:7], v[144:147], a[148:151], a[4:7]\n\t"
    "buffer_load_dwordx4 v[92:95], v241, s[52:55], s31 offen offset:1024 sc1\n\t"
    "v_subrev_u32 v194, 98304, v194\n\t"
    "v_mfma_f32_16x16x32_bf16 a[8:11], v[152:155], a[128:131], a[8:11]\n\t"
    "v_subrev_u32 v195, 98304, v195\n\t"
    "v_mfma_f32_16x16x32_bf16 a[8:11], v[156:159], a[152:155], a[8:11]\n\t"
    "v_mfma_f32_16x16x32_bf16 a[12:15], v[156:159], a[156:159], a[12:15]\n\t"
    "v_mfma_f32_16x16x32_bf16 a[12:15], v[152:155], a[132:135], a[12:15]\n\t"
    "v_mfma_f32_16x16x32_bf16 a[16:19], v[152:155], a[136:139], a[16:19]\n\t"
    "v_mfma_f32_16x16x32_bf16 a[16:19], v[156:159], a[160:163], a[16:19]\n\t"
    "v_mfma_f32_16x16x32_bf16 a[20:23], v[156:159], a[164:167], a[20:23]\n\t"
    "v_mfma_f32_16x16x32_bf16 a[20:23], v[152:155], a[140:143], a[20:23]\n\t"
    "v_mfma_f32_16x16x32_bf16 a[24:27], v[152:155], a[144:147], a[24:27]\n\t"
    "v_mfma_f32_16x16x32_bf16 a[24:27], v[156:159], a[168:171], a[24:27]\n\t"
    "v_mfma_f32_16x16x32_bf16 a[28:31], v[156:159], a[172:175], a[28:31]\n\t"
    "v_mfma_f32_16x16x32_bf16 a[28:31], v[152:155], a[148:151], a[28:31]\n\t"
    "s_waitcnt lgkmcnt(0)\n\t"
    "s_barrier\n\t"
    "v_mfma_f32_16x16x32_bf16 a[32:35], v[160:163], a[128:131], a[32:35]\n\t"
    "ds_read_b64_tr_b16 v[128:129], v192\n\t"
    "v_mfma_f32_16x16x32_bf16 a[32:35], v[164:167], a[152:155], a[32:35]\n\t"
    "v_mfma_f32_16x16x32_bf16 a[36:39], v[164:167], a[156:159], a[36:39]\n\t"
    "ds_read_b64_tr_b16 v[130:131], v192 offset:1024\n\t"
    "s_add_u32 s24, s77, 196608\n\t"
    "v_mfma_f32_16x16x32_bf16 a[36:39], v[160:163], a[132:135], a[36:39]\n\t"
    "buffer_store_dwordx4 v[108:111], v229, s[52:55], s24 offen offset:3072 sc1\n\t"
    "v_mfma_f32_16x16x32_bf16 a[40:43], v[160:163], a[136:139], a[40:43]\n\t"
    "ds_read_b64_tr_b16 v[132:133], v192 offset:2048\n\t"
    "v_mfma_f32_16x16x32_bf16 a[40:43], v[164:167], a[160:163], a[40:43]\n\t"
    "v_mfma_f32_16x16x32_bf16 a[44:47], v[164:167], a[164:167], a[44:47]\n\t"
    "ds_read_b64_tr_b16 v[134:135], v192 offset:3072\n\t"
    "v_mfma_f32_16x16x32_bf16 a[44:47], v[160:163], a[140:143], a[44:47]\n\t"
    "buffer_store_dwordx4 v[112:115], v241, s[52:55], s24 offen sc1\n\t"
    "v_mfma_f32_16x16x32_bf16 a[48:51], v[160:163], a[144:147], a[48:51]\n\t"
    "ds_read_b64_tr_b16 v[136:137], v193\n\t"
    "v_mfma_f32_16x16x32_bf16 a[48:51], v[164:167], a[168:171], a[48:51]\n\t"
    "v_mfma_f32_16x16x32_bf16 a[52:55], v[164:167], a[172:175], a[52:55]\n\t"
    "ds_read_b64_tr_b16 v[138:139], v193 offset:1024\n\t"
    "v_mfma_f32_16x16x32_bf16 a[52:55], v[160:163], a[148:151], a[52:55]\n\t"
    "buffer_store_dwordx4 v[116:119], v241, s[52:55], s24 offen offset:1024 sc1\n\t"
    "v_mfma_f32_16x16x32_bf16 a[56:59], v[168:171], a[128:131], a[56:59]\n\t"
    "ds_read_b64_tr_b16 v[140:141], v193 offset:2048\n\t"
    "v_mfma_f32_16x16x32_bf16 a[56:59], v[172:175], a[152:155], a[56:59]\n\t"
    "v_mfma_f32_16x16x32_bf16 a[60:63], v[172:175], a[156:159], a[60:63]\n\t"
    "ds_read_b64_tr_b16 v[142:143], v193 offset:3072\n\t"
    "s_cmp_le_u32 s63, 6\n\t"
    "s_addc_u32 s31, 6, 0\n\t"
    "s_mul_i32 s31, s31, 12288\n\t"
    "s_add_u32 s31, s31, 0\n\t"
    "s_nop 0\n\t"
    "v_mfma_f32_16x16x32_bf16 a[60:63], v[168:171], a[132:135], a[60:63]\n\t"
    "buffer_load_dwordx4 v[0:3], v229, s[52:55], s31 offen sc1\n\t"
    "v_mfma_f32_16x16x32_bf16 a[64:67], v[168:171], a[136:139], a[64:67]\n\t"
    "ds_read_b64_tr_b16 v[144:145], v194\n\t"
    "v_mfma_f32_16x16x32_bf16 a[64:67], v[172:175], a[160:163], a[64:67]\n\t"
    "v_mfma_f32_16x16x32_bf16 a[68:71], v[172:175], a[164:167], a[68:71]\n\t"
    "ds_read_b64_tr_b16 v[146:147], v194 offset:1024\n\t"
    "v_mfma_f32_16x16x32_bf16 a[68:71], v[168:171], a[140:143], a[68:71]\n\t"
    "buffer_load_dwordx4 v[4:7], v229, s[52:55], s31 offen offset:1024 sc1\n\t"
    "v_mfma_f32_16x16x32_bf16 a[72:75], v[168:171], a[144:147], a[72:75]\n\t"
    "ds_read_b64_tr_b16 v[148:149], v194 offset:2048\n\t"
    "v_mfma_f32_16x16x32_bf16 a[72:75], v[172:175], a[168:171], a[72:75]\n\t"
    "v_mfma_f32_16x16x32_bf16 a[76:79], v[172:175], a[172:175], a[76:79]\n\t"
    "ds_read_b64_tr_b16 v[150:151], v194 offset:3072\n\t"
    "v_mfma_f32_16x16x32_bf16 a[76:79], v[168:171], a[148:151], a[76:79]\n\t"
    "buffer_load_dwordx4 v[8:11], v229, s[52:55], s31 offen offset:2048 sc1\n\t"
    "v_mfma_f32_16x16x32_bf16 a[80:83], v[176:179], a[128:131], a[80:83]\n\t"
    "ds_read_b64_tr_b16 v[152:153], v195\n\t"
    "v_mfma_f32_16x16x32_bf16 a[80:83], v[180:183], a[152:155], a[80:83]\n\t"
    "v_mfma_f32_16x16x32_bf16 a[84:87], v[180:183], a[156:159], a[84:87]\n\t"
    "ds_read_b64_tr_b16 v[154:155], v195 offset:1024\n\t"
    "v_mfma_f32_16x16x32_bf16 a[84:87], v[176:179], a[132:135], a[84:87]\n\t"
    "buffer_load_dwordx4 v[12:15], v229, s[52:55], s31 offen offset:3072 sc1\n\t"
    "v_mfma_f32_16x16x32_bf16 a[88:91], v[176:179], a[136:139], a[88:91]\n\t"
    "ds_read_b64_tr_b16 v[156:157], v195 offset:2048\n\t"
    "v_mfma_f32_16x16x32_bf16 a[88:91], v[180:183], a[160:163], a[88:91]\n\t"
    "v_mfma_f32_16x16x32_bf16 a[92:95], v[180:183], a[164:167], a[92:95]\n\t"
    "ds_read_b64_tr_b16 v[158:159], v195 offset:3072\n\t"
    "v_mfma_f32_16x16x32_bf16 a[92:95], v[176:179], a[140:143], a[92:95]\n\t"
    "buffer_load_dwordx4 v[16:19], v241, s[52:55], s31 offen sc1\n\t"
    "v_mfma_f32_16x16x32_bf16 a[96:99], v[176:179], a[144:147], a[96:99]\n\t"
    "v_mfma_f32_16x16x32_bf16 a[96:99], v[180:183], a[168:171], a[96:99]\n\t"
    "v_mfma_f32_16x16x32_bf16 a[100:103], v[180:183], a[172:175], a[100:103]\n\t"
    "v_mfma_f32_16x16x32_bf16 a[100:103], v[176:179], a[148:151], a[100:103]\n\t"
    "buffer_load_dwordx4 v[20:23], v241, s[52:55], s31 offen offset:1024 sc1\n\t"
    "v_mfma_f32_16x16x32_bf16 a[104:107], v[184:187], a[128:131], a[104:107]\n\t"
    "v_mfma_f32_16x16x32_bf16 a[104:107], v[188:191], a[152:155], a[104:107]\n\t"
    "v_mfma_f32_16x16x32_bf16 a[108:111], v[188:191], a[156:159], a[108:111]\n\t"
    "v_mfma_f32_16x16x32_bf16 a[108:111], v[184:187], a[132:135], a[108:111]\n\t"
    "v_mfma_f32_16x16x32_bf16 a[112:115], v[184:187], a[136:139], a[112:115]\n\t"
    "v_mfma_f32_16x16x32_bf16 a[112:115], v[188:191], a[160:163], a[112:115]\n\t"
    "v_mfma_f32_16x16x32_bf16 a[116:119], v[188:191], a[164:167], a[116:119]\n\t"
    "v_mfma_f32_16x16x32_bf16 a[116:119], v[184:187], a[140:143], a[116:119]\n\t"
    "v_mfma_f32_16x16x32_bf16 a[120:123], v[184:187], a[144:147], a[120:123]\n\t"
    "v_mfma_f32_16x16x32_bf16 a[120:123], v[188:191], a[168:171], a[120:123]\n\t"
    "v_mfma_f32_16x16x32_bf16 a[124:127], v[188:191], a[172:175], a[124:127]\n\t"
    "s_mov_b32 s25, 254\n\t"
    "s_mov_b32 s30, 2\n\t"
    "s_mov_b32 s23, 2\n\t"
    "s_waitcnt vmcnt(18)\n\t"
    "v_sub_u32 v254, v254, v231\n\t"
    "v_cmp_gt_u32 vcc, s30, v254\n\t"
    "s_nop 1\n\t"
    "s_and_b32 s26, vcc_lo, s25\n\t"
    "s_cbranch_scc1 .Lk_spin\n\t"
    ".Lk_spinret_2:\n\t"
    "v_mfma_f32_16x16x32_bf16 a[124:127], v[184:187], a[148:151], a[124:127]\n\t"
    "s_waitcnt vmcnt(9) lgkmcnt(0)\n\t"
    "s_barrier\n\t"
    "v_mfma_f32_16x16x32_bf16 a[192:195], v[128:131], v[48:51], a[192:195]\n\t"
    "ds_read_b64_tr_b16 v[160:161], v192 offset:4096\n\t"
    "v_mfma_f32_16x16x32_bf16 a[192:195], v[132:135], v[72:75], a[192:195]\n\t"
    "v_mfma_f32_16x16x32_bf16 a[196:199], v[132:135], v[76:79], a[196:199]\n\t"
    "ds_read_b64_tr_b16 v[162:163], v192 offset:5120\n\t"
    "s_cmp_le_u32 s63, 0\n\t"
    "s_addc_u32 s31, 0, 0\n\t"
    "s_mul_i32 s31, s31, 12288\n\t"
    "s_add_u32 s31, s31, 98304\n\t"
    "v_mfma_f32_16x16x32_bf16 a[196:199], v[128:131], v[52:55], a[196:199]\n\t"
    "buffer_load_dwordx4 v[24:27], v229, s[52:55], s31 offen sc1\n\t"
    "v_mfma_f32_16x16x32_bf16 a[200:203], v[128:131], v[56:59], a[200:203]\n\t"
    "ds_read_b64_tr_b16 v[164:165], v192 offset:6144\n\t"
    "v_mfma_f32_16x16x32_bf16 a[200:203], v[132:135], v[80:83], a[200:203]\n\t"
    "v_mfma_f32_16x16x32_bf16 a[204:207], v[132:135], v[84:87], a[204:207]\n\t"
    "ds_read_b64_tr_b16 v[166:167], v192 offset:7168\n\t"
    "v_mfma_f32_16x16x32_bf16 a[204:207], v[128:131], v[60:63], a[204:207]\n\t"
    "buffer_load_dwordx4 v[28:31], v229, s[52:55], s31 offen offset:1024 sc1\n\t"
    "v_mfma_f32_16x16x32_bf16 a[208:211], v[128:131], v[64:67], a[208:211]\n\t"
    "ds_read_b64_tr_b16 v[168:169], v193 offset:4096\n\t"
    "v_mfma_f32_16x16x32_bf16 a[208:211], v[132:135], v[88:91], a[208:211]\n\t"
    "v_mfma_f32_16x16x32_bf16 a[212:215], v[132:135], v[92:95], a[212:215]\n\t"
    "ds_read_b64_tr_b16 v[170:171], v193 offset:5120\n\t"
    "v_mfma_f32_16x16x32_bf16 a[212:215], v[128:131], v[68:71], a[212:215]\n\t"
    "buffer_load_dwordx4 v[32:35], v229, s[52:55], s31 offen offset:2048 sc1\n\t"
    "v_mfma_f32_16x16x32_bf16 a[216:219], v[136:139], v[48:51], a[216:219]\n\t"
    "ds_read_b64_tr_b16 v[172:173], v193 offset:6144\n\t"
    "v_mfma_f32_16x16x32_bf16 a[216:219], v[140:143], v[72:75], a[216:219]\n\t"
    "v_mfma_f32_16x16x32_bf16 a[220:223], v[140:143], v[76:79], a[220:223]\n\t"
    "ds_read_b64_tr_b16 v[174:175], v193 offset:7168\n\t"
    "v_mfma_f32_16x16x32_bf16 a[220:223], v[136:139], v[52:55], a[220:223]\n\t"
    "buffer_load_dwordx4 v[36:39], v229, s[52:55], s31 offen offset:3072 sc1\n\t"
    "v_mfma_f32_16x16x32_bf16 a[224:227], v[136:139], v[56:59], a[224:227]\n\t"
    "ds_read_b64_tr_b16 v[176:177], v194 offset:4096\n\t"
    "v_mfma_f32_16x16x32_bf16 a[224:227], v[140:143], v[80:83], a[224:227]\n\t"
    "v_mfma_f32_16x16x32_bf16 a[228:231], v[140:143], v[84:87], a[228:231]\n\t"
    "ds_read_b64_tr_b16 v[178:179], v194 offset:5120\n\t"
    "v_mfma_f32_16x16x32_bf16 a[228:231], v[136:139], v[60:63], a[228:231]\n\t"
    "buffer_load_dwordx4 v[40:43], v241, s[52:55], s31 offen sc1\n\t"
    "v_mfma_f32_16x16x32_bf16 a[232:235], v[136:139], v[64:67], a[232:235]\n\t"
    "ds_read_b64_tr_b16 v[180:181], v194 offset:6144\n\t"
    "v_mfma_f32_16x16x32_bf16 a[232:235], v[140:143], v[88:91], a[232:235]\n\t"
    "v_mfma_f32_16x16x32_bf16 a[236:239], v[140:143], v[92:95], a[236:239]\n\t"
    "ds_read_b64_tr_b16 v[182:183], v194 offset:7168\n\t"
    "v_mfma_f32_16x16x32_bf16 a[236:239], v[136:139], v[68:71], a[236:239]\n\t"
    "buffer_load_dwordx4 v[44:47], v241, s[52:55], s31 offen offset:1024 sc1\n\t"
    "v_mfma_f32_16x16x32_bf16 a[240:243], v[144:147], v[48:51], a[240:243]\n\t"
    "ds_read_b64_tr_b16 v[184:185], v195 offset:4096\n\t"
    "v_mfma_f32_16x16x32_bf16 a[240:243], v[148:151], v[72:75], a[240:243]\n\t"
    "v_mfma_f32_16x16x32_bf16 a[244:247], v[148:151], v[76:79], a[244:247]\n\t"
    "ds_read_b64_tr_b16 v[186:187], v195 offset:5120\n\t"
    "s_add_u32 m0, s58, 4096\n\t"
    "v_mfma_f32_16x16x32_bf16 a[244:247], v[144:147], v[52:55], a[244:247]\n\t"
    "buffer_load_dwordx4 v196, s[64:67], s28 offen lds\n\t"
    "v_mfma_f32_16x16x32_bf16 a[248:251], v[144:147], v[56:59], a[248:251]\n\t"
    "ds_read_b64_tr_b16 v[188:189], v195 offset:6144\n\t"
    "v_mfma_f32_16x16x32_bf16 a[248:251], v[148:151], v[80:83], a[248:251]\n\t"
    "v_mfma_f32_16x16x32_bf16 a[252:255], v[148:151], v[84:87], a[252:255]\n\t"
    "ds_read_b64_tr_b16 v[190:191], v195 offset:7168\n\t"
    "v_mfma_f32_16x16x32_bf16 a[252:255], v[144:147], v[60:63], a[252:255]\n\t"
    "buffer_load_dwordx4 v197, s[64:67], s28 offen offset:1024 lds\n\t"
    "v_mfma_f32_16x16x32_bf16 a[0:3], v[144:147], v[64:67], a[0:3]\n\t"
    "v_mfma_f32_16x16x32_bf16 a[0:3], v[148:151], v[88:91], a[0:3]\n\t"
    "v_mfma_f32_16x16x32_bf16 a[4:7], v[148:151], v[92:95], a[4:7]\n\t"
    "s_add_u32 m0, s58, 6144\n\t"
    "v_mfma_f32_16x16x32_bf16 a[4:7], v[144:147], v[68:71], a[4:7]\n\t"
    "buffer_load_dwordx4 v196, s[64:67], s29 offen lds\n\t"
    "v_mfma_f32_16x16x32_bf16 a[8:11], v[152:155], v[48:51], a[8:11]\n\t"
    "v_mfma_f32_16x16x32_bf16 a[8:11], v[156:159], v[72:75], a[8:11]\n\t"
    "v_mfma_f32_16x16x32_bf16 a[12:15], v[156:159], v[76:79], a[12:15]\n\t"
    "v_mfma_f32_16x16x32_bf16 a[12:15], v[152:155], v[52:55], a[12:15]\n\t"
    "buffer_load_dwordx4 v197, s[64:67], s29 offen offset:1024 lds\n\t"
    "v_mfma_f32_16x16x32_bf16 a[16:19], v[152:155], v[56:59], a[16:19]\n\t"
    "v_mfma_f32_16x16x32_bf16 a[16:19], v[156:159], v[80:83], a[16:19]\n\t"
    "v_mfma_f32_16x16x32_bf16 a[20:23], v[156:159], v[84:87], a[20:23]\n\t"
    "v_mfma_f32_16x16x32_bf16 a[20:23], v[152:155], v[60:63], a[20:23]\n\t"
    "v_mfma_f32_16x16x32_bf16 a[24:27], v[152:155], v[64:67], a[24:27]\n\t"
    "v_mfma_f32_16x16x32_bf16 a[24:27], v[156:159], v[88:91], a[24:27]\n\t"
    "v_mfma_f32_16x16x32_bf16 a[28:31], v[156:159], v[92:95], a[28:31]\n\t"
    "v_mfma_f32_16x16x32_bf16 a[28:31], v[152:155], v[68:71], a[28:31]\n\t"
    "s_waitcnt lgkmcnt(0)\n\t"
    "s_barrier\n\t"
    "v_mfma_f32_16x16x32_bf16 a[32:35], v[160:163], v[48:51], a[32:35]\n\t"
    "ds_read_b64_tr_b16 v[128:129], v192 offset:32768\n\t"
    "v_mfma_f32_16x16x32_bf16 a[32:35], v[164:167], v[72:75], a[32:35]\n\t"
    "v_mfma_f32_16x16x32_bf16 a[36:39], v[164:167], v[76:79], a[36:39]\n\t"
    "ds_read_b64_tr_b16 v[130:131], v192 offset:33792\n\t"
    "s_cmp_le_u32 s63, 1\n\t"
    "s_addc_u32 s31, 1, 0\n\t"
    "s_mul_i32 s31, s31, 12288\n\t"
    "s_add_u32 s31, s31, 98304\n\t"
    "v_mfma_f32_16x16x32_bf16 a[36:39], v[160:163], v[52:55], a[36:39]\n\t"
    "buffer_load_dwordx4 a[128:131], v229, s[52:55], s31 offen sc1\n\t"
    "v_mfma_f32_16x16x32_bf16 a[40:43], v[160:163], v[56:59], a[40:43]\n\t"
    "ds_read_b64_tr_b16 v[132:133], v192 offset:34816\n\t"
    "v_mfma_f32_16x16x32_bf16 a[40:43], v[164:167], v[80:83], a[40:43]\n\t"
    "v_mfma_f32_16x16x32_bf16 a[44:47], v[164:167], v[84:87], a[44:47]\n\t"
    "ds_read_b64_tr_b16 v[134:135], v192 offset:35840\n\t"
    "v_mfma_f32_16x16x32_bf16 a[44:47], v[160:163], v[60:63], a[44:47]\n\t"
    "buffer_load_dwordx4 a[132:135], v229, s[52:55], s31 offen offset:1024 sc1\n\t"
    "v_mfma_f32_16x16x32_bf16 a[48:51], v[160:163], v[64:67], a[48:51]\n\t"
    "ds_read_b64_tr_b16 v[136:137], v193 offset:32768\n\t"
    "v_mfma_f32_16x16x32_bf16 a[48:51], v[164:167], v[88:91], a[48:51]\n\t"
    "v_mfma_f32_16x16x32_bf16 a[52:55], v[164:167], v[92:95], a[52:55]\n\t"
    "ds_read_b64_tr_b16 v[138:139], v193 offset:33792\n\t"
    "v_mfma_f32_16x16x32_bf16 a[52:55], v[160:163], v[68:71], a[52:55]\n\t"
    "buffer_load_dwordx4 a[136:139], v229, s[52:55], s31 offen offset:2048 sc1\n\t"
    "v_mfma_f32_16x16x32_bf16 a[56:59], v[168:171], v[48:51], a[56:59]\n\t"
    "ds_read_b64_tr_b16 v[140:141], v193 offset:34816\n\t"
    "v_mfma_f32_16x16x32_bf16 a[56:59], v[172:175], v[72:75], a[56:59]\n\t"
    "v_mfma_f32_16x16x32_bf16 a[60:63], v[172:175], v[76:79], a[60:63]\n\t"
    "ds_read_b64_tr_b16 v[142:143], v193 offset:35840\n\t"
    "v_mfma_f32_16x16x32_bf16 a[60:63], v[168:171], v[52:55], a[60:63]\n\t"
    "buffer_load_dwordx4 a[140:143], v229, s[52:55], s31 offen offset:3072 sc1\n\t"
    "v_mfma_f32_16x16x32_bf16 a[64:67], v[168:171], v[56:59], a[64:67]\n\t"
    "ds_read_b64_tr_b16 v[144:145], v194 offset:32768\n\t"
    "v_mfma_f32_16x16x32_bf16 a[64:67], v[172:175], v[80:83], a[64:67]\n\t"
    "v_mfma_f32_16x16x32_bf16 a[68:71], v[172:175], v[84:87], a[68:71]\n\t"
    "ds_read_b64_tr_b16 v[146:147], v194 offset:33792\n\t"
    "v_mfma_f32_16x16x32_bf16 a[68:71], v[168:171], v[60:63], a[68:71]\n\t"
    "buffer_load_dwordx4 a[144:147], v241, s[52:55], s31 offen sc1\n\t"
    "v_mfma_f32_16x16x32_bf16 a[72:75], v[168:171], v[64:67], a[72:75]\n\t"
    "ds_read_b64_tr_b16 v[148:149], v194 offset:34816\n\t"
    "v_mfma_f32_16x16x32_bf16 a[72:75], v[172:175], v[88:91], a[72:75]\n\t"
    "v_mfma_f32_16x16x32_bf16 a[76:79], v[172:175], v[92:95], a[76:79]\n\t"
    "ds_read_b64_tr_b16 v[150:151], v194 offset:35840\n\t"
    "v_mfma_f32_16x16x32_bf16 a[76:79], v[168:171], v[68:71], a[76:79]\n\t"
    "buffer_load_dwordx4 a[148:151], v241, s[52:55], s31 offen offset:1024 sc1\n\t"
    "v_mfma_f32_16x16x32_bf16 a[80:83], v[176:179], v[48:51], a[80:83]\n\t"
    "ds_read_b64_tr_b16 v[152:153], v195 offset:32768\n\t"
    "v_mfma_f32_16x16x32_bf16 a[80:83], v[180:183], v[72:75], a[80:83]\n\t"
    "v_mfma_f32_16x16x32_bf16 a[84:87], v[180:183], v[76:79], a[84:87]\n\t"
    "ds_read_b64_tr_b16 v[154:155], v195 offset:33792\n\t"
    "s_cmp_le_u32 s63, 3\n\t"
    "s_addc_u32 s28, 3, 0\n\t"
    "s_lshl_b32 s28, s28, 19\n\t"
    "s_nop 0\n\t"
    "s_add_u32 s28, s28, 65536\n\t"
    "s_add_u32 m0, s58, 32768\n\t"
    "v_mfma_f32_16x16x32_bf16 a[84:87], v[176:179], v[52:55], a[84:87]\n\t"
    "buffer_load_dwordx4 v198, s[64:67], s28 offen lds\n\t"
    "v_mfma_f32_16x16x32_bf16 a[88:91], v[176:179], v[56:59], a[88:91]\n\t"
    "ds_read_b64_tr_b16 v[156:157], v195 offset:34816\n\t"
    "v_mfma_f32_16x16x32_bf16 a[88:91], v[180:183], v[80:83], a[88:91]\n\t"
    "v_mfma_f32_16x16x32_bf16 a[92:95], v[180:183], v[84:87], a[92:95]\n\t"
    "ds_read_b64_tr_b16 v[158:159], v195 offset:35840\n\t"
    "v_mfma_f32_16x16x32_bf16 a[92:95], v[176:179], v[60:63], a[92:95]\n\t"
    "buffer_load_dwordx4 v199, s[64:67], s28 offen offset:1024 lds\n\t"
    "v_mfma_f32_16x16x32_bf16 a[96:99], v[176:179], v[64:67], a[96:99]\n\t"
    "v_mfma_f32_16x16x32_bf16 a[96:99], v[180:183], v[88:91], a[96:99]\n\t"
    "v_mfma_f32_16x16x32_bf16 a[100:103], v[180:183], v[92:95], a[100:103]\n\t"
    "s_cmp_le_u32 s63, 4\n\t"
    "s_addc_u32 s29, 4, 0\n\t"
    "s_lshl_b32 s29, s29, 19\n\t"
    "s_nop 0\n\t"
    "s_add_u32 s29, s29, 65536\n\t"
    "s_add_u32 m0, s58, 34816\n\t"
    "v_mfma_f32_16x16x32_bf16 a[100:103], v[176:179], v[68:71], a[100:103]\n\t"
    "buffer_load_dwordx4 v198, s[64:67], s29 offen lds\n\t"
    "v_mfma_f32_16x16x32_bf16 a[104:107], v[184:187], v[48:51], a[104:107]\n\t"
    "v_mfma_f32_16x16x32_bf16 a[104:107], v[188:191], v[72:75], a[104:107]\n\t"
    "v_mfma_f32_16x16x32_bf16 a[108:111], v[188:191], v[76:79], a[108:111]\n\t"
    "v_mfma_f32_16x16x32_bf16 a[108:111], v[184:187], v[52:55], a[108:111]\n\t"
    "buffer_load_dwordx4 v199, s[64:67], s29 offen offset:1024 lds\n\t"
    "v_mfma_f32_16x16x32_bf16 a[112:115], v[184:187], v[56:59], a[112:115]\n\t"
    "v_mfma_f32_16x16x32_bf16 a[112:115], v[188:191], v[80:83], a[112:115]\n\t"
    "v_mfma_f32_16x16x32_bf16 a[116:119], v[188:191], v[84:87], a[116:119]\n\t"
    "v_mfma_f32_16x16x32_bf16 a[116:119], v[184:187], v[60:63], a[116:119]\n\t"
    "v_mfma_f32_16x16x32_bf16 a[120:123], v[184:187], v[64:67], a[120:123]\n\t"
    "s_waitcnt vmcnt(26)\n\t"
    "s_mov_b64 exec, 1\n\t"
    "buffer_atomic_add v247, off, s[68:71], s78 sc1\n\t"
    "s_mov_b64 exec, -1\n\t"
    "s_nop 0\n\t"
    "v_mfma_f32_16x16x32_bf16 a[120:123], v[188:191], v[88:91], a[120:123]\n\t"
    "v_mfma_f32_16x16x32_bf16 a[124:127], v[188:191], v[92:95], a[124:127]\n\t"
    "v_mfma_f32_16x16x32_bf16 a[124:127], v[184:187], v[68:71], a[124:127]\n\t"
    "s_waitcnt vmcnt(15) lgkmcnt(0)\n\t"
    "s_barrier\n\t"
    "v_mfma_f32_16x16x32_bf16 a[192:195], v[128:131], v[0:3], a[192:195]\n\t"
    "ds_read_b64_tr_b16 v[160:161], v192 offset:36864\n\t"
    "v_mfma_f32_16x16x32_bf16 a[192:195], v[132:135], v[24:27], a[192:195]\n\t"
    "v_mfma_f32_16x16x32_bf16 a[196:199], v[132:135], v[28:31], a[196:199]\n\t"
    "ds_read_b64_tr_b16 v[162:163], v192 offset:37888\n\t"
    "s_cmp_le_u32 s63, 2\n\t"
    "s_addc_u32 s31, 2, 0\n\t"
    "s_mul_i32 s31, s31, 12288\n\t"
    "s_add_u32 s31, s31, 98304\n\t"
    "v_mfma_f32_16x16x32_bf16 a[196:199], v[128:131], v[4:7], a[196:199]\n\t"
    "buffer_load_dwordx4 a[152:155], v229, s[52:55], s31 offen sc1\n\t"
    "v_mfma_f32_16x16x32_bf16 a[200:203], v[128:131], v[8:11], a[200:203]\n\t"
    "ds_read_b64_tr_b16 v[164:165], v192 offset:38912\n\t"
    "v_mfma_f32_16x16x32_bf16 a[200:203], v[132:135], v[32:35], a[200:203]\n\t"
    "v_mfma_f32_16x16x32_bf16 a[204:207], v[132:135], v[36:39], a[204:207]\n\t"
    "ds_read_b64_tr_b16 v[166:167], v192 offset:39936\n\t"
    "v_mfma_f32_16x16x32_bf16 a[204:207], v[128:131], v[12:15], a[204:207]\n\t"
    "buffer_load_dwordx4 a[156:159], v229, s[52:55], s31 offen offset:1024 sc1\n\t"
    "v_mfma_f32_16x16x32_bf16 a[208:211], v[128:131], v[16:19], a[208:211]\n\t"
    "ds_read_b64_tr_b16 v[168:169], v193 offset:36864\n\t"
    "v_mfma_f32_16x16x32_bf16 a[208:211], v[132:135], v[40:43], a[208:211]\n\t"
    "v_mfma_f32_16x16x32_bf16 a[212:215], v[132:135], v[44:47], a[212:215]\n\t"
    "ds_read_b64_tr_b16 v[170:171], v193 offset:37888\n\t"
    "v_mfma_f32_16x16x32_bf16 a[212:215], v[128:131], v[20:23], a[212:215]\n\t"
    "buffer_load_dwordx4 a[160:163], v229, s[52:55], s31 offen offset:2048 sc1\n\t"
    "v_mfma_f32_16x16x32_bf16 a[216:219], v[136:139], v[0:3], a[216:219]\n\t"
    "ds_read_b64_tr_b16 v[172:173], v193 offset:38912\n\t"
    "v_mfma_f32_16x16x32_bf16 a[216:219], v[140:143], v[24:27], a[216:219]\n\t"
    "v_mfma_f32_16x16x32_bf16 a[220:223], v[140:143], v[28:31], a[220:223]\n\t"
    "ds_read_b64_tr_b16 v[174:175], v193 offset:39936\n\t"
    "v_mfma_f32_16x16x32_bf16 a[220:223], v[136:139], v[4:7], a[220:223]\n\t"
    "buffer_load_dwordx4 a[164:167], v229, s[52:55], s31 offen offset:3072 sc1\n\t"
    "v_mfma_f32_16x16x32_bf16 a[224:227], v[136:139], v[8:11], a[224:227]\n\t"
    "ds_read_b64_tr_b16 v[176:177], v194 offset:36864\n\t"
    "v_mfma_f32_16x16x32_bf16 a[224:227], v[140:143], v[32:35], a[224:227]\n\t"
    "v_mfma_f32_16x16x32_bf16 a[228:231], v[140:143], v[36:39], a[228:231]\n\t"
    "ds_read_b64_tr_b16 v[178:179], v194 offset:37888\n\t"
    "v_mfma_f32_16x16x32_bf16 a[228:231], v[136:139], v[12:15], a[228:231]\n\t"
    "buffer_load_dwordx4 a[168:171], v241, s[52:55], s31 offen sc1\n\t"
    "v_mfma_f32_16x16x32_bf16 a[232:235], v[136:139], v[16:19], a[232:235]\n\t"
    "ds_read_b64_tr_b16 v[180:181], v194 offset:38912\n\t"
    "v_mfma_f32_16x16x32_bf16 a[232:235], v[140:143], v[40:43], a[232:235]\n\t"
    "v_mfma_f32_16x16x32_bf16 a[236:239], v[140:143], v[44:47], a[236:239]\n\t"
    "ds_read_b64_tr_b16 v[182:183], v194 offset:39936\n\t"
    "v_mfma_f32_16x16x32_bf16 a[236:239], v[136:139], v[20:23], a[236:239]\n\t"
    "buffer_load_dwordx4 a[172:175], v241, s[52:55], s31 offen offset:1024 sc1\n\t"
    "v_mfma_f32_16x16x32_bf16 a[240:243], v[144:147], v[0:3], a[240:243]\n\t"
    "ds_read_b64_tr_b16 v[184:185], v195 offset:36864\n\t"
    "v_mfma_f32_16x16x32_bf16 a[240:243], v[148:151], v[24:27], a[240:243]\n\t"
    "v_mfma_f32_16x16x32_bf16 a[244:247], v[148:151], v[28:31], a[244:247]\n\t"
    "ds_read_b64_tr_b16 v[186:187], v195 offset:37888\n\t"
    "s_add_u32 m0, s58, 36864\n\t"
    "v_mfma_f32_16x16x32_bf16 a[244:247], v[144:147], v[4:7], a[244:247]\n\t"
    "buffer_load_dwordx4 v196, s[64:67], s28 offen lds\n\t"
    "v_mfma_f32_16x16x32_bf16 a[248:251], v[144:147], v[8:11], a[248:251]\n\t"
    "ds_read_b64_tr_b16 v[188:189], v195 offset:38912\n\t"
    "v_mfma_f32_16x16x32_bf16 a[248:251], v[148:151], v[32:35], a[248:251]\n\t"
    "v_mfma_f32_16x16x32_bf16 a[252:255], v[148:151], v[36:39], a[252:255]\n\t"
    "ds_read_b64_tr_b16 v[190:191], v195 offset:39936\n\t"
    "v_mfma_f32_16x16x32_bf16 a[252:255], v[144:147], v[12:15], a[252:255]\n\t"
    "buffer_load_dwordx4 v197, s[64:67], s28 offen offset:1024 lds\n\t"
    "v_mfma_f32_16x16x32_bf16 a[0:3], v[144:147], v[16:19], a[0:3]\n\t"
    "v_mfma_f32_16x16x32_bf16 a[0:3], v[148:151], v[40:43], a[0:3]\n\t"
    "v_add_u32 v192, 98304, v192\n\t"
    "v_mfma_f32_16x16x32_bf16 a[4:7], v[148:151], v[44:47], a[4:7]\n\t"
    "v_add_u32 v193, 98304, v193\n\t"
    "s_add_u32 m0, s58, 38912\n\t"
    "v_mfma_f32_16x16x32_bf16 a[4:7], v[144:147], v[20:23], a[4:7]\n\t"
    "buffer_load_dwordx4 v196, s[64:67], s29 offen lds\n\t"
    "v_add_u32 v194, 98304, v194\n\t"
    "v_mfma_f32_16x16x32_bf16 a[8:11], v[152:155], v[0:3], a[8:11]\n\t"
    "v_add_u32 v195, 98304, v195\n\t"
    "v_mfma_f32_16x16x32_bf16 a[8:11], v[156:159], v[24:27], a[8:11]\n\t"
    "v_mfma_f32_16x16x32_bf16 a[12:15], v[156:159], v[28:31], a[12:15]\n\t"
    "v_mfma_f32_16x16x32_bf16 a[12:15], v[152:155], v[4:7], a[12:15]\n\t"
    "buffer_load_dwordx4 v197, s[64:67], s29 offen offset:1024 lds\n\t"
    "v_mfma_f32_16x16x32_bf16 a[16:19], v[152:155], v[8:11], a[16:19]\n\t"
    "v_mfma_f32_16x16x32_bf16 a[16:19], v[156:159], v[32:35], a[16:19]\n\t"
    "v_mfma_f32_16x16x32_bf16 a[20:23], v[156:159], v[36:39], a[20:23]\n\t"
    "v_mfma_f32_16x16x32_bf16 a[20:23], v[152:155], v[12:15], a[20:23]\n\t"
    "v_mfma_f32_16x16x32_bf16 a[24:27], v[152:155], v[16:19], a[24:27]\n\t"
    "v_mfma_f32_16x16x32_bf16 a[24:27], v[156:159], v[40:43], a[24:27]\n\t"
    "v_mfma_f32_16x16x32_bf16 a[28:31], v[156:159], v[44:47], a[28:31]\n\t"
    "v_mfma_f32_16x16x32_bf16 a[28:31], v[152:155], v[20:23], a[28:31]\n\t"
    "s_waitcnt lgkmcnt(0)\n\t"
    "s_barrier\n\t"
    "v_mfma_f32_16x16x32_bf16 a[32:35], v[160:163], v[0:3], a[32:35]\n\t"
    "ds_read_b64_tr_b16 v[128:129], v192\n\t"
    "v_mfma_f32_16x16x32_bf16 a[32:35], v[164:167], v[24:27], a[32:35]\n\t"
    "v_mfma_f32_16x16x32_bf16 a[36:39], v[164:167], v[28:31], a[36:39]\n\t"
    "ds_read_b64_tr_b16 v[130:131], v192 offset:1024\n\t"
    "s_cmp_le_u32 s63, 3\n\t"
    "s_addc_u32 s31, 3, 0\n\t"
    "s_mul_i32 s31, s31, 12288\n\t"
    "s_add_u32 s31, s31, 98304\n\t"
    "v_mfma_f32_16x16x32_bf16 a[36:39], v[160:163], v[4:7], a[36:39]\n\t"
    "buffer_load_dwordx4 v[48:51], v229, s[52:55], s31 offen sc1\n\t"
    "v_mfma_f32_16x16x32_bf16 a[40:43], v[160:163], v[8:11], a[40:43]\n\t"
    "ds_read_b64_tr_b16 v[132:133], v192 offset:2048\n\t"
    "v_mfma_f32_16x16x32_bf16 a[40:43], v[164:167], v[32:35], a[40:43]\n\t"
    "v_mfma_f32_16x16x32_bf16 a[44:47], v[164:167], v[36:39], a[44:47]\n\t"
    "ds_read_b64_tr_b16 v[134:135], v192 offset:3072\n\t"
    "v_mfma_f32_16x16x32_bf16 a[44:47], v[160:163], v[12:15], a[44:47]\n\t"
    "buffer_load_dwordx4 v[52:55], v229, s[52:55], s31 offen offset:1024 sc1\n\t"
    "v_mfma_f32_16x16x32_bf16 a[48:51], v[160:163], v[16:19], a[48:51]\n\t"
    "ds_read_b64_tr_b16 v[136:137], v193\n\t"
    "v_mfma_f32_16x16x32_bf16 a[48:51], v[164:167], v[40:43], a[48:51]\n\t"
    "v_mfma_f32_16x16x32_bf16 a[52:55], v[164:167], v[44:47], a[52:55]\n\t"
    "ds_read_b64_tr_b16 v[138:139], v193 offset:1024\n\t"
    "v_mfma_f32_16x16x32_bf16 a[52:55], v[160:163], v[20:23], a[52:55]\n\t"
    "buffer_load_dwordx4 v[56:59], v229, s[52:55], s31 offen offset:2048 sc1\n\t"
    "v_mfma_f32_16x16x32_bf16 a[56:59], v[168:171], v[0:3], a[56:59]\n\t"
    "ds_read_b64_tr_b16 v[140:141], v193 offset:2048\n\t"
    "v_mfma_f32_16x16x32_bf16 a[56:59], v[172:175], v[24:27], a[56:59]\n\t"
    "v_mfma_f32_16x16x32_bf16 a[60:63], v[172:175], v[28:31], a[60:63]\n\t"
    "ds_read_b64_tr_b16 v[142:143], v193 offset:3072\n\t"
    "v_mfma_f32_16x16x32_bf16 a[60:63], v[168:171], v[4:7], a[60:63]\n\t"
    "buffer_load_dwordx4 v[60:63], v229, s[52:55], s31 offen offset:3072 sc1\n\t"
    "v_mfma_f32_16x16x32_bf16 a[64:67], v[168:171], v[8:11], a[64:67]\n\t"
    "ds_read_b64_tr_b16 v[144:145], v194\n\t"
    "v_mfma_f32_16x16x32_bf16 a[64:67], v[172:175], v[32:35], a[64:67]\n\t"
    "v_mfma_f32_16x16x32_bf16 a[68:71], v[172:175], v[36:39], a[68:71]\n\t"
    "ds_read_b64_tr_b16 v[146:147], v194 offset:1024\n\t"
    "v_mfma_f32_16x16x32_bf16 a[68:71], v[168:171], v[12:15], a[68:71]\n\t"
    "buffer_load_dwordx4 v[64:67], v241, s[52:55], s31 offen sc1\n\t"
    "v_mfma_f32_16x16x32_bf16 a[72:75], v[168:171], v[16:19], a[72:75]\n\t"
    "ds_read_b64_tr_b16 v[148:149], v194 offset:2048\n\t"
    "v_mfma_f32_16x16x32_bf16 a[72:75], v[172:175], v[40:43], a[72:75]\n\t"
    "v_mfma_f32_16x16x32_bf16 a[76:79], v[172:175], v[44:47], a[76:79]\n\t"
    "ds_read_b64_tr_b16 v[150:151], v194 offset:3072\n\t"
    "v_mfma_f32_16x16x32_bf16 a[76:79], v[168:171], v[20:23], a[76:79]\n\t"
    "buffer_load_dwordx4 v[68:71], v241, s[52:55], s31 offen offset:1024 sc1\n\t"
    "v_mfma_f32_16x16x32_bf16 a[80:83], v[176:179], v[0:3], a[80:83]\n\t"
    "ds_read_b64_tr_b16 v[152:153], v195\n\t"
    "v_mfma_f32_16x16x32_bf16 a[80:83], v[180:183], v[24:27], a[80:83]\n\t"
    "v_mfma_f32_16x16x32_bf16 a[84:87], v[180:183], v[28:31], a[84:87]\n\t"
    "ds_read_b64_tr_b16 v[154:155], v195 offset:1024\n\t"
    "s_cmp_le_u32 s63, 5\n\t"
    "s_addc_u32 s28, 5, 0\n\t"
    "s_lshl_b32 s28, s28, 19\n\t"
    "s_add_u32 s28, s28, 65536\n\t"
    "s_add_u32 m0, s59, 0\n\t"
    "v_mfma_f32_16x16x32_bf16 a[84:87], v[176:179], v[4:7], a[84:87]\n\t"
    "buffer_load_dwordx4 v198, s[64:67], s28 offen lds\n\t"
    "v_mfma_f32_16x16x32_bf16 a[88:91], v[176:179], v[8:11], a[88:91]\n\t"
    "ds_read_b64_tr_b16 v[156:157], v195 offset:2048\n\t"
    "v_mfma_f32_16x16x32_bf16 a[88:91], v[180:183], v[32:35], a[88:91]\n\t"
    "v_mfma_f32_16x16x32_bf16 a[92:95], v[180:183], v[36:39], a[92:95]\n\t"
    "ds_read_b64_tr_b16 v[158:159], v195 offset:3072\n\t"
    "v_mfma_f32_16x16x32_bf16 a[92:95], v[176:179], v[12:15], a[92:95]\n\t"
    "buffer_load_dwordx4 v199, s[64:67], s28 offen offset:1024 lds\n\t"
    "v_mfma_f32_16x16x32_bf16 a[96:99], v[176:179], v[16:19], a[96:99]\n\t"
    "v_mfma_f32_16x16x32_bf16 a[96:99], v[180:183], v[40:43], a[96:99]\n\t"
    "v_mfma_f32_16x16x32_bf16 a[100:103], v[180:183], v[44:47], a[100:103]\n\t"
    "s_cmp_le_u32 s63, 6\n\t"
    "s_addc_u32 s29, 6, 0\n\t"
    "s_lshl_b32 s29, s29, 19\n\t"
    "s_nop 0\n\t"
    "s_add_u32 s29, s29, 65536\n\t"
    "s_add_u32 m0, s59, 2048\n\t"
    "v_mfma_f32_16x16x32_bf16 a[100:103], v[176:179], v[20:23], a[100:103]\n\t"
    "buffer_load_dwordx4 v198, s[64:67], s29 offen lds\n\t"
    "v_mfma_f32_16x16x32_bf16 a[104:107], v[184:187], v[0:3], a[104:107]\n\t"
    "v_mfma_f32_16x16x32_bf16 a[104:107], v[188:191], v[24:27], a[104:107]\n\t"
    "v_mfma_f32_16x16x32_bf16 a[108:111], v[188:191], v[28:31], a[108:111]\n\t"
    "v_mfma_f32_16x16x32_bf16 a[108:111], v[184:187], v[4:7], a[108:111]\n\t"
    "buffer_load_dwordx4 v199, s[64:67], s29 offen offset:1024 lds\n\t"
    "v_mfma_f32_16x16x32_bf16 a[112:115], v[184:187], v[8:11], a[112:115]\n\t"
    "v_mfma_f32_16x16x32_bf16 a[112:115], v[188:191], v[32:35], a[112:115]\n\t"
    "v_mfma_f32_16x16x32_bf16 a[116:119], v[188:191], v[36:39], a[116:119]\n\t"
    "v_mfma_f32_16x16x32_bf16 a[116:119], v[184:187], v[12:15], a[116:119]\n\t"
    "v_mfma_f32_16x16x32_bf16 a[120:123], v[184:187], v[16:19], a[120:123]\n\t"
    "v_mfma_f32_16x16x32_bf16 a[120:123], v[188:191], v[40:43], a[120:123]\n\t"
    "v_mfma_f32_16x16x32_bf16 a[124:127], v[188:191], v[44:47], a[124:127]\n\t"
    "v_mfma_f32_16x16x32_bf16 a[124:127], v[184:187], v[20:23], a[124:127]\n\t"
    "s_waitcnt vmcnt(14) lgkmcnt(0)\n\t"
    "s_barrier\n\t"
    "v_mfma_f32_16x16x32_bf16 a[192:195], v[128:131], a[128:131], a[192:195]\n\t"
    "ds_read_b64_tr_b16 v[160:161], v192 offset:4096\n\t"
    "v_mfma_f32_16x16x32_bf16 a[192:195], v[132:135], a[152:155], a[192:195]\n\t"
    "v_mfma_f32_16x16x32_bf16 a[196:199], v[132:135], a[156:159], a[196:199]\n\t"
    "ds_read_b64_tr_b16 v[162:163], v192 offset:5120\n\t"
    "s_cmp_le_u32 s63, 4\n\t"
    "s_addc_u32 s31, 4, 0\n\t"
    "s_mul_i32 s31, s31, 12288\n\t"
    "s_add_u32 s31, s31, 98304\n\t"
    "v_mfma_f32_16x16x32_bf16 a[196:199], v[128:131], a[132:135], a[196:199]\n\t"
    "buffer_load_dwordx4 v[72:75], v229, s[52:55], s31 offen sc1\n\t"
    "v_mfma_f32_16x16x32_bf16 a[200:203], v[128:131], a[136:139], a[200:203]\n\t"
    "ds_read_b64_tr_b16 v[164:165], v192 offset:6144\n\t"
    "v_mfma_f32_16x16x32_bf16 a[200:203], v[132:135], a[160:163], a[200:203]\n\t"
    "v_mfma_f32_16x16x32_bf16 a[204:207], v[132:135], a[164:167], a[204:207]\n\t"
    "ds_read_b64_tr_b16 v[166:167], v192 offset:7168\n\t"
    "v_mfma_f32_16x16x32_bf16 a[204:207], v[128:131], a[140:143], a[204:207]\n\t"
    "buffer_load_dwordx4 v[76:79], v229, s[52:55], s31 offen offset:1024 sc1\n\t"
    "v_mfma_f32_16x16x32_bf16 a[208:211], v[128:131], a[144:147], a[208:211]\n\t"
    "ds_read_b64_tr_b16 v[168:169], v193 offset:4096\n\t"
    "v_mfma_f32_16x16x32_bf16 a[208:211], v[132:135], a[168:171], a[208:211]\n\t"
    "v_mfma_f32_16x16x32_bf16 a[212:215], v[132:135], a[172:175], a[212:215]\n\t"
    "ds_read_b64_tr_b16 v[170:171], v193 offset:5120\n\t"
    "v_mfma_f32_16x16x32_bf16 a[212:215], v[128:131], a[148:151], a[212:215]\n\t"
    "buffer_load_dwordx4 v[80:83], v229, s[52:55], s31 offen offset:2048 sc1\n\t"
    "v_mfma_f32_16x16x32_bf16 a[216:219], v[136:139], a[128:131], a[216:219]\n\t"
    "ds_read_b64_tr_b16 v[172:173], v193 offset:6144\n\t"
    "v_mfma_f32_16x16x32_bf16 a[216:219], v[140:143], a[152:155], a[216:219]\n\t"
    "v_mfma_f32_16x16x32_bf16 a[220:223], v[140:143], a[156:159], a[220:223]\n\t"
    "ds_read_b64_tr_b16 v[174:175], v193 offset:7168\n\t"
    "v_mfma_f32_16x16x32_bf16 a[220:223], v[136:139], a[132:135], a[220:223]\n\t"
    "buffer_load_dwordx4 v[84:87], v229, s[52:55], s31 offen offset:3072 sc1\n\t"
    "v_mfma_f32_16x16x32_bf16 a[224:227], v[136:139], a[136:139], a[224:227]\n\t"
    "ds_read_b64_tr_b16 v[176:177], v194 offset:4096\n\t"
    "v_mfma_f32_16x16x32_bf16 a[224:227], v[140:143], a[160:163], a[224:227]\n\t"
    "v_mfma_f32_16x16x32_bf16 a[228:231], v[140:143], a[164:167], a[228:231]\n\t"
    "ds_read_b64_tr_b16 v[178:179], v194 offset:5120\n\t"
    "v_mfma_f32_16x16x32_bf16 a[228:231], v[136:139], a[140:143], a[228:231]\n\t"
    "buffer_load_dwordx4 v[88:91], v241, s[52:55], s31 offen sc1\n\t"
    "v_mfma_f32_16x16x32_bf16 a[232:235], v[136:139], a[144:147], a[232:235]\n\t"
    "ds_read_b64_tr_b16 v[180:181], v194 offset:6144\n\t"
    "v_mfma_f32_16x16x32_bf16 a[232:235], v[140:143], a[168:171], a[232:235]\n\t"
    "v_mfma_f32_16x16x32_bf16 a[236:239], v[140:143], a[172:175], a[236:239]\n\t"
    "ds_read_b64_tr_b16 v[182:183], v194 offset:7168\n\t"
    "v_mfma_f32_16x16x32_bf16 a[236:239], v[136:139], a[148:151], a[236:239]\n\t"
    "buffer_load_dwordx4 v[92:95], v241, s[52:55], s31 offen offset:1024 sc1\n\t"
    "v_mfma_f32_16x16x32_bf16 a[240:243], v[144:147], a[128:131], a[240:243]\n\t"
    "ds_read_b64_tr_b16 v[184:185], v195 offset:4096\n\t"
    "v_mfma_f32_16x16x32_bf16 a[240:243], v[148:151], a[152:155], a[240:243]\n\t"
    "v_mfma_f32_16x16x32_bf16 a[244:247], v[148:151], a[156:159], a[244:247]\n\t"
    "ds_read_b64_tr_b16 v[186:187], v195 offset:5120\n\t"
    "s_add_u32 m0, s59, 4096\n\t"
    "v_mfma_f32_16x16x32_bf16 a[244:247], v[144:147], a[132:135], a[244:247]\n\t"
    "buffer_load_dwordx4 v196, s[64:67], s28 offen lds\n\t"
    "v_mfma_f32_16x16x32_bf16 a[248:251], v[144:147], a[136:139], a[248:251]\n\t"
    "ds_read_b64_tr_b16 v[188:189], v195 offset:6144\n\t"
    "v_mfma_f32_16x16x32_bf16 a[248:251], v[148:151], a[160:163], a[248:251]\n\t"
    "v_mfma_f32_16x16x32_bf16 a[252:255], v[148:151], a[164:167], a[252:255]\n\t"
    "ds_read_b64_tr_b16 v[190:191], v195 offset:7168\n\t"
    "v_mfma_f32_16x16x32_bf16 a[252:255], v[144:147], a[140:143], a[252:255]\n\t"
    "buffer_load_dwordx4 v197, s[64:67], s28 offen offset:1024 lds\n\t"
    "v_mfma_f32_16x16x32_bf16 a[0:3], v[144:147], a[144:147], a[0:3]\n\t"
    "v_mfma_f32_16x16x32_bf16 a[0:3], v[148:151], a[168:171], a[0:3]\n\t"
    "v_mfma_f32_16x16x32_bf16 a[4:7], v[148:151], a[172:175], a[4:7]\n\t"
    "s_add_u32 m0, s59, 6144\n\t"
    "v_mfma_f32_16x16x32_bf16 a[4:7], v[144:147], a[148:151], a[4:7]\n\t"
    "buffer_load_dwordx4 v196, s[64:67], s29 offen lds\n\t"
    "v_mfma_f32_16x16x32_bf16 a[8:11], v[152:155], a[128:131], a[8:11]\n\t"
    "v_mfma_f32_16x16x32_bf16 a[8:11], v[156:159], a[152:155], a[8:11]\n\t"
    "v_mfma_f32_16x16x32_bf16 a[12:15], v[156:159], a[156:159], a[12:15]\n\t"
    "v_mfma_f32_16x16x32_bf16 a[12:15], v[152:155], a[132:135], a[12:15]\n\t"
    "buffer_load_dwordx4 v197, s[64:67], s29 offen offset:1024 lds\n\t"
    "v_mfma_f32_16x16x32_bf16 a[16:19], v[152:155], a[136:139], a[16:19]\n\t"
    "v_mfma_f32_16x16x32_bf16 a[16:19], v[156:159], a[160:163], a[16:19]\n\t"
    "v_mfma_f32_16x16x32_bf16 a[20:23], v[156:159], a[164:167], a[20:23]\n\t"
    "v_mfma_f32_16x16x32_bf16 a[20:23], v[152:155], a[140:143], a[20:23]\n\t"
    "v_mfma_f32_16x16x32_bf16 a[24:27], v[152:155], a[144:147], a[24:27]\n\t"
    "v_mfma_f32_16x16x32_bf16 a[24:27], v[156:159], a[168:171], a[24:27]\n\t"
    "v_mfma_f32_16x16x32_bf16 a[28:31], v[156:159], a[172:175], a[28:31]\n\t"
    "v_mfma_f32_16x16x32_bf16 a[28:31], v[152:155], a[148:151], a[28:31]\n\t"
    "s_waitcnt lgkmcnt(0)\n\t"
    "s_barrier\n\t"
    "v_mfma_f32_16x16x32_bf16 a[32:35], v[160:163], a[128:131], a[32:35]\n\t"
    "ds_read_b64_tr_b16 v[128:129], v192 offset:32768\n\t"
    "v_mfma_f32_16x16x32_bf16 a[32:35], v[164:167], a[152:155], a[32:35]\n\t"
    "buffer_load_dword v254, v230, s[68:71], 0 offen sc1\n\t"
    "v_mfma_f32_16x16x32_bf16 a[36:39], v[164:167], a[156:159], a[36:39]\n\t"
    "ds_read_b64_tr_b16 v[130:131], v192 offset:33792\n\t"
    "s_add_u32 s24, s77, 294912\n\t"
    "v_mfma_f32_16x16x32_bf16 a[36:39], v[160:163], a[132:135], a[36:39]\n\t"
    "buffer_store_dwordx4 v[120:123], v229, s[52:55], s24 offen sc1\n\t"
    "v_mfma_f32_16x16x32_bf16 a[40:43], v[160:163], a[136:139], a[40:43]\n\t"
    "ds_read_b64_tr_b16 v[132:133], v192 offset:34816\n\t"
    "v_mfma_f32_16x16x32_bf16 a[40:43], v[164:167], a[160:163], a[40:43]\n\t"
    "v_mfma_f32_16x16x32_bf16 a[44:47], v[164:167], a[164:167], a[44:47]\n\t"
    "ds_read_b64_tr_b16 v[134:135], v192 offset:35840\n\t"
    "v_mfma_f32_16x16x32_bf16 a[44:47], v[160:163], a[140:143], a[44:47]\n\t"
    "buffer_store_dwordx4 v[124:127], v229, s[52:55], s24 offen offset:1024 sc1\n\t"
    "v_mfma_f32_16x16x32_bf16 a[48:51], v[160:163], a[144:147], a[48:51]\n\t"
    "ds_read_b64_tr_b16 v[136:137], v193 offset:32768\n\t"
    "v_mfma_f32_16x16x32_bf16 a[48:51], v[164:167], a[168:171], a[48:51]\n\t"
    "v_mfma_f32_16x16x32_bf16 a[52:55], v[164:167], a[172:175], a[52:55]\n\t"
    "ds_read_b64_tr_b16 v[138:139], v193 offset:33792\n\t"
    "v_mfma_f32_16x16x32_bf16 a[52:55], v[160:163], a[148:151], a[52:55]\n\t"
    "buffer_store_dwordx4 v[208:211], v229, s[52:55], s24 offen offset:2048 sc1\n\t"
    "v_mfma_f32_16x16x32_bf16 a[56:59], v[168:171], a[128:131], a[56:59]\n\t"
    "ds_read_b64_tr_b16 v[140:141], v193 offset:34816\n\t"
    "v_mfma_f32_16x16x32_bf16 a[56:59], v[172:175], a[152:155], a[56:59]\n\t"
    "v_mfma_f32_16x16x32_bf16 a[60:63], v[172:175], a[156:159], a[60:63]\n\t"
    "ds_read_b64_tr_b16 v[142:143], v193 offset:35840\n\t"
    "s_cmp_le_u32 s63, 5\n\t"
    "s_addc_u32 s31, 5, 0\n\t"
    "s_mul_i32 s31, s31, 12288\n\t"
    "s_add_u32 s31, s31, 98304\n\t"
    "v_mfma_f32_16x16x32_bf16 a[60:63], v[168:171], a[132:135], a[60:63]\n\t"
    "buffer_load_dwordx4 v[0:3], v229, s[52:55], s31 offen sc1\n\t"
    "v_mfma_f32_16x16x32_bf16 a[64:67], v[168:171], a[136:139], a[64:67]\n\t"
    "ds_read_b64_tr_b16 v[144:145], v194 offset:32768\n\t"
    "v_mfma_f32_16x16x32_bf16 a[64:67], v[172:175], a[160:163], a[64:67]\n\t"
    "v_mfma_f32_16x16x32_bf16 a[68:71], v[172:175], a[164:167], a[68:71]\n\t"
    "ds_read_b64_tr_b16 v[146:147], v194 offset:33792\n\t"
    "v_mfma_f32_16x16x32_bf16 a[68:71], v[168:171], a[140:143], a[68:71]\n\t"
    "buffer_load_dwordx4 v[4:7], v229, s[52:55], s31 offen offset:1024 sc1\n\t"
    "v_mfma_f32_16x16x32_bf16 a[72:75], v[168:171], a[144:147], a[72:75]\n\t"
    "ds_read_b64_tr_b16 v[148:149], v194 offset:34816\n\t"
    "v_mfma_f32_16x16x32_bf16 a[72:75], v[172:175], a[168:171], a[72:75]\n\t"
    "v_mfma_f32_16x16x32_bf16 a[76:79], v[172:175], a[172:175], a[76:79]\n\t"
    "ds_read_b64_tr_b16 v[150:151], v194 offset:35840\n\t"
    "v_mfma_f32_16x16x32_bf16 a[76:79], v[168:171], a[148:151], a[76:79]\n\t"
    "buffer_load_dwordx4 v[8:11], v229, s[52:55], s31 offen offset:2048 sc1\n\t"
    "v_mfma_f32_16x16x32_bf16 a[80:83], v[176:179], a[128:131], a[80:83]\n\t"
    "ds_read_b64_tr_b16 v[152:153], v195 offset:32768\n\t"
    "v_mfma_f32_16x16x32_bf16 a[80:83], v[180:183], a[152:155], a[80:83]\n\t"
    "v_mfma_f32_16x16x32_bf16 a[84:87], v[180:183], a[156:159], a[84:87]\n\t"
    "ds_read_b64_tr_b16 v[154:155], v195 offset:33792\n\t"
    "v_mfma_f32_16x16x32_bf16 a[84:87], v[176:179], a[132:135], a[84:87]\n\t"
    "buffer_load_dwordx4 v[12:15], v229, s[52:55], s31 offen offset:3072 sc1\n\t"
    "v_mfma_f32_16x16x32_bf16 a[88:91], v[176:179], a[136:139], a[88:91]\n\t"
    "ds_read_b64_tr_b16 v[156:157], v195 offset:34816\n\t"
    "v_mfma_f32_16x16x32_bf16 a[88:91], v[180:183], a[160:163], a[88:91]\n\t"
    "v_mfma_f32_16x16x32_bf16 a[92:95], v[180:183], a[164:167], a[92:95]\n\t"
    "ds_read_b64_tr_b16 v[158:159], v195 offset:35840\n\t"
    "v_mfma_f32_16x16x32_bf16 a[92:95], v[176:179], a[140:143], a[92:95]\n\t"
    "buffer_load_dwordx4 v[16:19], v241, s[52:55], s31 offen sc1\n\t"
    "v_mfma_f32_16x16x32_bf16 a[96:99], v[176:179], a[144:147], a[96:99]\n\t"
    "v_mfma_f32_16x16x32_bf16 a[96:99], v[180:183], a[168:171], a[96:99]\n\t"
    "v_mfma_f32_16x16x32_bf16 a[100:103], v[180:183], a[172:175], a[100:103]\n\t"
    "v_mfma_f32_16x16x32_bf16 a[100:103], v[176:179], a[148:151], a[100:103]\n\t"
    "buffer_load_dwordx4 v[20:23], v241, s[52:55], s31 offen offset:1024 sc1\n\t"
    "v_mfma_f32_16x16x32_bf16 a[104:107], v[184:187], a[128:131], a[104:107]\n\t"
    "v_mfma_f32_16x16x32_bf16 a[104:107], v[188:191], a[152:155], a[104:107]\n\t"
    "v_mfma_f32_16x16x32_bf16 a[108:111], v[188:191], a[156:159], a[108:111]\n\t"
    "v_mfma_f32_16x16x32_bf16 a[108:111], v[184:187], a[132:135], a[108:111]\n\t"
    "v_mfma_f32_16x16x32_bf16 a[112:115], v[184:187], a[136:139], a[112:115]\n\t"
    "v_mfma_f32_16x16x32_bf16 a[112:115], v[188:191], a[160:163], a[112:115]\n\t"
    "v_mfma_f32_16x16x32_bf16 a[116:119], v[188:191], a[164:167], a[116:119]\n\t"
    "v_mfma_f32_16x16x32_bf16 a[116:119], v[184:187], a[140:143], a[116:119]\n\t"
    "v_mfma_f32_16x16x32_bf16 a[120:123], v[184:187], a[144:147], a[120:123]\n\t"
    "v_mfma_f32_16x16x32_bf16 a[120:123], v[188:191], a[168:171], a[120:123]\n\t"
    "v_mfma_f32_16x16x32_bf16 a[124:127], v[188:191], a[172:175], a[124:127]\n\t"
    "v_mfma_f32_16x16x32_bf16 a[124:127], v[184:187], a[148:151], a[124:127]\n\t"
    "s_waitcnt vmcnt(14) lgkmcnt(0)\n\t"
    "s_barrier\n\t"
    "v_mfma_f32_16x16x32_bf16 a[192:195], v[128:131], v[48:51], a[192:195]\n\t"
    "ds_read_b64_tr_b16 v[160:161], v192 offset:36864\n\t"
    "s_add_u32 s24, s77, 294912\n\t"
    "v_mfma_f32_16x16x32_bf16 a[192:195], v[132:135], v[72:75], a[192:195]\n\t"
    "buffer_store_dwordx4 v[212:215], v229, s[52:55], s24 offen offset:3072 sc1\n\t"
    "v_mfma_f32_16x16x32_bf16 a[196:199], v[132:135], v[76:79], a[196:199]\n\t"
    "ds_read_b64_tr_b16 v[162:163], v192 offset:37888\n\t"
    "v_mfma_f32_16x16x32_bf16 a[196:199], v[128:131], v[52:55], a[196:199]\n\t"
    "v_mfma_f32_16x16x32_bf16 a[200:203], v[128:131], v[56:59], a[200:203]\n\t"
    "ds_read_b64_tr_b16 v[164:165], v192 offset:38912\n\t"
    "v_mfma_f32_16x16x32_bf16 a[200:203], v[132:135], v[80:83], a[200:203]\n\t"
    "buffer_store_dwordx4 v[216:219], v241, s[52:55], s24 offen sc1\n\t"
    "v_mfma_f32_16x16x32_bf16 a[204:207], v[132:135], v[84:87], a[204:207]\n\t"
    "ds_read_b64_tr_b16 v[166:167], v192 offset:39936\n\t"
    "v_mfma_f32_16x16x32_bf16 a[204:207], v[128:131], v[60:63], a[204:207]\n\t"
    "v_mfma_f32_16x16x32_bf16 a[208:211], v[128:131], v[64:67], a[208:211]\n\t"
    "ds_read_b64_tr_b16 v[168:169], v193 offset:36864\n\t"
    "v_mfma_f32_16x16x32_bf16 a[208:211], v[132:135], v[88:91], a[208:211]\n\t"
    "buffer_store_dwordx4 v[220:223], v241, s[52:55], s24 offen offset:1024 sc1\n\t"
    "v_mfma_f32_16x16x32_bf16 a[212:215], v[132:135], v[92:95], a[212:215]\n\t"
    "ds_read_b64_tr_b16 v[170:171], v193 offset:37888\n\t"
    "v_mfma_f32_16x16x32_bf16 a[212:215], v[128:131], v[68:71], a[212:215]\n\t"
    "v_mfma_f32_16x16x32_bf16 a[216:219], v[136:139], v[48:51], a[216:219]\n\t"
    "ds_read_b64_tr_b16 v[172:173], v193 offset:38912\n\t"
    "s_cmp_le_u32 s63, 6\n\t"
    "s_addc_u32 s31, 6, 0\n\t"
    "s_mul_i32 s31, s31, 12288\n\t"
    "s_add_u32 s31, s31, 98304\n\t"
    "v_mfma_f32_16x16x32_bf16 a[216:219], v[140:143], v[72:75], a[216:219]\n\t"
    "buffer_load_dwordx4 v[24:27], v229, s[52:55], s31 offen sc1\n\t"
    "v_mfma_f32_16x16x32_bf16 a[220:223], v[140:143], v[76:79], a[220:223]\n\t"
    "ds_read_b64_tr_b16 v[174:175], v193 offset:39936\n\t"
    "v_mfma_f32_16x16x32_bf16 a[220:223], v[136:139], v[52:55], a[220:223]\n\t"
    "v_mfma_f32_16x16x32_bf16 a[224:227], v[136:139], v[56:59], a[224:227]\n\t"
    "ds_read_b64_tr_b16 v[176:177], v194 offset:36864\n\t"
    "v_mfma_f32_16x16x32_bf16 a[224:227], v[140:143], v[80:83], a[224:227]\n\t"
    "buffer_load_dwordx4 v[28:31], v229, s[52:55], s31 offen offset:1024 sc1\n\t"
    "v_mfma_f32_16x16x32_bf16 a[228:231], v[140:143], v[84:87], a[228:231]\n\t"
    "ds_read_b64_tr_b16 v[178:179], v194 offset:37888\n\t"
    "v_mfma_f32_16x16x32_bf16 a[228:231], v[136:139], v[60:63], a[228:231]\n\t"
    "buffer_load_dwordx4 v[32:35], v229, s[52:55], s31 offen offset:2048 sc1\n\t"
    "v_mfma_f32_16x16x32_bf16 a[232:235], v[136:139], v[64:67], a[232:235]\n\t"
    "ds_read_b64_tr_b16 v[180:181], v194 offset:38912\n\t"
    "v_mfma_f32_16x16x32_bf16 a[232:235], v[140:143], v[88:91], a[232:235]\n\t"
    "v_mfma_f32_16x16x32_bf16 a[236:239], v[140:143], v[92:95], a[236:239]\n\t"
    "ds_read_b64_tr_b16 v[182:183], v194 offset:39936\n\t"
    "v_mfma_f32_16x16x32_bf16 a[236:239], v[136:139], v[68:71], a[236:239]\n\t"
    "buffer_load_dwordx4 v[36:39], v229, s[52:55], s31 offen offset:3072 sc1\n\t"
    "v_mfma_f32_16x16x32_bf16 a[240:243], v[144:147], v[48:51], a[240:243]\n\t"
    "ds_read_b64_tr_b16 v[184:185], v195 offset:36864\n\t"
    "v_mfma_f32_16x16x32_bf16 a[240:243], v[148:151], v[72:75], a[240:243]\n\t"
    "v_mfma_f32_16x16x32_bf16 a[244:247], v[148:151], v[76:79], a[244:247]\n\t"
    "ds_read_b64_tr_b16 v[186:187], v195 offset:37888\n\t"
    "v_mfma_f32_16x16x32_bf16 a[244:247], v[144:147], v[52:55], a[244:247]\n\t"
    "buffer_load_dwordx4 v[40:43], v241, s[52:55], s31 offen sc1\n\t"
    "v_mfma_f32_16x16x32_bf16 a[248:251], v[144:147], v[56:59], a[248:251]\n\t"
    "ds_read_b64_tr_b16 v[188:189], v195 offset:38912\n\t"
    "v_mfma_f32_16x16x32_bf16 a[248:251], v[148:151], v[80:83], a[248:251]\n\t"
    "v_mfma_f32_16x16x32_bf16 a[252:255], v[148:151], v[84:87], a[252:255]\n\t"
    "ds_read_b64_tr_b16 v[190:191], v195 offset:39936\n\t"
    "s_cmp_le_u32 s63, 0\n\t"
    "s_addc_u32 s28, 0, 0\n\t"
    "s_lshl_b32 s28, s28, 19\n\t"
    "s_nop 0\n\t"
    "s_add_u32 s28, s28, 131072\n\t"
    "s_add_u32 m0, s59, 32768\n\t"
    "v_mfma_f32_16x16x32_bf16 a[252:255], v[144:147], v[60:63], a[252:255]\n\t"
    "buffer_load_dwordx4 v198, s[64:67], s28 offen lds\n\t"
    "v_mfma_f32_16x16x32_bf16 a[0:3], v[144:147], v[64:67], a[0:3]\n\t"
    "v_mfma_f32_16x16x32_bf16 a[0:3], v[148:151], v[88:91], a[0:3]\n\t"
    "v_subrev_u32 v192, 98304, v192\n\t"
    "v_mfma_f32_16x16x32_bf16 a[4:7], v[148:151], v[92:95], a[4:7]\n\t"
    "buffer_load_dwordx4 v199, s[64:67], s28 offen offset:1024 lds\n\t"
    "v_subrev_u32 v193, 98304, v193\n\t"
    "v_mfma_f32_16x16x32_bf16 a[4:7], v[144:147], v[68:71], a[4:7]\n\t"
    "v_subrev_u32 v194, 98304, v194\n\t"
    "v_mfma_f32_16x16x32_bf16 a[8:11], v[152:155], v[48:51], a[8:11]\n\t"
    "v_subrev_u32 v195, 98304, v195\n\t"
    "v_mfma_f32_16x16x32_bf16 a[8:11], v[156:159], v[72:75], a[8:11]\n\t"
    "s_cmp_le_u32 s63, 1\n\t"
    "s_addc_u32 s29, 1, 0\n\t"
    "s_lshl_b32 s29, s29, 19\n\t"
    "s_nop 0\n\t"
    "s_add_u32 s29, s29, 131072\n\t"
    "s_add_u32 m0, s59, 34816\n\t"
    "v_mfma_f32_16x16x32_bf16 a[12:15], v[156:159], v[76:79], a[12:15]\n\t"
    "buffer_load_dwordx4 v198, s[64:67], s29 offen lds\n\t"
    "v_mfma_f32_16x16x32_bf16 a[12:15], v[152:155], v[52:55], a[12:15]\n\t"
    "v_mfma_f32_16x16x32_bf16 a[16:19], v[152:155], v[56:59], a[16:19]\n\t"
    "v_mfma_f32_16x16x32_bf16 a[16:19], v[156:159], v[80:83], a[16:19]\n\t"
    "v_mfma_f32_16x16x32_bf16 a[20:23], v[156:159], v[84:87], a[20:23]\n\t"
    "buffer_load_dwordx4 v199, s[64:67], s29 offen offset:1024 lds\n\t"
    "v_mfma_f32_16x16x32_bf16 a[20:23], v[152:155], v[60:63], a[20:23]\n\t"
    "v_mfma_f32_16x16x32_bf16 a[24:27], v[152:155], v[64:67], a[24:27]\n\t"
    "v_mfma_f32_16x16x32_bf16 a[24:27], v[156:159], v[88:91], a[24:27]\n\t"
    "v_mfma_f32_16x16x32_bf16 a[28:31], v[156:159], v[92:95], a[28:31]\n\t"
    "s_mov_b32 s25, 254\n\t"
    "s_mov_b32 s30, 3\n\t"
    "s_mov_b32 s23, 3\n\t"
    "s_waitcnt vmcnt(21)\n\t"
    "v_sub_u32 v254, v254, v231\n\t"
    "v_cmp_gt_u32 vcc, s30, v254\n\t"
    "s_nop 1\n\t"
    "s_and_b32 s26, vcc_lo, s25\n\t"
    "s_cbranch_scc1 .Lk_spin\n\t"
    ".Lk_spinret_3:\n\t"
    "v_mfma_f32_16x16x32_bf16 a[28:31], v[152:155], v[68:71], a[28:31]\n\t"
    "s_waitcnt lgkmcnt(0)\n\t"
    "s_barrier\n\t"
    "v_mfma_f32_16x16x32_bf16 a[32:35], v[160:163], v[48:51], a[32:35]\n\t"
    "ds_read_b64_tr_b16 v[128:129], v192\n\t"
    "v_mfma_f32_16x16x32_bf16 a[32:35], v[164:167], v[72:75], a[32:35]\n\t"
    "buffer_load_dwordx4 v[44:47], v241, s[52:55], s31 offen offset:1024 sc1\n\t"
    "v_mfma_f32_16x16x32_bf16 a[36:39], v[164:167], v[76:79], a[36:39]\n\t"
    "ds_read_b64_tr_b16 v[130:131], v192 offset:1024\n\t"
    "v_mfma_f32_16x16x32_bf16 a[36:39], v[160:163], v[52:55], a[36:39]\n\t"
    "v_mfma_f32_16x16x32_bf16 a[40:43], v[160:163], v[56:59], a[40:43]\n\t"
    "ds_read_b64_tr_b16 v[132:133], v192 offset:2048\n\t"
    "s_cmp_le_u32 s63, 0\n\t"
    "s_addc_u32 s31, 0, 0\n\t"
    "s_mul_i32 s31, s31, 12288\n\t"
    "s_add_u32 s31, s31, 196608\n\t"
    "v_mfma_f32_16x16x32_bf16 a[40:43], v[164:167], v[80:83], a[40:43]\n\t"
    "buffer_load_dwordx4 a[128:131], v229, s[52:55], s31 offen sc1\n\t"
    "v_mfma_f32_16x16x32_bf16 a[44:47], v[164:167], v[84:87], a[44:47]\n\t"
    "ds_read_b64_tr_b16 v[134:135], v192 offset:3072\n\t"
    "v_mfma_f32_16x16x32_bf16 a[44:47], v[160:163], v[60:63], a[44:47]\n\t"
    "v_mfma_f32_16x16x32_bf16 a[48:51], v[160:163], v[64:67], a[48:51]\n\t"
    "ds_read_b64_tr_b16 v[136:137], v193\n\t"
    "v_mfma_f32_16x16x32_bf16 a[48:51], v[164:167], v[88:91], a[48:51]\n\t"
    "buffer_load_dwordx4 a[132:135], v229, s[52:55], s31 offen offset:1024 sc1\n\t"
    "v_mfma_f32_16x16x32_bf16 a[52:55], v[164:167], v[92:95], a[52:55]\n\t"
    "ds_read_b64_tr_b16 v[138:139], v193 offset:1024\n\t"
    "v_mfma_f32_16x16x32_bf16 a[52:55], v[160:163], v[68:71], a[52:55]\n\t"
    "v_mfma_f32_16x16x32_bf16 a[56:59], v[168:171], v[48:51], a[56:59]\n\t"
    "ds_read_b64_tr_b16 v[140:141], v193 offset:2048\n\t"
    "v_mfma_f32_16x16x32_bf16 a[56:59], v[172:175], v[72:75], a[56:59]\n\t"
    "buffer_load_dwordx4 a[136:139], v229, s[52:55], s31 offen offset:2048 sc1\n\t"
    "v_mfma_f32_16x16x32_bf16 a[60:63], v[172:175], v[76:79], a[60:63]\n\t"
    "ds_read_b64_tr_b16 v[142:143], v193 offset:3072\n\t"
    "v_mfma_f32_16x16x32_bf16 a[60:63], v[168:171], v[52:55], a[60:63]\n\t"
    "v_mfma_f32_16x16x32_bf16 a[64:67], v[168:171], v[56:59], a[64:67]\n\t"
    "ds_read_b64_tr_b16 v[144:145], v194\n\t"
    "v_mfma_f32_16x16x32_bf16 a[64:67], v[172:175], v[80:83], a[64:67]\n\t"
    "buffer_load_dwordx4 a[140:143], v229, s[52:55], s31 offen offset:3072 sc1\n\t"
    "v_mfma_f32_16x16x32_bf16 a[68:71], v[172:175], v[84:87], a[68:71]\n\t"
    "ds_read_b64_tr_b16 v[146:147], v194 offset:1024\n\t"
    "v_mfma_f32_16x16x32_bf16 a[68:71], v[168:171], v[60:63], a[68:71]\n\t"
    "v_mfma_f32_16x16x32_bf16 a[72:75], v[168:171], v[64:67], a[72:75]\n\t"
    "ds_read_b64_tr_b16 v[148:149], v194 offset:2048\n\t"
    "v_mfma_f32_16x16x32_bf16 a[72:75], v[172:175], v[88:91], a[72:75]\n\t"
    "buffer_load_dwordx4 a[144:147], v241, s[52:55], s31 offen sc1\n\t"
    "v_mfma_f32_16x16x32_bf16 a[76:79], v[172:175], v[92:95], a[76:79]\n\t"
    "ds_read_b64_tr_b16 v[150:151], v194 offset:3072\n\t"
    "v_mfma_f32_16x16x32_bf16 a[76:79], v[168:171], v[68:71], a[76:79]\n\t"
    "v_mfma_f32_16x16x32_bf16 a[80:83], v[176:179], v[48:51], a[80:83]\n\t"
    "ds_read_b64_tr_b16 v[152:153], v195\n\t"
    "v_mfma_f32_16x16x32_bf16 a[80:83], v[180:183], v[72:75], a[80:83]\n\t"
    "buffer_load_dwordx4 a[148:151], v241, s[52:55], s31 offen offset:1024 sc1\n\t"
    "v_mfma_f32_16x16x32_bf16 a[84:87], v[180:183], v[76:79], a[84:87]\n\t"
    "ds_read_b64_tr_b16 v[154:155], v195 offset:1024\n\t"
    "v_mfma_f32_16x16x32_bf16 a[84:87], v[176:179], v[52:55], a[84:87]\n\t"
    "v_mfma_f32_16x16x32_bf16 a[88:91], v[176:179], v[56:59], a[88:91]\n\t"
    "ds_read_b64_tr_b16 v[156:157], v195 offset:2048\n\t"
    "s_add_u32 m0, s59, 36864\n\t"
    "v_mfma_f32_16x16x32_bf16 a[88:91], v[180:183], v[80:83], a[88:91]\n\t"
    "buffer_load_dwordx4 v196, s[64:67], s28 offen lds\n\t"
    "v_mfma_f32_16x16x32_bf16 a[92:95], v[180:183], v[84:87], a[92:95]\n\t"
    "ds_read_b64_tr_b16 v[158:159], v195 offset:3072\n\t"
    "v_mfma_f32_16x16x32_bf16 a[92:95], v[176:179], v[60:63], a[92:95]\n\t"
    "v_mfma_f32_16x16x32_bf16 a[96:99], v[176:179], v[64:67], a[96:99]\n\t"
    "v_mfma_f32_16x16x32_bf16 a[96:99], v[180:183], v[88:91], a[96:99]\n\t"
    "buffer_load_dwordx4 v197, s[64:67], s28 offen offset:1024 lds\n\t"
    "v_mfma_f32_16x16x32_bf16 a[100:103], v[180:183], v[92:95], a[100:103]\n\t"
    "v_mfma_f32_16x16x32_bf16 a[100:103], v[176:179], v[68:71], a[100:103]\n\t"
    "v_mfma_f32_16x16x32_bf16 a[104:107], v[184:187], v[48:51], a[104:107]\n\t"
    "s_add_u32 m0, s59, 38912\n\t"
    "v_mfma_f32_16x16x32_bf16 a[104:107], v[188:191], v[72:75], a[104:107]\n\t"
    "buffer_load_dwordx4 v196, s[64:67], s29 offen lds\n\t"
    "v_mfma_f32_16x16x32_bf16 a[108:111], v[188:191], v[76:79], a[108:111]\n\t"
    "v_mfma_f32_16x16x32_bf16 a[108:111], v[184:187], v[52:55], a[108:111]\n\t"
    "v_mfma_f32_16x16x32_bf16 a[112:115], v[184:187], v[56:59], a[112:115]\n\t"
    "v_mfma_f32_16x16x32_bf16 a[112:115], v[188:191], v[80:83], a[112:115]\n\t"
    "buffer_load_dwordx4 v197, s[64:67], s29 offen offset:1024 lds\n\t"
    "v_mfma_f32_16x16x32_bf16 a[116:119], v[188:191], v[84:87], a[116:119]\n\t"
    "v_mfma_f32_16x16x32_bf16 a[116:119], v[184:187], v[60:63], a[116:119]\n\t"
    "v_mfma_f32_16x16x32_bf16 a[120:123], v[184:187], v[64:67], a[120:123]\n\t"
    "v_mfma_f32_16x16x32_bf16 a[120:123], v[188:191], v[88:91], a[120:123]\n\t"
    "v_mfma_f32_16x16x32_bf16 a[124:127], v[188:191], v[92:95], a[124:127]\n\t"
    "v_mfma_f32_16x16x32_bf16 a[124:127], v[184:187], v[68:71], a[124:127]\n\t"
    "s_waitcnt vmcnt(10) lgkmcnt(0)\n\t"
    "s_barrier\n\t"
    "v_mfma_f32_16x16x32_bf16 a[192:195], v[128:131], v[0:3], a[192:195]\n\t"
    "ds_read_b64_tr_b16 v[160:161], v192 offset:4096\n\t"
    "v_mfma_f32_16x16x32_bf16 a[192:195], v[132:135], v[24:27], a[192:195]\n\t"
    "v_mfma_f32_16x16x32_bf16 a[196:199], v[132:135], v[28:31], a[196:199]\n\t"
    "ds_read_b64_tr_b16 v[162:163], v192 offset:5120\n\t"
    "s_cmp_le_u32 s63, 1\n\t"
    "s_addc_u32 s31, 1, 0\n\t"
    "s_mul_i32 s31, s31, 12288\n\t"
    "s_add_u32 s31, s31, 196608\n\t"
    "v_mfma_f32_16x16x32_bf16 a[196:199], v[128:131], v[4:7], a[196:199]\n\t"
    "buffer_load_dwordx4 a[152:155], v229, s[52:55], s31 offen sc1\n\t"
    "v_mfma_f32_16x16x32_bf16 a[200:203], v[128:131], v[8:11], a[200:203]\n\t"
    "ds_read_b64_tr_b16 v[164:165], v192 offset:6144\n\t"
    "v_mfma_f32_16x16x32_bf16 a[200:203], v[132:135], v[32:35], a[200:203]\n\t"
    "v_mfma_f32_16x16x32_bf16 a[204:207], v[132:135], v[36:39], a[204:207]\n\t"
    "ds_read_b64_tr_b16 v[166:167], v192 offset:7168\n\t"
    "v_mfma_f32_16x16x32_bf16 a[204:207], v[128:131], v[12:15], a[204:207]\n\t"
    "buffer_load_dwordx4 a[156:159], v229, s[52:55], s31 offen offset:1024 sc1\n\t"
    "v_mfma_f32_16x16x32_bf16 a[208:211], v[128:131], v[16:19], a[208:211]\n\t"
    "ds_read_b64_tr_b16 v[168:169], v193 offset:4096\n\t"
    "v_mfma_f32_16x16x32_bf16 a[208:211], v[132:135], v[40:43], a[208:211]\n\t"
    "v_mfma_f32_16x16x32_bf16 a[212:215], v[132:135], v[44:47], a[212:215]\n\t"
    "ds_read_b64_tr_b16 v[170:171], v193 offset:5120\n\t"
    "v_mfma_f32_16x16x32_bf16 a[212:215], v[128:131], v[20:23], a[212:215]\n\t"
    "buffer_load_dwordx4 a[160:163], v229, s[52:55], s31 offen offset:2048 sc1\n\t"
    "v_mfma_f32_16x16x32_bf16 a[216:219], v[136:139], v[0:3], a[216:219]\n\t"
    "ds_read_b64_tr_b16 v[172:173], v193 offset:6144\n\t"
    "v_mfma_f32_16x16x32_bf16 a[216:219], v[140:143], v[24:27], a[216:219]\n\t"
    "v_mfma_f32_16x16x32_bf16 a[220:223], v[140:143], v[28:31], a[220:223]\n\t"
    "ds_read_b64_tr_b16 v[174:175], v193 offset:7168\n\t"
    "v_mfma_f32_16x16x32_bf16 a[220:223], v[136:139], v[4:7], a[220:223]\n\t"
    "buffer_load_dwordx4 a[164:167], v229, s[52:55], s31 offen offset:3072 sc1\n\t"
    "v_mfma_f32_16x16x32_bf16 a[224:227], v[136:139], v[8:11], a[224:227]\n\t"
    "ds_read_b64_tr_b16 v[176:177], v194 offset:4096\n\t"
    "v_mfma_f32_16x16x32_bf16 a[224:227], v[140:143], v[32:35], a[224:227]\n\t"
    "v_mfma_f32_16x16x32_bf16 a[228:231], v[140:143], v[36:39], a[228:231]\n\t"
    "ds_read_b64_tr_b16 v[178:179], v194 offset:5120\n\t"
    "v_mfma_f32_16x16x32_bf16 a[228:231], v[136:139], v[12:15], a[228:231]\n\t"
    "buffer_load_dwordx4 a[168:171], v241, s[52:55], s31 offen sc1\n\t"
    "v_mfma_f32_16x16x32_bf16 a[232:235], v[136:139], v[16:19], a[232:235]\n\t"
    "ds_read_b64_tr_b16 v[180:181], v194 offset:6144\n\t"
    "v_mfma_f32_16x16x32_bf16 a[232:235], v[140:143], v[40:43], a[232:235]\n\t"
    "v_mfma_f32_16x16x32_bf16 a[236:239], v[140:143], v[44:47], a[236:239]\n\t"
    "ds_read_b64_tr_b16 v[182:183], v194 offset:7168\n\t"
    "v_mfma_f32_16x16x32_bf16 a[236:239], v[136:139], v[20:23], a[236:239]\n\t"
    "buffer_load_dwordx4 a[172:175], v241, s[52:55], s31 offen offset:1024 sc1\n\t"
    "v_mfma_f32_16x16x32_bf16 a[240:243], v[144:147], v[0:3], a[240:243]\n\t"
    "ds_read_b64_tr_b16 v[184:185], v195 offset:4096\n\t"
    "v_mfma_f32_16x16x32_bf16 a[240:243], v[148:151], v[24:27], a[240:243]\n\t"
    "v_mfma_f32_16x16x32_bf16 a[244:247], v[148:151], v[28:31], a[244:247]\n\t"
    "ds_read_b64_tr_b16 v[186:187], v195 offset:5120\n\t"
    "s_cmp_le_u32 s63, 2\n\t"
    "s_addc_u32 s28, 2, 0\n\t"
    "s_lshl_b32 s28, s28, 19\n\t"
    "s_add_u32 s28, s28, 131072\n\t"
    "s_add_u32 m0, s58, 0\n\t"
    "v_mfma_f32_16x16x32_bf16 a[244:247], v[144:147], v[4:7], a[244:247]\n\t"
    "buffer_load_dwordx4 v198, s[64:67], s28 offen lds\n\t"
    "v_mfma_f32_16x16x32_bf16 a[248:251], v[144:147], v[8:11], a[248:251]\n\t"
    "ds_read_b64_tr_b16 v[188:189], v195 offset:6144\n\t"
    "v_mfma_f32_16x16x32_bf16 a[248:251], v[148:151], v[32:35], a[248:251]\n\t"
    "v_mfma_f32_16x16x32_bf16 a[252:255], v[148:151], v[36:39], a[252:255]\n\t"
    "ds_read_b64_tr_b16 v[190:191], v195 offset:7168\n\t"
    "v_mfma_f32_16x16x32_bf16 a[252:255], v[144:147], v[12:15], a[252:255]\n\t"
    "buffer_load_dwordx4 v199, s[64:67], s28 offen offset:1024 lds\n\t"
    "v_mfma_f32_16x16x32_bf16 a[0:3], v[144:147], v[16:19], a[0:3]\n\t"
    "v_mfma_f32_16x16x32_bf16 a[0:3], v[148:151], v[40:43], a[0:3]\n\t"
    "v_mfma_f32_16x16x32_bf16 a[4:7], v[148:151], v[44:47], a[4:7]\n\t"
    "s_cmp_le_u32 s63, 3\n\t"
    "s_addc_u32 s29, 3, 0\n\t"
    "s_lshl_b32 s29, s29, 19\n\t"
    "s_nop 0\n\t"
    "s_add_u32 s29, s29, 131072\n\t"
    "s_add_u32 m0, s58, 2048\n\t"
    "v_mfma_f32_16x16x32_bf16 a[4:7], v[144:147], v[20:23], a[4:7]\n\t"
    "buffer_load_dwordx4 v198, s[64:67], s29 offen lds\n\t"
    "v_mfma_f32_16x16x32_bf16 a[8:11], v[152:155], v[0:3], a[8:11]\n\t"
    "v_mfma_f32_16x16x32_bf16 a[8:11], v[156:159], v[24:27], a[8:11]\n\t"
    "v_mfma_f32_16x16x32_bf16 a[12:15], v[156:159], v[28:31], a[12:15]\n\t"
    "v_mfma_f32_16x16x32_bf16 a[12:15], v[152:155], v[4:7], a[12:15]\n\t"
    "buffer_load_dwordx4 v199, s[64:67], s29 offen offset:1024 lds\n\t"
    "v_mfma_f32_16x16x32_bf16 a[16:19], v[152:155], v[8:11], a[16:19]\n\t"
    "v_mfma_f32_16x16x32_bf16 a[16:19], v[156:159], v[32:35], a[16:19]\n\t"
    "v_mfma_f32_16x16x32_bf16 a[20:23], v[156:159], v[36:39], a[20:23]\n\t"
    "v_mfma_f32_16x16x32_bf16 a[20:23], v[152:155], v[12:15], a[20:23]\n\t"
    "v_mfma_f32_16x16x32_bf16 a[24:27], v[152:155], v[16:19], a[24:27]\n\t"
    "s_mov_b64 exec, 1\n\t"
    "s_nop 0\n\t"
    "buffer_atomic_add v247, off, s[68:71], s78 sc1\n\t"
    "s_mov_b64 exec, -1\n\t"
    "s_nop 0\n\t"
    "v_mfma_f32_16x16x32_bf16 a[24:27], v[156:159], v[40:43], a[24:27]\n\t"
    "v_mfma_f32_16x16x32_bf16 a[28:31], v[156:159], v[44:47], a[28:31]\n\t"
    "v_mfma_f32_16x16x32_bf16 a[28:31], v[152:155], v[20:23], a[28:31]\n\t"
    "s_waitcnt lgkmcnt(0)\n\t"
    "s_barrier\n\t"
    "v_mfma_f32_16x16x32_bf16 a[32:35], v[160:163], v[0:3], a[32:35]\n\t"
    "ds_read_b64_tr_b16 v[128:129], v192 offset:32768\n\t"
    "s_cmp_le_u32 s63, 2\n\t"
    "s_addc_u32 s31, 2, 0\n\t"
    "s_mul_i32 s31, s31, 12288\n\t"
    "s_add_u32 s31, s31, 196608\n\t"
    "v_mfma_f32_16x16x32_bf16 a[32:35], v[164:167], v[24:27], a[32:35]\n\t"
    "buffer_load_dwordx4 v[48:51], v229, s[52:55], s31 offen sc1\n\t"
    "v_mfma_f32_16x16x32_bf16 a[36:39], v[164:167], v[28:31], a[36:39]\n\t"
    "ds_read_b64_tr_b16 v[130:131], v192 offset:33792\n\t"
    "v_mfma_f32_16x16x32_bf16 a[36:39], v[160:163], v[4:7], a[36:39]\n\t"
    "v_mfma_f32_16x16x32_bf16 a[40:43], v[160:163], v[8:11], a[40:43]\n\t"
    "ds_read_b64_tr_b16 v[132:133], v192 offset:34816\n\t"
    "v_mfma_f32_16x16x32_bf16 a[40:43], v[164:167], v[32:35], a[40:43]\n\t"
    "buffer_load_dwordx4 v[52:55], v229, s[52:55], s31 offen offset:1024 sc1\n\t"
    "v_mfma_f32_16x16x32_bf16 a[44:47], v[164:167], v[36:39], a[44:47]\n\t"
    "ds_read_b64_tr_b16 v[134:135], v192 offset:35840\n\t"
    "v_mfma_f32_16x16x32_bf16 a[44:47], v[160:163], v[12:15], a[44:47]\n\t"
    "v_mfma_f32_16x16x32_bf16 a[48:51], v[160:163], v[16:19], a[48:51]\n\t"
    "ds_read_b64_tr_b16 v[136:137], v193 offset:32768\n\t"
    "v_mfma_f32_16x16x32_bf16 a[48:51], v[164:167], v[40:43], a[48:51]\n\t"
    "buffer_load_dwordx4 v[56:59], v229, s[52:55], s31 offen offset:2048 sc1\n\t"
    "v_mfma_f32_16x16x32_bf16 a[52:55], v[164:167], v[44:47], a[52:55]\n\t"
    "ds_read_b64_tr_b16 v[138:139], v193 offset:33792\n\t"
    "v_mfma_f32_16x16x32_bf16 a[52:55], v[160:163], v[20:23], a[52:55]\n\t"
    "v_mfma_f32_16x16x32_bf16 a[56:59], v[168:171], v[0:3], a[56:59]\n\t"
    "ds_read_b64_tr_b16 v[140:141], v193 offset:34816\n\t"
    "v_mfma_f32_16x16x32_bf16 a[56:59], v[172:175], v[24:27], a[56:59]\n\t"
    "buffer_load_dwordx4 v[60:63], v229, s[52:55], s31 offen offset:3072 sc1\n\t"
    "v_mfma_f32_16x16x32_bf16 a[60:63], v[172:175], v[28:31], a[60:63]\n\t"
    "ds_read_b64_tr_b16 v[142:143], v193 offset:35840\n\t"
    "v_mfma_f32_16x16x32_bf16 a[60:63], v[168:171], v[4:7], a[60:63]\n\t"
    "v_mfma_f32_16x16x32_bf16 a[64:67], v[168:171], v[8:11], a[64:67]\n\t"
    "ds_read_b64_tr_b16 v[144:145], v194 offset:32768\n\t"
    "s_add_u32 m0, s58, 4096\n\t"
    "v_mfma_f32_16x16x32_bf16 a[64:67], v[172:175], v[32:35], a[64:67]\n\t"
    "buffer_load_dwordx4 v196, s[64:67], s28 offen lds\n\t"
    "v_mfma_f32_16x16x32_bf16 a[68:71], v[172:175], v[36:39], a[68:71]\n\t"
    "ds_read_b64_tr_b16 v[146:147], v194 offset:33792\n\t"
    "v_mfma_f32_16x16x32_bf16 a[68:71], v[168:171], v[12:15], a[68:71]\n\t"
    "buffer_load_dwordx4 v197, s[64:67], s28 offen offset:1024 lds\n\t"
    "v_mfma_f32_16x16x32_bf16 a[72:75], v[168:171], v[16:19], a[72:75]\n\t"
    "ds_read_b64_tr_b16 v[148:149], v194 offset:34816\n\t"
    "v_mfma_f32_16x16x32_bf16 a[72:75], v[172:175], v[40:43], a[72:75]\n\t"
    "v_mfma_f32_16x16x32_bf16 a[76:79], v[172:175], v[44:47], a[76:79]\n\t"
    "ds_read_b64_tr_b16 v[150:151], v194 offset:35840\n\t"
    "s_add_u32 m0, s58, 6144\n\t"
    "v_mfma_f32_16x16x32_bf16 a[76:79], v[168:171], v[20:23], a[76:79]\n\t"
    "buffer_load_dwordx4 v196, s[64:67], s29 offen lds\n\t"
    "v_mfma_f32_16x16x32_bf16 a[80:83], v[176:179], v[0:3], a[80:83]\n\t"
    "ds_read_b64_tr_b16 v[152:153], v195 offset:32768\n\t"
    "v_mfma_f32_16x16x32_bf16 a[80:83], v[180:183], v[24:27], a[80:83]\n\t"
    "v_mfma_f32_16x16x32_bf16 a[84:87], v[180:183], v[28:31], a[84:87]\n\t"
    "ds_read_b64_tr_b16 v[154:155], v195 offset:33792\n\t"
    "v_mfma_f32_16x16x32_bf16 a[84:87], v[176:179], v[4:7], a[84:87]\n\t"
    "buffer_load_dwordx4 v197, s[64:67], s29 offen offset:1024 lds\n\t"
    "v_mfma_f32_16x16x32_bf16 a[88:91], v[176:179], v[8:11], a[88:91]\n\t"
    "ds_read_b64_tr_b16 v[156:157], v195 offset:34816\n\t"
    "v_mfma_f32_16x16x32_bf16 a[88:91], v[180:183], v[32:35], a[88:91]\n\t"
    "v_mfma_f32_16x16x32_bf16 a[92:95], v[180:183], v[36:39], a[92:95]\n\t"
    "ds_read_b64_tr_b16 v[158:159], v195 offset:35840\n\t"
    "s_cmp_le_u32 s63, 4\n\t"
    "s_addc_u32 s28, 4, 0\n\t"
    "s_lshl_b32 s28, s28, 19\n\t"
    "s_nop 0\n\t"
    "s_add_u32 s28, s28, 131072\n\t"
    "s_add_u32 m0, s58, 32768\n\t"
    "v_mfma_f32_16x16x32_bf16 a[92:95], v[176:179], v[12:15], a[92:95]\n\t"
    "buffer_load_dwordx4 v198, s[64:67], s28 offen lds\n\t"
    "v_mfma_f32_16x16x32_bf16 a[96:99], v[176:179], v[16:19], a[96:99]\n\t"
    "v_mfma_f32_16x16x32_bf16 a[96:99], v[180:183], v[40:43], a[96:99]\n\t"
    "v_mfma_f32_16x16x32_bf16 a[100:103], v[180:183], v[44:47], a[100:103]\n\t"
    "buffer_load_dwordx4 v199, s[64:67], s28 offen offset:1024 lds\n\t"
    "v_mfma_f32_16x16x32_bf16 a[100:103], v[176:179], v[20:23], a[100:103]\n\t"
    "v_mfma_f32_16x16x32_bf16 a[104:107], v[184:187], v[0:3], a[104:107]\n\t"
    "v_mfma_f32_16x16x32_bf16 a[104:107], v[188:191], v[24:27], a[104:107]\n\t"
    "s_cmp_le_u32 s63, 5\n\t"
    "s_addc_u32 s29, 5, 0\n\t"
    "s_lshl_b32 s29, s29, 19\n\t"
    "s_nop 0\n\t"
    "s_add_u32 s29, s29, 131072\n\t"
    "s_add_u32 m0, s58, 34816\n\t"
    "v_mfma_f32_16x16x32_bf16 a[108:111], v[188:191], v[28:31], a[108:111]\n\t"
    "buffer_load_dwordx4 v198, s[64:67], s29 offen lds\n\t"
    "v_mfma_f32_16x16x32_bf16 a[108:111], v[184:187], v[4:7], a[108:111]\n\t"
    "v_mfma_f32_16x16x32_bf16 a[112:115], v[184:187], v[8:11], a[112:115]\n\t"
    "v_mfma_f32_16x16x32_bf16 a[112:115], v[188:191], v[32:35], a[112:115]\n\t"
    "v_mfma_f32_16x16x32_bf16 a[116:119], v[188:191], v[36:39], a[116:119]\n\t"
    "buffer_load_dwordx4 v199, s[64:67], s29 offen offset:1024 lds\n\t"
    "v_mfma_f32_16x16x32_bf16 a[116:119], v[184:187], v[12:15], a[116:119]\n\t"
    "v_mfma_f32_16x16x32_bf16 a[120:123], v[184:187], v[16:19], a[120:123]\n\t"
    "v_mfma_f32_16x16x32_bf16 a[120:123], v[188:191], v[40:43], a[120:123]\n\t"
    "v_mfma_f32_16x16x32_bf16 a[124:127], v[188:191], v[44:47], a[124:127]\n\t"
    "v_mfma_f32_16x16x32_bf16 a[124:127], v[184:187], v[20:23], a[124:127]\n\t"
    "s_waitcnt vmcnt(17) lgkmcnt(0)\n\t"
    "s_barrier\n\t"
    "v_mfma_f32_16x16x32_bf16 a[192:195], v[128:131], a[128:131], a[192:195]\n\t"
    "ds_read_b64_tr_b16 v[160:161], v192 offset:36864\n\t"
    "v_mfma_f32_16x16x32_bf16 a[192:195], v[132:135], a[152:155], a[192:195]\n\t"
    "buffer_load_dwordx4 v[64:67], v241, s[52:55], s31 offen sc1\n\t"
    "v_mfma_f32_16x16x32_bf16 a[196:199], v[132:135], a[156:159], a[196:199]\n\t"
    "ds_read_b64_tr_b16 v[162:163], v192 offset:37888\n\t"
    "v_mfma_f32_16x16x32_bf16 a[196:199], v[128:131], a[132:135], a[196:199]\n\t"
    "v_mfma_f32_16x16x32_bf16 a[200:203], v[128:131], a[136:139], a[200:203]\n\t"
    "ds_read_b64_tr_b16 v[164:165], v192 offset:38912\n\t"
    "v_mfma_f32_16x16x32_bf16 a[200:203], v[132:135], a[160:163], a[200:203]\n\t"
    "buffer_load_dwordx4 v[68:71], v241, s[52:55], s31 offen offset:1024 sc1\n\t"
    "v_mfma_f32_16x16x32_bf16 a[204:207], v[132:135], a[164:167], a[204:207]\n\t"
    "ds_read_b64_tr_b16 v[166:167], v192 offset:39936\n\t"
    "v_mfma_f32_16x16x32_bf16 a[204:207], v[128:131], a[140:143], a[204:207]\n\t"
    "v_mfma_f32_16x16x32_bf16 a[208:211], v[128:131], a[144:147], a[208:211]\n\t"
    "ds_read_b64_tr_b16 v[168:169], v193 offset:36864\n\t"
    "s_cmp_le_u32 s63, 3\n\t"
    "s_addc_u32 s31, 3, 0\n\t"
    "s_mul_i32 s31, s31, 12288\n\t"
    "s_add_u32 s31, s31, 196608\n\t"
    "v_mfma_f32_16x16x32_bf16 a[208:211], v[132:135], a[168:171], a[208:211]\n\t"
    "buffer_load_dwordx4 v[72:75], v229, s[52:55], s31 offen sc1\n\t"
    "v_mfma_f32_16x16x32_bf16 a[212:215], v[132:135], a[172:175], a[212:215]\n\t"
    "ds_read_b64_tr_b16 v[170:171], v193 offset:37888\n\t"
    "v_mfma_f32_16x16x32_bf16 a[212:215], v[128:131], a[148:151], a[212:215]\n\t"
    "v_mfma_f32_16x16x32_bf16 a[216:219], v[136:139], a[128:131], a[216:219]\n\t"
    "ds_read_b64_tr_b16 v[172:173], v193 offset:38912\n\t"
    "v_mfma_f32_16x16x32_bf16 a[216:219], v[140:143], a[152:155], a[216:219]\n\t"
    "buffer_load_dwordx4 v[76:79], v229, s[52:55], s31 offen offset:1024 sc1\n\t"
    "v_mfma_f32_16x16x32_bf16 a[220:223], v[140:143], a[156:159], a[220:223]\n\t"
    "ds_read_b64_tr_b16 v[174:175], v193 offset:39936\n\t"
    "v_mfma_f32_16x16x32_bf16 a[220:223], v[136:139], a[132:135], a[220:223]\n\t"
    "v_mfma_f32_16x16x32_bf16 a[224:227], v[136:139], a[136:139], a[224:227]\n\t"
    "ds_read_b64_tr_b16 v[176:177], v194 offset:36864\n\t"
    "v_mfma_f32_16x16x32_bf16 a[224:227], v[140:143], a[160:163], a[224:227]\n\t"
    "buffer_load_dwordx4 v[80:83], v229, s[52:55], s31 offen offset:2048 sc1\n\t"
    "v_mfma_f32_16x16x32_bf16 a[228:231], v[140:143], a[164:167], a[228:231]\n\t"
    "ds_read_b64_tr_b16 v[178:179], v194 offset:37888\n\t"
    "v_mfma_f32_16x16x32_bf16 a[228:231], v[136:139], a[140:143], a[228:231]\n\t"
    "buffer_load_dwordx4 v[84:87], v229, s[52:55], s31 offen offset:3072 sc1\n\t"
    "v_mfma_f32_16x16x32_bf16 a[232:235], v[136:139], a[144:147], a[232:235]\n\t"
    "ds_read_b64_tr_b16 v[180:181], v194 offset:38912\n\t"
    "v_mfma_f32_16x16x32_bf16 a[232:235], v[140:143], a[168:171], a[232:235]\n\t"
    "v_mfma_f32_16x16x32_bf16 a[236:239], v[140:143], a[172:175], a[236:239]\n\t"
    "ds_read_b64_tr_b16 v[182:183], v194 offset:39936\n\t"
    "v_mfma_f32_16x16x32_bf16 a[236:239], v[136:139], a[148:151], a[236:239]\n\t"
    "buffer_load_dwordx4 v[88:91], v241, s[52:55], s31 offen sc1\n\t"
    "v_mfma_f32_16x16x32_bf16 a[240:243], v[144:147], a[128:131], a[240:243]\n\t"
    "ds_read_b64_tr_b16 v[184:185], v195 offset:36864\n\t"
    "v_mfma_f32_16x16x32_bf16 a[240:243], v[148:151], a[152:155], a[240:243]\n\t"
    "v_mfma_f32_16x16x32_bf16 a[244:247], v[148:151], a[156:159], a[244:247]\n\t"
    "ds_read_b64_tr_b16 v[186:187], v195 offset:37888\n\t"
    "v_mfma_f32_16x16x32_bf16 a[244:247], v[144:147], a[132:135], a[244:247]\n\t"
    "buffer_load_dwordx4 v[92:95], v241, s[52:55], s31 offen offset:1024 sc1\n\t"
    "v_mfma_f32_16x16x32_bf16 a[248:251], v[144:147], a[136:139], a[248:251]\n\t"
    "ds_read_b64_tr_b16 v[188:189], v195 offset:38912\n\t"
    "v_mfma_f32_16x16x32_bf16 a[248:251], v[148:151], a[160:163], a[248:251]\n\t"
    "v_mfma_f32_16x16x32_bf16 a[252:255], v[148:151], a[164:167], a[252:255]\n\t"
    "ds_read_b64_tr_b16 v[190:191], v195 offset:39936\n\t"
    "s_add_u32 m0, s58, 36864\n\t"
    "v_mfma_f32_16x16x32_bf16 a[252:255], v[144:147], a[140:143], a[252:255]\n\t"
    "buffer_load_dwordx4 v196, s[64:67], s28 offen lds\n\t"
    "v_mfma_f32_16x16x32_bf16 a[0:3], v[144:147], a[144:147], a[0:3]\n\t"
    "v_mfma_f32_16x16x32_bf16 a[0:3], v[148:151], a[168:171], a[0:3]\n\t"
    "v_add_u32 v192, 98304, v192\n\t"
    "v_mfma_f32_16x16x32_bf16 a[4:7], v[148:151], a[172:175], a[4:7]\n\t"
    "buffer_load_dwordx4 v197, s[64:67], s28 offen offset:1024 lds\n\t"
    "v_add_u32 v193, 98304, v193\n\t"
    "v_mfma_f32_16x16x32_bf16 a[4:7], v[144:147], a[148:151], a[4:7]\n\t"
    "v_add_u32 v194, 98304, v194\n\t"
    "v_mfma_f32_16x16x32_bf16 a[8:11], v[152:155], a[128:131], a[8:11]\n\t"
    "v_add_u32 v195, 98304, v195\n\t"
    "v_mfma_f32_16x16x32_bf16 a[8:11], v[156:159], a[152:155], a[8:11]\n\t"
    "s_add_u32 m0, s58, 38912\n\t"
    "v_mfma_f32_16x16x32_bf16 a[12:15], v[156:159], a[156:159], a[12:15]\n\t"
    "buffer_load_dwordx4 v196, s[64:67], s29 offen lds\n\t"
    "v_mfma_f32_16x16x32_bf16 a[12:15], v[152:155], a[132:135], a[12:15]\n\t"
    "v_mfma_f32_16x16x32_bf16 a[16:19], v[152:155], a[136:139], a[16:19]\n\t"
    "v_mfma_f32_16x16x32_bf16 a[16:19], v[156:159], a[160:163], a[16:19]\n\t"
    "v_mfma_f32_16x16x32_bf16 a[20:23], v[156:159], a[164:167], a[20:23]\n\t"
    "buffer_load_dwordx4 v197, s[64:67], s29 offen offset:1024 lds\n\t"
    "v_mfma_f32_16x16x32_bf16 a[20:23], v[152:155], a[140:143], a[20:23]\n\t"
    "v_mfma_f32_16x16x32_bf16 a[24:27], v[152:155], a[144:147], a[24:27]\n\t"
    "v_mfma_f32_16x16x32_bf16 a[24:27], v[156:159], a[168:171], a[24:27]\n\t"
    "v_mfma_f32_16x16x32_bf16 a[28:31], v[156:159], a[172:175], a[28:31]\n\t"
    "v_mfma_f32_16x16x32_bf16 a[28:31], v[152:155], a[148:151], a[28:31]\n\t"
    "s_waitcnt vmcnt(25) lgkmcnt(0)\n\t"
    "s_barrier\n\t"
    "v_mfma_f32_16x16x32_bf16 a[32:35], v[160:163], a[128:131], a[32:35]\n\t"
    "ds_read_b64_tr_b16 v[128:129], v192\n\t"
    "v_mfma_f32_16x16x32_bf16 a[32:35], v[164:167], a[152:155], a[32:35]\n\t"
    "v_mfma_f32_16x16x32_bf16 a[36:39], v[164:167], a[156:159], a[36:39]\n\t"
    "ds_read_b64_tr_b16 v[130:131], v192 offset:1024\n\t"
    "s_cmp_le_u32 s63, 4\n\t"
    "s_addc_u32 s31, 4, 0\n\t"
    "s_mul_i32 s31, s31, 12288\n\t"
    "s_add_u32 s31, s31, 196608\n\t"
    "v_mfma_f32_16x16x32_bf16 a[36:39], v[160:163], a[132:135], a[36:39]\n\t"
    "buffer_load_dwordx4 v[0:3], v229, s[52:55], s31 offen sc1\n\t"
    "v_mfma_f32_16x16x32_bf16 a[40:43], v[160:163], a[136:139], a[40:43]\n\t"
    "ds_read_b64_tr_b16 v[132:133], v192 offset:2048\n\t"
    "v_mfma_f32_16x16x32_bf16 a[40:43], v[164:167], a[160:163], a[40:43]\n\t"
    "v_mfma_f32_16x16x32_bf16 a[44:47], v[164:167], a[164:167], a[44:47]\n\t"
    "ds_read_b64_tr_b16 v[134:135], v192 offset:3072\n\t"
    "v_mfma_f32_16x16x32_bf16 a[44:47], v[160:163], a[140:143], a[44:47]\n\t"
    "buffer_load_dwordx4 v[4:7], v229, s[52:55], s31 offen offset:1024 sc1\n\t"
    "v_mfma_f32_16x16x32_bf16 a[48:51], v[160:163], a[144:147], a[48:51]\n\t"
    "ds_read_b64_tr_b16 v[136:137], v193\n\t"
    "v_mfma_f32_16x16x32_bf16 a[48:51], v[164:167], a[168:171], a[48:51]\n\t"
    "v_mfma_f32_16x16x32_bf16 a[52:55], v[164:167], a[172:175], a[52:55]\n\t"
    "ds_read_b64_tr_b16 v[138:139], v193 offset:1024\n\t"
    "v_mfma_f32_16x16x32_bf16 a[52:55], v[160:163], a[148:151], a[52:55]\n\t"
    "buffer_load_dwordx4 v[8:11], v229, s[52:55], s31 offen offset:2048 sc1\n\t"
    "v_mfma_f32_16x16x32_bf16 a[56:59], v[168:171], a[128:131], a[56:59]\n\t"
    "ds_read_b64_tr_b16 v[140:141], v193 offset:2048\n\t"
    "v_mfma_f32_16x16x32_bf16 a[56:59], v[172:175], a[152:155], a[56:59]\n\t"
    "v_mfma_f32_16x16x32_bf16 a[60:63], v[172:175], a[156:159], a[60:63]\n\t"
    "ds_read_b64_tr_b16 v[142:143], v193 offset:3072\n\t"
    "v_mfma_f32_16x16x32_bf16 a[60:63], v[168:171], a[132:135], a[60:63]\n\t"
    "buffer_load_dwordx4 v[12:15], v229, s[52:55], s31 offen offset:3072 sc1\n\t"
    "v_mfma_f32_16x16x32_bf16 a[64:67], v[168:171], a[136:139], a[64:67]\n\t"
    "ds_read_b64_tr_b16 v[144:145], v194\n\t"
    "v_mfma_f32_16x16x32_bf16 a[64:67], v[172:175], a[160:163], a[64:67]\n\t"
    "v_mfma_f32_16x16x32_bf16 a[68:71], v[172:175], a[164:167], a[68:71]\n\t"
    "ds_read_b64_tr_b16 v[146:147], v194 offset:1024\n\t"
    "v_mfma_f32_16x16x32_bf16 a[68:71], v[168:171], a[140:143], a[68:71]\n\t"
    "buffer_load_dwordx4 v[16:19], v241, s[52:55], s31 offen sc1\n\t"
    "v_mfma_f32_16x16x32_bf16 a[72:75], v[168:171], a[144:147], a[72:75]\n\t"
    "ds_read_b64_tr_b16 v[148:149], v194 offset:2048\n\t"
    "v_mfma_f32_16x16x32_bf16 a[72:75], v[172:175], a[168:171], a[72:75]\n\t"
    "v_mfma_f32_16x16x32_bf16 a[76:79], v[172:175], a[172:175], a[76:79]\n\t"
    "ds_read_b64_tr_b16 v[150:151], v194 offset:3072\n\t"
    "v_mfma_f32_16x16x32_bf16 a[76:79], v[168:171], a[148:151], a[76:79]\n\t"
    "buffer_load_dwordx4 v[20:23], v241, s[52:55], s31 offen offset:1024 sc1\n\t"
    "v_mfma_f32_16x16x32_bf16 a[80:83], v[176:179], a[128:131], a[80:83]\n\t"
    "ds_read_b64_tr_b16 v[152:153], v195\n\t"
    "v_mfma_f32_16x16x32_bf16 a[80:83], v[180:183], a[152:155], a[80:83]\n\t"
    "v_mfma_f32_16x16x32_bf16 a[84:87], v[180:183], a[156:159], a[84:87]\n\t"
    "ds_read_b64_tr_b16 v[154:155], v195 offset:1024\n\t"
    "s_cmp_le_u32 s63, 6\n\t"
    "s_addc_u32 s28, 6, 0\n\t"
    "s_lshl_b32 s28, s28, 19\n\t"
    "s_add_u32 s28, s28, 131072\n\t"
    "s_add_u32 m0, s59, 0\n\t"
    "v_mfma_f32_16x16x32_bf16 a[84:87], v[176:179], a[132:135], a[84:87]\n\t"
    "buffer_load_dwordx4 v198, s[64:67], s28 offen lds\n\t"
    "v_mfma_f32_16x16x32_bf16 a[88:91], v[176:179], a[136:139], a[88:91]\n\t"
    "ds_read_b64_tr_b16 v[156:157], v195 offset:2048\n\t"
    "v_mfma_f32_16x16x32_bf16 a[88:91], v[180:183], a[160:163], a[88:91]\n\t"
    "v_mfma_f32_16x16x32_bf16 a[92:95], v[180:183], a[164:167], a[92:95]\n\t"
    "ds_read_b64_tr_b16 v[158:159], v195 offset:3072\n\t"
    "v_mfma_f32_16x16x32_bf16 a[92:95], v[176:179], a[140:143], a[92:95]\n\t"
    "buffer_load_dwordx4 v199, s[64:67], s28 offen offset:1024 lds\n\t"
    "v_mfma_f32_16x16x32_bf16 a[96:99], v[176:179], a[144:147], a[96:99]\n\t"
    "v_mfma_f32_16x16x32_bf16 a[96:99], v[180:183], a[168:171], a[96:99]\n\t"
    "v_mfma_f32_16x16x32_bf16 a[100:103], v[180:183], a[172:175], a[100:103]\n\t"
    "s_cmp_le_u32 s63, 0\n\t"
    "s_addc_u32 s29, 0, 0\n\t"
    "s_lshl_b32 s29, s29, 19\n\t"
    "s_nop 0\n\t"
    "s_add_u32 s29, s29, 196608\n\t"
    "s_add_u32 m0, s59, 2048\n\t"
    "v_mfma_f32_16x16x32_bf16 a[100:103], v[176:179], a[148:151], a[100:103]\n\t"
    "buffer_load_dwordx4 v198, s[64:67], s29 offen lds\n\t"
    "v_mfma_f32_16x16x32_bf16 a[104:107], v[184:187], a[128:131], a[104:107]\n\t"
    "v_mfma_f32_16x16x32_bf16 a[104:107], v[188:191], a[152:155], a[104:107]\n\t"
    "v_mfma_f32_16x16x32_bf16 a[108:111], v[188:191], a[156:159], a[108:111]\n\t"
    "v_mfma_f32_16x16x32_bf16 a[108:111], v[184:187], a[132:135], a[108:111]\n\t"
    "buffer_load_dwordx4 v199, s[64:67], s29 offen offset:1024 lds\n\t"
    "v_mfma_f32_16x16x32_bf16 a[112:115], v[184:187], a[136:139], a[112:115]\n\t"
    "v_mfma_f32_16x16x32_bf16 a[112:115], v[188:191], a[160:163], a[112:115]\n\t"
    "v_mfma_f32_16x16x32_bf16 a[116:119], v[188:191], a[164:167], a[116:119]\n\t"
    "v_mfma_f32_16x16x32_bf16 a[116:119], v[184:187], a[140:143], a[116:119]\n\t"
    "v_mfma_f32_16x16x32_bf16 a[120:123], v[184:187], a[144:147], a[120:123]\n\t"
    "v_mfma_f32_16x16x32_bf16 a[120:123], v[188:191], a[168:171], a[120:123]\n\t"
    "v_mfma_f32_16x16x32_bf16 a[124:127], v[188:191], a[172:175], a[124:127]\n\t"
    "v_mfma_f32_16x16x32_bf16 a[124:127], v[184:187], a[148:151], a[124:127]\n\t"
    "s_waitcnt vmcnt(14) lgkmcnt(0)\n\t"
    "s_barrier\n\t"
    "v_mfma_f32_16x16x32_bf16 a[192:195], v[128:131], v[48:51], a[192:195]\n\t"
    "ds_read_b64_tr_b16 v[160:161], v192 offset:4096\n\t"
    "s_cmp_le_u32 s63, 5\n\t"
    "s_addc_u32 s31, 5, 0\n\t"
    "s_mul_i32 s31, s31, 12288\n\t"
    "s_add_u32 s31, s31, 196608\n\t"
    "v_mfma_f32_16x16x32_bf16 a[192:195], v[132:135], v[72:75], a[192:195]\n\t"
    "buffer_load_dwordx4 v[24:27], v229, s[52:55], s31 offen sc1\n\t"
    "v_mfma_f32_16x16x32_bf16 a[196:199], v[132:135], v[76:79], a[196:199]\n\t"
    "ds_read_b64_tr_b16 v[162:163], v192 offset:5120\n\t"
    "v_mfma_f32_16x16x32_bf16 a[196:199], v[128:131], v[52:55], a[196:199]\n\t"
    "buffer_load_dword v254, v230, s[68:71], 0 offen sc1\n\t"
    "v_mfma_f32_16x16x32_bf16 a[200:203], v[128:131], v[56:59], a[200:203]\n\t"
    "ds_read_b64_tr_b16 v[164:165], v192 offset:6144\n\t"
    "v_mfma_f32_16x16x32_bf16 a[200:203], v[132:135], v[80:83], a[200:203]\n\t"
    "buffer_load_dwordx4 v[28:31], v229, s[52:55], s31 offen offset:1024 sc1\n\t"
    "v_mfma_f32_16x16x32_bf16 a[204:207], v[132:135], v[84:87], a[204:207]\n\t"
    "ds_read_b64_tr_b16 v[166:167], v192 offset:7168\n\t"
    "v_mfma_f32_16x16x32_bf16 a[204:207], v[128:131], v[60:63], a[204:207]\n\t"
    "v_mfma_f32_16x16x32_bf16 a[208:211], v[128:131], v[64:67], a[208:211]\n\t"
    "ds_read_b64_tr_b16 v[168:169], v193 offset:4096\n\t"
    "v_mfma_f32_16x16x32_bf16 a[208:211], v[132:135], v[88:91], a[208:211]\n\t"
    "buffer_load_dwordx4 v[32:35], v229, s[52:55], s31 offen offset:2048 sc1\n\t"
    "v_mfma_f32_16x16x32_bf16 a[212:215], v[132:135], v[92:95], a[212:215]\n\t"
    "ds_read_b64_tr_b16 v[170:171], v193 offset:5120\n\t"
    "v_mfma_f32_16x16x32_bf16 a[212:215], v[128:131], v[68:71], a[212:215]\n\t"
    "v_mfma_f32_16x16x32_bf16 a[216:219], v[136:139], v[48:51], a[216:219]\n\t"
    "ds_read_b64_tr_b16 v[172:173], v193 offset:6144\n\t"
    "v_mfma_f32_16x16x32_bf16 a[216:219], v[140:143], v[72:75], a[216:219]\n\t"
    "buffer_load_dwordx4 v[36:39], v229, s[52:55], s31 offen offset:3072 sc1\n\t"
    "v_mfma_f32_16x16x32_bf16 a[220:223], v[140:143], v[76:79], a[220:223]\n\t"
    "ds_read_b64_tr_b16 v[174:175], v193 offset:7168\n\t"
    "v_mfma_f32_16x16x32_bf16 a[220:223], v[136:139], v[52:55], a[220:223]\n\t"
    "v_mfma_f32_16x16x32_bf16 a[224:227], v[136:139], v[56:59], a[224:227]\n\t"
    "ds_read_b64_tr_b16 v[176:177], v194 offset:4096\n\t"
    "s_add_u32 m0, s59, 4096\n\t"
    "v_mfma_f32_16x16x32_bf16 a[224:227], v[140:143], v[80:83], a[224:227]\n\t"
    "buffer_load_dwordx4 v196, s[64:67], s28 offen lds\n\t"
    "v_mfma_f32_16x16x32_bf16 a[228:231], v[140:143], v[84:87], a[228:231]\n\t"
    "ds_read_b64_tr_b16 v[178:179], v194 offset:5120\n\t"
    "v_mfma_f32_16x16x32_bf16 a[228:231], v[136:139], v[60:63], a[228:231]\n\t"
    "buffer_load_dwordx4 v197, s[64:67], s28 offen offset:1024 lds\n\t"
    "v_mfma_f32_16x16x32_bf16 a[232:235], v[136:139], v[64:67], a[232:235]\n\t"
    "ds_read_b64_tr_b16 v[180:181], v194 offset:6144\n\t"
    "v_mfma_f32_16x16x32_bf16 a[232:235], v[140:143], v[88:91], a[232:235]\n\t"
    "v_mfma_f32_16x16x32_bf16 a[236:239], v[140:143], v[92:95], a[236:239]\n\t"
    "ds_read_b64_tr_b16 v[182:183], v194 offset:7168\n\t"
    "s_add_u32 m0, s59, 6144\n\t"
    "v_mfma_f32_16x16x32_bf16 a[236:239], v[136:139], v[68:71], a[236:239]\n\t"
    "buffer_load_dwordx4 v196, s[64:67], s29 offen lds\n\t"
    "v_mfma_f32_16x16x32_bf16 a[240:243], v[144:147], v[48:51], a[240:243]\n\t"
    "ds_read_b64_tr_b16 v[184:185], v195 offset:4096\n\t"
    "v_mfma_f32_16x16x32_bf16 a[240:243], v[148:151], v[72:75], a[240:243]\n\t"
    "v_mfma_f32_16x16x32_bf16 a[244:247], v[148:151], v[76:79], a[244:247]\n\t"
    "ds_read_b64_tr_b16 v[186:187], v195 offset:5120\n\t"
    "v_mfma_f32_16x16x32_bf16 a[244:247], v[144:147], v[52:55], a[244:247]\n\t"
    "buffer_load_dwordx4 v197, s[64:67], s29 offen offset:1024 lds\n\t"
    "v_mfma_f32_16x16x32_bf16 a[248:251], v[144:147], v[56:59], a[248:251]\n\t"
    "ds_read_b64_tr_b16 v[188:189], v195 offset:6144\n\t"
    "v_mfma_f32_16x16x32_bf16 a[248:251], v[148:151], v[80:83], a[248:251]\n\t"
    "v_mfma_f32_16x16x32_bf16 a[252:255], v[148:151], v[84:87], a[252:255]\n\t"
    "ds_read_b64_tr_b16 v[190:191], v195 offset:7168\n\t"
    "s_cmp_le_u32 s63, 1\n\t"
    "s_addc_u32 s28, 1, 0\n\t"
    "s_lshl_b32 s28, s28, 19\n\t"
    "s_nop 0\n\t"
    "s_add_u32 s28, s28, 196608\n\t"
    "s_add_u32 m0, s59, 32768\n\t"
    "v_mfma_f32_16x16x32_bf16 a[252:255], v[144:147], v[60:63], a[252:255]\n\t"
    "buffer_load_dwordx4 v198, s[64:67], s28 offen lds\n\t"
    "v_mfma_f32_16x16x32_bf16 a[0:3], v[144:147], v[64:67], a[0:3]\n\t"
    "v_mfma_f32_16x16x32_bf16 a[0:3], v[148:151], v[88:91], a[0:3]\n\t"
    "v_mfma_f32_16x16x32_bf16 a[4:7], v[148:151], v[92:95], a[4:7]\n\t"
    "buffer_load_dwordx4 v199, s[64:67], s28 offen offset:1024 lds\n\t"
    "v_mfma_f32_16x16x32_bf16 a[4:7], v[144:147], v[68:71], a[4:7]\n\t"
    "v_mfma_f32_16x16x32_bf16 a[8:11], v[152:155], v[48:51], a[8:11]\n\t"
    "v_mfma_f32_16x16x32_bf16 a[8:11], v[156:159], v[72:75], a[8:11]\n\t"
    "s_cmp_le_u32 s63, 2\n\t"
    "s_addc_u32 s29, 2, 0\n\t"
    "s_lshl_b32 s29, s29, 19\n\t"
    "s_nop 0\n\t"
    "s_add_u32 s29, s29, 196608\n\t"
    "s_add_u32 m0, s59, 34816\n\t"
    "v_mfma_f32_16x16x32_bf16 a[12:15], v[156:159], v[76:79], a[12:15]\n\t"
    "buffer_load_dwordx4 v198, s[64:67], s29 offen lds\n\t"
    "v_mfma_f32_16x16x32_bf16 a[12:15], v[152:155], v[52:55], a[12:15]\n\t"
    "v_mfma_f32_16x16x32_bf16 a[16:19], v[152:155], v[56:59], a[16:19]\n\t"
    "v_mfma_f32_16x16x32_bf16 a[16:19], v[156:159], v[80:83], a[16:19]\n\t"
    "v_mfma_f32_16x16x32_bf16 a[20:23], v[156:159], v[84:87], a[20:23]\n\t"
    "buffer_load_dwordx4 v199, s[64:67], s29 offen offset:1024 lds\n\t"
    "v_mfma_f32_16x16x32_bf16 a[20:23], v[152:155], v[60:63], a[20:23]\n\t"
    "v_mfma_f32_16x16x32_bf16 a[24:27], v[152:155], v[64:67], a[24:27]\n\t"
    "v_mfma_f32_16x16x32_bf16 a[24:27], v[156:159], v[88:91], a[24:27]\n\t"
    "v_mfma_f32_16x16x32_bf16 a[28:31], v[156:159], v[92:95], a[28:31]\n\t"
    "v_mfma_f32_16x16x32_bf16 a[28:31], v[152:155], v[68:71], a[28:31]\n\t"
    "s_waitcnt lgkmcnt(0)\n\t"
    "s_barrier\n\t"
    "v_mfma_f32_16x16x32_bf16 a[32:35], v[160:163], v[48:51], a[32:35]\n\t"
    "ds_read_b64_tr_b16 v[128:129], v192 offset:32768\n\t"
    "v_mfma_f32_16x16x32_bf16 a[32:35], v[164:167], v[72:75], a[32:35]\n\t"
    "buffer_load_dwordx4 v[40:43], v241, s[52:55], s31 offen sc1\n\t"
    "v_mfma_f32_16x16x32_bf16 a[36:39], v[164:167], v[76:79], a[36:39]\n\t"
    "ds_read_b64_tr_b16 v[130:131], v192 offset:33792\n\t"
    "v_mfma_f32_16x16x32_bf16 a[36:39], v[160:163], v[52:55], a[36:39]\n\t"
    "v_mfma_f32_16x16x32_bf16 a[40:43], v[160:163], v[56:59], a[40:43]\n\t"
    "ds_read_b64_tr_b16 v[132:133], v192 offset:34816\n\t"
    "v_mfma_f32_16x16x32_bf16 a[40:43], v[164:167], v[80:83], a[40:43]\n\t"
    "buffer_load_dwordx4 v[44:47], v241, s[52:55], s31 offen offset:1024 sc1\n\t"
    "v_mfma_f32_16x16x32_bf16 a[44:47], v[164:167], v[84:87], a[44:47]\n\t"
    "ds_read_b64_tr_b16 v[134:135], v192 offset:35840\n\t"
    "v_mfma_f32_16x16x32_bf16 a[44:47], v[160:163], v[60:63], a[44:47]\n\t"
    "v_mfma_f32_16x16x32_bf16 a[48:51], v[160:163], v[64:67], a[48:51]\n\t"
    "ds_read_b64_tr_b16 v[136:137], v193 offset:32768\n\t"
    "s_cmp_le_u32 s63, 6\n\t"
    "s_addc_u32 s31, 6, 0\n\t"
    "s_mul_i32 s31, s31, 12288\n\t"
    "s_add_u32 s31, s31, 196608\n\t"
    "v_mfma_f32_16x16x32_bf16 a[48:51], v[164:167], v[88:91], a[48:51]\n\t"
    "buffer_load_dwordx4 a[128:131], v229, s[52:55], s31 offen sc1\n\t"
    "v_mfma_f32_16x16x32_bf16 a[52:55], v[164:167], v[92:95], a[52:55]\n\t"
    "ds_read_b64_tr_b16 v[138:139], v193 offset:33792\n\t"
    "v_mfma_f32_16x16x32_bf16 a[52:55], v[160:163], v[68:71], a[52:55]\n\t"
    "v_mfma_f32_16x16x32_bf16 a[56:59], v[168:171], v[48:51], a[56:59]\n\t"
    "ds_read_b64_tr_b16 v[140:141], v193 offset:34816\n\t"
    "v_mfma_f32_16x16x32_bf16 a[56:59], v[172:175], v[72:75], a[56:59]\n\t"
    "buffer_load_dwordx4 a[132:135], v229, s[52:55], s31 offen offset:1024 sc1\n\t"
    "v_mfma_f32_16x16x32_bf16 a[60:63], v[172:175], v[76:79], a[60:63]\n\t"
    "ds_read_b64_tr_b16 v[142:143], v193 offset:35840\n\t"
    "v_mfma_f32_16x16x32_bf16 a[60:63], v[168:171], v[52:55], a[60:63]\n\t"
    "v_mfma_f32_16x16x32_bf16 a[64:67], v[168:171], v[56:59], a[64:67]\n\t"
    "ds_read_b64_tr_b16 v[144:145], v194 offset:32768\n\t"
    "v_mfma_f32_16x16x32_bf16 a[64:67], v[172:175], v[80:83], a[64:67]\n\t"
    "buffer_load_dwordx4 a[136:139], v229, s[52:55], s31 offen offset:2048 sc1\n\t"
    "v_mfma_f32_16x16x32_bf16 a[68:71], v[172:175], v[84:87], a[68:71]\n\t"
    "ds_read_b64_tr_b16 v[146:147], v194 offset:33792\n\t"
    "v_mfma_f32_16x16x32_bf16 a[68:71], v[168:171], v[60:63], a[68:71]\n\t"
    "buffer_load_dwordx4 a[140:143], v229, s[52:55], s31 offen offset:3072 sc1\n\t"
    "v_mfma_f32_16x16x32_bf16 a[72:75], v[168:171], v[64:67], a[72:75]\n\t"
    "ds_read_b64_tr_b16 v[148:149], v194 offset:34816\n\t"
    "v_mfma_f32_16x16x32_bf16 a[72:75], v[172:175], v[88:91], a[72:75]\n\t"
    "v_mfma_f32_16x16x32_bf16 a[76:79], v[172:175], v[92:95], a[76:79]\n\t"
    "ds_read_b64_tr_b16 v[150:151], v194 offset:35840\n\t"
    "v_mfma_f32_16x16x32_bf16 a[76:79], v[168:171], v[68:71], a[76:79]\n\t"
    "buffer_load_dwordx4 a[144:147], v241, s[52:55], s31 offen sc1\n\t"
    "v_mfma_f32_16x16x32_bf16 a[80:83], v[176:179], v[48:51], a[80:83]\n\t"
    "ds_read_b64_tr_b16 v[152:153], v195 offset:32768\n\t"
    "v_mfma_f32_16x16x32_bf16 a[80:83], v[180:183], v[72:75], a[80:83]\n\t"
    "v_mfma_f32_16x16x32_bf16 a[84:87], v[180:183], v[76:79], a[84:87]\n\t"
    "ds_read_b64_tr_b16 v[154:155], v195 offset:33792\n\t"
    "v_mfma_f32_16x16x32_bf16 a[84:87], v[176:179], v[52:55], a[84:87]\n\t"
    "buffer_load_dwordx4 a[148:151], v241, s[52:55], s31 offen offset:1024 sc1\n\t"
    "v_mfma_f32_16x16x32_bf16 a[88:91], v[176:179], v[56:59], a[88:91]\n\t"
    "ds_read_b64_tr_b16 v[156:157], v195 offset:34816\n\t"
    "v_mfma_f32_16x16x32_bf16 a[88:91], v[180:183], v[80:83], a[88:91]\n\t"
    "v_mfma_f32_16x16x32_bf16 a[92:95], v[180:183], v[84:87], a[92:95]\n\t"
    "ds_read_b64_tr_b16 v[158:159], v195 offset:35840\n\t"
    "s_add_u32 m0, s59, 36864\n\t"
    "v_mfma_f32_16x16x32_bf16 a[92:95], v[176:179], v[60:63], a[92:95]\n\t"
    "buffer_load_dwordx4 v196, s[64:67], s28 offen lds\n\t"
    "v_mfma_f32_16x16x32_bf16 a[96:99], v[176:179], v[64:67], a[96:99]\n\t"
    "v_mfma_f32_16x16x32_bf16 a[96:99], v[180:183], v[88:91], a[96:99]\n\t"
    "v_mfma_f32_16x16x32_bf16 a[100:103], v[180:183], v[92:95], a[100:103]\n\t"
    "buffer_load_dwordx4 v197, s[64:67], s28 offen offset:1024 lds\n\t"
    "v_mfma_f32_16x16x32_bf16 a[100:103], v[176:179], v[68:71], a[100:103]\n\t"
    "v_mfma_f32_16x16x32_bf16 a[104:107], v[184:187], v[48:51], a[104:107]\n\t"
    "v_mfma_f32_16x16x32_bf16 a[104:107], v[188:191], v[72:75], a[104:107]\n\t"
    "s_add_u32 m0, s59, 38912\n\t"
    "v_mfma_f32_16x16x32_bf16 a[108:111], v[188:191], v[76:79], a[108:111]\n\t"
    "buffer_load_dwordx4 v196, s[64:67], s29 offen lds\n\t"
    "v_mfma_f32_16x16x32_bf16 a[108:111], v[184:187], v[52:55], a[108:111]\n\t"
    "v_mfma_f32_16x16x32_bf16 a[112:115], v[184:187], v[56:59], a[112:115]\n\t"
    "v_mfma_f32_16x16x32_bf16 a[112:115], v[188:191], v[80:83], a[112:115]\n\t"
    "v_mfma_f32_16x16x32_bf16 a[116:119], v[188:191], v[84:87], a[116:119]\n\t"
    "buffer_load_dwordx4 v197, s[64:67], s29 offen offset:1024 lds\n\t"
    "v_mfma_f32_16x16x32_bf16 a[116:119], v[184:187], v[60:63], a[116:119]\n\t"
    "v_mfma_f32_16x16x32_bf16 a[120:123], v[184:187], v[64:67], a[120:123]\n\t"
    "v_mfma_f32_16x16x32_bf16 a[120:123], v[188:191], v[88:91], a[120:123]\n\t"
    "v_mfma_f32_16x16x32_bf16 a[124:127], v[188:191], v[92:95], a[124:127]\n\t"
    "s_mov_b32 s25, 254\n\t"
    "s_mov_b32 s30, 4\n\t"
    "s_mov_b32 s23, 4\n\t"
    "s_waitcnt vmcnt(23)\n\t"
    "v_sub_u32 v254, v254, v231\n\t"
    "v_cmp_gt_u32 vcc, s30, v254\n\t"
    "s_nop 1\n\t"
    "s_and_b32 s26, vcc_lo, s25\n\t"
    "s_cbranch_scc1 .Lk_spin\n\t"
    ".Lk_spinret_4:\n\t"
    "v_mfma_f32_16x16x32_bf16 a[124:127], v[184:187], v[68:71], a[124:127]\n\t"
    "s_waitcnt vmcnt(10) lgkmcnt(0)\n\t"
    "s_barrier\n\t"
    "v_mfma_f32_16x16x32_bf16 a[192:195], v[128:131], v[0:3], a[192:195]\n\t"
    "ds_read_b64_tr_b16 v[160:161], v192 offset:36864\n\t"
    "v_mfma_f32_16x16x32_bf16 a[192:195], v[132:135], v[24:27], a[192:195]\n\t"
    "v_mfma_f32_16x16x32_bf16 a[196:199], v[132:135], v[28:31], a[196:199]\n\t"
    "ds_read_b64_tr_b16 v[162:163], v192 offset:37888\n\t"
    "s_cmp_le_u32 s63, 0\n\t"
    "s_addc_u32 s31, 0, 0\n\t"
    "s_mul_i32 s31, s31, 12288\n\t"
    "s_add_u32 s31, s31, 294912\n\t"
    "v_mfma_f32_16x16x32_bf16 a[196:199], v[128:131], v[4:7], a[196:199]\n\t"
    "buffer_load_dwordx4 a[152:155], v229, s[52:55], s31 offen sc1\n\t"
    "v_mfma_f32_16x16x32_bf16 a[200:203], v[128:131], v[8:11], a[200:203]\n\t"
    "ds_read_b64_tr_b16 v[164:165], v192 offset:38912\n\t"
    "v_mfma_f32_16x16x32_bf16 a[200:203], v[132:135], v[32:35], a[200:203]\n\t"
    "v_mfma_f32_16x16x32_bf16 a[204:207], v[132:135], v[36:39], a[204:207]\n\t"
    "ds_read_b64_tr_b16 v[166:167], v192 offset:39936\n\t"
    "v_mfma_f32_16x16x32_bf16 a[204:207], v[128:131], v[12:15], a[204:207]\n\t"
    "buffer_load_dwordx4 a[156:159], v229, s[52:55], s31 offen offset:1024 sc1\n\t"
    "v_mfma_f32_16x16x32_bf16 a[208:211], v[128:131], v[16:19], a[208:211]\n\t"
    "ds_read_b64_tr_b16 v[168:169], v193 offset:36864\n\t"
    "v_mfma_f32_16x16x32_bf16 a[208:211], v[132:135], v[40:43], a[208:211]\n\t"
    "v_mfma_f32_16x16x32_bf16 a[212:215], v[132:135], v[44:47], a[212:215]\n\t"
    "ds_read_b64_tr_b16 v[170:171], v193 offset:37888\n\t"
    "v_mfma_f32_16x16x32_bf16 a[212:215], v[128:131], v[20:23], a[212:215]\n\t"
    "buffer_load_dwordx4 a[160:163], v229, s[52:55], s31 offen offset:2048 sc1\n\t"
    "v_mfma_f32_16x16x32_bf16 a[216:219], v[136:139], v[0:3], a[216:219]\n\t"
    "ds_read_b64_tr_b16 v[172:173], v193 offset:38912\n\t"
    "v_mfma_f32_16x16x32_bf16 a[216:219], v[140:143], v[24:27], a[216:219]\n\t"
    "v_mfma_f32_16x16x32_bf16 a[220:223], v[140:143], v[28:31], a[220:223]\n\t"
    "ds_read_b64_tr_b16 v[174:175], v193 offset:39936\n\t"
    "v_mfma_f32_16x16x32_bf16 a[220:223], v[136:139], v[4:7], a[220:223]\n\t"
    "buffer_load_dwordx4 a[164:167], v229, s[52:55], s31 offen offset:3072 sc1\n\t"
    "v_mfma_f32_16x16x32_bf16 a[224:227], v[136:139], v[8:11], a[224:227]\n\t"
    "ds_read_b64_tr_b16 v[176:177], v194 offset:36864\n\t"
    "v_mfma_f32_16x16x32_bf16 a[224:227], v[140:143], v[32:35], a[224:227]\n\t"
    "v_mfma_f32_16x16x32_bf16 a[228:231], v[140:143], v[36:39], a[228:231]\n\t"
    "ds_read_b64_tr_b16 v[178:179], v194 offset:37888\n\t"
    "v_mfma_f32_16x16x32_bf16 a[228:231], v[136:139], v[12:15], a[228:231]\n\t"
    "buffer_load_dwordx4 a[168:171], v241, s[52:55], s31 offen sc1\n\t"
    "v_mfma_f32_16x16x32_bf16 a[232:235], v[136:139], v[16:19], a[232:235]\n\t"
    "ds_read_b64_tr_b16 v[180:181], v194 offset:38912\n\t"
    "v_mfma_f32_16x16x32_bf16 a[232:235], v[140:143], v[40:43], a[232:235]\n\t"
    "v_mfma_f32_16x16x32_bf16 a[236:239], v[140:143], v[44:47], a[236:239]\n\t"
    "ds_read_b64_tr_b16 v[182:183], v194 offset:39936\n\t"
    "v_mfma_f32_16x16x32_bf16 a[236:239], v[136:139], v[20:23], a[236:239]\n\t"
    "buffer_load_dwordx4 a[172:175], v241, s[52:55], s31 offen offset:1024 sc1\n\t"
    "v_mfma_f32_16x16x32_bf16 a[240:243], v[144:147], v[0:3], a[240:243]\n\t"
    "ds_read_b64_tr_b16 v[184:185], v195 offset:36864\n\t"
    "v_mfma_f32_16x16x32_bf16 a[240:243], v[148:151], v[24:27], a[240:243]\n\t"
    "v_mfma_f32_16x16x32_bf16 a[244:247], v[148:151], v[28:31], a[244:247]\n\t"
    "ds_read_b64_tr_b16 v[186:187], v195 offset:37888\n\t"
    "s_cmp_le_u32 s63, 3\n\t"
    "s_addc_u32 s28, 3, 0\n\t"
    "s_lshl_b32 s28, s28, 19\n\t"
    "s_add_u32 s28, s28, 196608\n\t"
    "s_add_u32 m0, s58, 0\n\t"
    "v_mfma_f32_16x16x32_bf16 a[244:247], v[144:147], v[4:7], a[244:247]\n\t"
    "buffer_load_dwordx4 v198, s[64:67], s28 offen lds\n\t"
    "v_mfma_f32_16x16x32_bf16 a[248:251], v[144:147], v[8:11], a[248:251]\n\t"
    "ds_read_b64_tr_b16 v[188:189], v195 offset:38912\n\t"
    "v_mfma_f32_16x16x32_bf16 a[248:251], v[148:151], v[32:35], a[248:251]\n\t"
    "v_mfma_f32_16x16x32_bf16 a[252:255], v[148:151], v[36:39], a[252:255]\n\t"
    "ds_read_b64_tr_b16 v[190:191], v195 offset:39936\n\t"
    "v_mfma_f32_16x16x32_bf16 a[252:255], v[144:147], v[12:15], a[252:255]\n\t"
    "buffer_load_dwordx4 v199, s[64:67], s28 offen offset:1024 lds\n\t"
    "v_mfma_f32_16x16x32_bf16 a[0:3], v[144:147], v[16:19], a[0:3]\n\t"
    "v_mfma_f32_16x16x32_bf16 a[0:3], v[148:151], v[40:43], a[0:3]\n\t"
    "v_subrev_u32 v192, 98304, v192\n\t"
    "v_mfma_f32_16x16x32_bf16 a[4:7], v[148:151], v[44:47], a[4:7]\n\t"
    "v_subrev_u32 v193, 98304, v193\n\t"
    "s_cmp_le_u32 s63, 4\n\t"
    "s_addc_u32 s29, 4, 0\n\t"
    "s_lshl_b32 s29, s29, 19\n\t"
    "s_nop 0\n\t"
    "s_add_u32 s29, s29, 196608\n\t"
    "s_add_u32 m0, s58, 2048\n\t"
    "v_mfma_f32_16x16x32_bf16 a[4:7], v[144:147], v[20:23], a[4:7]\n\t"
    "buffer_load_dwordx4 v198, s[64:67], s29 offen lds\n\t"
    "v_subrev_u32 v194, 98304, v194\n\t"
    "v_mfma_f32_16x16x32_bf16 a[8:11], v[152:155], v[0:3], a[8:11]\n\t"
    "v_subrev_u32 v195, 98304, v195\n\t"
    "v_mfma_f32_16x16x32_bf16 a[8:11], v[156:159], v[24:27], a[8:11]\n\t"
    "v_mfma_f32_16x16x32_bf16 a[12:15], v[156:159], v[28:31], a[12:15]\n\t"
    "v_mfma_f32_16x16x32_bf16 a[12:15], v[152:155], v[4:7], a[12:15]\n\t"
    "buffer_load_dwordx4 v199, s[64:67], s29 offen offset:1024 lds\n\t"
    "v_mfma_f32_16x16x32_bf16 a[16:19], v[152:155], v[8:11], a[16:19]\n\t"
    "v_mfma_f32_16x16x32_bf16 a[16:19], v[156:159], v[32:35], a[16:19]\n\t"
    "v_mfma_f32_16x16x32_bf16 a[20:23], v[156:159], v[36:39], a[20:23]\n\t"
    "v_mfma_f32_16x16x32_bf16 a[20:23], v[152:155], v[12:15], a[20:23]\n\t"
    "v_mfma_f32_16x16x32_bf16 a[24:27], v[152:155], v[16:19], a[24:27]\n\t"
    "v_mfma_f32_16x16x32_bf16 a[24:27], v[156:159], v[40:43], a[24:27]\n\t"
    "v_mfma_f32_16x16x32_bf16 a[28:31], v[156:159], v[44:47], a[28:31]\n\t"
    "v_mfma_f32_16x16x32_bf16 a[28:31], v[152:155], v[20:23], a[28:31]\n\t"
    "s_waitcnt lgkmcnt(0)\n\t"
    "s_barrier\n\t"
    "v_mfma_f32_16x16x32_bf16 a[32:35], v[160:163], v[0:3], a[32:35]\n\t"
    "ds_read_b64_tr_b16 v[128:129], v192\n\t"
    "v_mfma_f32_16x16x32_bf16 a[32:35], v[164:167], v[24:27], a[32:35]\n\t"
    "v_mfma_f32_16x16x32_bf16 a[36:39], v[164:167], v[28:31], a[36:39]\n\t"
    "ds_read_b64_tr_b16 v[130:131], v192 offset:1024\n\t"
    "s_cmp_le_u32 s63, 1\n\t"
    "s_addc_u32 s31, 1, 0\n\t"
    "s_mul_i32 s31, s31, 12288\n\t"
    "s_add_u32 s31, s31, 294912\n\t"
    "v_mfma_f32_16x16x32_bf16 a[36:39], v[160:163], v[4:7], a[36:39]\n\t"
    "buffer_load_dwordx4 v[48:51], v229, s[52:55], s31 offen sc1\n\t"
    "v_mfma_f32_16x16x32_bf16 a[40:43], v[160:163], v[8:11], a[40:43]\n\t"
    "ds_read_b64_tr_b16 v[132:133], v192 offset:2048\n\t"
    "v_mfma_f32_16x16x32_bf16 a[40:43], v[164:167], v[32:35], a[40:43]\n\t"
    "v_mfma_f32_16x16x32_bf16 a[44:47], v[164:167], v[36:39], a[44:47]\n\t"
    "ds_read_b64_tr_b16 v[134:135], v192 offset:3072\n\t"
    "v_mfma_f32_16x16x32_bf16 a[44:47], v[160:163], v[12:15], a[44:47]\n\t"
    "buffer_load_dwordx4 v[52:55], v229, s[52:55], s31 offen offset:1024 sc1\n\t"
    "v_mfma_f32_16x16x32_bf16 a[48:51], v[160:163], v[16:19], a[48:51]\n\t"
    "ds_read_b64_tr_b16 v[136:137], v193\n\t"
    "v_mfma_f32_16x16x32_bf16 a[48:51], v[164:167], v[40:43], a[48:51]\n\t"
    "v_mfma_f32_16x16x32_bf16 a[52:55], v[164:167], v[44:47], a[52:55]\n\t"
    "ds_read_b64_tr_b16 v[138:139], v193 offset:1024\n\t"
    "v_mfma_f32_16x16x32_bf16 a[52:55], v[160:163], v[20:23], a[52:55]\n\t"
    "buffer_load_dwordx4 v[56:59], v229, s[52:55], s31 offen offset:2048 sc1\n\t"
    "v_mfma_f32_16x16x32_bf16 a[56:59], v[168:171], v[0:3], a[56:59]\n\t"
    "ds_read_b64_tr_b16 v[140:141], v193 offset:2048\n\t"
    "v_mfma_f32_16x16x32_bf16 a[56:59], v[172:175], v[24:27], a[56:59]\n\t"
    "v_mfma_f32_16x16x32_bf16 a[60:63], v[172:175], v[28:31], a[60:63]\n\t"
    "ds_read_b64_tr_b16 v[142:143], v193 offset:3072\n\t"
    "v_mfma_f32_16x16x32_bf16 a[60:63], v[168:171], v[4:7], a[60:63]\n\t"
    "buffer_load_dwordx4 v[60:63], v229, s[52:55], s31 offen offset:3072 sc1\n\t"
    "v_mfma_f32_16x16x32_bf16 a[64:67], v[168:171], v[8:11], a[64:67]\n\t"
    "ds_read_b64_tr_b16 v[144:145], v194\n\t"
    "v_mfma_f32_16x16x32_bf16 a[64:67], v[172:175], v[32:35], a[64:67]\n\t"
    "v_mfma_f32_16x16x32_bf16 a[68:71], v[172:175], v[36:39], a[68:71]\n\t"
    "ds_read_b64_tr_b16 v[146:147], v194 offset:1024\n\t"
    "v_mfma_f32_16x16x32_bf16 a[68:71], v[168:171], v[12:15], a[68:71]\n\t"
    "buffer_load_dwordx4 v[64:67], v241, s[52:55], s31 offen sc1\n\t"
    "v_mfma_f32_16x16x32_bf16 a[72:75], v[168:171], v[16:19], a[72:75]\n\t"
    "ds_read_b64_tr_b16 v[148:149], v194 offset:2048\n\t"
    "v_mfma_f32_16x16x32_bf16 a[72:75], v[172:175], v[40:43], a[72:75]\n\t"
    "v_mfma_f32_16x16x32_bf16 a[76:79], v[172:175], v[44:47], a[76:79]\n\t"
    "ds_read_b64_tr_b16 v[150:151], v194 offset:3072\n\t"
    "v_mfma_f32_16x16x32_bf16 a[76:79], v[168:171], v[20:23], a[76:79]\n\t"
    "buffer_load_dwordx4 v[68:71], v241, s[52:55], s31 offen offset:1024 sc1\n\t"
    "v_mfma_f32_16x16x32_bf16 a[80:83], v[176:179], v[0:3], a[80:83]\n\t"
    "ds_read_b64_tr_b16 v[152:153], v195\n\t"
    "v_mfma_f32_16x16x32_bf16 a[80:83], v[180:183], v[24:27], a[80:83]\n\t"
    "v_mfma_f32_16x16x32_bf16 a[84:87], v[180:183], v[28:31], a[84:87]\n\t"
    "ds_read_b64_tr_b16 v[154:155], v195 offset:1024\n\t"
    "s_cmp_le_u32 s63, 5\n\t"
    "s_addc_u32 s28, 5, 0\n\t"
    "s_lshl_b32 s28, s28, 19\n\t"
    "s_nop 0\n\t"
    "s_add_u32 s28, s28, 196608\n\t"
    "s_add_u32 m0, s58, 4096\n\t"
    "v_mfma_f32_16x16x32_bf16 a[84:87], v[176:179], v[4:7], a[84:87]\n\t"
    "buffer_load_dwordx4 v198, s[64:67], s28 offen lds\n\t"
    "v_mfma_f32_16x16x32_bf16 a[88:91], v[176:179], v[8:11], a[88:91]\n\t"
    "ds_read_b64_tr_b16 v[156:157], v195 offset:2048\n\t"
    "v_mfma_f32_16x16x32_bf16 a[88:91], v[180:183], v[32:35], a[88:91]\n\t"
    "v_mfma_f32_16x16x32_bf16 a[92:95], v[180:183], v[36:39], a[92:95]\n\t"
    "ds_read_b64_tr_b16 v[158:159], v195 offset:3072\n\t"
    "v_mfma_f32_16x16x32_bf16 a[92:95], v[176:179], v[12:15], a[92:95]\n\t"
    "buffer_load_dwordx4 v199, s[64:67], s28 offen offset:1024 lds\n\t"
    "v_mfma_f32_16x16x32_bf16 a[96:99], v[176:179], v[16:19], a[96:99]\n\t"
    "v_mfma_f32_16x16x32_bf16 a[96:99], v[180:183], v[40:43], a[96:99]\n\t"
    "v_mfma_f32_16x16x32_bf16 a[100:103], v[180:183], v[44:47], a[100:103]\n\t"
    "s_cmp_le_u32 s63, 6\n\t"
    "s_addc_u32 s29, 6, 0\n\t"
    "s_lshl_b32 s29, s29, 19\n\t"
    "s_nop 0\n\t"
    "s_add_u32 s29, s29, 196608\n\t"
    "s_add_u32 m0, s58, 6144\n\t"
    "v_mfma_f32_16x16x32_bf16 a[100:103], v[176:179], v[20:23], a[100:103]\n\t"
    "buffer_load_dwordx4 v198, s[64:67], s29 offen lds\n\t"
    "v_mfma_f32_16x16x32_bf16 a[104:107], v[184:187], v[0:3], a[104:107]\n\t"
    "v_mfma_f32_16x16x32_bf16 a[104:107], v[188:191], v[24:27], a[104:107]\n\t"
    "v_mfma_f32_16x16x32_bf16 a[108:111], v[188:191], v[28:31], a[108:111]\n\t"
    "v_mfma_f32_16x16x32_bf16 a[108:111], v[184:187], v[4:7], a[108:111]\n\t"
    "buffer_load_dwordx4 v199, s[64:67], s29 offen offset:1024 lds\n\t"
    "v_mfma_f32_16x16x32_bf16 a[112:115], v[184:187], v[8:11], a[112:115]\n\t"
    "v_mfma_f32_16x16x32_bf16 a[112:115], v[188:191], v[32:35], a[112:115]\n\t"
    "v_mfma_f32_16x16x32_bf16 a[116:119], v[188:191], v[36:39], a[116:119]\n\t"
    "v_mfma_f32_16x16x32_bf16 a[116:119], v[184:187], v[12:15], a[116:119]\n\t"
    "v_mfma_f32_16x16x32_bf16 a[120:123], v[184:187], v[16:19], a[120:123]\n\t"
    "v_mfma_f32_16x16x32_bf16 a[120:123], v[188:191], v[40:43], a[120:123]\n\t"
    "v_mfma_f32_16x16x32_bf16 a[124:127], v[188:191], v[44:47], a[124:127]\n\t"
    "v_mfma_f32_16x16x32_bf16 a[124:127], v[184:187], v[20:23], a[124:127]\n\t"
    "s_waitcnt vmcnt(14) lgkmcnt(0)\n\t"
    "s_barrier\n\t"
    "v_mfma_f32_16x16x32_bf16 a[192:195], v[128:131], a[128:131], a[192:195]\n\t"
    "ds_read_b64_tr_b16 v[160:161], v192 offset:4096\n\t"
    "s_cmp_le_u32 s63, 2\n\t"
    "s_addc_u32 s31, 2, 0\n\t"
    "s_mul_i32 s31, s31, 12288\n\t"
    "s_add_u32 s31, s31, 294912\n\t"
    "v_mfma_f32_16x16x32_bf16 a[192:195], v[132:135], a[152:155], a[192:195]\n\t"
    "buffer_load_dwordx4 v[72:75], v229, s[52:55], s31 offen sc1\n\t"
    "v_mfma_f32_16x16x32_bf16 a[196:199], v[132:135], a[156:159], a[196:199]\n\t"
    "ds_read_b64_tr_b16 v[162:163], v192 offset:5120\n\t"
    "v_mfma_f32_16x16x32_bf16 a[196:199], v[128:131], a[132:135], a[196:199]\n\t"
    "v_mfma_f32_16x16x32_bf16 a[200:203], v[128:131], a[136:139], a[200:203]\n\t"
    "ds_read_b64_tr_b16 v[164:165], v192 offset:6144\n\t"
    "v_mfma_f32_16x16x32_bf16 a[200:203], v[132:135], a[160:163], a[200:203]\n\t"
    "buffer_load_dwordx4 v[76:79], v229, s[52:55], s31 offen offset:1024 sc1\n\t"
    "v_mfma_f32_16x16x32_bf16 a[204:207], v[132:135], a[164:167], a[204:207]\n\t"
    "ds_read_b64_tr_b16 v[166:167], v192 offset:7168\n\t"
    "v_mfma_f32_16x16x32_bf16 a[204:207], v[128:131], a[140:143], a[204:207]\n\t"
    "v_mfma_f32_16x16x32_bf16 a[208:211], v[128:131], a[144:147], a[208:211]\n\t"
    "ds_read_b64_tr_b16 v[168:169], v193 offset:4096\n\t"
    "v_mfma_f32_16x16x32_bf16 a[208:211], v[132:135], a[168:171], a[208:211]\n\t"
    "buffer_load_dwordx4 v[80:83], v229, s[52:55], s31 offen offset:2048 sc1\n\t"
    "v_mfma_f32_16x16x32_bf16 a[212:215], v[132:135], a[172:175], a[212:215]\n\t"
    "ds_read_b64_tr_b16 v[170:171], v193 offset:5120\n\t"
    "v_mfma_f32_16x16x32_bf16 a[212:215], v[128:131], a[148:151], a[212:215]\n\t"
    "v_mfma_f32_16x16x32_bf16 a[216:219], v[136:139], a[128:131], a[216:219]\n\t"
    "ds_read_b64_tr_b16 v[172:173], v193 offset:6144\n\t"
    "v_mfma_f32_16x16x32_bf16 a[216:219], v[140:143], a[152:155], a[216:219]\n\t"
    "buffer_load_dwordx4 v[84:87], v229, s[52:55], s31 offen offset:3072 sc1\n\t"
    "v_mfma_f32_16x16x32_bf16 a[220:223], v[140:143], a[156:159], a[220:223]\n\t"
    "ds_read_b64_tr_b16 v[174:175], v193 offset:7168\n\t"
    "v_mfma_f32_16x16x32_bf16 a[220:223], v[136:139], a[132:135], a[220:223]\n\t"
    "v_mfma_f32_16x16x32_bf16 a[224:227], v[136:139], a[136:139], a[224:227]\n\t"
    "ds_read_b64_tr_b16 v[176:177], v194 offset:4096\n\t"
    "v_mfma_f32_16x16x32_bf16 a[224:227], v[140:143], a[160:163], a[224:227]\n\t"
    "buffer_load_dwordx4 v[88:91], v241, s[52:55], s31 offen sc1\n\t"
    "v_mfma_f32_16x16x32_bf16 a[228:231], v[140:143], a[164:167], a[228:231]\n\t"
    "ds_read_b64_tr_b16 v[178:179], v194 offset:5120\n\t"
    "v_mfma_f32_16x16x32_bf16 a[228:231], v[136:139], a[140:143], a[228:231]\n\t"
    "buffer_load_dwordx4 v[92:95], v241, s[52:55], s31 offen offset:1024 sc1\n\t"
    "v_mfma_f32_16x16x32_bf16 a[232:235], v[136:139], a[144:147], a[232:235]\n\t"
    "ds_read_b64_tr_b16 v[180:181], v194 offset:6144\n\t"
    "v_mfma_f32_16x16x32_bf16 a[232:235], v[140:143], a[168:171], a[232:235]\n\t"
    "v_mfma_f32_16x16x32_bf16 a[236:239], v[140:143], a[172:175], a[236:239]\n\t"
    "ds_read_b64_tr_b16 v[182:183], v194 offset:7168\n\t"
    "s_cmp_le_u32 s63, 3\n\t"
    "s_addc_u32 s31, 3, 0\n\t"
    "s_mul_i32 s31, s31, 12288\n\t"
    "s_add_u32 s31, s31, 294912\n\t"
    "v_mfma_f32_16x16x32_bf16 a[236:239], v[136:139], a[148:151], a[236:239]\n\t"
    "buffer_load_dwordx4 v[0:3], v229, s[52:55], s31 offen sc1\n\t"
    "v_mfma_f32_16x16x32_bf16 a[240:243], v[144:147], a[128:131], a[240:243]\n\t"
    "ds_read_b64_tr_b16 v[184:185], v195 offset:4096\n\t"
    "v_mfma_f32_16x16x32_bf16 a[240:243], v[148:151], a[152:155], a[240:243]\n\t"
    "v_mfma_f32_16x16x32_bf16 a[244:247], v[148:151], a[156:159], a[244:247]\n\t"
    "ds_read_b64_tr_b16 v[186:187], v195 offset:5120\n\t"
    "v_mfma_f32_16x16x32_bf16 a[244:247], v[144:147], a[132:135], a[244:247]\n\t"
    "buffer_load_dwordx4 v[4:7], v229, s[52:55], s31 offen offset:1024 sc1\n\t"
    "v_mfma_f32_16x16x32_bf16 a[248:251], v[144:147], a[136:139], a[248:251]\n\t"
    "ds_read_b64_tr_b16 v[188:189], v195 offset:6144\n\t"
    "v_mfma_f32_16x16x32_bf16 a[248:251], v[148:151], a[160:163], a[248:251]\n\t"
    "v_mfma_f32_16x16x32_bf16 a[252:255], v[148:151], a[164:167], a[252:255]\n\t"
    "ds_read_b64_tr_b16 v[190:191], v195 offset:7168\n\t"
    "s_cmp_le_u32 s63, 3\n\t"
    "s_addc_u32 s28, 3, 0\n\t"
    "s_lshl_b32 s28, s28, 19\n\t"
    "s_nop 0\n\t"
    "s_add_u32 s28, s28, 196608\n\t"
    "s_add_u32 m0, s58, 32768\n\t"
    "v_mfma_f32_16x16x32_bf16 a[252:255], v[144:147], a[140:143], a[252:255]\n\t"
    "buffer_load_dwordx4 v196, s[64:67], s28 offen lds\n\t"
    "v_mfma_f32_16x16x32_bf16 a[0:3], v[144:147], a[144:147], a[0:3]\n\t"
    "v_mfma_f32_16x16x32_bf16 a[0:3], v[148:151], a[168:171], a[0:3]\n\t"
    "v_mfma_f32_16x16x32_bf16 a[4:7], v[148:151], a[172:175], a[4:7]\n\t"
    "buffer_load_dwordx4 v197, s[64:67], s28 offen offset:1024 lds\n\t"
    "v_mfma_f32_16x16x32_bf16 a[4:7], v[144:147], a[148:151], a[4:7]\n\t"
    "v_mfma_f32_16x16x32_bf16 a[8:11], v[152:155], a[128:131], a[8:11]\n\t"
    "v_mfma_f32_16x16x32_bf16 a[8:11], v[156:159], a[152:155], a[8:11]\n\t"
    "s_cmp_le_u32 s63, 4\n\t"
    "s_addc_u32 s29, 4, 0\n\t"
    "s_lshl_b32 s29, s29, 19\n\t"
    "s_nop 0\n\t"
    "s_add_u32 s29, s29, 196608\n\t"
    "s_add_u32 m0, s58, 34816\n\t"
    "v_mfma_f32_16x16x32_bf16 a[12:15], v[156:159], a[156:159], a[12:15]\n\t"
    "buffer_load_dwordx4 v196, s[64:67], s29 offen lds\n\t"
    "v_mfma_f32_16x16x32_bf16 a[12:15], v[152:155], a[132:135], a[12:15]\n\t"
    "v_mfma_f32_16x16x32_bf16 a[16:19], v[152:155], a[136:139], a[16:19]\n\t"
    "v_mfma_f32_16x16x32_bf16 a[16:19], v[156:159], a[160:163], a[16:19]\n\t"
    "v_mfma_f32_16x16x32_bf16 a[20:23], v[156:159], a[164:167], a[20:23]\n\t"
    "buffer_load_dwordx4 v197, s[64:67], s29 offen offset:1024 lds\n\t"
    "v_mfma_f32_16x16x32_bf16 a[20:23], v[152:155], a[140:143], a[20:23]\n\t"
    "v_mfma_f32_16x16x32_bf16 a[24:27], v[152:155], a[144:147], a[24:27]\n\t"
    "v_mfma_f32_16x16x32_bf16 a[24:27], v[156:159], a[168:171], a[24:27]\n\t"
    "v_mfma_f32_16x16x32_bf16 a[28:31], v[156:159], a[172:175], a[28:31]\n\t"
    "v_mfma_f32_16x16x32_bf16 a[28:31], v[152:155], a[148:151], a[28:31]\n\t"
    "s_waitcnt lgkmcnt(0)\n\t"
    "s_barrier\n\t"
    "v_mfma_f32_16x16x32_bf16 a[32:35], v[160:163], a[128:131], a[32:35]\n\t"
    "ds_read_b64_tr_b16 v[128:129], v192 offset:32768\n\t"
    "v_mfma_f32_16x16x32_bf16 a[32:35], v[164:167], a[152:155], a[32:35]\n\t"
    "buffer_load_dwordx4 v[8:11], v229, s[52:55], s31 offen offset:2048 sc1\n\t"
    "v_mfma_f32_16x16x32_bf16 a[36:39], v[164:167], a[156:159], a[36:39]\n\t"
    "ds_read_b64_tr_b16 v[130:131], v192 offset:33792\n\t"
    "v_mfma_f32_16x16x32_bf16 a[36:39], v[160:163], a[132:135], a[36:39]\n\t"
    "v_mfma_f32_16x16x32_bf16 a[40:43], v[160:163], a[136:139], a[40:43]\n\t"
    "ds_read_b64_tr_b16 v[132:133], v192 offset:34816\n\t"
    "v_mfma_f32_16x16x32_bf16 a[40:43], v[164:167], a[160:163], a[40:43]\n\t"
    "buffer_load_dwordx4 v[12:15], v229, s[52:55], s31 offen offset:3072 sc1\n\t"
    "v_mfma_f32_16x16x32_bf16 a[44:47], v[164:167], a[164:167], a[44:47]\n\t"
    "ds_read_b64_tr_b16 v[134:135], v192 offset:35840\n\t"
    "v_mfma_f32_16x16x32_bf16 a[44:47], v[160:163], a[140:143], a[44:47]\n\t"
    "v_mfma_f32_16x16x32_bf16 a[48:51], v[160:163], a[144:147], a[48:51]\n\t"
    "ds_read_b64_tr_b16 v[136:137], v193 offset:32768\n\t"
    "v_mfma_f32_16x16x32_bf16 a[48:51], v[164:167], a[168:171], a[48:51]\n\t"
    "buffer_load_dwordx4 v[16:19], v241, s[52:55], s31 offen sc1\n\t"
    "v_mfma_f32_16x16x32_bf16 a[52:55], v[164:167], a[172:175], a[52:55]\n\t"
    "ds_read_b64_tr_b16 v[138:139], v193 offset:33792\n\t"
    "v_mfma_f32_16x16x32_bf16 a[52:55], v[160:163], a[148:151], a[52:55]\n\t"
    "v_mfma_f32_16x16x32_bf16 a[56:59], v[168:171], a[128:131], a[56:59]\n\t"
    "ds_read_b64_tr_b16 v[140:141], v193 offset:34816\n\t"
    "v_mfma_f32_16x16x32_bf16 a[56:59], v[172:175], a[152:155], a[56:59]\n\t"
    "buffer_load_dwordx4 v[20:23], v241, s[52:55], s31 offen offset:1024 sc1\n\t"
    "v_mfma_f32_16x16x32_bf16 a[60:63], v[172:175], a[156:159], a[60:63]\n\t"
    "ds_read_b64_tr_b16 v[142:143], v193 offset:35840\n\t"
    "v_mfma_f32_16x16x32_bf16 a[60:63], v[168:171], a[132:135], a[60:63]\n\t"
    "v_mfma_f32_16x16x32_bf16 a[64:67], v[168:171], a[136:139], a[64:67]\n\t"
    "ds_read_b64_tr_b16 v[144:145], v194 offset:32768\n\t"
    "s_cmp_le_u32 s63, 4\n\t"
    "s_addc_u32 s31, 4, 0\n\t"
    "s_mul_i32 s31, s31, 12288\n\t"
    "s_add_u32 s31, s31, 294912\n\t"
    "v_mfma_f32_16x16x32_bf16 a[64:67], v[172:175], a[160:163], a[64:67]\n\t"
    "buffer_load_dwordx4 v[24:27], v229, s[52:55], s31 offen sc1\n\t"
    "v_mfma_f32_16x16x32_bf16 a[68:71], v[172:175], a[164:167], a[68:71]\n\t"
    "ds_read_b64_tr_b16 v[146:147], v194 offset:33792\n\t"
    "v_mfma_f32_16x16x32_bf16 a[68:71], v[168:171], a[140:143], a[68:71]\n\t"
    "buffer_load_dwordx4 v[28:31], v229, s[52:55], s31 offen offset:1024 sc1\n\t"
    "v_mfma_f32_16x16x32_bf16 a[72:75], v[168:171], a[144:147], a[72:75]\n\t"
    "ds_read_b64_tr_b16 v[148:149], v194 offset:34816\n\t"
    "v_mfma_f32_16x16x32_bf16 a[72:75], v[172:175], a[168:171], a[72:75]\n\t"
    "v_mfma_f32_16x16x32_bf16 a[76:79], v[172:175], a[172:175], a[76:79]\n\t"
    "ds_read_b64_tr_b16 v[150:151], v194 offset:35840\n\t"
    "v_mfma_f32_16x16x32_bf16 a[76:79], v[168:171], a[148:151], a[76:79]\n\t"
    "buffer_load_dwordx4 v[32:35], v229, s[52:55], s31 offen offset:2048 sc1\n\t"
    "v_mfma_f32_16x16x32_bf16 a[80:83], v[176:179], a[128:131], a[80:83]\n\t"
    "ds_read_b64_tr_b16 v[152:153], v195 offset:32768\n\t"
    "v_mfma_f32_16x16x32_bf16 a[80:83], v[180:183], a[152:155], a[80:83]\n\t"
    "v_mfma_f32_16x16x32_bf16 a[84:87], v[180:183], a[156:159], a[84:87]\n\t"
    "ds_read_b64_tr_b16 v[154:155], v195 offset:33792\n\t"
    "v_mfma_f32_16x16x32_bf16 a[84:87], v[176:179], a[132:135], a[84:87]\n\t"
    "buffer_load_dwordx4 v[36:39], v229, s[52:55], s31 offen offset:3072 sc1\n\t"
    "v_mfma_f32_16x16x32_bf16 a[88:91], v[176:179], a[136:139], a[88:91]\n\t"
    "ds_read_b64_tr_b16 v[156:157], v195 offset:34816\n\t"
    "v_mfma_f32_16x16x32_bf16 a[88:91], v[180:183], a[160:163], a[88:91]\n\t"
    "v_mfma_f32_16x16x32_bf16 a[92:95], v[180:183], a[164:167], a[92:95]\n\t"
    "ds_read_b64_tr_b16 v[158:159], v195 offset:35840\n\t"
    "s_cmp_le_u32 s63, 5\n\t"
    "s_addc_u32 s28, 5, 0\n\t"
    "s_lshl_b32 s28, s28, 19\n\t"
    "s_nop 0\n\t"
    "s_add_u32 s28, s28, 196608\n\t"
    "s_add_u32 m0, s58, 36864\n\t"
    "v_mfma_f32_16x16x32_bf16 a[92:95], v[176:179], a[140:143], a[92:95]\n\t"
    "buffer_load_dwordx4 v196, s[64:67], s28 offen lds\n\t"
    "v_mfma_f32_16x16x32_bf16 a[96:99], v[176:179], a[144:147], a[96:99]\n\t"
    "v_mfma_f32_16x16x32_bf16 a[96:99], v[180:183], a[168:171], a[96:99]\n\t"
    "v_mfma_f32_16x16x32_bf16 a[100:103], v[180:183], a[172:175], a[100:103]\n\t"
    "buffer_load_dwordx4 v197, s[64:67], s28 offen offset:1024 lds\n\t"
    "v_mfma_f32_16x16x32_bf16 a[100:103], v[176:179], a[148:151], a[100:103]\n\t"
    "v_mfma_f32_16x16x32_bf16 a[104:107], v[184:187], a[128:131], a[104:107]\n\t"
    "v_mfma_f32_16x16x32_bf16 a[104:107], v[188:191], a[152:155], a[104:107]\n\t"
    "s_cmp_le_u32 s63, 6\n\t"
    "s_addc_u32 s29, 6, 0\n\t"
    "s_lshl_b32 s29, s29, 19\n\t"
    "s_nop 0\n\t"
    "s_add_u32 s29, s29, 196608\n\t"
    "s_add_u32 m0, s58, 38912\n\t"
    "v_mfma_f32_16x16x32_bf16 a[108:111], v[188:191], a[156:159], a[108:111]\n\t"
    "buffer_load_dwordx4 v196, s[64:67], s29 offen lds\n\t"
    "v_mfma_f32_16x16x32_bf16 a[108:111], v[184:187], a[132:135], a[108:111]\n\t"
    "v_mfma_f32_16x16x32_bf16 a[112:115], v[184:187], a[136:139], a[112:115]\n\t"
    "v_mfma_f32_16x16x32_bf16 a[112:115], v[188:191], a[160:163], a[112:115]\n\t"
    "v_mfma_f32_16x16x32_bf16 a[116:119], v[188:191], a[164:167], a[116:119]\n\t"
    "buffer_load_dwordx4 v197, s[64:67], s29 offen offset:1024 lds\n\t"
    "v_mfma_f32_16x16x32_bf16 a[116:119], v[184:187], a[140:143], a[116:119]\n\t"
    "v_mfma_f32_16x16x32_bf16 a[120:123], v[184:187], a[144:147], a[120:123]\n\t"
    "s_add_u32 s78, s78, 65536\n\t"
    "v_mfma_f32_16x16x32_bf16 a[120:123], v[188:191], a[168:171], a[120:123]\n\t"
    "v_mfma_f32_16x16x32_bf16 a[124:127], v[188:191], a[172:175], a[124:127]\n\t"
    "v_mfma_f32_16x16x32_bf16 a[124:127], v[184:187], a[148:151], a[124:127]\n\t"
    "s_waitcnt vmcnt(18) lgkmcnt(0)\n\t"
    "s_barrier\n\t"
    "v_mfma_f32_16x16x32_bf16 a[192:195], v[128:131], v[48:51], a[192:195]\n\t"
    "ds_read_b64_tr_b16 v[160:161], v192 offset:36864\n\t"
    "v_mfma_f32_16x16x32_bf16 a[192:195], v[132:135], v[72:75], a[192:195]\n\t"
    "buffer_load_dwordx4 v[40:43], v241, s[52:55], s31 offen sc1\n\t"
    "v_mfma_f32_16x16x32_bf16 a[196:199], v[132:135], v[76:79], a[196:199]\n\t"
    "ds_read_b64_tr_b16 v[162:163], v192 offset:37888\n\t"
    "v_mfma_f32_16x16x32_bf16 a[196:199], v[128:131], v[52:55], a[196:199]\n\t"
    "v_mfma_f32_16x16x32_bf16 a[200:203], v[128:131], v[56:59], a[200:203]\n\t"
    "ds_read_b64_tr_b16 v[164:165], v192 offset:38912\n\t"
    "v_mfma_f32_16x16x32_bf16 a[200:203], v[132:135], v[80:83], a[200:203]\n\t"
    "buffer_load_dwordx4 v[44:47], v241, s[52:55], s31 offen offset:1024 sc1\n\t"
    "v_mfma_f32_16x16x32_bf16 a[204:207], v[132:135], v[84:87], a[204:207]\n\t"
    "ds_read_b64_tr_b16 v[166:167], v192 offset:39936\n\t"
    "v_mfma_f32_16x16x32_bf16 a[204:207], v[128:131], v[60:63], a[204:207]\n\t"
    "v_mfma_f32_16x16x32_bf16 a[208:211], v[128:131], v[64:67], a[208:211]\n\t"
    "ds_read_b64_tr_b16 v[168:169], v193 offset:36864\n\t"
    "s_cmp_le_u32 s63, 5\n\t"
    "s_addc_u32 s31, 5, 0\n\t"
    "s_mul_i32 s31, s31, 12288\n\t"
    "s_add_u32 s31, s31, 294912\n\t"
    "v_mfma_f32_16x16x32_bf16 a[208:211], v[132:135], v[88:91], a[208:211]\n\t"
    "buffer_load_dwordx4 a[128:131], v229, s[52:55], s31 offen sc1\n\t"
    "v_mfma_f32_16x16x32_bf16 a[212:215], v[132:135], v[92:95], a[212:215]\n\t"
    "ds_read_b64_tr_b16 v[170:171], v193 offset:37888\n\t"
    "v_mfma_f32_16x16x32_bf16 a[212:215], v[128:131], v[68:71], a[212:215]\n\t"
    "v_mfma_f32_16x16x32_bf16 a[216:219], v[136:139], v[48:51], a[216:219]\n\t"
    "ds_read_b64_tr_b16 v[172:173], v193 offset:38912\n\t"
    "v_mfma_f32_16x16x32_bf16 a[216:219], v[140:143], v[72:75], a[216:219]\n\t"
    "buffer_load_dwordx4 a[132:135], v229, s[52:55], s31 offen offset:1024 sc1\n\t"
    "v_mfma_f32_16x16x32_bf16 a[220:223], v[140:143], v[76:79], a[220:223]\n\t"
    "ds_read_b64_tr_b16 v[174:175], v193 offset:39936\n\t"
    "v_mfma_f32_16x16x32_bf16 a[220:223], v[136:139], v[52:55], a[220:223]\n\t"
    "v_mfma_f32_16x16x32_bf16 a[224:227], v[136:139], v[56:59], a[224:227]\n\t"
    "ds_read_b64_tr_b16 v[176:177], v194 offset:36864\n\t"
    "v_mfma_f32_16x16x32_bf16 a[224:227], v[140:143], v[80:83], a[224:227]\n\t"
    "buffer_load_dwordx4 a[136:139], v229, s[52:55], s31 offen offset:2048 sc1\n\t"
    "v_mfma_f32_16x16x32_bf16 a[228:231], v[140:143], v[84:87], a[228:231]\n\t"
    "ds_read_b64_tr_b16 v[178:179], v194 offset:37888\n\t"
    "v_mfma_f32_16x16x32_bf16 a[228:231], v[136:139], v[60:63], a[228:231]\n\t"
    "buffer_load_dwordx4 a[140:143], v229, s[52:55], s31 offen offset:3072 sc1\n\t"
    "v_mfma_f32_16x16x32_bf16 a[232:235], v[136:139], v[64:67], a[232:235]\n\t"
    "ds_read_b64_tr_b16 v[180:181], v194 offset:38912\n\t"
    "v_mfma_f32_16x16x32_bf16 a[232:235], v[140:143], v[88:91], a[232:235]\n\t"
    "v_mfma_f32_16x16x32_bf16 a[236:239], v[140:143], v[92:95], a[236:239]\n\t"
    "ds_read_b64_tr_b16 v[182:183], v194 offset:39936\n\t"
    "v_mfma_f32_16x16x32_bf16 a[236:239], v[136:139], v[68:71], a[236:239]\n\t"
    "buffer_load_dwordx4 a[144:147], v241, s[52:55], s31 offen sc1\n\t"
    "v_mfma_f32_16x16x32_bf16 a[240:243], v[144:147], v[48:51], a[240:243]\n\t"
    "ds_read_b64_tr_b16 v[184:185], v195 offset:36864\n\t"
    "v_mfma_f32_16x16x32_bf16 a[240:243], v[148:151], v[72:75], a[240:243]\n\t"
    "v_mfma_f32_16x16x32_bf16 a[244:247], v[148:151], v[76:79], a[244:247]\n\t"
    "ds_read_b64_tr_b16 v[186:187], v195 offset:37888\n\t"
    "v_mfma_f32_16x16x32_bf16 a[244:247], v[144:147], v[52:55], a[244:247]\n\t"
    "buffer_load_dwordx4 a[148:151], v241, s[52:55], s31 offen offset:1024 sc1\n\t"
    "v_mfma_f32_16x16x32_bf16 a[248:251], v[144:147], v[56:59], a[248:251]\n\t"
    "ds_read_b64_tr_b16 v[188:189], v195 offset:38912\n\t"
    "v_mfma_f32_16x16x32_bf16 a[248:251], v[148:151], v[80:83], a[248:251]\n\t"
    "v_mfma_f32_16x16x32_bf16 a[252:255], v[148:151], v[84:87], a[252:255]\n\t"
    "ds_read_b64_tr_b16 v[190:191], v195 offset:39936\n\t"
    "s_cmp_le_u32 s63, 6\n\t"
    "s_addc_u32 s31, 6, 0\n\t"
    "s_mul_i32 s31, s31, 12288\n\t"
    "s_add_u32 s31, s31, 294912\n\t"
    "v_mfma_f32_16x16x32_bf16 a[252:255], v[144:147], v[60:63], a[252:255]\n\t"
    "buffer_load_dwordx4 a[152:155], v229, s[52:55], s31 offen sc1\n\t"
    "v_mfma_f32_16x16x32_bf16 a[0:3], v[144:147], v[64:67], a[0:3]\n\t"
    "v_mfma_f32_16x16x32_bf16 a[0:3], v[148:151], v[88:91], a[0:3]\n\t"
    "v_add_u32 v192, 98304, v192\n\t"
    "v_mfma_f32_16x16x32_bf16 a[4:7], v[148:151], v[92:95], a[4:7]\n\t"
    "buffer_load_dwordx4 a[156:159], v229, s[52:55], s31 offen offset:1024 sc1\n\t"
    "v_add_u32 v193, 98304, v193\n\t"
    "v_mfma_f32_16x16x32_bf16 a[4:7], v[144:147], v[68:71], a[4:7]\n\t"
    "v_add_u32 v194, 98304, v194\n\t"
    "v_mfma_f32_16x16x32_bf16 a[8:11], v[152:155], v[48:51], a[8:11]\n\t"
    "v_add_u32 v195, 98304, v195\n\t"
    "v_mfma_f32_16x16x32_bf16 a[8:11], v[156:159], v[72:75], a[8:11]\n\t"
    "v_mfma_f32_16x16x32_bf16 a[12:15], v[156:159], v[76:79], a[12:15]\n\t"
    "buffer_load_dwordx4 a[160:163], v229, s[52:55], s31 offen offset:2048 sc1\n\t"
    "v_mfma_f32_16x16x32_bf16 a[12:15], v[152:155], v[52:55], a[12:15]\n\t"
    "v_mfma_f32_16x16x32_bf16 a[16:19], v[152:155], v[56:59], a[16:19]\n\t"
    "v_mfma_f32_16x16x32_bf16 a[16:19], v[156:159], v[80:83], a[16:19]\n\t"
    "v_mfma_f32_16x16x32_bf16 a[20:23], v[156:159], v[84:87], a[20:23]\n\t"
    "buffer_load_dwordx4 a[164:167], v229, s[52:55], s31 offen offset:3072 sc1\n\t"
    "v_mfma_f32_16x16x32_bf16 a[20:23], v[152:155], v[60:63], a[20:23]\n\t"
    "v_mfma_f32_16x16x32_bf16 a[24:27], v[152:155], v[64:67], a[24:27]\n\t"
    "v_mfma_f32_16x16x32_bf16 a[24:27], v[156:159], v[88:91], a[24:27]\n\t"
    "v_mfma_f32_16x16x32_bf16 a[28:31], v[156:159], v[92:95], a[28:31]\n\t"
    "v_mfma_f32_16x16x32_bf16 a[28:31], v[152:155], v[68:71], a[28:31]\n\t"
    "s_waitcnt vmcnt(12) lgkmcnt(0)\n\t"
    "s_barrier\n\t"
    "v_mfma_f32_16x16x32_bf16 a[32:35], v[160:163], v[48:51], a[32:35]\n\t"
    "ds_read_b64_tr_b16 v[128:129], v194\n\t"
    "v_mfma_f32_16x16x32_bf16 a[32:35], v[164:167], v[72:75], a[32:35]\n\t"
    "v_mfma_f32_16x16x32_bf16 a[36:39], v[164:167], v[76:79], a[36:39]\n\t"
    "ds_read_b64_tr_b16 v[130:131], v194 offset:1024\n\t"
    "v_mfma_f32_16x16x32_bf16 a[36:39], v[160:163], v[52:55], a[36:39]\n\t"
    "buffer_load_dwordx4 a[168:171], v241, s[52:55], s31 offen sc1\n\t"
    "v_mfma_f32_16x16x32_bf16 a[40:43], v[160:163], v[56:59], a[40:43]\n\t"
    "ds_read_b64_tr_b16 v[132:133], v194 offset:2048\n\t"
    "v_mfma_f32_16x16x32_bf16 a[40:43], v[164:167], v[80:83], a[40:43]\n\t"
    "v_mfma_f32_16x16x32_bf16 a[44:47], v[164:167], v[84:87], a[44:47]\n\t"
    "ds_read_b64_tr_b16 v[134:135], v194 offset:3072\n\t"
    "v_mfma_f32_16x16x32_bf16 a[44:47], v[160:163], v[60:63], a[44:47]\n\t"
    "buffer_load_dwordx4 a[172:175], v241, s[52:55], s31 offen offset:1024 sc1\n\t"
    "v_mfma_f32_16x16x32_bf16 a[48:51], v[160:163], v[64:67], a[48:51]\n\t"
    "ds_read_b64_tr_b16 v[136:137], v194 offset:4096\n\t"
    "v_mfma_f32_16x16x32_bf16 a[48:51], v[164:167], v[88:91], a[48:51]\n\t"
    "v_mfma_f32_16x16x32_bf16 a[52:55], v[164:167], v[92:95], a[52:55]\n\t"
    "ds_read_b64_tr_b16 v[138:139], v194 offset:5120\n\t"
    "v_mfma_f32_16x16x32_bf16 a[52:55], v[160:163], v[68:71], a[52:55]\n\t"
    "v_mfma_f32_16x16x32_bf16 a[56:59], v[168:171], v[48:51], a[56:59]\n\t"
    "ds_read_b64_tr_b16 v[140:141], v194 offset:6144\n\t"
    "v_mfma_f32_16x16x32_bf16 a[56:59], v[172:175], v[72:75], a[56:59]\n\t"
    "v_mfma_f32_16x16x32_bf16 a[60:63], v[172:175], v[76:79], a[60:63]\n\t"
    "ds_read_b64_tr_b16 v[142:143], v194 offset:7168\n\t"
    "v_mfma_f32_16x16x32_bf16 a[60:63], v[168:171], v[52:55], a[60:63]\n\t"
    "v_mfma_f32_16x16x32_bf16 a[64:67], v[168:171], v[56:59], a[64:67]\n\t"
    "ds_read_b64_tr_b16 v[144:145], v194 offset:32768\n\t"
    "v_mfma_f32_16x16x32_bf16 a[64:67], v[172:175], v[80:83], a[64:67]\n\t"
    "v_mfma_f32_16x16x32_bf16 a[68:71], v[172:175], v[84:87], a[68:71]\n\t"
    "ds_read_b64_tr_b16 v[146:147], v194 offset:33792\n\t"
    "v_mfma_f32_16x16x32_bf16 a[68:71], v[168:171], v[60:63], a[68:71]\n\t"
    "v_mfma_f32_16x16x32_bf16 a[72:75], v[168:171], v[64:67], a[72:75]\n\t"
    "ds_read_b64_tr_b16 v[148:149], v194 offset:34816\n\t"
    "v_mfma_f32_16x16x32_bf16 a[72:75], v[172:175], v[88:91], a[72:75]\n\t"
    "v_mfma_f32_16x16x32_bf16 a[76:79], v[172:175], v[92:95], a[76:79]\n\t"
    "ds_read_b64_tr_b16 v[150:151], v194 offset:35840\n\t"
    "v_mfma_f32_16x16x32_bf16 a[76:79], v[168:171], v[68:71], a[76:79]\n\t"
    "v_mfma_f32_16x16x32_bf16 a[80:83], v[176:179], v[48:51], a[80:83]\n\t"
    "ds_read_b64_tr_b16 v[152:153], v194 offset:36864\n\t"
    "v_mfma_f32_16x16x32_bf16 a[80:83], v[180:183], v[72:75], a[80:83]\n\t"
    "v_mfma_f32_16x16x32_bf16 a[84:87], v[180:183], v[76:79], a[84:87]\n\t"
    "ds_read_b64_tr_b16 v[154:155], v194 offset:37888\n\t"
    "v_mfma_f32_16x16x32_bf16 a[84:87], v[176:179], v[52:55], a[84:87]\n\t"
    "v_mfma_f32_16x16x32_bf16 a[88:91], v[176:179], v[56:59], a[88:91]\n\t"
    "ds_read_b64_tr_b16 v[156:157], v194 offset:38912\n\t"
    "v_mfma_f32_16x16x32_bf16 a[88:91], v[180:183], v[80:83], a[88:91]\n\t"
    "v_mfma_f32_16x16x32_bf16 a[92:95], v[180:183], v[84:87], a[92:95]\n\t"
    "ds_read_b64_tr_b16 v[158:159], v194 offset:39936\n\t"
    "v_mfma_f32_16x16x32_bf16 a[92:95], v[176:179], v[60:63], a[92:95]\n\t"
    "v_mfma_f32_16x16x32_bf16 a[96:99], v[176:179], v[64:67], a[96:99]\n\t"
    "v_mfma_f32_16x16x32_bf16 a[96:99], v[180:183], v[88:91], a[96:99]\n\t"
    "v_mfma_f32_16x16x32_bf16 a[100:103], v[180:183], v[92:95], a[100:103]\n\t"
    "v_mfma_f32_16x16x32_bf16 a[100:103], v[176:179], v[68:71], a[100:103]\n\t"
    "v_mfma_f32_16x16x32_bf16 a[104:107], v[184:187], v[48:51], a[104:107]\n\t"
    "v_mfma_f32_16x16x32_bf16 a[104:107], v[188:191], v[72:75], a[104:107]\n\t"
    "v_mfma_f32_16x16x32_bf16 a[108:111], v[188:191], v[76:79], a[108:111]\n\t"
    "v_mfma_f32_16x16x32_bf16 a[108:111], v[184:187], v[52:55], a[108:111]\n\t"
    "v_mfma_f32_16x16x32_bf16 a[112:115], v[184:187], v[56:59], a[112:115]\n\t"
    "v_mfma_f32_16x16x32_bf16 a[112:115], v[188:191], v[80:83], a[112:115]\n\t"
    "v_mfma_f32_16x16x32_bf16 a[116:119], v[188:191], v[84:87], a[116:119]\n\t"
    "v_mfma_f32_16x16x32_bf16 a[116:119], v[184:187], v[60:63], a[116:119]\n\t"
    "v_mfma_f32_16x16x32_bf16 a[120:123], v[184:187], v[64:67], a[120:123]\n\t"
    "v_mfma_f32_16x16x32_bf16 a[120:123], v[188:191], v[88:91], a[120:123]\n\t"
    "v_mfma_f32_16x16x32_bf16 a[124:127], v[188:191], v[92:95], a[124:127]\n\t"
    "v_mfma_f32_16x16x32_bf16 a[124:127], v[184:187], v[68:71], a[124:127]\n\t"
    "s_waitcnt vmcnt(0) lgkmcnt(0)\n\t"
    "s_nop 0\n\t"
    "v_mfma_f32_16x16x32_bf16 a[240:243], v[128:131], v[0:3], a[240:243]\n\t"
    "ds_read_b64_tr_b16 v[160:161], v195\n\t"
    "v_mfma_f32_16x16x32_bf16 a[240:243], v[132:135], v[24:27], a[240:243]\n\t"
    "v_mfma_f32_16x16x32_bf16 a[240:243], v[136:139], a[128:131], a[240:243]\n\t"
    "ds_read_b64_tr_b16 v[162:163], v195 offset:1024\n\t"
    "v_mfma_f32_16x16x32_bf16 a[240:243], v[140:143], a[152:155], a[240:243]\n\t"
    "v_mfma_f32_16x16x32_bf16 a[244:247], v[140:143], a[156:159], a[244:247]\n\t"
    "v_mfma_f32_16x16x32_bf16 a[244:247], v[136:139], a[132:135], a[244:247]\n\t"
    "ds_read_b64_tr_b16 v[164:165], v195 offset:2048\n\t"
    "v_mfma_f32_16x16x32_bf16 a[244:247], v[132:135], v[28:31], a[244:247]\n\t"
    "v_mfma_f32_16x16x32_bf16 a[244:247], v[128:131], v[4:7], a[244:247]\n\t"
    "ds_read_b64_tr_b16 v[166:167], v195 offset:3072\n\t"
    "v_mfma_f32_16x16x32_bf16 a[248:251], v[128:131], v[8:11], a[248:251]\n\t"
    "v_mfma_f32_16x16x32_bf16 a[248:251], v[132:135], v[32:35], a[248:251]\n\t"
    "v_mfma_f32_16x16x32_bf16 a[248:251], v[136:139], a[136:139], a[248:251]\n\t"
    "ds_read_b64_tr_b16 v[168:169], v195 offset:4096\n\t"
    "v_mfma_f32_16x16x32_bf16 a[248:251], v[140:143], a[160:163], a[248:251]\n\t"
    "v_mfma_f32_16x16x32_bf16 a[252:255], v[140:143], a[164:167], a[252:255]\n\t"
    "v_mfma_f32_16x16x32_bf16 a[252:255], v[136:139], a[140:143], a[252:255]\n\t"
    "ds_read_b64_tr_b16 v[170:171], v195 offset:5120\n\t"
    "v_mfma_f32_16x16x32_bf16 a[252:255], v[132:135], v[36:39], a[252:255]\n\t"
    "ds_write_b128 v243, a[240:243] offset:12288\n\t"
    "v_mfma_f32_16x16x32_bf16 a[252:255], v[128:131], v[12:15], a[252:255]\n\t"
    "ds_read_b64_tr_b16 v[172:173], v195 offset:6144\n\t"
    "v_mfma_f32_16x16x32_bf16 a[0:3], v[128:131], v[16:19], a[0:3]\n\t"
    "v_mfma_f32_16x16x32_bf16 a[0:3], v[132:135], v[40:43], a[0:3]\n\t"
    "v_mfma_f32_16x16x32_bf16 a[0:3], v[136:139], a[144:147], a[0:3]\n\t"
    "ds_read_b64_tr_b16 v[174:175], v195 offset:7168\n\t"
    "v_mfma_f32_16x16x32_bf16 a[0:3], v[140:143], a[168:171], a[0:3]\n\t"
    "ds_write_b128 v243, a[244:247] offset:13312\n\t"
    "v_mfma_f32_16x16x32_bf16 a[4:7], v[140:143], a[172:175], a[4:7]\n\t"
    "v_mfma_f32_16x16x32_bf16 a[4:7], v[136:139], a[148:151], a[4:7]\n\t"
    "ds_read_b64_tr_b16 v[176:177], v195 offset:32768\n\t"
    "v_mfma_f32_16x16x32_bf16 a[4:7], v[132:135], v[44:47], a[4:7]\n\t"
    "v_mfma_f32_16x16x32_bf16 a[4:7], v[128:131], v[20:23], a[4:7]\n\t"
    "ds_read_b64_tr_b16 v[178:179], v195 offset:33792\n\t"
    "v_mfma_f32_16x16x32_bf16 a[80:83], v[144:147], v[0:3], a[80:83]\n\t"
    "ds_write_b128 v243, a[248:251] offset:14336\n\t"
    "v_mfma_f32_16x16x32_bf16 a[80:83], v[148:151], v[24:27], a[80:83]\n\t"
    "v_mfma_f32_16x16x32_bf16 a[80:83], v[152:155], a[128:131], a[80:83]\n\t"
    "ds_read_b64_tr_b16 v[180:181], v195 offset:34816\n\t"
    "v_mfma_f32_16x16x32_bf16 a[80:83], v[156:159], a[152:155], a[80:83]\n\t"
    "v_mfma_f32_16x16x32_bf16 a[84:87], v[156:159], a[156:159], a[84:87]\n\t"
    "ds_read_b64_tr_b16 v[182:183], v195 offset:35840\n\t"
    "v_mfma_f32_16x16x32_bf16 a[84:87], v[152:155], a[132:135], a[84:87]\n\t"
    "ds_write_b128 v243, a[252:255] offset:15360\n\t"
    "v_mfma_f32_16x16x32_bf16 a[84:87], v[148:151], v[28:31], a[84:87]\n\t"
    "v_mfma_f32_16x16x32_bf16 a[84:87], v[144:147], v[4:7], a[84:87]\n\t"
    "ds_read_b64_tr_b16 v[184:185], v195 offset:36864\n\t"
    "v_mfma_f32_16x16x32_bf16 a[88:91], v[144:147], v[8:11], a[88:91]\n\t"
    "v_mfma_f32_16x16x32_bf16 a[88:91], v[148:151], v[32:35], a[88:91]\n\t"
    "v_mfma_f32_16x16x32_bf16 a[88:91], v[152:155], a[136:139], a[88:91]\n\t"
    "ds_write_b128 v243, a[0:3] offset:16384\n\t"
    "v_mfma_f32_16x16x32_bf16 a[88:91], v[156:159], a[160:163], a[88:91]\n\t"
    "ds_read_b64_tr_b16 v[186:187], v195 offset:37888\n\t"
    "v_mfma_f32_16x16x32_bf16 a[92:95], v[156:159], a[164:167], a[92:95]\n\t"
    "ds_read_b64_tr_b16 v[188:189], v195 offset:38912\n\t"
    "v_mfma_f32_16x16x32_bf16 a[92:95], v[152:155], a[140:143], a[92:95]\n\t"
    "v_mfma_f32_16x16x32_bf16 a[92:95], v[148:151], v[36:39], a[92:95]\n\t"
    "v_mfma_f32_16x16x32_bf16 a[92:95], v[144:147], v[12:15], a[92:95]\n\t"
    "ds_write_b128 v243, a[4:7] offset:17408\n\t"
    "v_mfma_f32_16x16x32_bf16 a[96:99], v[144:147], v[16:19], a[96:99]\n\t"
    "ds_read_b64_tr_b16 v[190:191], v195 offset:39936\n\t"
    "v_mfma_f32_16x16x32_bf16 a[96:99], v[148:151], v[40:43], a[96:99]\n\t"
    "v_mfma_f32_16x16x32_bf16 a[96:99], v[152:155], a[144:147], a[96:99]\n\t"
    "v_mfma_f32_16x16x32_bf16 a[96:99], v[156:159], a[168:171], a[96:99]\n\t"
    "v_mfma_f32_16x16x32_bf16 a[100:103], v[156:159], a[172:175], a[100:103]\n\t"
    "v_mfma_f32_16x16x32_bf16 a[100:103], v[152:155], a[148:151], a[100:103]\n\t"
    "v_mfma_f32_16x16x32_bf16 a[100:103], v[148:151], v[44:47], a[100:103]\n\t"
    "v_mfma_f32_16x16x32_bf16 a[100:103], v[144:147], v[20:23], a[100:103]\n\t"
    "s_waitcnt lgkmcnt(0)\n\t"
    "s_barrier\n\t"
    "v_mfma_f32_16x16x32_bf16 a[8:11], v[160:163], v[0:3], a[8:11]\n\t"
    "ds_read_b128 v[48:51], v244 offset:12288\n\t"
    "v_mfma_f32_16x16x32_bf16 a[8:11], v[164:167], v[24:27], a[8:11]\n\t"
    "ds_read_b128 v[52:55], v244 offset:13312\n\t"
    "v_accvgpr_read_b32 v96, a80\n\t"
    "v_mfma_f32_16x16x32_bf16 a[8:11], v[168:171], a[128:131], a[8:11]\n\t"
    "ds_read_b128 v[56:59], v244 offset:14336\n\t"
    "v_accvgpr_read_b32 v97, a81\n\t"
    "v_mfma_f32_16x16x32_bf16 a[8:11], v[172:175], a[152:155], a[8:11]\n\t"
    "ds_read_b128 v[60:63], v244 offset:15360\n\t"
    "v_accvgpr_read_b32 v98, a82\n\t"
    "v_mfma_f32_16x16x32_bf16 a[12:15], v[172:175], a[156:159], a[12:15]\n\t"
    "ds_read_b128 v[64:67], v244 offset:16384\n\t"
    "v_accvgpr_read_b32 v99, a83\n\t"
    "v_mfma_f32_16x16x32_bf16 a[12:15], v[168:171], a[132:135], a[12:15]\n\t"
    "ds_read_b128 v[68:71], v244 offset:17408\n\t"
    "v_accvgpr_read_b32 v100, a84\n\t"
    "v_mfma_f32_16x16x32_bf16 a[12:15], v[164:167], v[28:31], a[12:15]\n\t"
    "v_accvgpr_read_b32 v101, a85\n\t"
    "v_accvgpr_read_b32 v102, a86\n\t"
    "v_mfma_f32_16x16x32_bf16 a[12:15], v[160:163], v[4:7], a[12:15]\n\t"
    "ds_read_b64_tr_b16 v[128:129], v192\n\t"
    "v_accvgpr_read_b32 v103, a87\n\t"
    "v_mfma_f32_16x16x32_bf16 a[16:19], v[160:163], v[8:11], a[16:19]\n\t"
    "v_accvgpr_read_b32 v104, a88\n\t"
    "v_accvgpr_read_b32 v105, a89\n\t"
    "v_mfma_f32_16x16x32_bf16 a[16:19], v[164:167], v[32:35], a[16:19]\n\t"
    "ds_read_b64_tr_b16 v[130:131], v192 offset:1024\n\t"
    "v_accvgpr_read_b32 v106, a90\n\t"
    "v_accvgpr_read_b32 v107, a91\n\t"
    "v_mfma_f32_16x16x32_bf16 a[16:19], v[168:171], a[136:139], a[16:19]\n\t"
    "v_accvgpr_read_b32 v108, a92\n\t"
    "v_accvgpr_read_b32 v109, a93\n\t"
    "v_mfma_f32_16x16x32_bf16 a[16:19], v[172:175], a[160:163], a[16:19]\n\t"
    "ds_read_b64_tr_b16 v[132:133], v192 offset:2048\n\t"
    "v_accvgpr_read_b32 v110, a94\n\t"
    "v_mfma_f32_16x16x32_bf16 a[20:23], v[172:175], a[164:167], a[20:23]\n\t"
    "v_accvgpr_read_b32 v111, a95\n\t"
    "v_accvgpr_read_b32 v112, a96\n\t"
    "v_mfma_f32_16x16x32_bf16 a[20:23], v[168:171], a[140:143], a[20:23]\n\t"
    "ds_read_b64_tr_b16 v[134:135], v192 offset:3072\n\t"
    "v_accvgpr_read_b32 v113, a97\n\t"
    "v_mfma_f32_16x16x32_bf16 a[20:23], v[164:167], v[36:39], a[20:23]\n\t"
    "ds_write_b128 v243, a[8:11] offset:18432\n\t"
    "v_accvgpr_read_b32 v114, a98\n\t"
    "v_mfma_f32_16x16x32_bf16 a[20:23], v[160:163], v[12:15], a[20:23]\n\t"
    "ds_read_b64_tr_b16 v[136:137], v192 offset:4096\n\t"
    "v_accvgpr_read_b32 v115, a99\n\t"
    "v_mfma_f32_16x16x32_bf16 a[24:27], v[160:163], v[16:19], a[24:27]\n\t"
    "v_accvgpr_read_b32 v116, a100\n\t"
    "v_accvgpr_read_b32 v117, a101\n\t"
    "v_mfma_f32_16x16x32_bf16 a[24:27], v[164:167], v[40:43], a[24:27]\n\t"
    "ds_read_b64_tr_b16 v[138:139], v192 offset:5120\n\t"
    "v_accvgpr_read_b32 v118, a102\n\t"
    "v_accvgpr_read_b32 v119, a103\n\t"
    "v_mfma_f32_16x16x32_bf16 a[24:27], v[168:171], a[144:147], a[24:27]\n\t"
    "s_waitcnt lgkmcnt(12)\n\t"
    "v_add_f32 v96, v96, v48\n\t"
    "v_add_f32_e64 v97, v97, v49\n\t"
    "v_mfma_f32_16x16x32_bf16 a[24:27], v[172:175], a[168:171], a[24:27]\n\t"
    "ds_write_b128 v243, a[12:15] offset:19456\n\t"
    "v_add_f32_e64 v98, v98, v50\n\t"
    "v_mfma_f32_16x16x32_bf16 a[28:31], v[172:175], a[172:175], a[28:31]\n\t"
    "ds_read_b64_tr_b16 v[140:141], v192 offset:6144\n\t"
    "v_add_f32_e64 v99, v99, v51\n\t"
    "v_mfma_f32_16x16x32_bf16 a[28:31], v[168:171], a[148:151], a[28:31]\n\t"
    "s_waitcnt lgkmcnt(13)\n\t"
    "v_add_f32 v100, v100, v52\n\t"
    "v_add_f32_e64 v101, v101, v53\n\t"
    "v_mfma_f32_16x16x32_bf16 a[28:31], v[164:167], v[44:47], a[28:31]\n\t"
    "ds_read_b64_tr_b16 v[142:143], v192 offset:7168\n\t"
    "v_add_f32_e64 v102, v102, v54\n\t"
    "v_mfma_f32_16x16x32_bf16 a[28:31], v[160:163], v[20:23], a[28:31]\n\t"
    "v_add_f32_e64 v103, v103, v55\n\t"
    "v_cvt_pk_bf16_f32 v72, v96, v97\n\t"
    "v_mfma_f32_16x16x32_bf16 a[104:107], v[176:179], v[0:3], a[104:107]\n\t"
    "ds_write_b128 v243, a[16:19] offset:20480\n\t"
    "v_cvt_pk_bf16_f32 v73, v98, v99\n\t"
    "v_mfma_f32_16x16x32_bf16 a[104:107], v[180:183], v[24:27], a[104:107]\n\t"
    "ds_read_b64_tr_b16 v[144:145], v193\n\t"
    "v_cvt_pk_bf16_f32 v74, v100, v101\n\t"
    "v_mfma_f32_16x16x32_bf16 a[104:107], v[184:187], a[128:131], a[104:107]\n\t"
    "ds_read_b64_tr_b16 v[146:147], v193 offset:1024\n\t"
    "v_cvt_pk_bf16_f32 v75, v102, v103\n\t"
    "v_mfma_f32_16x16x32_bf16 a[104:107], v[188:191], a[152:155], a[104:107]\n\t"
    "buffer_store_dwordx4 v[72:75], v229, s[68:71], s79 offen sc1\n\t"
    "s_waitcnt lgkmcnt(14)\n\t"
    "v_add_f32 v104, v104, v56\n\t"
    "v_mfma_f32_16x16x32_bf16 a[108:111], v[188:191], a[156:159], a[108:111]\n\t"
    "ds_read_b64_tr_b16 v[148:149], v193 offset:2048\n\t"
    "v_add_f32_e64 v105, v105, v57\n\t"
    "v_mfma_f32_16x16x32_bf16 a[108:111], v[184:187], a[132:135], a[108:111]\n\t"
    "ds_write_b128 v243, a[20:23] offset:21504\n\t"
    "v_add_f32_e64 v106, v106, v58\n\t"
    "v_mfma_f32_16x16x32_bf16 a[108:111], v[180:183], v[28:31], a[108:111]\n\t"
    "v_add_f32 v107, v107, v59\n\t"
    "v_add_f32 v108, v108, v60\n\t"
    "v_mfma_f32_16x16x32_bf16 a[108:111], v[176:179], v[4:7], a[108:111]\n\t"
    "ds_read_b64_tr_b16 v[150:151], v193 offset:3072\n\t"
    "v_add_f32_e64 v109, v109, v61\n\t"
    "v_mfma_f32_16x16x32_bf16 a[112:115], v[176:179], v[8:11], a[112:115]\n\t"
    "v_add_f32 v110, v110, v62\n\t"
    "v_add_f32 v111, v111, v63\n\t"
    "v_mfma_f32_16x16x32_bf16 a[112:115], v[180:183], v[32:35], a[112:115]\n\t"
    "ds_read_b64_tr_b16 v[152:153], v193 offset:4096\n\t"
    "v_cvt_pk_bf16_f32 v76, v104, v105\n\t"
    "v_mfma_f32_16x16x32_bf16 a[112:115], v[184:187], a[136:139], a[112:115]\n\t"
    "ds_write_b128 v243, a[24:27] offset:22528\n\t"
    "v_cvt_pk_bf16_f32 v77, v106, v107\n\t"
    "v_mfma_f32_16x16x32_bf16 a[112:115], v[188:191], a[160:163], a[112:115]\n\t"
    "ds_read_b64_tr_b16 v[154:155], v193 offset:5120\n\t"
    "v_cvt_pk_bf16_f32 v78, v108, v109\n\t"
    "v_cvt_pk_bf16_f32 v79, v110, v111\n\t"
    "v_mfma_f32_16x16x32_bf16 a[116:119], v[188:191], a[164:167], a[116:119]\n\t"
    "buffer_store_dwordx4 v[76:79], v229, s[68:71], s79 offen offset:1024 sc1\n\t"
    "v_mfma_f32_16x16x32_bf16 a[116:119], v[184:187], a[140:143], a[116:119]\n\t"
    "ds_read_b64_tr_b16 v[156:157], v193 offset:6144\n\t"
    "v_add_f32_e64 v112, v112, v64\n\t"
    "v_mfma_f32_16x16x32_bf16 a[116:119], v[180:183], v[36:39], a[116:119]\n\t"
    "v_add_f32 v113, v113, v65\n\t"
    "v_add_f32 v114, v114, v66\n\t"
    "v_mfma_f32_16x16x32_bf16 a[116:119], v[176:179], v[12:15], a[116:119]\n\t"
    "ds_write_b128 v243, a[28:31] offset:23552\n\t"
    "v_add_f32_e64 v115, v115, v67\n\t"
    "v_mfma_f32_16x16x32_bf16 a[120:123], v[176:179], v[16:19], a[120:123]\n\t"
    "ds_read_b64_tr_b16 v[158:159], v193 offset:7168\n\t"
    "s_waitcnt lgkmcnt(14)\n\t"
    "v_add_f32 v116, v116, v68\n\t"
    "v_mfma_f32_16x16x32_bf16 a[120:123], v[180:183], v[40:43], a[120:123]\n\t"
    "v_add_f32_e64 v117, v117, v69\n\t"
    "v_mfma_f32_16x16x32_bf16 a[120:123], v[184:187], a[144:147], a[120:123]\n\t"
    "v_add_f32 v118, v118, v70\n\t"
    "v_add_f32 v119, v119, v71\n\t"
    "v_mfma_f32_16x16x32_bf16 a[120:123], v[188:191], a[168:171], a[120:123]\n\t"
    "v_cvt_pk_bf16_f32 v80, v112, v113\n\t"
    "v_cvt_pk_bf16_f32 v81, v114, v115\n\t"
    "v_mfma_f32_16x16x32_bf16 a[124:127], v[188:191], a[172:175], a[124:127]\n\t"
    "v_cvt_pk_bf16_f32 v82, v116, v117\n\t"
    "v_cvt_pk_bf16_f32 v83, v118, v119\n\t"
    "v_mfma_f32_16x16x32_bf16 a[124:127], v[184:187], a[148:151], a[124:127]\n\t"
    "buffer_store_dwordx4 v[80:83], v229, s[68:71], s79 offen offset:2048 sc1\n\t"
    "v_mfma_f32_16x16x32_bf16 a[124:127], v[180:183], v[44:47], a[124:127]\n\t"
    "v_mfma_f32_16x16x32_bf16 a[124:127], v[176:179], v[20:23], a[124:127]\n\t"
    "s_waitcnt lgkmcnt(0)\n\t"
    "s_barrier\n\t"
    "v_mfma_f32_16x16x32_bf16 a[192:195], v[128:131], v[0:3], a[192:195]\n\t"
    "ds_read_b128 v[48:51], v244 offset:18432\n\t"
    "v_mfma_f32_16x16x32_bf16 a[192:195], v[132:135], v[24:27], a[192:195]\n\t"
    "ds_read_b128 v[52:55], v244 offset:19456\n\t"
    "v_accvgpr_read_b32 v96, a104\n\t"
    "v_accvgpr_read_b32 v97, a105\n\t"
    "v_mfma_f32_16x16x32_bf16 a[192:195], v[136:139], a[128:131], a[192:195]\n\t"
    "ds_read_b128 v[56:59], v244 offset:20480\n\t"
    "v_accvgpr_read_b32 v98, a106\n\t"
    "v_accvgpr_read_b32 v99, a107\n\t"
    "v_accvgpr_read_b32 v100, a108\n\t"
    "v_mfma_f32_16x16x32_bf16 a[192:195], v[140:143], a[152:155], a[192:195]\n\t"
    "ds_read_b128 v[60:63], v244 offset:21504\n\t"
    "v_accvgpr_read_b32 v101, a109\n\t"
    "v_accvgpr_read_b32 v102, a110\n\t"
    "v_mfma_f32_16x16x32_bf16 a[196:199], v[140:143], a[156:159], a[196:199]\n\t"
    "ds_read_b128 v[64:67], v244 offset:22528\n\t"
    "v_accvgpr_read_b32 v103, a111\n\t"
    "v_accvgpr_read_b32 v104, a112\n\t"
    "v_accvgpr_read_b32 v105, a113\n\t"
    "v_mfma_f32_16x16x32_bf16 a[196:199], v[136:139], a[132:135], a[196:199]\n\t"
    "ds_read_b128 v[68:71], v244 offset:23552\n\t"
    "v_accvgpr_read_b32 v106, a114\n\t"
    "v_accvgpr_read_b32 v107, a115\n\t"
    "v_accvgpr_read_b32 v108, a116\n\t"
    "v_mfma_f32_16x16x32_bf16 a[196:199], v[132:135], v[28:31], a[196:199]\n\t"
    "v_accvgpr_read_b32 v109, a117\n\t"
    "v_accvgpr_read_b32 v110, a118\n\t"
    "v_accvgpr_read_b32 v111, a119\n\t"
    "v_mfma_f32_16x16x32_bf16 a[196:199], v[128:131], v[4:7], a[196:199]\n\t"
    "ds_read_b64_tr_b16 v[160:161], v192 offset:32768\n\t"
    "v_accvgpr_read_b32 v112, a120\n\t"
    "v_accvgpr_read_b32 v113, a121\n\t"
    "v_accvgpr_read_b32 v114, a122\n\t"
    "v_accvgpr_read_b32 v115, a123\n\t"
    "v_mfma_f32_16x16x32_bf16 a[200:203], v[128:131], v[8:11], a[200:203]\n\t"
    "v_accvgpr_read_b32 v116, a124\n\t"
    "v_accvgpr_read_b32 v117, a125\n\t"
    "v_accvgpr_read_b32 v118, a126\n\t"
    "v_accvgpr_read_b32 v119, a127\n\t"
    "v_mfma_f32_16x16x32_bf16 a[200:203], v[132:135], v[32:35], a[200:203]\n\t"
    "ds_read_b64_tr_b16 v[162:163], v192 offset:33792\n\t"
    "s_waitcnt lgkmcnt(7)\n\t"
    "v_add_f32 v96, v96, v48\n\t"
    "v_add_f32 v97, v97, v49\n\t"
    "v_add_f32 v98, v98, v50\n\t"
    "v_mfma_f32_16x16x32_bf16 a[200:203], v[136:139], a[136:139], a[200:203]\n\t"
    "v_add_f32 v99, v99, v51\n\t"
    "s_waitcnt lgkmcnt(6)\n\t"
    "v_add_f32 v100, v100, v52\n\t"
    "v_add_f32 v101, v101, v53\n\t"
    "v_mfma_f32_16x16x32_bf16 a[200:203], v[140:143], a[160:163], a[200:203]\n\t"
    "ds_read_b64_tr_b16 v[164:165], v192 offset:34816\n\t"
    "v_add_f32 v102, v102, v54\n\t"
    "v_add_f32 v103, v103, v55\n\t"
    "v_cvt_pk_bf16_f32 v72, v96, v97\n\t"
    "v_mfma_f32_16x16x32_bf16 a[204:207], v[140:143], a[164:167], a[204:207]\n\t"
    "v_cvt_pk_bf16_f32 v73, v98, v99\n\t"
    "v_cvt_pk_bf16_f32 v74, v100, v101\n\t"
    "v_cvt_pk_bf16_f32 v75, v102, v103\n\t"
    "v_mfma_f32_16x16x32_bf16 a[204:207], v[136:139], a[140:143], a[204:207]\n\t"
    "buffer_store_dwordx4 v[72:75], v229, s[68:71], s79 offen offset:3072 sc1\n\t"
    "s_waitcnt lgkmcnt(6)\n\t"
    "v_add_f32 v104, v104, v56\n\t"
    "v_add_f32_e64 v105, v105, v57\n\t"
    "v_mfma_f32_16x16x32_bf16 a[204:207], v[132:135], v[36:39], a[204:207]\n\t"
    "ds_read_b64_tr_b16 v[166:167], v192 offset:35840\n\t"
    "v_add_f32 v106, v106, v58\n\t"
    "v_add_f32 v107, v107, v59\n\t"
    "s_waitcnt lgkmcnt(6)\n\t"
    "v_add_f32 v108, v108, v60\n\t"
    "v_mfma_f32_16x16x32_bf16 a[204:207], v[128:131], v[12:15], a[204:207]\n\t"
    "ds_read_b64_tr_b16 v[168:169], v192 offset:36864\n\t"
    "v_add_f32 v109, v109, v61\n\t"
    "v_add_f32 v110, v110, v62\n\t"
    "v_add_f32_e64 v111, v111, v63\n\t"
    "v_mfma_f32_16x16x32_bf16 a[208:211], v[128:131], v[16:19], a[208:211]\n\t"
    "v_cvt_pk_bf16_f32 v76, v104, v105\n\t"
    "v_cvt_pk_bf16_f32 v77, v106, v107\n\t"
    "v_cvt_pk_bf16_f32 v78, v108, v109\n\t"
    "v_cvt_pk_bf16_f32 v79, v110, v111\n\t"
    "v_mfma_f32_16x16x32_bf16 a[208:211], v[132:135], v[40:43], a[208:211]\n\t"
    "buffer_store_dwordx4 v[76:79], v241, s[68:71], s79 offen sc1\n\t"
    "s_waitcnt lgkmcnt(6)\n\t"
    "v_add_f32 v112, v112, v64\n\t"
    "v_add_f32_e64 v113, v113, v65\n\t"
    "v_mfma_f32_16x16x32_bf16 a[208:211], v[136:139], a[144:147], a[208:211]\n\t"
    "ds_read_b64_tr_b16 v[170:171], v192 offset:37888\n\t"
    "v_add_f32 v114, v114, v66\n\t"
    "v_add_f32 v115, v115, v67\n\t"
    "s_waitcnt lgkmcnt(6)\n\t"
    "v_add_f32 v116, v116, v68\n\t"
    "v_mfma_f32_16x16x32_bf16 a[208:211], v[140:143], a[168:171], a[208:211]\n\t"
    "v_add_f32 v117, v117, v69\n\t"
    "v_add_f32 v118, v118, v70\n\t"
    "v_add_f32_e64 v119, v119, v71\n\t"
    "v_mfma_f32_16x16x32_bf16 a[212:215], v[140:143], a[172:175], a[212:215]\n\t"
    "ds_read_b64_tr_b16 v[172:173], v192 offset:38912\n\t"
    "v_cvt_pk_bf16_f32 v80, v112, v113\n\t"
    "v_cvt_pk_bf16_f32 v81, v114, v115\n\t"
    "v_cvt_pk_bf16_f32 v82, v116, v117\n\t"
    "v_cvt_pk_bf16_f32 v83, v118, v119\n\t"
    "v_mfma_f32_16x16x32_bf16 a[212:215], v[136:139], a[148:151], a[212:215]\n\t"
    "buffer_store_dwordx4 v[80:83], v241, s[68:71], s79 offen offset:1024 sc1\n\t"
    "v_mfma_f32_16x16x32_bf16 a[212:215], v[132:135], v[44:47], a[212:215]\n\t"
    "ds_write_b128 v243, a[192:195]\n\t"
    "v_mfma_f32_16x16x32_bf16 a[212:215], v[128:131], v[20:23], a[212:215]\n\t"
    "ds_read_b64_tr_b16 v[174:175], v192 offset:39936\n\t"
    "v_mfma_f32_16x16x32_bf16 a[216:219], v[144:147], v[0:3], a[216:219]\n\t"
    "ds_read_b64_tr_b16 v[176:177], v193 offset:32768\n\t"
    "v_mfma_f32_16x16x32_bf16 a[216:219], v[148:151], v[24:27], a[216:219]\n\t"
    "ds_write_b128 v243, a[196:199] offset:1024\n\t"
    "v_mfma_f32_16x16x32_bf16 a[216:219], v[152:155], a[128:131], a[216:219]\n\t"
    "ds_read_b64_tr_b16 v[178:179], v193 offset:33792\n\t"
    "v_mfma_f32_16x16x32_bf16 a[216:219], v[156:159], a[152:155], a[216:219]\n\t"
    "v_mfma_f32_16x16x32_bf16 a[220:223], v[156:159], a[156:159], a[220:223]\n\t"
    "ds_read_b64_tr_b16 v[180:181], v193 offset:34816\n\t"
    "v_mfma_f32_16x16x32_bf16 a[220:223], v[152:155], a[132:135], a[220:223]\n\t"
    "ds_write_b128 v243, a[200:203] offset:2048\n\t"
    "v_mfma_f32_16x16x32_bf16 a[220:223], v[148:151], v[28:31], a[220:223]\n\t"
    "v_mfma_f32_16x16x32_bf16 a[220:223], v[144:147], v[4:7], a[220:223]\n\t"
    "ds_read_b64_tr_b16 v[182:183], v193 offset:35840\n\t"
    "v_mfma_f32_16x16x32_bf16 a[224:227], v[144:147], v[8:11], a[224:227]\n\t"
    "v_mfma_f32_16x16x32_bf16 a[224:227], v[148:151], v[32:35], a[224:227]\n\t"
    "ds_write_b128 v243, a[204:207] offset:3072\n\t"
    "v_mfma_f32_16x16x32_bf16 a[224:227], v[152:155], a[136:139], a[224:227]\n\t"
    "ds_read_b64_tr_b16 v[184:185], v193 offset:36864\n\t"
    "v_mfma_f32_16x16x32_bf16 a[224:227], v[156:159], a[160:163], a[224:227]\n\t"
    "ds_read_b64_tr_b16 v[186:187], v193 offset:37888\n\t"
    "v_mfma_f32_16x16x32_bf16 a[228:231], v[156:159], a[164:167], a[228:231]\n\t"
    "ds_write_b128 v243, a[208:211] offset:4096\n\t"
    "v_mfma_f32_16x16x32_bf16 a[228:231], v[152:155], a[140:143], a[228:231]\n\t"
    "ds_read_b64_tr_b16 v[188:189], v193 offset:38912\n\t"
    "v_mfma_f32_16x16x32_bf16 a[228:231], v[148:151], v[36:39], a[228:231]\n\t"
    "v_mfma_f32_16x16x32_bf16 a[228:231], v[144:147], v[12:15], a[228:231]\n\t"
    "ds_read_b64_tr_b16 v[190:191], v193 offset:39936\n\t"
    "v_mfma_f32_16x16x32_bf16 a[232:235], v[144:147], v[16:19], a[232:235]\n\t"
    "ds_write_b128 v243, a[212:215] offset:5120\n\t"
    "v_mfma_f32_16x16x32_bf16 a[232:235], v[148:151], v[40:43], a[232:235]\n\t"
    "v_mfma_f32_16x16x32_bf16 a[232:235], v[152:155], a[144:147], a[232:235]\n\t"
    "v_mfma_f32_16x16x32_bf16 a[232:235], v[156:159], a[168:171], a[232:235]\n\t"
    "v_mfma_f32_16x16x32_bf16 a[236:239], v[156:159], a[172:175], a[236:239]\n\t"
    "v_mfma_f32_16x16x32_bf16 a[236:239], v[152:155], a[148:151], a[236:239]\n\t"
    "v_mfma_f32_16x16x32_bf16 a[236:239], v[148:151], v[44:47], a[236:239]\n\t"
    "v_mfma_f32_16x16x32_bf16 a[236:239], v[144:147], v[20:23], a[236:239]\n\t"
    "s_waitcnt lgkmcnt(0)\n\t"
    "s_nop 0\n\t"
    "v_mfma_f32_16x16x32_bf16 a[32:35], v[160:163], v[0:3], a[32:35]\n\t"
    "v_mfma_f32_16x16x32_bf16 a[32:35], v[164:167], v[24:27], a[32:35]\n\t"
    "ds_write_b128 v243, a[216:219] offset:6144\n\t"
    "v_mfma_f32_16x16x32_bf16 a[32:35], v[168:171], a[128:131], a[32:35]\n\t"
    "v_mfma_f32_16x16x32_bf16 a[32:35], v[172:175], a[152:155], a[32:35]\n\t"
    "v_mfma_f32_16x16x32_bf16 a[36:39], v[172:175], a[156:159], a[36:39]\n\t"
    "ds_write_b128 v243, a[220:223] offset:7168\n\t"
    "v_mfma_f32_16x16x32_bf16 a[36:39], v[168:171], a[132:135], a[36:39]\n\t"
    "v_mfma_f32_16x16x32_bf16 a[36:39], v[164:167], v[28:31], a[36:39]\n\t"
    "s_waitcnt vmcnt(0)\n\t"
    "s_mov_b64 exec, 1\n\t"
    "buffer_atomic_add v247, off, s[68:71], s78 sc1\n\t"
    "s_mov_b64 exec, -1\n\t"
    "s_nop 0\n\t"
    "s_xor_b32 s79, s79, 4194304\n\t"
    "v_mfma_f32_16x16x32_bf16 a[36:39], v[160:163], v[4:7], a[36:39]\n\t"
    "v_mfma_f32_16x16x32_bf16 a[40:43], v[160:163], v[8:11], a[40:43]\n\t"
    "ds_write_b128 v243, a[224:227] offset:8192\n\t"
    "v_mfma_f32_16x16x32_bf16 a[40:43], v[164:167], v[32:35], a[40:43]\n\t"
    "v_mfma_f32_16x16x32_bf16 a[40:43], v[168:171], a[136:139], a[40:43]\n\t"
    "v_mfma_f32_16x16x32_bf16 a[40:43], v[172:175], a[160:163], a[40:43]\n\t"
    "ds_write_b128 v243, a[228:231] offset:9216\n\t"
    "v_mfma_f32_16x16x32_bf16 a[44:47], v[172:175], a[164:167], a[44:47]\n\t"
    "v_mfma_f32_16x16x32_bf16 a[44:47], v[168:171], a[140:143], a[44:47]\n\t"
    "v_mfma_f32_16x16x32_bf16 a[44:47], v[164:167], v[36:39], a[44:47]\n\t"
    "v_mfma_f32_16x16x32_bf16 a[44:47], v[160:163], v[12:15], a[44:47]\n\t"
    "ds_write_b128 v243, a[232:235] offset:10240\n\t"
    "v_mfma_f32_16x16x32_bf16 a[48:51], v[160:163], v[16:19], a[48:51]\n\t"
    "v_mfma_f32_16x16x32_bf16 a[48:51], v[164:167], v[40:43], a[48:51]\n\t"
    "v_mfma_f32_16x16x32_bf16 a[48:51], v[168:171], a[144:147], a[48:51]\n\t"
    "ds_write_b128 v243, a[236:239] offset:11264\n\t"
    "v_mfma_f32_16x16x32_bf16 a[48:51], v[172:175], a[168:171], a[48:51]\n\t"
    "v_mfma_f32_16x16x32_bf16 a[52:55], v[172:175], a[172:175], a[52:55]\n\t"
    "v_mfma_f32_16x16x32_bf16 a[52:55], v[168:171], a[148:151], a[52:55]\n\t"
    "v_mfma_f32_16x16x32_bf16 a[52:55], v[164:167], v[44:47], a[52:55]\n\t"
    "v_mfma_f32_16x16x32_bf16 a[52:55], v[160:163], v[20:23], a[52:55]\n\t"
    "v_mfma_f32_16x16x32_bf16 a[56:59], v[176:179], v[0:3], a[56:59]\n\t"
    "v_accvgpr_read_b32 v96, a32\n\t"
    "v_accvgpr_read_b32 v97, a33\n\t"
    "v_mfma_f32_16x16x32_bf16 a[56:59], v[180:183], v[24:27], a[56:59]\n\t"
    "s_waitcnt lgkmcnt(0)\n\t"
    "s_barrier\n\t"
    "v_accvgpr_read_b32 v98, a34\n\t"
    "v_accvgpr_read_b32 v99, a35\n\t"
    "v_mfma_f32_16x16x32_bf16 a[56:59], v[184:187], a[128:131], a[56:59]\n\t"
    "ds_read_b128 v[48:51], v244\n\t"
    "v_accvgpr_read_b32 v100, a36\n\t"
    "v_accvgpr_read_b32 v101, a37\n\t"
    "v_mfma_f32_16x16x32_bf16 a[56:59], v[188:191], a[152:155], a[56:59]\n\t"
    "ds_read_b128 v[52:55], v244 offset:1024\n\t"
    "v_accvgpr_read_b32 v102, a38\n\t"
    "v_accvgpr_read_b32 v103, a39\n\t"
    "v_mfma_f32_16x16x32_bf16 a[60:63], v[188:191], a[156:159], a[60:63]\n\t"
    "ds_read_b128 v[56:59], v244 offset:2048\n\t"
    "v_accvgpr_read_b32 v104, a40\n\t"
    "v_accvgpr_read_b32 v105, a41\n\t"
    "v_mfma_f32_16x16x32_bf16 a[60:63], v[184:187], a[132:135], a[60:63]\n\t"
    "ds_read_b128 v[60:63], v244 offset:3072\n\t"
    "v_accvgpr_read_b32 v106, a42\n\t"
    "v_accvgpr_read_b32 v107, a43\n\t"
    "v_mfma_f32_16x16x32_bf16 a[60:63], v[180:183], v[28:31], a[60:63]\n\t"
    "ds_read_b128 v[64:67], v244 offset:4096\n\t"
    "v_accvgpr_read_b32 v108, a44\n\t"
    "v_accvgpr_read_b32 v109, a45\n\t"
    "v_mfma_f32_16x16x32_bf16 a[60:63], v[176:179], v[4:7], a[60:63]\n\t"
    "ds_read_b128 v[68:71], v244 offset:5120\n\t"
    "v_accvgpr_read_b32 v110, a46\n\t"
    "v_accvgpr_read_b32 v111, a47\n\t"
    "v_mfma_f32_16x16x32_bf16 a[64:67], v[176:179], v[8:11], a[64:67]\n\t"
    "ds_read_b128 v[72:75], v244 offset:6144\n\t"
    "v_accvgpr_read_b32 v112, a48\n\t"
    "v_accvgpr_read_b32 v113, a49\n\t"
    "v_mfma_f32_16x16x32_bf16 a[64:67], v[180:183], v[32:35], a[64:67]\n\t"
    "ds_read_b128 v[76:79], v244 offset:7168\n\t"
    "v_accvgpr_read_b32 v114, a50\n\t"
    "v_accvgpr_read_b32 v115, a51\n\t"
    "v_mfma_f32_16x16x32_bf16 a[64:67], v[184:187], a[136:139], a[64:67]\n\t"
    "buffer_load_dword v254, v230, s[68:71], 0 offen sc1\n\t"
    "v_accvgpr_read_b32 v116, a52\n\t"
    "v_accvgpr_read_b32 v117, a53\n\t"
    "v_mfma_f32_16x16x32_bf16 a[64:67], v[188:191], a[160:163], a[64:67]\n\t"
    "ds_read_b128 v[80:83], v244 offset:8192\n\t"
    "v_accvgpr_read_b32 v118, a54\n\t"
    "v_accvgpr_read_b32 v119, a55\n\t"
    "v_mfma_f32_16x16x32_bf16 a[68:71], v[188:191], a[164:167], a[68:71]\n\t"
    "ds_read_b128 v[84:87], v244 offset:9216\n\t"
    "s_waitcnt lgkmcnt(9)\n\t"
    "v_add_f32 v96, v96, v48\n\t"
    "v_mfma_f32_16x16x32_bf16 a[68:71], v[184:187], a[140:143], a[68:71]\n\t"
    "ds_read_b128 v[88:91], v244 offset:10240\n\t"
    "v_add_f32_e64 v97, v97, v49\n\t"
    "v_mfma_f32_16x16x32_bf16 a[68:71], v[180:183], v[36:39], a[68:71]\n\t"
    "ds_read_b128 v[92:95], v244 offset:11264\n\t"
    "v_add_f32_e64 v98, v98, v50\n\t"
    "v_mfma_f32_16x16x32_bf16 a[68:71], v[176:179], v[12:15], a[68:71]\n\t"
    "v_add_f32 v99, v99, v51\n\t"
    "s_waitcnt lgkmcnt(10)\n\t"
    "v_add_f32 v100, v100, v52\n\t"
    "v_add_f32 v101, v101, v53\n\t"
    "v_mfma_f32_16x16x32_bf16 a[72:75], v[176:179], v[16:19], a[72:75]\n\t"
    "v_add_f32 v102, v102, v54\n\t"
    "v_add_f32 v103, v103, v55\n\t"
    "v_mfma_f32_16x16x32_bf16 a[72:75], v[180:183], v[40:43], a[72:75]\n\t"
    "s_waitcnt lgkmcnt(9)\n\t"
    "v_add_f32 v104, v104, v56\n\t"
    "v_add_f32_e64 v105, v105, v57\n\t"
    "v_mfma_f32_16x16x32_bf16 a[72:75], v[184:187], a[144:147], a[72:75]\n\t"
    "v_add_f32 v106, v106, v58\n\t"
    "v_add_f32 v107, v107, v59\n\t"
    "v_mfma_f32_16x16x32_bf16 a[72:75], v[188:191], a[168:171], a[72:75]\n\t"
    "s_waitcnt lgkmcnt(8)\n\t"
    "v_add_f32 v108, v108, v60\n\t"
    "v_add_f32 v109, v109, v61\n\t"
    "v_add_f32 v110, v110, v62\n\t"
    "v_mfma_f32_16x16x32_bf16 a[76:79], v[188:191], a[172:175], a[76:79]\n\t"
    "v_add_f32 v111, v111, v63\n\t"
    "s_waitcnt lgkmcnt(7)\n\t"
    "v_add_f32_e64 v112, v112, v64\n\t"
    "v_mfma_f32_16x16x32_bf16 a[76:79], v[184:187], a[148:151], a[76:79]\n\t"
    "v_add_f32 v113, v113, v65\n\t"
    "v_add_f32 v114, v114, v66\n\t"
    "v_mfma_f32_16x16x32_bf16 a[76:79], v[180:183], v[44:47], a[76:79]\n\t"
    "v_add_f32 v115, v115, v67\n\t"
    "s_waitcnt lgkmcnt(6)\n\t"
    "v_add_f32_e64 v116, v116, v68\n\t"
    "v_mfma_f32_16x16x32_bf16 a[76:79], v[176:179], v[20:23], a[76:79]\n\t"
    "v_add_f32 v117, v117, v69\n\t"
    "v_add_f32 v118, v118, v70\n\t"
    "v_add_f32 v119, v119, v71\n\t"
    "s_mov_b32 s25, 256\n\t"
    "s_mov_b32 s30, 1\n\t"
    "s_mov_b32 s23, 5\n\t"
    "s_waitcnt vmcnt(0)\n\t"
    "v_sub_u32 v254, v254, v231\n\t"
    "v_cmp_gt_u32 vcc, s30, v254\n\t"
    "s_nop 1\n\t"
    "s_and_b32 s26, vcc_lo, s25\n\t"
    "s_cbranch_scc1 .Lk_spin\n\t"
    ".Lk_spinret_5:\n\t"
    "s_barrier\n\t"
    "buffer_load_dwordx4 v[0:3], v229, s[68:71], s79 offen sc1\n\t"
    "buffer_load_dwordx4 v[12:15], v229, s[68:71], s79 offen offset:3072 sc1\n\t"
    "buffer_load_dwordx4 v[4:7], v229, s[68:71], s79 offen offset:1024 sc1\n\t"
    "buffer_load_dwordx4 v[16:19], v241, s[68:71], s79 offen sc1\n\t"
    "buffer_load_dwordx4 v[8:11], v229, s[68:71], s79 offen offset:2048 sc1\n\t"
    "buffer_load_dwordx4 v[20:23], v241, s[68:71], s79 offen offset:1024 sc1\n\t"
    "v_accvgpr_read_b32 v128, a56\n\t"
    "v_accvgpr_read_b32 v129, a57\n\t"
    "v_accvgpr_read_b32 v130, a58\n\t"
    "v_accvgpr_read_b32 v131, a59\n\t"
    "v_accvgpr_read_b32 v132, a60\n\t"
    "v_accvgpr_read_b32 v133, a61\n\t"
    "v_accvgpr_read_b32 v134, a62\n\t"
    "v_accvgpr_read_b32 v135, a63\n\t"
    "v_accvgpr_read_b32 v136, a64\n\t"
    "v_accvgpr_read_b32 v137, a65\n\t"
    "v_accvgpr_read_b32 v138, a66\n\t"
    "v_accvgpr_read_b32 v139, a67\n\t"
    "v_accvgpr_read_b32 v140, a68\n\t"
    "v_accvgpr_read_b32 v141, a69\n\t"
    "v_accvgpr_read_b32 v142, a70\n\t"
    "v_accvgpr_read_b32 v143, a71\n\t"
    "v_accvgpr_read_b32 v144, a72\n\t"
    "v_accvgpr_read_b32 v145, a73\n\t"
    "v_accvgpr_read_b32 v146, a74\n\t"
    "v_accvgpr_read_b32 v147, a75\n\t"
    "v_accvgpr_read_b32 v148, a76\n\t"
    "v_accvgpr_read_b32 v149, a77\n\t"
    "v_accvgpr_read_b32 v150, a78\n\t"
    "v_accvgpr_read_b32 v151, a79\n\t"
    "s_waitcnt lgkmcnt(5)\n\t"
    "v_pk_add_f32 v[128:129], v[128:129], v[72:73]\n\t"
    "v_pk_add_f32 v[130:131], v[130:131], v[74:75]\n\t"
    "s_waitcnt lgkmcnt(4)\n\t"
    "v_pk_add_f32 v[132:133], v[132:133], v[76:77]\n\t"
    "v_pk_add_f32 v[134:135], v[134:135], v[78:79]\n\t"
    "s_waitcnt lgkmcnt(3)\n\t"
    "v_pk_add_f32 v[136:137], v[136:137], v[80:81]\n\t"
    "v_pk_add_f32 v[138:139], v[138:139], v[82:83]\n\t"
    "s_waitcnt lgkmcnt(2)\n\t"
    "v_pk_add_f32 v[140:141], v[140:141], v[84:85]\n\t"
    "v_pk_add_f32 v[142:143], v[142:143], v[86:87]\n\t"
    "s_waitcnt lgkmcnt(1)\n\t"
    "v_pk_add_f32 v[144:145], v[144:145], v[88:89]\n\t"
    "v_pk_add_f32 v[146:147], v[146:147], v[90:91]\n\t"
    "s_waitcnt lgkmcnt(0)\n\t"
    "v_pk_add_f32 v[148:149], v[148:149], v[92:93]\n\t"
    "v_pk_add_f32 v[150:151], v[150:151], v[94:95]\n\t"
    "s_mov_b32 s26, 0xffff0000\n\t"
    "s_waitcnt vmcnt(5)\n\t"
    "v_lshlrev_b32 v120, 16, v0\n\t"
    "v_and_b32 v121, s26, v0\n\t"
    "v_lshlrev_b32 v122, 16, v1\n\t"
    "v_and_b32 v123, s26, v1\n\t"
    "v_pk_add_f32 v[96:97], v[96:97], v[120:121]\n\t"
    "v_pk_add_f32 v[98:99], v[98:99], v[122:123]\n\t"
    "v_cvt_pk_bf16_f32 v24, v96, v97\n\t"
    "v_cvt_pk_bf16_f32 v25, v98, v99\n\t"
    "s_waitcnt vmcnt(4)\n\t"
    "v_lshlrev_b32 v120, 16, v12\n\t"
    "v_and_b32 v121, s26, v12\n\t"
    "v_lshlrev_b32 v122, 16, v13\n\t"
    "v_and_b32 v123, s26, v13\n\t"
    "v_pk_add_f32 v[128:129], v[128:129], v[120:121]\n\t"
    "v_pk_add_f32 v[130:131], v[130:131], v[122:123]\n\t"
    "v_cvt_pk_bf16_f32 v26, v128, v129\n\t"
    "v_cvt_pk_bf16_f32 v27, v130, v131\n\t"
    "s_mov_b32 s24, 0\n\t"
    "v_permlane16_swap_b32 v24, v26\n\t"
    "v_permlane16_swap_b32 v25, v27\n\t"
    "s_nop 0\n\t"
    "buffer_store_dwordx4 v[24:27], v245, s[72:75], s24 offen sc1\n\t"
    "v_lshlrev_b32 v120, 16, v2\n\t"
    "v_and_b32 v121, s26, v2\n\t"
    "v_lshlrev_b32 v122, 16, v3\n\t"
    "v_and_b32 v123, s26, v3\n\t"
    "v_pk_add_f32 v[100:101], v[100:101], v[120:121]\n\t"
    "v_pk_add_f32 v[102:103], v[102:103], v[122:123]\n\t"
    "v_cvt_pk_bf16_f32 v28, v100, v101\n\t"
    "v_cvt_pk_bf16_f32 v29, v102, v103\n\t"
    "v_lshlrev_b32 v120, 16, v14\n\t"
    "v_and_b32 v121, s26, v14\n\t"
    "v_lshlrev_b32 v122, 16, v15\n\t"
    "v_and_b32 v123, s26, v15\n\t"
    "v_pk_add_f32 v[132:133], v[132:133], v[120:121]\n\t"
    "v_pk_add_f32 v[134:135], v[134:135], v[122:123]\n\t"
    "v_cvt_pk_bf16_f32 v30, v132, v133\n\t"
    "v_cvt_pk_bf16_f32 v31, v134, v135\n\t"
    "s_mov_b32 s24, 65536\n\t"
    "v_permlane16_swap_b32 v28, v30\n\t"
    "v_permlane16_swap_b32 v29, v31\n\t"
    "buffer_store_dwordx4 v[28:31], v245, s[72:75], s24 offen sc1\n\t"
    "s_waitcnt vmcnt(5)\n\t"
    "v_lshlrev_b32 v120, 16, v4\n\t"
    "v_and_b32 v121, s26, v4\n\t"
    "v_lshlrev_b32 v122, 16, v5\n\t"
    "v_and_b32 v123, s26, v5\n\t"
    "v_pk_add_f32 v[104:105], v[104:105], v[120:121]\n\t"
    "v_pk_add_f32 v[106:107], v[106:107], v[122:123]\n\t"
    "v_cvt_pk_bf16_f32 v32, v104, v105\n\t"
    "v_cvt_pk_bf16_f32 v33, v106, v107\n\t"
    "s_waitcnt vmcnt(4)\n\t"
    "v_lshlrev_b32 v120, 16, v16\n\t"
    "v_and_b32 v121, s26, v16\n\t"
    "v_lshlrev_b32 v122, 16, v17\n\t"
    "v_and_b32 v123, s26, v17\n\t"
    "v_pk_add_f32 v[136:137], v[136:137], v[120:121]\n\t"
    "v_pk_add_f32 v[138:139], v[138:139], v[122:123]\n\t"
    "v_cvt_pk_bf16_f32 v34, v136, v137\n\t"
    "v_cvt_pk_bf16_f32 v35, v138, v139\n\t"
    "s_mov_b32 s24, 131072\n\t"
    "v_permlane16_swap_b32 v32, v34\n\t"
    "v_permlane16_swap_b32 v33, v35\n\t"
    "buffer_store_dwordx4 v[32:35], v245, s[72:75], s24 offen sc1\n\t"
    "v_lshlrev_b32 v120, 16, v6\n\t"
    "v_and_b32 v121, s26, v6\n\t"
    "v_lshlrev_b32 v122, 16, v7\n\t"
    "v_and_b32 v123, s26, v7\n\t"
    "v_pk_add_f32 v[108:109], v[108:109], v[120:121]\n\t"
    "v_pk_add_f32 v[110:111], v[110:111], v[122:123]\n\t"
    "v_cvt_pk_bf16_f32 v36, v108, v109\n\t"
    "v_cvt_pk_bf16_f32 v37, v110, v111\n\t"
    "v_lshlrev_b32 v120, 16, v18\n\t"
    "v_and_b32 v121, s26, v18\n\t"
    "v_lshlrev_b32 v122, 16, v19\n\t"
    "v_and_b32 v123, s26, v19\n\t"
    "v_pk_add_f32 v[140:141], v[140:141], v[120:121]\n\t"
    "v_pk_add_f32 v[142:143], v[142:143], v[122:123]\n\t"
    "v_cvt_pk_bf16_f32 v38, v140, v141\n\t"
    "v_cvt_pk_bf16_f32 v39, v142, v143\n\t"
    "s_mov_b32 s24, 196608\n\t"
    "v_permlane16_swap_b32 v36, v38\n\t"
    "v_permlane16_swap_b32 v37, v39\n\t"
    "buffer_store_dwordx4 v[36:39], v245, s[72:75], s24 offen sc1\n\t"
    "s_waitcnt vmcnt(5)\n\t"
    "v_lshlrev_b32 v120, 16, v8\n\t"
    "v_and_b32 v121, s26, v8\n\t"
    "v_lshlrev_b32 v122, 16, v9\n\t"
    "v_and_b32 v123, s26, v9\n\t"
    "v_pk_add_f32 v[112:113], v[112:113], v[120:121]\n\t"
    "v_pk_add_f32 v[114:115], v[114:115], v[122:123]\n\t"
    "v_cvt_pk_bf16_f32 v40, v112, v113\n\t"
    "v_cvt_pk_bf16_f32 v41, v114, v115\n\t"
    "s_waitcnt vmcnt(4)\n\t"
    "v_lshlrev_b32 v120, 16, v20\n\t"
    "v_and_b32 v121, s26, v20\n\t"
    "v_lshlrev_b32 v122, 16, v21\n\t"
    "v_and_b32 v123, s26, v21\n\t"
    "v_pk_add_f32 v[144:145], v[144:145], v[120:121]\n\t"
    "v_pk_add_f32 v[146:147], v[146:147], v[122:123]\n\t"
    "v_cvt_pk_bf16_f32 v42, v144, v145\n\t"
    "v_cvt_pk_bf16_f32 v43, v146, v147\n\t"
    "s_mov_b32 s24, 262144\n\t"
    "v_permlane16_swap_b32 v40, v42\n\t"
    "v_permlane16_swap_b32 v41, v43\n\t"
    "buffer_store_dwordx4 v[40:43], v245, s[72:75], s24 offen sc1\n\t"
    "v_lshlrev_b32 v120, 16, v10\n\t"
    "v_and_b32 v121, s26, v10\n\t"
    "v_lshlrev_b32 v122, 16, v11\n\t"
    "v_and_b32 v123, s26, v11\n\t"
    "v_pk_add_f32 v[116:117], v[116:117], v[120:121]\n\t"
    "v_pk_add_f32 v[118:119], v[118:119], v[122:123]\n\t"
    "v_cvt_pk_bf16_f32 v44, v116, v117\n\t"
    "v_cvt_pk_bf16_f32 v45, v118, v119\n\t"
    "v_lshlrev_b32 v120, 16, v22\n\t"
    "v_and_b32 v121, s26, v22\n\t"
    "v_lshlrev_b32 v122, 16, v23\n\t"
    "v_and_b32 v123, s26, v23\n\t"
    "v_pk_add_f32 v[148:149], v[148:149], v[120:121]\n\t"
    "v_pk_add_f32 v[150:151], v[150:151], v[122:123]\n\t"
    "v_cvt_pk_bf16_f32 v46, v148, v149\n\t"
    "v_cvt_pk_bf16_f32 v47, v150, v151\n\t"
    "s_mov_b32 s24, 327680\n\t"
    "v_permlane16_swap_b32 v44, v46\n\t"
    "v_permlane16_swap_b32 v45, v47\n\t"
    "buffer_store_dwordx4 v[44:47], v245, s[72:75], s24 offen sc1\n\t"
    "s_nop 0\n\t"
    "s_endpgm\n\t"
    ".Lk_spin:\n\t"
    "buffer_load_dword v254, v230, s[68:71], 0 offen sc1\n\t"
    "s_sub_u32 s27, s27, 1\n\t"
    "s_cbranch_scc1 .Lk_spinto\n\t"
    "s_waitcnt vmcnt(0)\n\t"
    "v_sub_u32 v254, v254, v231\n\t"
    "v_cmp_gt_u32 vcc, s30, v254\n\t"
    "s_nop 1\n\t"
    "s_and_b32 s26, vcc_lo, s25\n\t"
    "s_cbranch_scc1 .Lk_spin\n\t"
    "s_branch .Lk_spinout\n\t"
    ".Lk_spinto:\n\t"
    "s_mov_b32 s27, 0\n\t"
    "s_waitcnt vmcnt(0)\n\t"
    ".Lk_spinout:\n\t"
    "s_cmp_eq_u32 s23, 0\n\t"
    "s_cbranch_scc1 .Lk_spinret_0\n\t"
    "s_cmp_eq_u32 s23, 1\n\t"
    "s_cbranch_scc1 .Lk_spinret_1\n\t"
    "s_cmp_eq_u32 s23, 2\n\t"
    "s_cbranch_scc1 .Lk_spinret_2\n\t"
    "s_cmp_eq_u32 s23, 3\n\t"
    "s_cbranch_scc1 .Lk_spinret_3\n\t"
    "s_cmp_eq_u32 s23, 4\n\t"
    "s_cbranch_scc1 .Lk_spinret_4\n\t"
    "s_cmp_eq_u32 s23, 5\n\t"
    "s_cbranch_scc1 .Lk_spinret_5\n\t"
    ".Lk_rare:\n\t"
    ".Lk_adj:\n\t"
    "s_nop 3\n\t"
    "v_max3_f32 v206, v96, v97, 0\n\t"
    "v_max3_f32 v206, v206, v98, v99\n\t"
    "v_max3_f32 v206, v206, v100, v101\n\t"
    "v_max3_f32 v206, v206, v102, v103\n\t"
    "v_max3_f32 v207, v104, v105, 0\n\t"
    "v_max3_f32 v207, v207, v106, v107\n\t"
    "v_max3_f32 v207, v207, v108, v109\n\t"
    "v_max3_f32 v207, v207, v110, v111\n\t"
    "v_max3_f32 v208, v112, v113, 0\n\t"
    "v_max3_f32 v208, v208, v114, v115\n\t"
    "v_max3_f32 v208, v208, v116, v117\n\t"
    "v_max3_f32 v208, v208, v118, v119\n\t"
    "v_max3_f32 v209, v120, v121, 0\n\t"
    "v_max3_f32 v209, v209, v122, v123\n\t"
    "v_max3_f32 v209, v209, v124, v125\n\t"
    "v_max3_f32 v209, v209, v126, v127\n\t"
    "v_max3_f32 v210, v128, v129, 0\n\t"
    "v_max3_f32 v210, v210, v130, v131\n\t"
    "v_max3_f32 v210, v210, v132, v133\n\t"
    "v_max3_f32 v210, v210, v134, v135\n\t"
    "v_max3_f32 v211, v136, v137, 0\n\t"
    "v_max3_f32 v211, v211, v138, v139\n\t"
    "v_max3_f32 v211, v211, v140, v141\n\t"
    "v_max3_f32 v211, v211, v142, v143\n\t"
    "v_mov_b32 v200, v206\n\t"
    "v_mov_b32 v201, v207\n\t"
    "v_mov_b32 v202, v208\n\t"
    "v_mov_b32 v203, v209\n\t"
    "v_mov_b32 v204, v210\n\t"
    "v_mov_b32 v205, v211\n\t"
    "v_permlane32_swap_b32 v206, v200\n\t"
    "v_permlane32_swap_b32 v207, v201\n\t"
    "v_permlane32_swap_b32 v208, v202\n\t"
    "v_permlane32_swap_b32 v209, v203\n\t"
    "v_permlane32_swap_b32 v210, v204\n\t"
    "v_permlane32_swap_b32 v211, v205\n\t"
    "v_max_f32 v206, v206, v200\n\t"
    "v_max_f32 v207, v207, v201\n\t"
    "v_max_f32 v208, v208, v202\n\t"
    "v_max_f32 v209, v209, v203\n\t"
    "v_max_f32 v210, v210, v204\n\t"
    "v_max_f32 v211, v211, v205\n\t"
    "v_mov_b32 v200, v206\n\t"
    "v_mov_b32 v201, v207\n\t"
    "v_mov_b32 v202, v208\n\t"
    "v_mov_b32 v203, v209\n\t"
    "v_mov_b32 v204, v210\n\t"
    "v_mov_b32 v205, v211\n\t"
    "v_permlane16_swap_b32 v206, v200\n\t"
    "v_permlane16_swap_b32 v207, v201\n\t"
    "v_permlane16_swap_b32 v208, v202\n\t"
    "v_permlane16_swap_b32 v209, v203\n\t"
    "v_permlane16_swap_b32 v210, v204\n\t"
    "v_permlane16_swap_b32 v211, v205\n\t"
    "v_max_f32 v206, v206, v200\n\t"
    "v_max_f32 v207, v207, v201\n\t"
    "v_max_f32 v208, v208, v202\n\t"
    "v_max_f32 v209, v209, v203\n\t"
    "v_max_f32 v210, v210, v204\n\t"
    "v_max_f32 v211, v211, v205\n\t"
    "v_sub_f32 v168, v168, v206\n\t"
    "v_sub_f32 v169, v169, v206\n\t"
    "v_sub_f32 v170, v170, v206\n\t"
    "v_sub_f32 v171, v171, v206\n\t"
    "v_exp_f32 v200, -v206\n\t"
    "v_sub_f32 v172, v172, v207\n\t"
    "v_sub_f32 v173, v173, v207\n\t"
    "v_sub_f32 v174, v174, v207\n\t"
    "v_sub_f32 v175, v175, v207\n\t"
    "v_exp_f32 v201, -v207\n\t"
    "v_sub_f32 v176, v176, v208\n\t"
    "v_sub_f32 v177, v177, v208\n\t"
    "v_sub_f32 v178, v178, v208\n\t"
    "v_sub_f32 v179, v179, v208\n\t"
    "v_exp_f32 v202, -v208\n\t"
    "v_sub_f32 v180, v180, v209\n\t"
    "v_sub_f32 v181, v181, v209\n\t"
    "v_sub_f32 v182, v182, v209\n\t"
    "v_sub_f32 v183, v183, v209\n\t"
    "v_exp_f32 v203, -v209\n\t"
    "v_sub_f32 v184, v184, v210\n\t"
    "v_sub_f32 v185, v185, v210\n\t"
    "v_sub_f32 v186, v186, v210\n\t"
    "v_sub_f32 v187, v187, v210\n\t"
    "v_exp_f32 v204, -v210\n\t"
    "v_sub_f32 v188, v188, v211\n\t"
    "v_sub_f32 v189, v189, v211\n\t"
    "v_sub_f32 v190, v190, v211\n\t"
    "v_sub_f32 v191, v191, v211\n\t"
    "v_exp_f32 v205, -v211\n\t"
    "v_sub_f32 v96, v96, v206\n\t"
    "v_sub_f32 v97, v97, v206\n\t"
    "v_sub_f32 v98, v98, v206\n\t"
    "v_sub_f32 v99, v99, v206\n\t"
    "v_sub_f32 v100, v100, v206\n\t"
    "v_sub_f32 v101, v101, v206\n\t"
    "v_sub_f32 v102, v102, v206\n\t"
    "v_sub_f32 v103, v103, v206\n\t"
    "v_sub_f32 v104, v104, v207\n\t"
    "v_sub_f32 v105, v105, v207\n\t"
    "v_sub_f32 v106, v106, v207\n\t"
    "v_sub_f32 v107, v107, v207\n\t"
    "v_sub_f32 v108, v108, v207\n\t"
    "v_sub_f32 v109, v109, v207\n\t"
    "v_sub_f32 v110, v110, v207\n\t"
    "v_sub_f32 v111, v111, v207\n\t"
    "v_sub_f32 v112, v112, v208\n\t"
    "v_sub_f32 v113, v113, v208\n\t"
    "v_sub_f32 v114, v114, v208\n\t"
    "v_sub_f32 v115, v115, v208\n\t"
    "v_sub_f32 v116, v116, v208\n\t"
    "v_sub_f32 v117, v117, v208\n\t"
    "v_sub_f32 v118, v118, v208\n\t"
    "v_sub_f32 v119, v119, v208\n\t"
    "v_sub_f32 v120, v120, v209\n\t"
    "v_sub_f32 v121, v121, v209\n\t"
    "v_sub_f32 v122, v122, v209\n\t"
    "v_sub_f32 v123, v123, v209\n\t"
    "v_sub_f32 v124, v124, v209\n\t"
    "v_sub_f32 v125, v125, v209\n\t"
    "v_sub_f32 v126, v126, v209\n\t"
    "v_sub_f32 v127, v127, v209\n\t"
    "v_sub_f32 v128, v128, v210\n\t"
    "v_sub_f32 v129, v129, v210\n\t"
    "v_sub_f32 v130, v130, v210\n\t"
    "v_sub_f32 v131, v131, v210\n\t"
    "v_sub_f32 v132, v132, v210\n\t"
    "v_sub_f32 v133, v133, v210\n\t"
    "v_sub_f32 v134, v134, v210\n\t"
    "v_sub_f32 v135, v135, v210\n\t"
    "v_sub_f32 v136, v136, v211\n\t"
    "v_sub_f32 v137, v137, v211\n\t"
    "v_sub_f32 v138, v138, v211\n\t"
    "v_sub_f32 v139, v139, v211\n\t"
    "v_sub_f32 v140, v140, v211\n\t"
    "v_sub_f32 v141, v141, v211\n\t"
    "v_sub_f32 v142, v142, v211\n\t"
    "v_sub_f32 v143, v143, v211\n\t"
    "v_mul_f32 v248, v248, v200\n\t"
    "v_mul_f32 v249, v249, v201\n\t"
    "v_mul_f32 v250, v250, v202\n\t"
    "v_mul_f32 v251, v251, v203\n\t"
    "v_mul_f32 v252, v252, v204\n\t"
    "v_mul_f32 v253, v253, v205\n\t"
    "s_mov_b32 s22, 1\n\t"
    "s_nop 3\n\t"
    "s_cmp_eq_u32 s23, 0\n\t"
    "s_cbranch_scc1 .Lk_adjret_e\n\t"
    "s_cmp_eq_u32 s23, 1\n\t"
    "s_cbranch_scc1 .Lk_adjret_o\n\t"
    "s_cmp_eq_u32 s23, 2\n\t"
    "s_cbranch_scc1 .Lk_adjret_p\n\t"
    "s_cmp_eq_u32 s23, 3\n\t"
    "s_cbranch_scc1 .Lk_adjret_f\n\t"
    "s_cmp_eq_u32 s23, 4\n\t"
    "s_cbranch_scc1 .Lk_adjret_g\n\t"
    "s_cmp_eq_u32 s23, 5\n\t"
    "s_cbranch_scc1 .Lk_adjret_s2\n\t"
    "s_cmp_eq_u32 s23, 6\n\t"
    "s_cbranch_scc1 .Lk_adjret_s3\n\t"
    ".Lk_resc:\n\t"
    "s_nop 15\n\t"
    "v_accvgpr_read_b32 v206, a0\n\t"
    "v_accvgpr_read_b32 v207, a1\n\t"
    "v_accvgpr_read_b32 v208, a2\n\t"
    "v_accvgpr_read_b32 v209, a3\n\t"
    "v_mul_f32 v206, v206, v200\n\t"
    "v_mul_f32 v207, v207, v200\n\t"
    "v_mul_f32 v208, v208, v200\n\t"
    "v_mul_f32 v209, v209, v200\n\t"
    "v_accvgpr_write_b32 a0, v206\n\t"
    "v_accvgpr_write_b32 a1, v207\n\t"
    "v_accvgpr_write_b32 a2, v208\n\t"
    "v_accvgpr_write_b32 a3, v209\n\t"
    "v_accvgpr_read_b32 v206, a4\n\t"
    "v_accvgpr_read_b32 v207, a5\n\t"
    "v_accvgpr_read_b32 v208, a6\n\t"
    "v_accvgpr_read_b32 v209, a7\n\t"
    "v_mul_f32 v206, v206, v201\n\t"
    "v_mul_f32 v207, v207, v201\n\t"
    "v_mul_f32 v208, v208, v201\n\t"
    "v_mul_f32 v209, v209, v201\n\t"
    "v_accvgpr_write_b32 a4, v206\n\t"
    "v_accvgpr_write_b32 a5, v207\n\t"
    "v_accvgpr_write_b32 a6, v208\n\t"
    "v_accvgpr_write_b32 a7, v209\n\t"
    "v_accvgpr_read_b32 v206, a8\n\t"
    "v_accvgpr_read_b32 v207, a9\n\t"
    "v_accvgpr_read_b32 v208, a10\n\t"
    "v_accvgpr_read_b32 v209, a11\n\t"
    "v_mul_f32 v206, v206, v202\n\t"
    "v_mul_f32 v207, v207, v202\n\t"
    "v_mul_f32 v208, v208, v202\n\t"
    "v_mul_f32 v209, v209, v202\n\t"
    "v_accvgpr_write_b32 a8, v206\n\t"
    "v_accvgpr_write_b32 a9, v207\n\t"
    "v_accvgpr_write_b32 a10, v208\n\t"
    "v_accvgpr_write_b32 a11, v209\n\t"
    "v_accvgpr_read_b32 v206, a12\n\t"
    "v_accvgpr_read_b32 v207, a13\n\t"
    "v_accvgpr_read_b32 v208, a14\n\t"
    "v_accvgpr_read_b32 v209, a15\n\t"
    "v_mul_f32 v206, v206, v203\n\t"
    "v_mul_f32 v207, v207, v203\n\t"
    "v_mul_f32 v208, v208, v203\n\t"
    "v_mul_f32 v209, v209, v203\n\t"
    "v_accvgpr_write_b32 a12, v206\n\t"
    "v_accvgpr_write_b32 a13, v207\n\t"
    "v_accvgpr_write_b32 a14, v208\n\t"
    "v_accvgpr_write_b32 a15, v209\n\t"
    "v_accvgpr_read_b32 v206, a16\n\t"
    "v_accvgpr_read_b32 v207, a17\n\t"
    "v_accvgpr_read_b32 v208, a18\n\t"
    "v_accvgpr_read_b32 v209, a19\n\t"
    "v_mul_f32 v206, v206, v204\n\t"
    "v_mul_f32 v207, v207, v204\n\t"
    "v_mul_f32 v208, v208, v204\n\t"
    "v_mul_f32 v209, v209, v204\n\t"
    "v_accvgpr_write_b32 a16, v206\n\t"
    "v_accvgpr_write_b32 a17, v207\n\t"
    "v_accvgpr_write_b32 a18, v208\n\t"
    "v_accvgpr_write_b32 a19, v209\n\t"
    "v_accvgpr_read_b32 v206, a20\n\t"
    "v_accvgpr_read_b32 v207, a21\n\t"
    "v_accvgpr_read_b32 v208, a22\n\t"
    "v_accvgpr_read_b32 v209, a23\n\t"
    "v_mul_f32 v206, v206, v205\n\t"
    "v_mul_f32 v207, v207, v205\n\t"
    "v_mul_f32 v208, v208, v205\n\t"
    "v_mul_f32 v209, v209, v205\n\t"
    "v_accvgpr_write_b32 a20, v206\n\t"
    "v_accvgpr_write_b32 a21, v207\n\t"
    "v_accvgpr_write_b32 a22, v208\n\t"
    "v_accvgpr_write_b32 a23, v209\n\t"
    "v_accvgpr_read_b32 v206, a24\n\t"
    "v_accvgpr_read_b32 v207, a25\n\t"
    "v_accvgpr_read_b32 v208, a26\n\t"
    "v_accvgpr_read_b32 v209, a27\n\t"
    "v_mul_f32 v206, v206, v200\n\t"
    "v_mul_f32 v207, v207, v200\n\t"
    "v_mul_f32 v208, v208, v200\n\t"
    "v_mul_f32 v209, v209, v200\n\t"
    "v_accvgpr_write_b32 a24, v206\n\t"
    "v_accvgpr_write_b32 a25, v207\n\t"
    "v_accvgpr_write_b32 a26, v208\n\t"
    "v_accvgpr_write_b32 a27, v209\n\t"
    "v_accvgpr_read_b32 v206, a28\n\t"
    "v_accvgpr_read_b32 v207, a29\n\t"
    "v_accvgpr_read_b32 v208, a30\n\t"
    "v_accvgpr_read_b32 v209, a31\n\t"
    "v_mul_f32 v206, v206, v201\n\t"
    "v_mul_f32 v207, v207, v201\n\t"
    "v_mul_f32 v208, v208, v201\n\t"
    "v_mul_f32 v209, v209, v201\n\t"
    "v_accvgpr_write_b32 a28, v206\n\t"
    "v_accvgpr_write_b32 a29, v207\n\t"
    "v_accvgpr_write_b32 a30, v208\n\t"
    "v_accvgpr_write_b32 a31, v209\n\t"
    "v_accvgpr_read_b32 v206, a32\n\t"
    "v_accvgpr_read_b32 v207, a33\n\t"
    "v_accvgpr_read_b32 v208, a34\n\t"
    "v_accvgpr_read_b32 v209, a35\n\t"
    "v_mul_f32 v206, v206, v202\n\t"
    "v_mul_f32 v207, v207, v202\n\t"
    "v_mul_f32 v208, v208, v202\n\t"
    "v_mul_f32 v209, v209, v202\n\t"
    "v_accvgpr_write_b32 a32, v206\n\t"
    "v_accvgpr_write_b32 a33, v207\n\t"
    "v_accvgpr_write_b32 a34, v208\n\t"
    "v_accvgpr_write_b32 a35, v209\n\t"
    "v_accvgpr_read_b32 v206, a36\n\t"
    "v_accvgpr_read_b32 v207, a37\n\t"
    "v_accvgpr_read_b32 v208, a38\n\t"
    "v_accvgpr_read_b32 v209, a39\n\t"
    "v_mul_f32 v206, v206, v203\n\t"
    "v_mul_f32 v207, v207, v203\n\t"
    "v_mul_f32 v208, v208, v203\n\t"
    "v_mul_f32 v209, v209, v203\n\t"
    "v_accvgpr_write_b32 a36, v206\n\t"
    "v_accvgpr_write_b32 a37, v207\n\t"
    "v_accvgpr_write_b32 a38, v208\n\t"
    "v_accvgpr_write_b32 a39, v209\n\t"
    "v_accvgpr_read_b32 v206, a40\n\t"
    "v_accvgpr_read_b32 v207, a41\n\t"
    "v_accvgpr_read_b32 v208, a42\n\t"
    "v_accvgpr_read_b32 v209, a43\n\t"
    "v_mul_f32 v206, v206, v204\n\t"
    "v_mul_f32 v207, v207, v204\n\t"
    "v_mul_f32 v208, v208, v204\n\t"
    "v_mul_f32 v209, v209, v204\n\t"
    "v_accvgpr_write_b32 a40, v206\n\t"
    "v_accvgpr_write_b32 a41, v207\n\t"
    "v_accvgpr_write_b32 a42, v208\n\t"
    "v_accvgpr_write_b32 a43, v209\n\t"
    "v_accvgpr_read_b32 v206, a44\n\t"
    "v_accvgpr_read_b32 v207, a45\n\t"
    "v_accvgpr_read_b32 v208, a46\n\t"
    "v_accvgpr_read_b32 v209, a47\n\t"
    "v_mul_f32 v206, v206, v205\n\t"
    "v_mul_f32 v207, v207, v205\n\t"
    "v_mul_f32 v208, v208, v205\n\t"
    "v_mul_f32 v209, v209, v205\n\t"
    "v_accvgpr_write_b32 a44, v206\n\t"
    "v_accvgpr_write_b32 a45, v207\n\t"
    "v_accvgpr_write_b32 a46, v208\n\t"
    "v_accvgpr_write_b32 a47, v209\n\t"
    "v_accvgpr_read_b32 v206, a48\n\t"
    "v_accvgpr_read_b32 v207, a49\n\t"
    "v_accvgpr_read_b32 v208, a50\n\t"
    "v_accvgpr_read_b32 v209, a51\n\t"
    "v_mul_f32 v206, v206, v200\n\t"
    "v_mul_f32 v207, v207, v200\n\t"
    "v_mul_f32 v208, v208, v200\n\t"
    "v_mul_f32 v209, v209, v200\n\t"
    "v_accvgpr_write_b32 a48, v206\n\t"
    "v_accvgpr_write_b32 a49, v207\n\t"
    "v_accvgpr_write_b32 a50, v208\n\t"
    "v_accvgpr_write_b32 a51, v209\n\t"
    "v_accvgpr_read_b32 v206, a52\n\t"
    "v_accvgpr_read_b32 v207, a53\n\t"
    "v_accvgpr_read_b32 v208, a54\n\t"
    "v_accvgpr_read_b32 v209, a55\n\t"
    "v_mul_f32 v206, v206, v201\n\t"
    "v_mul_f32 v207, v207, v201\n\t"
    "v_mul_f32 v208, v208, v201\n\t"
    "v_mul_f32 v209, v209, v201\n\t"
    "v_accvgpr_write_b32 a52, v206\n\t"
    "v_accvgpr_write_b32 a53, v207\n\t"
    "v_accvgpr_write_b32 a54, v208\n\t"
    "v_accvgpr_write_b32 a55, v209\n\t"
    "v_accvgpr_read_b32 v206, a56\n\t"
    "v_accvgpr_read_b32 v207, a57\n\t"
    "v_accvgpr_read_b32 v208, a58\n\t"
    "v_accvgpr_read_b32 v209, a59\n\t"
    "v_mul_f32 v206, v206, v202\n\t"
    "v_mul_f32 v207, v207, v202\n\t"
    "v_mul_f32 v208, v208, v202\n\t"
    "v_mul_f32 v209, v209, v202\n\t"
    "v_accvgpr_write_b32 a56, v206\n\t"
    "v_accvgpr_write_b32 a57, v207\n\t"
    "v_accvgpr_write_b32 a58, v208\n\t"
    "v_accvgpr_write_b32 a59, v209\n\t"
    "v_accvgpr_read_b32 v206, a60\n\t"
    "v_accvgpr_read_b32 v207, a61\n\t"
    "v_accvgpr_read_b32 v208, a62\n\t"
    "v_accvgpr_read_b32 v209, a63\n\t"
    "v_mul_f32 v206, v206, v203\n\t"
    "v_mul_f32 v207, v207, v203\n\t"
    "v_mul_f32 v208, v208, v203\n\t"
    "v_mul_f32 v209, v209, v203\n\t"
    "v_accvgpr_write_b32 a60, v206\n\t"
    "v_accvgpr_write_b32 a61, v207\n\t"
    "v_accvgpr_write_b32 a62, v208\n\t"
    "v_accvgpr_write_b32 a63, v209\n\t"
    "v_accvgpr_read_b32 v206, a64\n\t"
    "v_accvgpr_read_b32 v207, a65\n\t"
    "v_accvgpr_read_b32 v208, a66\n\t"
    "v_accvgpr_read_b32 v209, a67\n\t"
    "v_mul_f32 v206, v206, v204\n\t"
    "v_mul_f32 v207, v207, v204\n\t"
    "v_mul_f32 v208, v208, v204\n\t"
    "v_mul_f32 v209, v209, v204\n\t"
    "v_accvgpr_write_b32 a64, v206\n\t"
    "v_accvgpr_write_b32 a65, v207\n\t"
    "v_accvgpr_write_b32 a66, v208\n\t"
    "v_accvgpr_write_b32 a67, v209\n\t"
    "v_accvgpr_read_b32 v206, a68\n\t"
    "v_accvgpr_read_b32 v207, a69\n\t"
    "v_accvgpr_read_b32 v208, a70\n\t"
    "v_accvgpr_read_b32 v209, a71\n\t"
    "v_mul_f32 v206, v206, v205\n\t"
    "v_mul_f32 v207, v207, v205\n\t"
    "v_mul_f32 v208, v208, v205\n\t"
    "v_mul_f32 v209, v209, v205\n\t"
    "v_accvgpr_write_b32 a68, v206\n\t"
    "v_accvgpr_write_b32 a69, v207\n\t"
    "v_accvgpr_write_b32 a70, v208\n\t"
    "v_accvgpr_write_b32 a71, v209\n\t"
    "v_accvgpr_read_b32 v206, a72\n\t"
    "v_accvgpr_read_b32 v207, a73\n\t"
    "v_accvgpr_read_b32 v208, a74\n\t"
    "v_accvgpr_read_b32 v209, a75\n\t"
    "v_mul_f32 v206, v206, v200\n\t"
    "v_mul_f32 v207, v207, v200\n\t"
    "v_mul_f32 v208, v208, v200\n\t"
    "v_mul_f32 v209, v209, v200\n\t"
    "v_accvgpr_write_b32 a72, v206\n\t"
    "v_accvgpr_write_b32 a73, v207\n\t"
    "v_accvgpr_write_b32 a74, v208\n\t"
    "v_accvgpr_write_b32 a75, v209\n\t"
    "v_accvgpr_read_b32 v206, a76\n\t"
    "v_accvgpr_read_b32 v207, a77\n\t"
    "v_accvgpr_read_b32 v208, a78\n\t"
    "v_accvgpr_read_b32 v209, a79\n\t"
    "v_mul_f32 v206, v206, v201\n\t"
    "v_mul_f32 v207, v207, v201\n\t"
    "v_mul_f32 v208, v208, v201\n\t"
    "v_mul_f32 v209, v209, v201\n\t"
    "v_accvgpr_write_b32 a76, v206\n\t"
    "v_accvgpr_write_b32 a77, v207\n\t"
    "v_accvgpr_write_b32 a78, v208\n\t"
    "v_accvgpr_write_b32 a79, v209\n\t"
    "v_accvgpr_read_b32 v206, a80\n\t"
    "v_accvgpr_read_b32 v207, a81\n\t"
    "v_accvgpr_read_b32 v208, a82\n\t"
    "v_accvgpr_read_b32 v209, a83\n\t"
    "v_mul_f32 v206, v206, v202\n\t"
    "v_mul_f32 v207, v207, v202\n\t"
    "v_mul_f32 v208, v208, v202\n\t"
    "v_mul_f32 v209, v209, v202\n\t"
    "v_accvgpr_write_b32 a80, v206\n\t"
    "v_accvgpr_write_b32 a81, v207\n\t"
    "v_accvgpr_write_b32 a82, v208\n\t"
    "v_accvgpr_write_b32 a83, v209\n\t"
    "v_accvgpr_read_b32 v206, a84\n\t"
    "v_accvgpr_read_b32 v207, a85\n\t"
    "v_accvgpr_read_b32 v208, a86\n\t"
    "v_accvgpr_read_b32 v209, a87\n\t"
    "v_mul_f32 v206, v206, v203\n\t"
    "v_mul_f32 v207, v207, v203\n\t"
    "v_mul_f32 v208, v208, v203\n\t"
    "v_mul_f32 v209, v209, v203\n\t"
    "v_accvgpr_write_b32 a84, v206\n\t"
    "v_accvgpr_write_b32 a85, v207\n\t"
    "v_accvgpr_write_b32 a86, v208\n\t"
    "v_accvgpr_write_b32 a87, v209\n\t"
    "v_accvgpr_read_b32 v206, a88\n\t"
    "v_accvgpr_read_b32 v207, a89\n\t"
    "v_accvgpr_read_b32 v208, a90\n\t"
    "v_accvgpr_read_b32 v209, a91\n\t"
    "v_mul_f32 v206, v206, v204\n\t"
    "v_mul_f32 v207, v207, v204\n\t"
    "v_mul_f32 v208, v208, v204\n\t"
    "v_mul_f32 v209, v209, v204\n\t"
    "v_accvgpr_write_b32 a88, v206\n\t"
    "v_accvgpr_write_b32 a89, v207\n\t"
    "v_accvgpr_write_b32 a90, v208\n\t"
    "v_accvgpr_write_b32 a91, v209\n\t"
    "v_accvgpr_read_b32 v206, a92\n\t"
    "v_accvgpr_read_b32 v207, a93\n\t"
    "v_accvgpr_read_b32 v208, a94\n\t"
    "v_accvgpr_read_b32 v209, a95\n\t"
    "v_mul_f32 v206, v206, v205\n\t"
    "v_mul_f32 v207, v207, v205\n\t"
    "v_mul_f32 v208, v208, v205\n\t"
    "v_mul_f32 v209, v209, v205\n\t"
    "v_accvgpr_write_b32 a92, v206\n\t"
    "v_accvgpr_write_b32 a93, v207\n\t"
    "v_accvgpr_write_b32 a94, v208\n\t"
    "v_accvgpr_write_b32 a95, v209\n\t"
    "v_accvgpr_read_b32 v206, a96\n\t"
    "v_accvgpr_read_b32 v207, a97\n\t"
    "v_accvgpr_read_b32 v208, a98\n\t"
    "v_accvgpr_read_b32 v209, a99\n\t"
    "v_mul_f32 v206, v206, v200\n\t"
    "v_mul_f32 v207, v207, v200\n\t"
    "v_mul_f32 v208, v208, v200\n\t"
    "v_mul_f32 v209, v209, v200\n\t"
    "v_accvgpr_write_b32 a96, v206\n\t"
    "v_accvgpr_write_b32 a97, v207\n\t"
    "v_accvgpr_write_b32 a98, v208\n\t"
    "v_accvgpr_write_b32 a99, v209\n\t"
    "v_accvgpr_read_b32 v206, a100\n\t"
    "v_accvgpr_read_b32 v207, a101\n\t"
    "v_accvgpr_read_b32 v208, a102\n\t"
    "v_accvgpr_read_b32 v209, a103\n\t"
    "v_mul_f32 v206, v206, v201\n\t"
    "v_mul_f32 v207, v207, v201\n\t"
    "v_mul_f32 v208, v208, v201\n\t"
    "v_mul_f32 v209, v209, v201\n\t"
    "v_accvgpr_write_b32 a100, v206\n\t"
    "v_accvgpr_write_b32 a101, v207\n\t"
    "v_accvgpr_write_b32 a102, v208\n\t"
    "v_accvgpr_write_b32 a103, v209\n\t"
    "v_accvgpr_read_b32 v206, a104\n\t"
    "v_accvgpr_read_b32 v207, a105\n\t"
    "v_accvgpr_read_b32 v208, a106\n\t"
    "v_accvgpr_read_b32 v209, a107\n\t"
    "v_mul_f32 v206, v206, v202\n\t"
    "v_mul_f32 v207, v207, v202\n\t"
    "v_mul_f32 v208, v208, v202\n\t"
    "v_mul_f32 v209, v209, v202\n\t"
    "v_accvgpr_write_b32 a104, v206\n\t"
    "v_accvgpr_write_b32 a105, v207\n\t"
    "v_accvgpr_write_b32 a106, v208\n\t"
    "v_accvgpr_write_b32 a107, v209\n\t"
    "v_accvgpr_read_b32 v206, a108\n\t"
    "v_accvgpr_read_b32 v207, a109\n\t"
    "v_accvgpr_read_b32 v208, a110\n\t"
    "v_accvgpr_read_b32 v209, a111\n\t"
    "v_mul_f32 v206, v206, v203\n\t"
    "v_mul_f32 v207, v207, v203\n\t"
    "v_mul_f32 v208, v208, v203\n\t"
    "v_mul_f32 v209, v209, v203\n\t"
    "v_accvgpr_write_b32 a108, v206\n\t"
    "v_accvgpr_write_b32 a109, v207\n\t"
    "v_accvgpr_write_b32 a110, v208\n\t"
    "v_accvgpr_write_b32 a111, v209\n\t"
    "v_accvgpr_read_b32 v206, a112\n\t"
    "v_accvgpr_read_b32 v207, a113\n\t"
    "v_accvgpr_read_b32 v208, a114\n\t"
    "v_accvgpr_read_b32 v209, a115\n\t"
    "v_mul_f32 v206, v206, v204\n\t"
    "v_mul_f32 v207, v207, v204\n\t"
    "v_mul_f32 v208, v208, v204\n\t"
    "v_mul_f32 v209, v209, v204\n\t"
    "v_accvgpr_write_b32 a112, v206\n\t"
    "v_accvgpr_write_b32 a113, v207\n\t"
    "v_accvgpr_write_b32 a114, v208\n\t"
    "v_accvgpr_write_b32 a115, v209\n\t"
    "v_accvgpr_read_b32 v206, a116\n\t"
    "v_accvgpr_read_b32 v207, a117\n\t"
    "v_accvgpr_read_b32 v208, a118\n\t"
    "v_accvgpr_read_b32 v209, a119\n\t"
    "v_mul_f32 v206, v206, v205\n\t"
    "v_mul_f32 v207, v207, v205\n\t"
    "v_mul_f32 v208, v208, v205\n\t"
    "v_mul_f32 v209, v209, v205\n\t"
    "v_accvgpr_write_b32 a116, v206\n\t"
    "v_accvgpr_write_b32 a117, v207\n\t"
    "v_accvgpr_write_b32 a118, v208\n\t"
    "v_accvgpr_write_b32 a119, v209\n\t"
    "v_accvgpr_read_b32 v206, a120\n\t"
    "v_accvgpr_read_b32 v207, a121\n\t"
    "v_accvgpr_read_b32 v208, a122\n\t"
    "v_accvgpr_read_b32 v209, a123\n\t"
    "v_mul_f32 v206, v206, v200\n\t"
    "v_mul_f32 v207, v207, v200\n\t"
    "v_mul_f32 v208, v208, v200\n\t"
    "v_mul_f32 v209, v209, v200\n\t"
    "v_accvgpr_write_b32 a120, v206\n\t"
    "v_accvgpr_write_b32 a121, v207\n\t"
    "v_accvgpr_write_b32 a122, v208\n\t"
    "v_accvgpr_write_b32 a123, v209\n\t"
    "v_accvgpr_read_b32 v206, a124\n\t"
    "v_accvgpr_read_b32 v207, a125\n\t"
    "v_accvgpr_read_b32 v208, a126\n\t"
    "v_accvgpr_read_b32 v209, a127\n\t"
    "v_mul_f32 v206, v206, v201\n\t"
    "v_mul_f32 v207, v207, v201\n\t"
    "v_mul_f32 v208, v208, v201\n\t"
    "v_mul_f32 v209, v209, v201\n\t"
    "v_accvgpr_write_b32 a124, v206\n\t"
    "v_accvgpr_write_b32 a125, v207\n\t"
    "v_accvgpr_write_b32 a126, v208\n\t"
    "v_accvgpr_write_b32 a127, v209\n\t"
    "v_accvgpr_read_b32 v206, a128\n\t"
    "v_accvgpr_read_b32 v207, a129\n\t"
    "v_accvgpr_read_b32 v208, a130\n\t"
    "v_accvgpr_read_b32 v209, a131\n\t"
    "v_mul_f32 v206, v206, v202\n\t"
    "v_mul_f32 v207, v207, v202\n\t"
    "v_mul_f32 v208, v208, v202\n\t"
    "v_mul_f32 v209, v209, v202\n\t"
    "v_accvgpr_write_b32 a128, v206\n\t"
    "v_accvgpr_write_b32 a129, v207\n\t"
    "v_accvgpr_write_b32 a130, v208\n\t"
    "v_accvgpr_write_b32 a131, v209\n\t"
    "v_accvgpr_read_b32 v206, a132\n\t"
    "v_accvgpr_read_b32 v207, a133\n\t"
    "v_accvgpr_read_b32 v208, a134\n\t"
    "v_accvgpr_read_b32 v209, a135\n\t"
    "v_mul_f32 v206, v206, v203\n\t"
    "v_mul_f32 v207, v207, v203\n\t"
    "v_mul_f32 v208, v208, v203\n\t"
    "v_mul_f32 v209, v209, v203\n\t"
    "v_accvgpr_write_b32 a132, v206\n\t"
    "v_accvgpr_write_b32 a133, v207\n\t"
    "v_accvgpr_write_b32 a134, v208\n\t"
    "v_accvgpr_write_b32 a135, v209\n\t"
    "v_accvgpr_read_b32 v206, a136\n\t"
    "v_accvgpr_read_b32 v207, a137\n\t"
    "v_accvgpr_read_b32 v208, a138\n\t"
    "v_accvgpr_read_b32 v209, a139\n\t"
    "v_mul_f32 v206, v206, v204\n\t"
    "v_mul_f32 v207, v207, v204\n\t"
    "v_mul_f32 v208, v208, v204\n\t"
    "v_mul_f32 v209, v209, v204\n\t"
    "v_accvgpr_write_b32 a136, v206\n\t"
    "v_accvgpr_write_b32 a137, v207\n\t"
    "v_accvgpr_write_b32 a138, v208\n\t"
    "v_accvgpr_write_b32 a139, v209\n\t"
    "v_accvgpr_read_b32 v206, a140\n\t"
    "v_accvgpr_read_b32 v207, a141\n\t"
    "v_accvgpr_read_b32 v208, a142\n\t"
    "v_accvgpr_read_b32 v209, a143\n\t"
    "v_mul_f32 v206, v206, v205\n\t"
    "v_mul_f32 v207, v207, v205\n\t"
    "v_mul_f32 v208, v208, v205\n\t"
    "v_mul_f32 v209, v209, v205\n\t"
    "v_accvgpr_write_b32 a140, v206\n\t"
    "v_accvgpr_write_b32 a141, v207\n\t"
    "v_accvgpr_write_b32 a142, v208\n\t"
    "v_accvgpr_write_b32 a143, v209\n\t"
    "v_accvgpr_read_b32 v206, a144\n\t"
    "v_accvgpr_read_b32 v207, a145\n\t"
    "v_accvgpr_read_b32 v208, a146\n\t"
    "v_accvgpr_read_b32 v209, a147\n\t"
    "v_mul_f32 v206, v206, v200\n\t"
    "v_mul_f32 v207, v207, v200\n\t"
    "v_mul_f32 v208, v208, v200\n\t"
    "v_mul_f32 v209, v209, v200\n\t"
    "v_accvgpr_write_b32 a144, v206\n\t"
    "v_accvgpr_write_b32 a145, v207\n\t"
    "v_accvgpr_write_b32 a146, v208\n\t"
    "v_accvgpr_write_b32 a147, v209\n\t"
    "v_accvgpr_read_b32 v206, a148\n\t"
    "v_accvgpr_read_b32 v207, a149\n\t"
    "v_accvgpr_read_b32 v208, a150\n\t"
    "v_accvgpr_read_b32 v209, a151\n\t"
    "v_mul_f32 v206, v206, v201\n\t"
    "v_mul_f32 v207, v207, v201\n\t"
    "v_mul_f32 v208, v208, v201\n\t"
    "v_mul_f32 v209, v209, v201\n\t"
    "v_accvgpr_write_b32 a148, v206\n\t"
    "v_accvgpr_write_b32 a149, v207\n\t"
    "v_accvgpr_write_b32 a150, v208\n\t"
    "v_accvgpr_write_b32 a151, v209\n\t"
    "v_accvgpr_read_b32 v206, a152\n\t"
    "v_accvgpr_read_b32 v207, a153\n\t"
    "v_accvgpr_read_b32 v208, a154\n\t"
    "v_accvgpr_read_b32 v209, a155\n\t"
    "v_mul_f32 v206, v206, v202\n\t"
    "v_mul_f32 v207, v207, v202\n\t"
    "v_mul_f32 v208, v208, v202\n\t"
    "v_mul_f32 v209, v209, v202\n\t"
    "v_accvgpr_write_b32 a152, v206\n\t"
    "v_accvgpr_write_b32 a153, v207\n\t"
    "v_accvgpr_write_b32 a154, v208\n\t"
    "v_accvgpr_write_b32 a155, v209\n\t"
    "v_accvgpr_read_b32 v206, a156\n\t"
    "v_accvgpr_read_b32 v207, a157\n\t"
    "v_accvgpr_read_b32 v208, a158\n\t"
    "v_accvgpr_read_b32 v209, a159\n\t"
    "v_mul_f32 v206, v206, v203\n\t"
    "v_mul_f32 v207, v207, v203\n\t"
    "v_mul_f32 v208, v208, v203\n\t"
    "v_mul_f32 v209, v209, v203\n\t"
    "v_accvgpr_write_b32 a156, v206\n\t"
    "v_accvgpr_write_b32 a157, v207\n\t"
    "v_accvgpr_write_b32 a158, v208\n\t"
    "v_accvgpr_write_b32 a159, v209\n\t"
    "v_accvgpr_read_b32 v206, a160\n\t"
    "v_accvgpr_read_b32 v207, a161\n\t"
    "v_accvgpr_read_b32 v208, a162\n\t"
    "v_accvgpr_read_b32 v209, a163\n\t"
    "v_mul_f32 v206, v206, v204\n\t"
    "v_mul_f32 v207, v207, v204\n\t"
    "v_mul_f32 v208, v208, v204\n\t"
    "v_mul_f32 v209, v209, v204\n\t"
    "v_accvgpr_write_b32 a160, v206\n\t"
    "v_accvgpr_write_b32 a161, v207\n\t"
    "v_accvgpr_write_b32 a162, v208\n\t"
    "v_accvgpr_write_b32 a163, v209\n\t"
    "v_accvgpr_read_b32 v206, a164\n\t"
    "v_accvgpr_read_b32 v207, a165\n\t"
    "v_accvgpr_read_b32 v208, a166\n\t"
    "v_accvgpr_read_b32 v209, a167\n\t"
    "v_mul_f32 v206, v206, v205\n\t"
    "v_mul_f32 v207, v207, v205\n\t"
    "v_mul_f32 v208, v208, v205\n\t"
    "v_mul_f32 v209, v209, v205\n\t"
    "v_accvgpr_write_b32 a164, v206\n\t"
    "v_accvgpr_write_b32 a165, v207\n\t"
    "v_accvgpr_write_b32 a166, v208\n\t"
    "v_accvgpr_write_b32 a167, v209\n\t"
    "v_accvgpr_read_b32 v206, a168\n\t"
    "v_accvgpr_read_b32 v207, a169\n\t"
    "v_accvgpr_read_b32 v208, a170\n\t"
    "v_accvgpr_read_b32 v209, a171\n\t"
    "v_mul_f32 v206, v206, v200\n\t"
    "v_mul_f32 v207, v207, v200\n\t"
    "v_mul_f32 v208, v208, v200\n\t"
    "v_mul_f32 v209, v209, v200\n\t"
    "v_accvgpr_write_b32 a168, v206\n\t"
    "v_accvgpr_write_b32 a169, v207\n\t"
    "v_accvgpr_write_b32 a170, v208\n\t"
    "v_accvgpr_write_b32 a171, v209\n\t"
    "v_accvgpr_read_b32 v206, a172\n\t"
    "v_accvgpr_read_b32 v207, a173\n\t"
    "v_accvgpr_read_b32 v208, a174\n\t"
    "v_accvgpr_read_b32 v209, a175\n\t"
    "v_mul_f32 v206, v206, v201\n\t"
    "v_mul_f32 v207, v207, v201\n\t"
    "v_mul_f32 v208, v208, v201\n\t"
    "v_mul_f32 v209, v209, v201\n\t"
    "v_accvgpr_write_b32 a172, v206\n\t"
    "v_accvgpr_write_b32 a173, v207\n\t"
    "v_accvgpr_write_b32 a174, v208\n\t"
    "v_accvgpr_write_b32 a175, v209\n\t"
    "v_accvgpr_read_b32 v206, a176\n\t"
    "v_accvgpr_read_b32 v207, a177\n\t"
    "v_accvgpr_read_b32 v208, a178\n\t"
    "v_accvgpr_read_b32 v209, a179\n\t"
    "v_mul_f32 v206, v206, v202\n\t"
    "v_mul_f32 v207, v207, v202\n\t"
    "v_mul_f32 v208, v208, v202\n\t"
    "v_mul_f32 v209, v209, v202\n\t"
    "v_accvgpr_write_b32 a176, v206\n\t"
    "v_accvgpr_write_b32 a177, v207\n\t"
    "v_accvgpr_write_b32 a178, v208\n\t"
    "v_accvgpr_write_b32 a179, v209\n\t"
    "v_accvgpr_read_b32 v206, a180\n\t"
    "v_accvgpr_read_b32 v207, a181\n\t"
    "v_accvgpr_read_b32 v208, a182\n\t"
    "v_accvgpr_read_b32 v209, a183\n\t"
    "v_mul_f32 v206, v206, v203\n\t"
    "v_mul_f32 v207, v207, v203\n\t"
    "v_mul_f32 v208, v208, v203\n\t"
    "v_mul_f32 v209, v209, v203\n\t"
    "v_accvgpr_write_b32 a180, v206\n\t"
    "v_accvgpr_write_b32 a181, v207\n\t"
    "v_accvgpr_write_b32 a182, v208\n\t"
    "v_accvgpr_write_b32 a183, v209\n\t"
    "v_accvgpr_read_b32 v206, a184\n\t"
    "v_accvgpr_read_b32 v207, a185\n\t"
    "v_accvgpr_read_b32 v208, a186\n\t"
    "v_accvgpr_read_b32 v209, a187\n\t"
    "v_mul_f32 v206, v206, v204\n\t"
    "v_mul_f32 v207, v207, v204\n\t"
    "v_mul_f32 v208, v208, v204\n\t"
    "v_mul_f32 v209, v209, v204\n\t"
    "v_accvgpr_write_b32 a184, v206\n\t"
    "v_accvgpr_write_b32 a185, v207\n\t"
    "v_accvgpr_write_b32 a186, v208\n\t"
    "v_accvgpr_write_b32 a187, v209\n\t"
    "v_accvgpr_read_b32 v206, a188\n\t"
    "v_accvgpr_read_b32 v207, a189\n\t"
    "v_accvgpr_read_b32 v208, a190\n\t"
    "v_accvgpr_read_b32 v209, a191\n\t"
    "v_mul_f32 v206, v206, v205\n\t"
    "v_mul_f32 v207, v207, v205\n\t"
    "v_mul_f32 v208, v208, v205\n\t"
    "v_mul_f32 v209, v209, v205\n\t"
    "v_accvgpr_write_b32 a188, v206\n\t"
    "v_accvgpr_write_b32 a189, v207\n\t"
    "v_accvgpr_write_b32 a190, v208\n\t"
    "v_accvgpr_write_b32 a191, v209\n\t"
    "s_mov_b32 s22, 0\n\t"
    "s_nop 3\n\t"
    "s_cmp_eq_u32 s23, 0\n\t"
    "s_cbranch_scc1 .Lk_rescret_e\n\t"
    "s_cmp_eq_u32 s23, 1\n\t"
    "s_cbranch_scc1 .Lk_rescret_o\n\t"
    "s_cmp_eq_u32 s23, 2\n\t"
    "s_cbranch_scc1 .Lk_rescret_p\n\t"
    "s_cmp_eq_u32 s23, 3\n\t"
    "s_cbranch_scc1 .Lk_rescret_f\n\t"
    "s_cmp_eq_u32 s23, 4\n\t"
    "s_cbranch_scc1 .Lk_rescret_g\n\t"
    "s_cmp_eq_u32 s23, 5\n\t"
    "s_cbranch_scc1 .Lk_rescret_s2\n\t"
    "s_cmp_eq_u32 s23, 6\n\t"
    "s_cbranch_scc1 .Lk_rescret_s3\n\t"
    ".Lk_end:\n\t"
    :
    : "{s[36:39]}"(srdX), "{s[40:43]}"(srdW), "{s[44:47]}"(srdK), "{s[48:51]}"(srdV), "{s[52:55]}"(srdOW), "{s[56:59]}"(m0s),
      "{s[60:63]}"(ofs), "{s[64:67]}"(srdWO), "{s[68:71]}"(srdF), "{s[72:75]}"(srdOut), "{s[76:79]}"(of2),
      "{v[192:199]}"(va), "{v[200:207]}"(vx), "{v[208:211]}"(vw), "{v[212:215]}"(vkd), "{v[216:223]}"(vk), "{v[224:231]}"(vv),
      "{v[240:247]}"(vo), "{v239}"(sink)
    : "v0","v1","v2","v3","v4","v5","v6","v7","v8","v9","v10","v11","v12","v13","v14","v15","v16","v17","v18","v19","v20","v21","v22","v23","v24","v25","v26","v27","v28","v29","v30","v31","v32","v33","v34","v35","v36","v37","v38","v39","v40","v41","v42","v43","v44","v45","v46","v47","v48","v49","v50","v51","v52","v53","v54","v55","v56","v57","v58","v59","v60","v61","v62","v63","v64","v65","v66","v67","v68","v69","v70","v71","v72","v73","v74","v75","v76","v77","v78","v79","v80","v81","v82","v83","v84","v85","v86","v87","v88","v89","v90","v91","v92","v93","v94","v95","v96","v97","v98","v99","v100","v101","v102","v103","v104","v105","v106","v107","v108","v109","v110","v111","v112","v113","v114","v115","v116","v117","v118","v119","v120","v121","v122","v123","v124","v125","v126","v127","v128","v129","v130","v131","v132","v133","v134","v135","v136","v137","v138","v139","v140","v141","v142","v143","v144","v145","v146","v147","v148","v149","v150","v151","v152","v153","v154","v155","v156","v157","v158","v159","v160","v161","v162","v163","v164","v165","v166","v167","v168","v169","v170","v171","v172","v173","v174","v175","v176","v177","v178","v179","v180","v181","v182","v183","v184","v185","v186","v187","v188","v189","v190","v191","v232","v233","v234","v235","v236","v237","v238","v248","v249","v250","v251","v252","v253","v254","v255","a0","a1","a2","a3","a4","a5","a6","a7","a8","a9","a10","a11","a12","a13","a14","a15","a16","a17","a18","a19","a20","a21","a22","a23","a24","a25","a26","a27","a28","a29","a30","a31","a32","a33","a34","a35","a36","a37","a38","a39","a40","a41","a42","a43","a44","a45","a46","a47","a48","a49","a50","a51","a52","a53","a54","a55","a56","a57","a58","a59","a60","a61","a62","a63","a64","a65","a66","a67","a68","a69","a70","a71","a72","a73","a74","a75","a76","a77","a78","a79","a80","a81","a82","a83","a84","a85","a86","a87","a88","a89","a90","a91","a92","a93","a94","a95","a96","a97","a98","a99","a100","a101","a102","a103","a104","a105","a106","a107","a108","a109","a110","a111","a112","a113","a114","a115","a116","a117","a118","a119","a120","a121","a122","a123","a124","a125","a126","a127","a128","a129","a130","a131","a132","a133","a134","a135","a136","a137","a138","a139","a140","a141","a142","a143","a144","a145","a146","a147","a148","a149","a150","a151","a152","a153","a154","a155","a156","a157","a158","a159","a160","a161","a162","a163","a164","a165","a166","a167","a168","a169","a170","a171","a172","a173","a174","a175","a176","a177","a178","a179","a180","a181","a182","a183","a184","a185","a186","a187","a188","a189","a190","a191","a192","a193","a194","a195","a196","a197","a198","a199","a200","a201","a202","a203","a204","a205","a206","a207","a208","a209","a210","a211","a212","a213","a214","a215","a216","a217","a218","a219","a220","a221","a222","a223","a224","a225","a226","a227","a228","a229","a230","a231","a232","a233","a234","a235","a236","a237","a238","a239","a240","a241","a242","a243","a244","a245","a246","a247","a248","a249","a250","a251","a252","a253","a254","a255","s11","s12","s13","s14","s15","s16","s17","s18","s19","s20","s21","s22","s23","s24","s25","s26","s27","s28","s29","s30","s31","vcc","scc","memory");
}

extern "C" void kernel_launch(void* const* d_in, const int* in_sizes, int n_in, void* d_out, int out_size, void* d_ws,
                              size_t ws_size, hipStream_t stream) {
  fused_attn_kernel<<<256, 256, 0, stream>>>((const char*)d_in[0], (const char*)d_in[1], (const char*)d_in[2], (const char*)d_in[3],
                                             (const char*)d_in[4], (const char*)d_in[5], (const char*)d_in[6], (const char*)d_in[7],
                                             (const char*)d_in[8], (const char*)d_in[9], (char*)d_out, (char*)d_ws);
}
```
